```python
import torch

def _softmax(x, axis=-1):
    return torch.softmax(x, dim=axis)
def _gelu(x):
    return torch.nn.functional.gelu(x, approximate="tanh")
def _silu(x):
    return torch.nn.functional.silu(x)
def _sigmoid(x):
    return torch.sigmoid(x)
def _layernorm(x, axis=-1, eps=1e-5):
    m = x.mean(dim=axis, keepdim=True)
    v = x.var(dim=axis, keepdim=True, unbiased=False)
    return (x - m) / torch.sqrt(v + eps)
def _scatter_add(tgt, idx, src, axis=0):
    return tgt.clone().index_add_(axis, idx, src)
def _segment_sum(src, seg, n):
    out = torch.zeros((n,) + src.shape[1:], dtype=src.dtype)
    return out.index_add_(0, seg, src)
def _pad(x, pw):
    flat = tuple(v for pair in reversed(pw) for v in pair)
    return torch.nn.functional.pad(x, flat)

kernel_name = 'concat_heads__softmax_full_mha__sliding_block__prompt'

def setup_inputs():

    _w = (lambda a: (lambda t: t.to(torch.bfloat16) if t.is_floating_point() else t)(torch.as_tensor(a)))
    def _A_setup():
        B, S, D = 1, 2048, 640
        return {
            "x": _w((lambda *s: torch.randn(*s, dtype=torch.bfloat16) * 0.2)(B, S, D)),
            "wqkv": _w((lambda *s: torch.randn(*s, dtype=torch.bfloat16) * 0.4)(D, 3 * D)),
            "wo": _w((lambda *s: torch.randn(*s, dtype=torch.bfloat16) * 0.1)(D, D)),
        }
    _A_in = _A_setup()

    def _B_setup():
        B, S, D = 1, 2048, 640
        return {
            "x": _w((lambda *s: torch.randn(*s, dtype=torch.bfloat16) * 0.4)(B, S, D)),
            "wqkv": _w((lambda *s: torch.randn(*s, dtype=torch.bfloat16) * 0.4)(D, 3 * D)),
            "wo": _w((lambda *s: torch.randn(*s, dtype=torch.bfloat16) * 0.05)(D, D)),
        }
    _B_in = _B_setup()

    return {
        "A_x": _A_in["x"],
        "A_wqkv": _A_in["wqkv"],
        "A_wo": _A_in["wo"],
        "B_x": _B_in["x"],
        "B_wqkv": _B_in["wqkv"],
        "B_wo": _B_in["wo"],
    }

def reference(A_x, A_wqkv, A_wo, B_x, B_wqkv, B_wo):
    def _A_ref(x, wqkv, wo):
        B, S, D = x.shape
        H = 32; Dh = D // H
        qkv = (lambda x, *d: x.permute(*d))((x @ wqkv).reshape(B, S, 3, H, Dh), 2, 0, 3, 1, 4)
        q, k, v = qkv[0], qkv[1], qkv[2]
        scale = Dh ** -0.5
        scores = torch.einsum("bhqd,bhkd->bhqk", q, k) * scale
        p = _softmax(scores, axis=-1)
        o = torch.einsum("bhqk,bhkd->bhqd", p, v)
        return (lambda x, *d: x.permute(*d))(o, 0, 2, 1, 3).reshape(B, S, D) @ wo

    def _B_ref(x, wqkv, wo):
        B, S, D = x.shape
        H = 32; Dh = D // H; W = 256
        qkv = (lambda x, *d: x.permute(*d))((x @ wqkv).reshape(B, S, 3, H, Dh), 2, 0, 3, 1, 4)
        q, k, v = qkv[0], qkv[1], qkv[2]
        scores = torch.einsum("bhqd,bhkd->bhqk", q, k) * (Dh ** -0.5)
        idx = torch.arange(S)
        mask = (idx[:, None] - idx[None, :] >= 0) & (idx[:, None] - idx[None, :] < W)
        scores = torch.where(mask[None, None], scores, -1e9)
        p = _softmax(scores, axis=-1)
        o = torch.einsum("bhqk,bhkd->bhqd", p, v)
        return (lambda x, *d: x.permute(*d))(o, 0, 2, 1, 3).reshape(B, S, D) @ wo

    a = _A_ref(A_x, A_wqkv, A_wo)
    b = _B_ref(B_x, B_wqkv, B_wo)
    return torch.concatenate([a, b], axis=-1)
```

```cpp
#include <hip/hip_runtime.h>
#include <hip/hip_bf16.h>
#include <stdint.h>

typedef __attribute__((ext_vector_type(8))) __bf16 bf16x8;
typedef __attribute__((ext_vector_type(16))) __bf16 bf16x16;
typedef __attribute__((ext_vector_type(4))) short s16x4;
typedef __attribute__((ext_vector_type(8))) short s16x8;
typedef __attribute__((ext_vector_type(2))) unsigned u32x2;
typedef __attribute__((ext_vector_type(4))) unsigned u32x4;
typedef __attribute__((ext_vector_type(4))) float f32x4;
typedef __attribute__((ext_vector_type(8))) float f32x8;
typedef __attribute__((ext_vector_type(16))) float f32x16;
typedef __attribute__((address_space(3))) const char* lds_cptr;

constexpr int SEQ = 2048, DM = 640, NQKV = 1920;
constexpr int TILE_B = 2624, K2_OFF = 1024, V_OFF = 1280, VG_B = 336;
constexpr int HEAD_B = 64 * TILE_B;
constexpr int QT_B = 1280, QHEAD_B = 64 * QT_B;
constexpr size_t KV_WS = 0, KV_SZ = (size_t)64 * HEAD_B;
constexpr size_t Q_WS = KV_WS + KV_SZ, Q_SZ = (size_t)64 * QHEAD_B;
constexpr size_t O_WS = Q_WS + Q_SZ;
constexpr float QSCALE = 0.22360679774997896f * 1.4426950408889634f;

#define SBAR() __builtin_amdgcn_sched_barrier(0)
#define PIN(x) asm volatile("" : "+v"(x))
#define WAITV_BAR(N) asm volatile("s_waitcnt vmcnt(" #N ") lgkmcnt(0)\n\ts_barrier" ::: "memory")

__device__ __forceinline__ void glds16(const void* sbase, unsigned voff, unsigned lds) {
  unsigned keep;
  asm volatile("s_mov_b32 %0, m0\n\ts_mov_b32 m0, %3\n\ts_nop 0\n\tglobal_load_lds_dwordx4 %1, %2\n\ts_mov_b32 m0, %0"
               : "=&s"(keep) : "v"(voff), "s"(sbase), "s"(lds) : "memory");
}
__device__ __forceinline__ unsigned pk_bf16(float lo, float hi) {
  typedef __attribute__((ext_vector_type(2))) float f32x2_;
  typedef __attribute__((ext_vector_type(2))) __bf16 bf16x2_;
  const f32x2_ v = {lo, hi};
  return __builtin_bit_cast(unsigned, __builtin_convertvector(v, bf16x2_));
}
__device__ __forceinline__ bf16x8 lds_b128(lds_cptr p) { return *(const __attribute__((address_space(3))) bf16x8*)p; }
__device__ __forceinline__ s16x4 lds_b64(lds_cptr p) { return *(const __attribute__((address_space(3))) s16x4*)p; }
__device__ __forceinline__ s16x4 lds_tr(lds_cptr p) {
  return __builtin_amdgcn_ds_read_tr16_b64_v4i16((__attribute__((address_space(3))) s16x4*)p);
}
__device__ __forceinline__ bf16x8 cat8(s16x4 a, s16x4 b) {
  s16x8 v = {a[0], a[1], a[2], a[3], b[0], b[1], b[2], b[3]};
  return __builtin_bit_cast(bf16x8, v);
}
#define MFMA32(a, b, c) __builtin_amdgcn_mfma_f32_32x32x16_bf16(a, b, c, 0, 0, 0)
#define MFMA32K8(a, b, c) __builtin_amdgcn_mfma_f32_32x32x8bf16_1k(a, b, c, 0, 0, 0)
#define MFMA16(a, b, c) __builtin_amdgcn_mfma_f32_16x16x32_bf16(a, b, c, 0, 0, 0)

constexpr int G1_SLOT = 49152;

template <bool VT>
__device__ __forceinline__ void g1_body(const char* __restrict__ X, const char* __restrict__ W, char* __restrict__ ws,
                                        char* lds, int br, int mt, int nt) {
  const int tid = threadIdx.x, lane = tid & 63;
  const int w = __builtin_amdgcn_readfirstlane(tid >> 6);
  const int wm = w & 3, wn = w >> 2;
  const int m0 = mt * 256, n0 = nt * 128;
  const unsigned lds0 = (unsigned)(uintptr_t)lds;
  const unsigned xoff0 = (lane >> 3) * 1280 + (((lane & 7) ^ (lane >> 4)) * 16);
  const unsigned xoff1 = (lane >> 3) * 1280 + (((lane & 7) ^ (4 + (lane >> 4))) * 16);
  const unsigned wrow = (lane >> 2) & 7;
  const unsigned woff = wrow * 3840 + (lane >> 5) * 64 + (((lane & 3) ^ ((2 * w + (wrow >> 2)) & 3)) * 16);
  const char* xb = X + (size_t)(m0 + 32 * w) * 1280;
  const char* wb = W + (size_t)(8 * w) * 3840 + n0 * 2;
  auto stage = [&](int kt, int sl) {
    const unsigned d = lds0 + sl * G1_SLOT;
    const char* xs = xb + kt * 128;
    glds16(xs, xoff0, d + w * 4096);
    glds16(xs + 8 * 1280, xoff1, d + w * 4096 + 1024);
    glds16(xs + 16 * 1280, xoff0, d + w * 4096 + 2048);
    glds16(xs + 24 * 1280, xoff1, d + w * 4096 + 3072);
    const char* wsrc = wb + (size_t)kt * 64 * 3840;
    glds16(wsrc, woff, d + 32768 + w * 2048);
    glds16(wsrc + 128, woff, d + 32768 + w * 2048 + 1024);
  };
  stage(0, 0); stage(1, 1); stage(2, 2);
  const int r32 = lane & 31, h = lane >> 5;
  const unsigned xrow = wm * 64 + r32;
  unsigned xa[4];
#pragma unroll
  for (int ks = 0; ks < 4; ++ks) xa[ks] = xrow * 128 + (((2 * ks + h) ^ ((xrow >> 1) & 7)) * 16);
  const int blk = (lane >> 4) & 1, q = (lane & 15) >> 2, p = lane & 3;
  unsigned wa[2];
#pragma unroll
  for (int t = 0; t < 2; ++t)
    wa[t] = 32768 + 2048 * h + 1024 * wn + 64 * (4 * t + q) + 16 * ((2 * blk + (p >> 1)) ^ ((2 * h + t) & 3)) + 8 * (p & 1);
  f32x16 acc[2][2] = {};
#pragma unroll
  for (int kt = 0; kt < 10; ++kt) {
    if (kt == 0) WAITV_BAR(12);
    else if (kt < 9) WAITV_BAR(6);
    else WAITV_BAR(0);
    if (kt >= 1 && kt + 2 < 10) stage(kt + 2, (kt + 2) % 3);
    lds_cptr base = (lds_cptr)lds + (kt % 3) * G1_SLOT;
#pragma unroll
    for (int ks = 0; ks < 4; ++ks) {
      bf16x8 xf[2], wf[2];
#pragma unroll
      for (int i = 0; i < 2; ++i) {
        xf[i] = lds_b128(base + xa[ks] + i * 4096);
        wf[i] = cat8(lds_tr(base + wa[0] + ks * 4096 + i * 512), lds_tr(base + wa[1] + ks * 4096 + i * 512));
      }
#pragma unroll
      for (int mi = 0; mi < 2; ++mi)
#pragma unroll
        for (int ni = 0; ni < 2; ++ni)
          acc[mi][ni] = VT ? MFMA32(xf[mi], wf[ni], acc[mi][ni]) : MFMA32(wf[ni], xf[mi], acc[mi][ni]);
    }
  }
  const int part = nt / 5;
  const int nf0 = n0 - part * 640 + wn * 64;
  const int tile0 = (m0 + wm * 64) >> 5;
  if (!VT) {
    const bool isq = (part == 0);
    const float sc = isq ? QSCALE : 1.0f;
    const int tstride = isq ? QT_B : TILE_B;
    const int hstride = 64 * tstride;
    char* dst = ws + (isq ? Q_WS : KV_WS) + (size_t)(br * 32) * hstride + (size_t)tile0 * tstride;
    const unsigned lo1 = r32 * 16 + h * 8;
    const unsigned lo2 = 1024 + r32 * 8 + h * hstride;
#pragma unroll
    for (int ni = 0; ni < 2; ++ni)
#pragma unroll
      for (int g = 0; g < 4; ++g) {
        const int n8 = nf0 + 32 * ni + 8 * g;
        const int P = n8 / 40, r40 = n8 - P * 40;
        const int head = 2 * P + (r40 >= 24 ? 1 : 0);
        const int uo = head * hstride + ((r40 == 8 || r40 == 32) ? 512 : 0);
        const unsigned lo = (r40 == 16) ? lo2 : lo1;
#pragma unroll
        for (int mi = 0; mi < 2; ++mi) {
          u32x2 v;
          v[0] = pk_bf16(acc[mi][ni][4 * g] * sc, acc[mi][ni][4 * g + 1] * sc);
          v[1] = pk_bf16(acc[mi][ni][4 * g + 2] * sc, acc[mi][ni][4 * g + 3] * sc);
          *(u32x2*)(dst + (uo + mi * tstride) + lo) = v;
        }
      }
  } else {
    char* dst = ws + KV_WS + (size_t)(br * 32) * HEAD_B + (size_t)tile0 * TILE_B + V_OFF;
#pragma unroll
    for (int ni = 0; ni < 2; ++ni) {
      const unsigned n = nf0 + 32 * ni + r32;
      const unsigned head = n / 20, d = n - head * 20;
      const unsigned lo = head * HEAD_B + d * 16 + h * 8;
#pragma unroll
      for (int mi = 0; mi < 2; ++mi)
#pragma unroll
        for (int g = 0; g < 4; ++g) {
          u32x2 v;
          v[0] = pk_bf16(acc[mi][ni][4 * g], acc[mi][ni][4 * g + 1]);
          v[1] = pk_bf16(acc[mi][ni][4 * g + 2], acc[mi][ni][4 * g + 3]);
          *(u32x2*)(dst + (mi * TILE_B + g * VG_B) + lo) = v;
        }
      if (d == 0) {
        const u32x2 ones = {0x3F803F80u, 0x3F803F80u};
#pragma unroll
        for (int mi = 0; mi < 2; ++mi)
#pragma unroll
          for (int g = 0; g < 4; ++g) *(u32x2*)(dst + (mi * TILE_B + g * VG_B + 320) + lo) = ones;
      }
    }
  }
}

__global__ void __launch_bounds__(512, 2)
k1_qkv(const char* __restrict__ xa, const char* __restrict__ wa, const char* __restrict__ xb, const char* __restrict__ wb,
       char* __restrict__ ws) {
  __shared__ __attribute__((aligned(16))) char lds[3 * G1_SLOT];
  const int bid = blockIdx.x, xcd = bid & 7, slot = bid >> 3;
  const int br = xcd >> 2, mt = ((xcd >> 1) & 1) * 4 + (slot & 3), nt = (xcd & 1) * 8 + (slot >> 2);
  if (nt >= 15) return;
  const char* X = br ? xb : xa;
  const char* W = br ? wb : wa;
  if (nt >= 10) g1_body<true>(X, W, ws, lds, br, mt, nt);
  else g1_body<false>(X, W, ws, lds, br, mt, nt);
}

constexpr int CHUNK_B = 16 * TILE_B;

__device__ __forceinline__ f32x16 smfmac(bf16x8 a, f32x8 p, f32x16 c) {
  return __builtin_amdgcn_smfmac_f32_32x32x32_bf16(a, __builtin_bit_cast(bf16x16, p), c, 0xDDDD, 0, 0);
}
__device__ __forceinline__ f32x8 lo8(const f32x16& s) { return __builtin_shufflevector(s, s, 0, 1, 2, 3, 4, 5, 6, 7); }
__device__ __forceinline__ f32x8 hi8(const f32x16& s) { return __builtin_shufflevector(s, s, 8, 9, 10, 11, 12, 13, 14, 15); }
#define MX3(a, b, c) __builtin_fmaxf(__builtin_fmaxf((a), (b)), (c))
__device__ __forceinline__ float max16(const f32x16& s, float m) {
#pragma unroll
  for (int r = 0; r < 16; r += 2) m = MX3(m, s[r], s[r + 1]);
  return m;
}
__device__ __forceinline__ float xhalf_max(float m) {
  auto rr = __builtin_amdgcn_permlane32_swap(__float_as_uint(m), __float_as_uint(m), false, false);
  return __builtin_fmaxf(__uint_as_float(rr[0]), __uint_as_float(rr[1]));
}
__device__ __forceinline__ s16x4 with_negmax(s16x4 q2, float m, int h) {
  u32x2 v = __builtin_bit_cast(u32x2, q2);
  const unsigned nm = pk_bf16(-m, 0.0f);
  v[0] = h ? nm : v[0];
  return __builtin_bit_cast(s16x4, v);
}
__device__ __forceinline__ void store_o(const f32x16& o, char* dst, int h) {
  const float l = h ? o[8] : o[12];
  const float rl = __builtin_amdgcn_rcpf(l);
  u32x2 v;
  v[0] = pk_bf16(o[0] * rl, o[1] * rl); v[1] = pk_bf16(o[2] * rl, o[3] * rl);
  *(u32x2*)(dst) = v;
  v[0] = pk_bf16(o[4] * rl, o[5] * rl); v[1] = pk_bf16(o[6] * rl, o[7] * rl);
  *(u32x2*)(dst + 16) = v;
  if (!h) {
    v[0] = pk_bf16(o[8] * rl, o[9] * rl); v[1] = pk_bf16(o[10] * rl, o[11] * rl);
    *(u32x2*)(dst + 32) = v;
  }
}

__global__ void __launch_bounds__(512, 2)
k2_attn(char* __restrict__ ws) {
  __shared__ __attribute__((aligned(16))) char lds[3 * CHUNK_B];
  const int tid = threadIdx.x, lane = tid & 63;
  const int w = __builtin_amdgcn_readfirstlane(tid >> 6);
  const int bid = blockIdx.x;
  const int head = (bid & 7) * 4 + ((bid >> 3) & 3), qb = bid >> 5;
  const int r32 = lane & 31, h = lane >> 5;
  const unsigned lds0 = (unsigned)(uintptr_t)lds;
  const char* kvA = ws + KV_WS + (size_t)head * HEAD_B;
  const char* kvB = ws + KV_WS + (size_t)(32 + head) * HEAD_B + (ptrdiff_t)(qb * 8 - 8) * TILE_B;
  const unsigned l16 = lane * 16;
  auto chunk_dma = [&](const char* src, int buf) {
    const char* s = src + w * 1024;
    const unsigned d = lds0 + buf * CHUNK_B + w * 1024;
#pragma unroll
    for (int j = 0; j < 5; ++j) glds16(s + j * 8192, l16, d + j * 8192);
    if (w == 0) glds16(s + 40960, l16, d + 40960);
  };
  const int qt = qb * 8 + w;
  const char* qA = ws + Q_WS + (size_t)head * QHEAD_B + (size_t)qt * QT_B;
  const char* qB = qA + (size_t)32 * QHEAD_B;
  bf16x8 q1A, q1B; s16x4 q2A, q2B;
  {
    const unsigned o2 = 1024 + r32 * 8;
    asm volatile("global_load_dwordx4 %0, %4, %6\n\tglobal_load_dwordx2 %1, %5, %6\n\t"
                 "global_load_dwordx4 %2, %4, %7\n\tglobal_load_dwordx2 %3, %5, %7"
                 : "=&v"(q1A), "=&v"(q2A), "=&v"(q1B), "=&v"(q2B)
                 : "v"(l16), "v"(o2), "s"(qA), "s"(qB) : "memory");
  }
  chunk_dma(kvA, 0);
  chunk_dma(kvA + CHUNK_B, 1);
  chunk_dma(kvA + 2 * CHUNK_B, 2);
  const unsigned k1o = l16;
  const unsigned k2o = h ? (V_OFF + 320) : (K2_OFF + r32 * 8);
  const unsigned dmap = r32 <= 20 ? r32 : (r32 < 28 ? 20 : 19);
  const unsigned vo = V_OFF + h * VG_B + dmap * 16;
  asm volatile("s_waitcnt vmcnt(10)\n\ts_barrier" : "+v"(q1A), "+v"(q2A), "+v"(q1B), "+v"(q2B) :: "memory");
  if (h) { q2A = s16x4{0, 0, 0, 0}; q2B = s16x4{0, 0, 0, 0}; }
  const f32x16 zero16 = {};
  {
    lds_cptr tp = (lds_cptr)lds;
    f32x16 s = MFMA32(lds_b128(tp + k1o), q1A, zero16);
    s = MFMA32K8(lds_b64(tp + k2o), q2A, s);
    float m = xhalf_max(max16(s, s[0]));
    q2A = with_negmax(q2A, m, h);
  }
  f32x16 o = {};
  for (int c = 0; c < 4; ++c) {
    lds_cptr cb = (lds_cptr)lds + (c == 3 ? 0 : c) * CHUNK_B;
#pragma unroll
    for (int t = 0; t < 16; ++t) {
      if (t == 8) {
        if (c == 0) WAITV_BAR(5); else WAITV_BAR(0);
        if (c == 1) chunk_dma(kvA + 3 * CHUNK_B, 0);
        if (c == 2) chunk_dma(kvB, 1);
      }
      lds_cptr tp = cb + t * TILE_B;
      f32x16 s = MFMA32(lds_b128(tp + k1o), q1A, zero16);
      s = MFMA32K8(lds_b64(tp + k2o), q2A, s);
#pragma unroll
      for (int r = 0; r < 16; ++r) s[r] = __builtin_amdgcn_exp2f(s[r]);
      o = smfmac(lds_b128(tp + vo), lo8(s), o);
      o = smfmac(lds_b128(tp + vo + 2 * VG_B), hi8(s), o);
    }
  }
  char* oA = ws + O_WS + (size_t)(qt * 32 + r32) * 1280 + head * 40 + h * 8;
  {
    const float l = h ? o[8] : o[12];
    float amax = 0.f;
#pragma unroll
    for (int r = 0; r < 12; ++r) amax = __builtin_fmaxf(amax, __builtin_fabsf(o[r]));
    const bool bad = !(l <= 1.8446744e19f) || !(l >= 5.4210109e-20f) || !(amax <= 3.0e38f);
    if (__builtin_expect(__any(bad), 0)) {
      if (h) q2A = s16x4{0, 0, 0, 0};
      float m = -INFINITY;
      for (int t = 0; t < 64; ++t) {
        const char* tp = kvA + t * TILE_B;
        f32x16 s = MFMA32(*(const bf16x8*)(tp + k1o), q1A, zero16);
        s = MFMA32K8(*(const s16x4*)(tp + k2o), q2A, s);
        m = max16(s, m);
      }
      m = xhalf_max(m);
      o = zero16;
      for (int t = 0; t < 64; ++t) {
        const char* tp = kvA + t * TILE_B;
        f32x16 s = MFMA32(*(const bf16x8*)(tp + k1o), q1A, zero16);
        s = MFMA32K8(*(const s16x4*)(tp + k2o), q2A, s);
#pragma unroll
        for (int r = 0; r < 16; ++r) s[r] = __builtin_amdgcn_exp2f(s[r] - m);
        o = smfmac(*(const bf16x8*)(tp + vo), lo8(s), o);
        o = smfmac(*(const bf16x8*)(tp + vo + 2 * VG_B), hi8(s), o);
      }
    }
  }
  store_o(o, oA, h);
  {
    f32x16 maskD, maskF;
#pragma unroll
    for (int r = 0; r < 16; ++r) {
      const int key = (r & 3) + 8 * (r >> 2) + 4 * h;
      maskD[r] = key <= r32 ? 0.f : -INFINITY;
      maskF[r] = key > r32 ? 0.f : -INFINITY;
    }
    lds_cptr cb = (lds_cptr)lds + CHUNK_B;
    const int tF = w, tD = w + 8;
    const int t0 = qb == 0 ? 8 : w + 1;
    float m;
    {
      lds_cptr tp = cb + tD * TILE_B;
      f32x16 s = MFMA32(lds_b128(tp + k1o), q1B, maskD);
      s = MFMA32K8(lds_b64(tp + k2o), q2B, s);
      m = max16(s, s[0]);
    }
    if (qb != 0) {
      lds_cptr tp = cb + tF * TILE_B;
      f32x16 s = MFMA32(lds_b128(tp + k1o), q1B, maskF);
      s = MFMA32K8(lds_b64(tp + k2o), q2B, s);
      m = max16(s, m);
    }
    for (int t = t0; t < tD; ++t) {
      lds_cptr tp = cb + t * TILE_B;
      f32x16 s = MFMA32(lds_b128(tp + k1o), q1B, zero16);
      s = MFMA32K8(lds_b64(tp + k2o), q2B, s);
      m = max16(s, m);
    }
    m = xhalf_max(m);
    q2B = with_negmax(q2B, m, h);
    f32x16 ob = {};
    auto tile = [&](int t, const f32x16& c) {
      lds_cptr tp = cb + t * TILE_B;
      f32x16 s = MFMA32(lds_b128(tp + k1o), q1B, c);
      s = MFMA32K8(lds_b64(tp + k2o), q2B, s);
#pragma unroll
      for (int r = 0; r < 16; ++r) s[r] = __builtin_amdgcn_exp2f(s[r]);
      ob = smfmac(lds_b128(tp + vo), lo8(s), ob);
      ob = smfmac(lds_b128(tp + vo + 2 * VG_B), hi8(s), ob);
    };
    tile(tD, maskD);
    if (qb != 0) tile(tF, maskF);
    for (int t = t0; t < tD; ++t) tile(t, zero16);
    store_o(ob, oA + (size_t)SEQ * 1280, h);
  }
}

constexpr int G3_SLOT = 28672;

__global__ void __launch_bounds__(256, 1)
k3_proj(const char* __restrict__ ws, const char* __restrict__ woa, const char* __restrict__ wob, char* __restrict__ out) {
  __shared__ __attribute__((aligned(16))) char lds[5 * G3_SLOT];
  const int tid = threadIdx.x, lane = tid & 63;
  const int w = __builtin_amdgcn_readfirstlane(tid >> 6);
  const int bid = blockIdx.x, xcd = bid & 7, slot = bid >> 3;
  const int br = xcd >> 2, mt = (xcd & 3) * 8 + (slot >> 2), nt = slot & 3;
  const int m0 = mt * 64, n0 = nt * 160;
  const int wm = w & 1, wn = w >> 1;
  const unsigned lds0 = (unsigned)(uintptr_t)lds;
  const char* O = ws + O_WS + (size_t)br * SEQ * 1280 + (size_t)(m0 + 16 * w) * 1280;
  const char* Wo = (br ? wob : woa) + n0 * 2;
  const unsigned xoff0 = (lane >> 3) * 1280 + (((lane & 7) ^ (lane >> 4)) * 16);
  const unsigned xoff1 = (lane >> 3) * 1280 + (((lane & 7) ^ (4 + (lane >> 4))) * 16);
  unsigned woff[5];
#pragma unroll
  for (int j = 0; j < 5; ++j) {
    const unsigned st = 2 * (5 * w + j) + (lane >> 5);
    const unsigned band = st / 5, cb = st - band * 5;
    const unsigned row = (lane >> 2) & 7;
    woff[j] = (band * 8 + row) * 1280 + cb * 64 + (((lane & 3) ^ ((2 * band + (row >> 2)) & 3)) * 16);
  }
  auto stage = [&](int kt, int sl) {
    const unsigned d = lds0 + sl * G3_SLOT;
    glds16(O + kt * 128, xoff0, d + w * 2048);
    glds16(O + kt * 128 + 8 * 1280, xoff1, d + w * 2048 + 1024);
    const char* wsrc = Wo + (size_t)kt * 64 * 1280;
#pragma unroll
    for (int j = 0; j < 5; ++j) glds16(wsrc, woff[j], d + 8192 + (5 * w + j) * 1024);
  };
  stage(0, 0); stage(1, 1); stage(2, 2); stage(3, 3); stage(4, 4);
  const int r16 = lane & 15, g = lane >> 4, q = r16 >> 2, p = lane & 3;
  const unsigned orow = wm * 32 + r16;
  unsigned oa[2];
#pragma unroll
  for (int ks = 0; ks < 2; ++ks) oa[ks] = orow * 128 + (((4 * ks + g) ^ ((orow >> 1) & 7)) * 16);
  f32x4 acc[2][5] = {};
#pragma unroll
  for (int kt = 0; kt < 10; ++kt) {
    if (kt == 0) WAITV_BAR(28);
    else if (kt <= 6) WAITV_BAR(21);
    else if (kt == 7) WAITV_BAR(14);
    else if (kt == 8) WAITV_BAR(7);
    else WAITV_BAR(0);
    if (kt >= 1 && kt + 4 < 10) stage(kt + 4, (kt + 4) % 5);
    lds_cptr base = (lds_cptr)lds + (kt % 5) * G3_SLOT;
#pragma unroll
    for (int ks = 0; ks < 2; ++ks) {
      bf16x8 of[2], wf[5];
#pragma unroll
      for (int mi = 0; mi < 2; ++mi) of[mi] = lds_b128(base + oa[ks] + mi * 2048);
#pragma unroll
      for (int i = 0; i < 5; ++i) {
        const int c16 = 5 * wn + i;
        const unsigned ch = 2 * c16 + (p >> 1);
        s16x4 f[2];
#pragma unroll
        for (int t = 0; t < 2; ++t)
          f[t] = lds_tr(base + 8192 + 2560 * (4 * ks + g) + 512 * (ch >> 2) + 64 * (4 * t + q) +
                        16 * ((ch & 3) ^ ((2 * g + t) & 3)) + 8 * (p & 1));
        wf[i] = cat8(f[0], f[1]);
      }
#pragma unroll
      for (int mi = 0; mi < 2; ++mi)
#pragma unroll
        for (int i = 0; i < 5; ++i) acc[mi][i] = MFMA16(wf[i], of[mi], acc[mi][i]);
    }
  }
  char* dst = out + (size_t)(m0 + wm * 32 + r16) * 2560 + (br * 640 + n0 + wn * 80 + 4 * g) * 2;
#pragma unroll
  for (int mi = 0; mi < 2; ++mi)
#pragma unroll
    for (int i = 0; i < 5; ++i) {
      u32x2 v;
      v[0] = pk_bf16(acc[mi][i][0], acc[mi][i][1]);
      v[1] = pk_bf16(acc[mi][i][2], acc[mi][i][3]);
      *(u32x2*)(dst + mi * 16 * 2560 + i * 32) = v;
    }
}

extern "C" void kernel_launch(void* const* d_in, const int* in_sizes, int n_in, void* d_out, int out_size, void* d_ws,
                              size_t ws_size, hipStream_t stream) {
  const char* xa = (const char*)d_in[0];
  const char* wa = (const char*)d_in[1];
  const char* woa = (const char*)d_in[2];
  const char* xb = (const char*)d_in[3];
  const char* wb = (const char*)d_in[4];
  const char* wob = (const char*)d_in[5];
  char* ws = (char*)d_ws;
  k1_qkv<<<256, 512, 0, stream>>>(xa, wa, xb, wb, ws);
  k2_attn<<<256, 512, 0, stream>>>(ws);
  k3_proj<<<256, 256, 0, stream>>>(ws, woa, wob, (char*)d_out);
}
```

```cpp
#include <hip/hip_runtime.h>
#include <hip/hip_bf16.h>
#include <stdint.h>
#include <type_traits>

typedef __attribute__((ext_vector_type(8))) __bf16 bf16x8;
typedef __attribute__((ext_vector_type(16))) __bf16 bf16x16;
typedef __attribute__((ext_vector_type(4))) short s16x4;
typedef __attribute__((ext_vector_type(8))) short s16x8;
typedef __attribute__((ext_vector_type(2))) unsigned u32x2;
typedef __attribute__((ext_vector_type(4))) unsigned u32x4;
typedef __attribute__((ext_vector_type(4))) float f32x4;
typedef __attribute__((ext_vector_type(8))) float f32x8;
typedef __attribute__((ext_vector_type(16))) float f32x16;
typedef __attribute__((ext_vector_type(32))) float f32x32;
typedef __attribute__((address_space(3))) const char* lds_cptr;
typedef __attribute__((address_space(3))) char* lds_ptr;

constexpr size_t QK_WS = 0, QK_BR = (size_t)160 * 32768;
constexpr size_t V_WS = 2 * QK_BR, V_BR = (size_t)8 * 256 * 1280;
constexpr size_t O_WS = V_WS + 2 * V_BR, O_BR = (size_t)80 * 32768;
constexpr size_t CTL_WS = O_WS + 2 * O_BR;
constexpr unsigned FQ_OFF = 4096, FO_OFF = FQ_OFF + 2 * 30 * 64 * 128;
constexpr float QSCALE = 0.22360679774997896f * 1.4426950408889634f;

#define STAMP_DECL unsigned stv = 0;
#define STAMP(i) do {} while (0)
#define STAMP_OUT(ws_, kid) do {} while (0)
__device__ __forceinline__ int lane_id() {
  int l; asm volatile("v_mbcnt_lo_u32_b32 %0, -1, 0\n\tv_mbcnt_hi_u32_b32 %0, -1, %0" : "=v"(l)); return l;
}
#define SBAR() __builtin_amdgcn_sched_barrier(0)
#define PIN(x) asm volatile("" : "+v"(x))
#define WAITV(N) asm volatile("s_waitcnt vmcnt(" #N ")" ::: "memory")
#define BARRIER() asm volatile("s_barrier" ::: "memory")
#define WAITV_BAR(N) asm volatile("s_waitcnt vmcnt(" #N ") lgkmcnt(0)\n\ts_barrier" ::: "memory")

__device__ __forceinline__ void glds16(const void* sbase, unsigned voff, unsigned lds) {
  unsigned keep;
  asm volatile("s_mov_b32 %0, m0\n\ts_mov_b32 m0, %3\n\ts_nop 0\n\tglobal_load_lds_dwordx4 %1, %2\n\ts_mov_b32 m0, %0"
               : "=&s"(keep) : "v"(voff), "s"(sbase), "s"(lds) : "memory");
}
__device__ __forceinline__ void glds16x4(const void* sbase, const unsigned (&voff)[4], unsigned lds) {
  unsigned keep;
  asm volatile("s_mov_b32 %0, m0\n\ts_mov_b32 m0, %6\n\ts_nop 0\n\tglobal_load_lds_dwordx4 %1, %5\n\t"
               "global_load_lds_dwordx4 %2, %5 offset:1024\n\tglobal_load_lds_dwordx4 %3, %5 offset:2048\n\t"
               "global_load_lds_dwordx4 %4, %5 offset:3072\n\ts_mov_b32 m0, %0"
               : "=&s"(keep) : "v"(voff[0]), "v"(voff[1]), "v"(voff[2]), "v"(voff[3]), "s"(sbase), "s"(lds) : "memory");
}
__device__ __forceinline__ unsigned pk_bf16(float lo, float hi) {
  typedef __attribute__((ext_vector_type(2))) float f32x2_;
  typedef __attribute__((ext_vector_type(2))) __bf16 bf16x2_;
  const f32x2_ v = {lo, hi};
  return __builtin_bit_cast(unsigned, __builtin_convertvector(v, bf16x2_));
}
#define ST_MODE 1
__device__ __forceinline__ void st16(char* sbase, unsigned voff, u32x4 v) {
  asm volatile("global_store_dwordx4 %0, %1, %2 sc1\n\ts_nop 1" :: "v"(voff), "v"(v), "s"(sbase) : "memory");
}
__device__ __forceinline__ void st8(char* sbase, unsigned voff, u32x2 v) {
  asm volatile("global_store_dwordx2 %0, %1, %2 sc1" :: "v"(voff), "v"(v), "s"(sbase) : "memory");
}
__device__ __forceinline__ bf16x8 lds_b128(lds_cptr p) { return *(const __attribute__((address_space(3))) bf16x8*)p; }
__device__ __forceinline__ s16x4 lds_b64(lds_cptr p) { return *(const __attribute__((address_space(3))) s16x4*)p; }
__device__ __forceinline__ s16x4 lds_tr(lds_cptr p) {
  return __builtin_amdgcn_ds_read_tr16_b64_v4i16((__attribute__((address_space(3))) s16x4*)p);
}
__device__ __forceinline__ bf16x8 cat8(s16x4 a, s16x4 b) {
  s16x8 v = {a[0], a[1], a[2], a[3], b[0], b[1], b[2], b[3]};
  return __builtin_bit_cast(bf16x8, v);
}
#define MFMA32(a, b, c) __builtin_amdgcn_mfma_f32_32x32x16_bf16(a, b, c, 0, 0, 0)
#define MFMA32K8(a, b, c) __builtin_amdgcn_mfma_f32_32x32x8bf16_1k(a, b, c, 0, 0, 0)
#define MFMA16(a, b, c) __builtin_amdgcn_mfma_f32_16x16x32_bf16(a, b, c, 0, 0, 0)

typedef __attribute__((ext_vector_type(8))) unsigned u32x8;
typedef __attribute__((ext_vector_type(16))) unsigned u32x16;
#define G1_ASM_MACROS \
    ".macro mm vt, c, x, w\n\t" \
    ".if (\\x) < 104\n\t"    \
    ".if \\vt\n\tv_mfma_f32_16x16x32_bf16 v[\\c:\\c+3], v[\\x:\\x+3], v[\\w:\\w+3], 0\n\t" \
    ".else\n\tv_mfma_f32_16x16x32_bf16 v[\\c:\\c+3], v[\\w:\\w+3], v[\\x:\\x+3], 0\n\t.endif\n\t" \
    ".elseif \\vt\n\tv_mfma_f32_16x16x32_bf16 v[\\c:\\c+3], v[\\x:\\x+3], v[\\w:\\w+3], v[\\c:\\c+3]\n\t" \
    ".else\n\tv_mfma_f32_16x16x32_bf16 v[\\c:\\c+3], v[\\w:\\w+3], v[\\x:\\x+3], v[\\c:\\c+3]\n\t.endif\n\t" \
    ".endm\n\t" \
    ".macro rx d, a, off\n\tds_read_b128 v[\\d:\\d+3], v\\a offset:\\off\n\tds_read_b128 v[\\d+4:\\d+7], v\\a offset:\\off+2048\n\t.endm\n\t" \
    ".macro rw d, a, off\n\tds_read_b64_tr_b16 v[\\d:\\d+1], v\\a offset:\\off\n\tds_read_b64_tr_b16 v[\\d+2:\\d+3], v\\a offset:\\off+512\n\t.endm\n\t" \
    ".macro bump lo, n, val\n\t.set bi_, \\lo\n\t.rept \\n\n\tv_add_u32 v[bi_], \\val, v[bi_]\n\t.set bi_, bi_+1\n\t.endr\n\t.endm\n\t"
#define G1_ASM_PURGE ".purgem mm\n\t.purgem rx\n\t.purgem rw\n\t.purgem bump\n\t"

#define G1_DEPTH 3
__device__ __forceinline__ void g1_pass1(int vt, f32x32& acc, f32x32& x0, f32x32& x1, f32x32& x2, f32x32& x3, f32x32& x4, u32x16& ad,
                                         const char* sA, const char* sB, unsigned off1, unsigned str1, unsigned off2, unsigned m0v,
                                         const char* ctl, unsigned long long& epoch) {
  unsigned keep, tmp;
  const unsigned sAl = (unsigned)(uintptr_t)sA, sAh = (unsigned)((uintptr_t)sA >> 32);
  const unsigned sBl = (unsigned)(uintptr_t)sB, sBh = (unsigned)((uintptr_t)sB >> 32);
  asm volatile(
      G1_ASM_MACROS
      ".macro p1body vt\n\t"
      ".set st_, 0\n\t"
      ".rept 10\n\t"
      ".set xr_, 96 + 16 * st_\n\t"
      ".set o_, ((st_ %% 5) & 1) * 32768\n\t"
      ".set n_, (((st_ + 1) %% 5) & 1) * 32768\n\t"
      "s_waitcnt lgkmcnt(6)\n\t"
      "mm \\vt, 0, xr_, 32\n\trw 48, 66, o_+16384+8192\n\t"
      "mm \\vt, 16, xr_+4, 32\n\trx xr_+8, 65, o_\n\ts_waitcnt lgkmcnt(8)\n\t"
      "mm \\vt, 4, xr_, 36\n\trw 52, 67, o_+16384+8192\n\t"
      "mm \\vt, 20, xr_+4, 36\n\trw 56, 68, o_+16384+8192\n\ts_waitcnt lgkmcnt(10)\n\t"
      "mm \\vt, 8, xr_, 40\n\trw 60, 69, o_+16384+8192\n\t"
      "mm \\vt, 24, xr_+4, 40\n\ts_waitcnt lgkmcnt(10)\n\t"
      "mm \\vt, 12, xr_, 44\n\t"
      ".if st_ < 10 - %c[dep]\n\t"
      "s_add_u32 s40, %[sAl], %[off1]\n\ts_addc_u32 s41, %[sAh], 0\n\t"
      "s_add_u32 %[off1], %[off1], %[str1]\n\ts_mul_i32 %[tmp], %[str1], 10\n\ts_cmp_eq_u32 %[off1], %[tmp]\n\ts_cselect_b32 %[off1], 0, %[off1]\n\t"
      ".else\n\t"
      "s_add_u32 s40, %[sBl], %[off2]\n\ts_addc_u32 s41, %[sBh], 0\n\t"
      "s_add_u32 %[off2], %[off2], 2*64*3840\n\ts_cmp_ge_u32 %[off2], 640*3840\n\ts_cselect_b32 %[tmp], 640*3840, 0\n\ts_sub_u32 %[off2], %[off2], %[tmp]\n\t"
      ".endif\n\t"
      "mm \\vt, 28, xr_+4, 44\n\t"
      "s_add_u32 m0, %[m0v], ((st_ + %c[dep]) %% 5) * 32768\n\t"
      ".if (st_ %% 5) == 1 || (st_ %% 5) == 3\n\tbump 64, 6, 0x10000\n\t.endif\n\t"
      ".if (st_ %% 5) == 4\n\tbump 64, 6, 0xfffe0000\n\t.endif\n\t"
      "s_waitcnt lgkmcnt(6)\n\t"
      "mm \\vt, 0, xr_+8, 48\n\t"
      "mm \\vt, 16, xr_+12, 48\n\t"
      "s_waitcnt vmcnt(4 * %c[dep] - 8) lgkmcnt(0)\n\ts_barrier\n\t"
      ".set vo_, 70 + 4 * ((st_ + %c[dep]) / 10)\n\t"
      "mm \\vt, 4, xr_+8, 52\n\t"
      ".if st_ < 9\n\trw 32, 66, n_+16384\n\t.endif\n\t"
      "global_load_lds_dwordx4 v[vo_], s[40:41]\n\t"
      "mm \\vt, 20, xr_+12, 52\n\t"
      ".if st_ < 9\n\trx xr_+16, 64, n_\n\t.endif\n\t"
      "global_load_lds_dwordx4 v[vo_+1], s[40:41] offset:1024\n\t"
      "mm \\vt, 8, xr_+8, 56\n\t"
      ".if st_ < 9\n\trw 36, 67, n_+16384\n\t.endif\n\t"
      "global_load_lds_dwordx4 v[vo_+2], s[40:41] offset:2048\n\t"
      "mm \\vt, 24, xr_+12, 56\n\t"
      ".if st_ < 9\n\trw 40, 68, n_+16384\n\t.endif\n\t"
      "global_load_lds_dwordx4 v[vo_+3], s[40:41] offset:3072\n\t"
      "mm \\vt, 12, xr_+8, 60\n\t"
      ".if st_ < 9\n\trw 44, 69, n_+16384\n\t.endif\n\t"
      "mm \\vt, 28, xr_+12, 60\n\t"
      ".set st_, st_ + 1\n\t"
      ".endr\n\t"
      ".endm\n\t"
      "rw 32, 66, 16384\n\trx 96, 64, 0\n\trw 36, 67, 16384\n\trw 40, 68, 16384\n\trw 44, 69, 16384\n\t"
      "s_load_dwordx2 %[ep], %[ctl], 0x0\n\t"
      "s_mov_b32 %[keep], m0\n\ts_add_u32 m0, %[m0v], 65536\n\t"
      "s_add_u32 s40, %[sAl], %[off1]\n\ts_addc_u32 s41, %[sAh], 0\n\t"
      "s_add_u32 %[off1], %[off1], %[str1]\n\ts_mul_i32 %[tmp], %[str1], 10\n\ts_cmp_eq_u32 %[off1], %[tmp]\n\ts_cselect_b32 %[off1], 0, %[off1]\n\t"
      "global_load_lds_dwordx4 v70, s[40:41]\n\tglobal_load_lds_dwordx4 v71, s[40:41] offset:1024\n\t"
      "global_load_lds_dwordx4 v72, s[40:41] offset:2048\n\tglobal_load_lds_dwordx4 v73, s[40:41] offset:3072\n\t"
      "s_cmp_lg_u32 %[vt], 0\n\ts_cbranch_scc1 1f\n\t"
      "p1body 0\n\ts_branch 2f\n\t"
      "1:\n\tp1body 1\n\t"
      "2:\n\ts_mov_b32 m0, %[keep]\n\ts_nop 7\n\t"
      ".purgem p1body\n\t" G1_ASM_PURGE
      : "=&{v[0:31]}"(acc), "=&{v[96:127]}"(x0), "=&{v[128:159]}"(x1), "=&{v[160:191]}"(x2), "=&{v[192:223]}"(x3), "=&{v[224:255]}"(x4),
        "+{v[64:79]}"(ad), [keep] "=&s"(keep), [tmp] "=&s"(tmp), [off1] "+s"(off1), [off2] "+s"(off2), [ep] "=&s"(epoch)
      : [sAl] "s"(sAl), [sAh] "s"(sAh), [sBl] "s"(sBl), [sBh] "s"(sBh), [str1] "s"(str1), [m0v] "s"(m0v), [vt] "s"(vt), [dep] "n"(G1_DEPTH), [ctl] "s"(ctl)
      : "memory", "scc", "s40", "s41", "v32", "v33", "v34", "v35", "v36", "v37", "v38", "v39", "v40", "v41", "v42", "v43", "v44", "v45", "v46", "v47",
        "v48", "v49", "v50", "v51", "v52", "v53", "v54", "v55", "v56", "v57", "v58", "v59", "v60", "v61", "v62", "v63");
}

#define G1_P2_MACROS \
      G1_ASM_MACROS \
      ".macro slot n\n\t.set n_, \\n\n\t"    \
      ".if n_ < 4\n\tglobal_store_dwordx4 v[72+n_], v[80+4*n_:83+4*n_], s[42:43] sc1\n\t.endif\n\t" \
      ".if n_ >= 4 && n_ < 12\n\t" \
      ".if (n_ & 3) == 0\n\t" \
      "s_add_u32 m0, s46, (2 + n_ / 4) * 32768\n\ts_add_u32 s40, s44, s47\n\ts_addc_u32 s41, s45, 0\n\t" \
      "s_add_u32 s47, s47, 2*64*3840\n\ts_cmp_ge_u32 s47, 640*3840\n\ts_cselect_b32 s48, 640*3840, 0\n\ts_sub_u32 s47, s47, s48\n\t" \
      ".endif\n\t" \
      "global_load_lds_dwordx4 v[68+(n_&3)], s[40:41] offset:1024*(n_&3)\n\t" \
      ".endif\n\t" \
      ".if n_ == 14\n\tv_mov_b32 v72, s50\n\tv_mov_b32 v73, s51\n\tv_mov_b32 v74, 0\n\ts_mov_b64 exec, 1\n\ts_waitcnt vmcnt(8)\n\t" \
      "global_store_dwordx2 v74, v[72:73], s[52:53] sc1\n\ts_mov_b64 exec, -1\n\t.endif\n\t" \
      ".if n_ == 26\n\tglobal_load_dwordx2 v[72:73], v76, s[54:55] sc1\n\t.endif\n\t" \
      ".endm\n\t" \
      ".macro p2body vt, k0, k1\n\t" \
      ".set k_, \\k0\n\t" \
      ".rept \\k1 - \\k0\n\t" \
      ".set xr_, 96 + 8 * k_\n\t" \
      ".set c_, 32 + 16 * (k_ & 1)\n\t" \
      ".set d_, 48 - 16 * (k_ & 1)\n\t" \
      ".set o_, ((k_ + 1) & 7) * 8192\n\t" \
      ".if k_ == 3\n\ts_waitcnt vmcnt(6)\n\ts_barrier\n\t.endif\n\t" \
      ".if k_ == 11\n\ts_waitcnt vmcnt(1)\n\ts_barrier\n\t.endif\n\t" \
      ".if \\vt == 2\n\tslot 2*k_\n\tslot 2*k_+1\n\t" \
      ".elseif k_ < 19\n\t" \
      ".if ((k_ + 1) & 7) == 0\n\tbump 64, 4, 0x10000\n\t.endif\n\t" \
      "s_waitcnt lgkmcnt(6)\n\t" \
      "mm \\vt, 0, xr_, c_\n\trw d_, 64, o_\n\t" \
      "mm \\vt, 16, xr_+4, c_\n\tslot 2*k_\n\ts_waitcnt lgkmcnt(6)\n\t" \
      "mm \\vt, 4, xr_, c_+4\n\trw d_+4, 65, o_\n\t" \
      "mm \\vt, 20, xr_+4, c_+4\n\ts_waitcnt lgkmcnt(6)\n\t" \
      "mm \\vt, 8, xr_, c_+8\n\trw d_+8, 66, o_\n\t" \
      "mm \\vt, 24, xr_+4, c_+8\n\tslot 2*k_+1\n\ts_waitcnt lgkmcnt(6)\n\t" \
      "mm \\vt, 12, xr_, c_+12\n\trw d_+12, 67, o_\n\t" \
      "mm \\vt, 28, xr_+4, c_+12\n\t" \
      ".else\n\t" \
      "s_waitcnt lgkmcnt(0)\n\t" \
      "mm \\vt, 0, xr_, c_\n\tmm \\vt, 16, xr_+4, c_\n\tmm \\vt, 4, xr_, c_+4\n\tmm \\vt, 20, xr_+4, c_+4\n\t" \
      "mm \\vt, 8, xr_, c_+8\n\tmm \\vt, 24, xr_+4, c_+8\n\tmm \\vt, 12, xr_, c_+12\n\tmm \\vt, 28, xr_+4, c_+12\n\t" \
      "s_nop 7\n\t" \
      ".endif\n\t" \
      ".set k_, k_ + 1\n\t" \
      ".endr\n\t" \
      ".endm\n\t"
#define G1_P2_PURGE ".purgem p2body\n\t.purgem slot\n\t" G1_ASM_PURGE
__device__ __forceinline__ void g1_pass2a(int vt, f32x32& acc, const f32x32& x0, const f32x32& x1, const f32x32& x2, const f32x32& x3,
                                          const f32x32& x4, f32x32& wf, u32x16& ad, const u32x16& pk, char* stb, const char* sB,
                                          unsigned m0v, unsigned off2, unsigned long long tag, char* flag, char* ctl) {
  unsigned keep;
  asm volatile(G1_P2_MACROS
      "rw 32, 64, 0\n\trw 36, 65, 0\n\trw 40, 66, 0\n\trw 44, 67, 0\n\t"
      "s_mov_b32 %[keep], m0\n\t"
      "s_cmp_eq_u32 %[vt], 1\n\ts_cbranch_scc1 1f\n\ts_cmp_eq_u32 %[vt], 2\n\ts_cbranch_scc1 3f\n\t"
      "p2body 0, 0, 15\n\ts_branch 2f\n\t"
      "3:\n\tp2body 2, 0, 15\n\ts_branch 2f\n\t"
      "1:\n\tp2body 1, 0, 15\n\t"
      "2:\n\ts_mov_b32 m0, %[keep]\n\ts_waitcnt vmcnt(0)\n\t"
      G1_P2_PURGE
      : "=&{v[0:31]}"(acc), "=&{v[32:63]}"(wf), "+{v[64:79]}"(ad), [keep] "=&s"(keep), "+{s47}"(off2)
      : "{v[80:95]}"(pk), "{v[96:127]}"(x0), "{v[128:159]}"(x1), "{v[160:191]}"(x2), "{v[192:223]}"(x3), "{v[224:255]}"(x4), [vt] "s"(vt),
        "{s[42:43]}"(stb), "{s[44:45]}"(sB), "{s46}"(m0v), "{s[50:51]}"(tag), "{s[52:53]}"(flag), "{s[54:55]}"(ctl)
      : "memory", "scc", "s40", "s41", "s48");
}
__device__ __forceinline__ void g1_pass2b(int vt, f32x32& acc, const f32x32& x3, const f32x32& x4, f32x32& wf, u32x4& wa) {
  asm volatile(G1_P2_MACROS
      "s_cmp_eq_u32 %[vt], 2\n\ts_cbranch_scc1 2f\n\ts_cmp_eq_u32 %[vt], 1\n\ts_cbranch_scc1 1f\n\t"
      "p2body 0, 15, 20\n\ts_branch 2f\n\t"
      "1:\n\tp2body 1, 15, 20\n\t"
      "2:\n\t"
      G1_P2_PURGE
      : "+{v[0:31]}"(acc), "+{v[32:63]}"(wf), "+{v[64:67]}"(wa)
      : "{v[192:223]}"(x3), "{v[224:255]}"(x4), [vt] "s"(vt)
      : "memory", "scc");
}

__device__ __forceinline__ void g1_pack(const f32x32& acc, int vt, bool isq, int f0, int lane, u32x16& pk, u32x4& so) {
  if (!vt) {
    const float sc = isq ? QSCALE : 1.0f;
#pragma unroll
    for (int ni = 0; ni < 4; ++ni) {
      const int a = 4 * ni, b = 16 + 4 * ni;
      const auto r0 = __builtin_amdgcn_permlane16_swap(pk_bf16(acc[a] * sc, acc[a + 1] * sc), pk_bf16(acc[b] * sc, acc[b + 1] * sc), false, false);
      const auto r1 = __builtin_amdgcn_permlane16_swap(pk_bf16(acc[a + 2] * sc, acc[a + 3] * sc), pk_bf16(acc[b + 2] * sc, acc[b + 3] * sc), false, false);
      pk[4 * ni] = r0[0]; pk[4 * ni + 1] = r1[0]; pk[4 * ni + 2] = r0[1]; pk[4 * ni + 3] = r1[1];
      so[ni] = ((f0 >> 3) + 2 * ni + (lane >> 5)) * 32768 + (lane & 31) * 16;
    }
  } else {
#pragma unroll
    for (int i = 0; i < 4; ++i) {
      const int a = 8 * i, b = a + 4;
      const auto r0 = __builtin_amdgcn_permlane16_swap(pk_bf16(acc[a], acc[a + 1]), pk_bf16(acc[b], acc[b + 1]), false, false);
      const auto r1 = __builtin_amdgcn_permlane16_swap(pk_bf16(acc[a + 2], acc[a + 3]), pk_bf16(acc[b + 2], acc[b + 3]), false, false);
      pk[4 * i] = r0[0]; pk[4 * i + 1] = r1[0]; pk[4 * i + 2] = r0[1]; pk[4 * i + 3] = r1[1];
      const unsigned f = f0 + 32 * (i & 1) + (lane & 31);
      so[i] = (f / 80) * (256 * 1280 - 1280) + f * 16 + (2 * (i >> 1) + (lane >> 5)) * 1280;
    }
  }
}
__device__ __forceinline__ char* g1_out(char* ws, int vt, int br, int tok0) {
  return vt ? ws + V_WS + br * V_BR + (tok0 >> 3) * 1280 : ws + QK_WS + br * QK_BR + tok0 * 16;
}

constexpr int TILE_B = 3584, K2_OFF = 1024, V_OFF = 1536;
constexpr int CHUNK_B = 8 * TILE_B;

__device__ __forceinline__ f32x16 smfmac(bf16x8 a, f32x8 p, f32x16 c) {
  return __builtin_amdgcn_smfmac_f32_32x32x32_bf16(a, __builtin_bit_cast(bf16x16, p), c, 0xDDDD, 0, 0);
}
__device__ __forceinline__ f32x8 lo8(const f32x16& s) { return __builtin_shufflevector(s, s, 0, 1, 2, 3, 4, 5, 6, 7); }
__device__ __forceinline__ f32x8 hi8(const f32x16& s) { return __builtin_shufflevector(s, s, 8, 9, 10, 11, 12, 13, 14, 15); }
#define MX3(a, b, c) __builtin_fmaxf(__builtin_fmaxf((a), (b)), (c))
__device__ __forceinline__ float max16(const f32x16& s, float m) {
#pragma unroll
  for (int r = 0; r < 16; r += 2) m = MX3(m, s[r], s[r + 1]);
  return m;
}
__device__ __forceinline__ float xhalf_max(float m) {
  auto rr = __builtin_amdgcn_permlane32_swap(__float_as_uint(m), __float_as_uint(m), false, false);
  return __builtin_fmaxf(__uint_as_float(rr[0]), __uint_as_float(rr[1]));
}
__device__ __forceinline__ s16x4 with_negmax(s16x4 q2, float m, int h) {
  u32x2 v = __builtin_bit_cast(u32x2, q2);
  const unsigned nm = pk_bf16(-m, 0.0f);
  v[0] = h ? nm : v[0];
  return __builtin_bit_cast(s16x4, v);
}
__device__ __forceinline__ float row_sum(const f32x16& o) {
  auto rr = __builtin_amdgcn_permlane32_swap(__float_as_uint(o[8]), __float_as_uint(o[8]), false, false);
  return __uint_as_float(rr[1]);
}
__device__ __forceinline__ void store_o(const f32x16& o, float l, char* base, unsigned lo1, unsigned lo2, int h) {
  const float rl = __builtin_amdgcn_rcpf(l);
  const unsigned a0 = pk_bf16(o[0] * rl, o[1] * rl), a1 = pk_bf16(o[2] * rl, o[3] * rl);
  const unsigned b0 = pk_bf16(o[4] * rl, o[5] * rl), b1 = pk_bf16(o[6] * rl, o[7] * rl);
  const auto r0 = __builtin_amdgcn_permlane32_swap(a0, b0, false, false);
  const auto r1 = __builtin_amdgcn_permlane32_swap(a1, b1, false, false);
  const u32x4 v = {r0[0], r1[0], r0[1], r1[1]};
  st16(base, lo1, v);
  const u32x2 c = {pk_bf16(o[8] * rl, o[9] * rl), pk_bf16(o[10] * rl, o[11] * rl)};
  if (!h) st8(base, lo2, c);
}

__device__ __forceinline__ bool flags_ok(unsigned a, unsigned b, unsigned long long tag) {
  return __all(a == (unsigned)tag && b == (unsigned)(tag >> 32));
}
__device__ __forceinline__ void poll_flags(const char* base, unsigned off, unsigned long long tag) {
  const unsigned long long t0 = __builtin_amdgcn_s_memrealtime();
  for (;;) {
    u32x2 a;
    asm volatile("global_load_dwordx2 %0, %1, %2 sc1\n\ts_waitcnt vmcnt(0)" : "=&v"(a) : "v"(off), "s"(base) : "memory");
    if (flags_ok(a[0], a[1], tag)) break;
    if (__builtin_amdgcn_s_memrealtime() - t0 > 20000000ull) break;
    __builtin_amdgcn_s_sleep(2);
  }
}
__device__ __forceinline__ void put_flag(char* p, unsigned long long tag, int lane) {
  if (lane == 0) __hip_atomic_store((unsigned long long*)p, tag, __ATOMIC_RELAXED, __HIP_MEMORY_SCOPE_AGENT);
}
__device__ __forceinline__ void tile_dma(const char* ks, const char* vs, unsigned d, u32x4 go) {
  unsigned keep;
  asm volatile("s_mov_b32 %0, m0\n\ts_mov_b32 m0, %7\n\ts_nop 0\n\t"
               "global_load_lds_dwordx4 %1, %5 sc1\n\t"
               "s_mov_b32 exec_hi, 0\n\t"
               "global_load_lds_dwordx4 %2, %5 offset:1024 sc1\n\t"
               "s_mov_b32 exec_lo, 0xfffff\n\ts_mov_b32 exec_hi, 0xfffff\n\t"
               "global_load_lds_dwordx4 %3, %6 offset:1536 sc1\n\t"
               "global_load_lds_dwordx4 %4, %6 offset:2560 sc1\n\t"
               "s_mov_b64 exec, -1\n\ts_mov_b32 m0, %0"
               : "=&s"(keep) : "v"(go[0]), "v"(go[1]), "v"(go[2]), "v"(go[3]), "s"(ks), "s"(vs), "s"(d) : "memory");
}
__device__ __forceinline__ void wo_dma(const char* src, unsigned d, const u32x8& wo) {
  unsigned keep;
  asm volatile("s_mov_b32 %0, m0\n\ts_mov_b32 m0, %7\n\ts_nop 0\n\t"
               "global_load_lds_dwordx4 %1, %6\n\tglobal_load_lds_dwordx4 %2, %6 offset:1024\n\t"
               "global_load_lds_dwordx4 %3, %6 offset:2048\n\tglobal_load_lds_dwordx4 %4, %6 offset:3072\n\t"
               "s_add_u32 m0, m0, 4096\n\ts_nop 0\n\tglobal_load_lds_dwordx4 %5, %6\n\ts_mov_b32 m0, %0"
               : "=&s"(keep) : "v"(wo[0]), "v"(wo[1]), "v"(wo[2]), "v"(wo[3]), "v"(wo[4]), "s"(src), "s"(d) : "memory", "scc");
}

#define AT_ASM_MACROS \
    ".macro xp d, s\n\tv_exp_f32 v[\\d], v[\\s]\n\tv_exp_f32 v[\\d+1], v[\\s+1]\n\tv_exp_f32 v[\\d+2], v[\\s+2]\n\tv_exp_f32 v[\\d+3], v[\\s+3]\n\t.endm\n\t" \
    ".macro smf o, v, p\n\tv_smfmac_f32_32x32x32_bf16 v[\\o:\\o+15], v[\\v:\\v+3], v[\\p:\\p+7], v76\n\t.endm\n\t" \
    ".macro qk1 s, k, q\n\tv_mfma_f32_32x32x16_bf16 v[\\s:\\s+15], v[\\k:\\k+3], v[\\q:\\q+3], 0\n\t.endm\n\t" \
    ".macro qk1c s, k, q, c\n\tv_mfma_f32_32x32x16_bf16 v[\\s:\\s+15], v[\\k:\\k+3], v[\\q:\\q+3], v[\\c:\\c+15]\n\t.endm\n\t" \
    ".macro qk2 s, k, q\n\tv_mfma_f32_32x32x8_bf16 v[\\s:\\s+15], v[\\k:\\k+1], v[\\q:\\q+1], v[\\s:\\s+15]\n\t.endm\n\t"
#define AT_ASM_PURGE ".purgem xp\n\t.purgem smf\n\t.purgem qk1\n\t.purgem qk1c\n\t.purgem qk2\n\t"

template <int J0, int J1>
__device__ __forceinline__ void at_a(f32x32& o, f32x16& s0, f32x16& pp, f32x32& fr, const u32x16& q, u32x16& ad, const char* ws,
                                     u32x8 so0, u32x2 so1, unsigned m0v, const char* ctl) {
  unsigned keep;
  asm volatile(AT_ASM_MACROS
      ".macro pos j\n\t.set sl_, ((\\j) >> 2) %% 5\n\t.set ar_, 128 + 3 * (sl_ >> 1)\n\t.set of_, (sl_ & 1) * 28672 + ((\\j) & 3) * 7168\n\t.endm\n\t"
      ".macro dma part\n\t"
      ".if ci_ >= 5\n\t"
      ".if \\part == 0\n\t"
      "s_add_u32 m0, s66, (ci_ - 5) * 28672\n\ts_add_u32 s40, s54, s[56+2*(ci_-5)]\n\ts_addc_u32 s41, s55, 0\n\t"
      "global_load_lds_dwordx4 v137, s[40:41] sc1\n\t"
      ".elseif \\part == 1\n\t"
      "s_mov_b32 exec_hi, 0\n\tglobal_load_lds_dwordx4 v138, s[40:41] offset:1024 sc1\n\ts_mov_b32 exec_hi, -1\n\t"
      "s_add_u32 s40, s54, s[57+2*(ci_-5)]\n\ts_addc_u32 s41, s55, 0\n\t"
      ".else\n\t"
      "s_mov_b32 exec_lo, 0xfffff\n\ts_mov_b32 exec_hi, 0xfffff\n\t"
      "global_load_lds_dwordx4 v139, s[40:41] offset:1536 sc1\n\tglobal_load_lds_dwordx4 v140, s[40:41] offset:2560 sc1\n\t"
      "s_mov_b64 exec, -1\n\t"
      ".endif\n\t.endif\n\t.endm\n\t"
      "s_mov_b32 %[keep], m0\n\t"
      ".set j_, %c[j0]\n\t"
      ".rept %c[j1] - %c[j0]\n\t"
      ".set a_, 80 + 14 * (j_ & 1)\n\t.set b_, 94 - 14 * (j_ & 1)\n\t"
      ".if j_ == 6\n\ts_waitcnt vmcnt(5)\n\ts_barrier\n\t.endif\n\t"
      ".if j_ == 14 || j_ == 22 || j_ == 30\n\ts_waitcnt vmcnt(0)\n\ts_barrier\n\t.endif\n\t"
      ".if (j_ & 1) == 0\n\ts_setprio 3 - (((j_ + 2) >> 1) & 3)\n\t.endif\n\t"
      ".set ci_, 0\n\t"
      ".if j_ == 6\n\t.set ci_, 5\n\t.endif\n\t.if j_ == 14\n\t.set ci_, 6\n\t.endif\n\t.if j_ == 15\n\t.set ci_, 7\n\t.endif\n\t"
      ".if j_ == 22\n\t.set ci_, 8\n\t.endif\n\t.if j_ == 23\n\t.set ci_, 9\n\t.endif\n\t"
      "pos j_+2\n\t"
      "qk1 48, a_, 68\n\t.if j_ < 30\n\tds_read_b128 v[a_:a_+3], v[ar_] offset:of_\n\t.endif\n\txp 112, 32\n\t"
      "qk2 48, a_+4, 74\n\t.if j_ < 30\n\tds_read_b64 v[a_+4:a_+5], v[ar_+1] offset:of_\n\t.endif\n\txp 116, 36\n\t"
      "pos j_+1\n\t"
      "smf 16, b_+10, 120\n\t.if j_ < 31\n\tds_read_b128 v[b_+10:b_+13], v[ar_+2] offset:of_+1024\n\t.endif\n\txp 120, 40\n\t"
      "smf 0, a_+6, 112\n\t.if j_ < 30\n\ts_waitcnt lgkmcnt(3)\n\t.elseif j_ == 30\n\ts_waitcnt lgkmcnt(1)\n\t.else\n\ts_waitcnt lgkmcnt(0)\n\t.endif\n\txp 124, 44\n\t"
      ".if j_ < 31\n\tqk1 32, b_, 64\n\t.endif\n\tdma 0\n\t"
      ".if j_ == 2\n\tglobal_load_dwordx2 v[142:143], v141, s[68:69] sc1\n\t.endif\n\t"
      "xp 112, 48\n\t"
      ".if j_ < 31\n\tqk2 32, b_+4, 72\n\t.endif\n\tdma 1\n\txp 116, 52\n\t"
      "smf 0, a_+10, 120\n\tdma 2\n\txp 120, 56\n\t"
      "pos j_+2\n\t"
      "smf 16, a_+6, 112\n\t.if j_ < 30\n\tds_read_b128 v[a_+6:a_+9], v[ar_+2] offset:of_\n\t.endif\n\txp 124, 60\n\t"
      ".set j_, j_ + 1\n\t"
      ".endr\n\t"
      ".if %c[j1] == 32\n\ts_setprio 0\n\ts_nop 0\n\tsmf 16, 104, 120\n\ts_nop 15\n\t.else\n\ts_waitcnt vmcnt(0)\n\t.endif\n\t"
      "s_mov_b32 m0, %[keep]\n\t"
      ".purgem pos\n\t.purgem dma\n\t" AT_ASM_PURGE
      : "+{v[0:31]}"(o), "+{v[32:47]}"(s0), "+{v[112:127]}"(pp), "+{v[80:111]}"(fr), "+{v[128:143]}"(ad), [keep] "=&s"(keep)
      : "{v[64:79]}"(q), "{s[54:55]}"(ws), "{s[56:63]}"(so0), "{s[64:65]}"(so1), "{s66}"(m0v), "{s[68:69]}"(ctl), [j0] "n"(J0), [j1] "n"(J1)
      : "memory", "scc", "s40", "s41", "v48", "v49", "v50", "v51", "v52", "v53", "v54", "v55", "v56", "v57", "v58", "v59", "v60", "v61", "v62", "v63");
}

__device__ __forceinline__ void at_b(f32x16& o, const f32x16& mF, const f32x16& mD, const u32x16& q, const u32x8& wo, const char* src,
                                     unsigned d0, unsigned d1, unsigned d2, unsigned long long tag, char* flag) {
  unsigned keep;
  asm volatile(AT_ASM_MACROS
      ".macro wslot n\n\t.set n_, \\n\n\t.set bk_, n_ / 5\n\t.set pc_, n_ %% 5\n\t"
      ".if n_ < 15\n\t"
      ".if bk_ == 2\n\ts_cmp_lt_i32 s46, 0\n\ts_cbranch_scc1 9f\n\t.endif\n\t"
      ".if pc_ == 0\n\ts_mov_b32 m0, s[44+bk_]\n\ts_add_u32 s40, s42, bk_ * 8 * 32 * 1280\n\ts_addc_u32 s41, s43, 0\n\t.endif\n\t"
      ".if pc_ == 4\n\ts_add_u32 m0, m0, 4096\n\ts_nop 0\n\t.endif\n\t"
      "global_load_lds_dwordx4 v[144+pc_], s[40:41] offset:1024*(pc_&3)\n\t"
      ".if bk_ == 2\n\t9:\n\t.endif\n\t"
      ".endif\n\t"
      ".if n_ == 4\n\tv_mov_b32 v110, s50\n\tv_mov_b32 v111, s51\n\tv_mov_b32 v108, 0\n\ts_mov_b64 exec, 1\n\ts_waitcnt vmcnt(5)\n\t"
      "global_store_dwordx2 v108, v[110:111], s[52:53] sc1\n\ts_mov_b64 exec, -1\n\t.endif\n\t"
      ".endm\n\t"
      "s_mov_b32 %[keep], m0\n\t"
      "ds_read_b128 v[80:83], v77\n\tds_read_b64 v[84:85], v78\n\tds_read_b128 v[94:97], v77 offset:3584\n\tds_read_b64 v[98:99], v78 offset:3584\n\t"
      "ds_read_b128 v[86:89], v79\n\tds_read_b128 v[100:103], v79 offset:3584\n\tds_read_b128 v[90:93], v79 offset:1024\n\t"
      "s_waitcnt lgkmcnt(5)\n\t"
      "qk1c 32, 80, 64, 16\n\tds_read_b128 v[80:83], v77 offset:7168\n\tqk2 32, 84, 72\n\tds_read_b64 v[84:85], v78 offset:7168\n\t"
      ".set i_, 0\n\t.rept 8\n\tv_mov_b64 v[i_:i_+1], 0\n\t.set i_, i_ + 2\n\t.endr\n\t"
      "s_waitcnt lgkmcnt(2)\n\t"
      ".set t_, 0\n\t"
      ".rept 9\n\t"
      ".set a_, 80 + 14 * (t_ & 1)\n\t.set b_, 94 - 14 * (t_ & 1)\n\t.set c_, 32 + 16 * (t_ & 1)\n\t.set d_, 48 - 16 * (t_ & 1)\n\t"
      ".if t_ < 7\n\tqk1 d_, b_, 64\n\t.elseif t_ == 7\n\tqk1c d_, b_, 64, 128\n\t.endif\n\t"
      ".if t_ < 6\n\tds_read_b128 v[b_:b_+3], v77 offset:(t_+3)*3584\n\t.endif\n\t"
      "xp 112, c_\n\t"
      ".if t_ < 8\n\tqk2 d_, b_+4, 72\n\t.endif\n\t"
      ".if t_ < 6\n\tds_read_b64 v[b_+4:b_+5], v78 offset:(t_+3)*3584\n\t.endif\n\t"
      "wslot 2*t_\n\txp 116, c_+4\n\t"
      ".if t_ > 0\n\tsmf 0, b_+10, 120\n\t.endif\n\t"
      ".if t_ < 8\n\tds_read_b128 v[b_+10:b_+13], v79 offset:(t_+1)*3584+1024\n\t.endif\n\t"
      "xp 120, c_+8\n\t"
      "smf 0, a_+6, 112\n\t"
      ".if t_ < 6\n\ts_waitcnt lgkmcnt(3)\n\t.elseif t_ < 8\n\ts_waitcnt lgkmcnt(1)\n\t.else\n\ts_waitcnt lgkmcnt(0)\n\t.endif\n\t"
      ".if t_ < 7\n\tds_read_b128 v[a_+6:a_+9], v79 offset:(t_+2)*3584\n\t.endif\n\t"
      "wslot 2*t_+1\n\txp 124, c_+12\n\t"
      ".set t_, t_ + 1\n\t"
      ".endr\n\t"
      "s_nop 1\n\tsmf 0, 90, 120\n\ts_mov_b32 m0, %[keep]\n\ts_nop 15\n\t"
      ".purgem wslot\n\t" AT_ASM_PURGE
      : "=&{v[0:15]}"(o), [keep] "=&s"(keep)
      : "{v[144:151]}"(wo), "{v[16:31]}"(mF), "{v[128:143]}"(mD), "{v[64:79]}"(q), "{s[42:43]}"(src), "{s44}"(d0), "{s45}"(d1), "{s46}"(d2),
        "{s[50:51]}"(tag), "{s[52:53]}"(flag)
      : "memory", "scc", "s40", "s41", "v109", "v110", "v111",
        "v32", "v33", "v34", "v35", "v36", "v37", "v38", "v39", "v40", "v41", "v42", "v43", "v44", "v45", "v46", "v47",
        "v48", "v49", "v50", "v51", "v52", "v53", "v54", "v55", "v56", "v57", "v58", "v59", "v60", "v61", "v62", "v63",
        "v80", "v81", "v82", "v83", "v84", "v85", "v86", "v87", "v88", "v89", "v90", "v91", "v92", "v93", "v94", "v95",
        "v96", "v97", "v98", "v99", "v100", "v101", "v102", "v103", "v104", "v105", "v106", "v107", "v108",
        "v112", "v113", "v114", "v115", "v116", "v117", "v118", "v119", "v120", "v121", "v122", "v123", "v124", "v125", "v126", "v127");
}

__device__ __forceinline__ void proj_mm(f32x32& acc, const f32x32& oa, const f32x8& ob, u32x4 wa) {
  asm volatile(
      ".macro rdw k\n\t.set d_, 80 + 20 * ((\\k) & 1)\n\t"
      ".if (\\k) < 6\n\t.set a_, 120\n\t.set o_, (\\k) * 5120\n\t.else\n\t.set a_, 122\n\t.set o_, ((\\k) - 6) * 5120\n\t.endif\n\t.endm\n\t"
      ".macro rd1 i\n\tds_read_b64_tr_b16 v[d_+4*\\i:d_+4*\\i+1], v[a_] offset:o_+32*\\i\n\tds_read_b64_tr_b16 v[d_+4*\\i+2:d_+4*\\i+3], v[a_+1] offset:o_+32*\\i\n\t.endm\n\t"
      ".macro pm i\n\t"
      ".if k_ == 0\n\tv_mfma_f32_16x16x32_bf16 v[4*\\i:4*\\i+3], v[c_+4*\\i:c_+4*\\i+3], v[32:35], 0\n\t"
      ".else\n\tv_mfma_f32_16x16x32_bf16 v[4*\\i:4*\\i+3], v[c_+4*\\i:c_+4*\\i+3], v[32+4*k_:35+4*k_], v[4*\\i:4*\\i+3]\n\t.endif\n\t"
      ".if k_ < 9\n\trd1 \\i\n\ts_waitcnt lgkmcnt(8)\n\t.elseif \\i == 0\n\ts_waitcnt lgkmcnt(0)\n\t.endif\n\t.endm\n\t"
      "rdw 0\n\trd1 0\n\trd1 1\n\trd1 2\n\trd1 3\n\trd1 4\n\ts_waitcnt lgkmcnt(8)\n\t"
      ".set k_, 0\n\t.rept 10\n\t.set c_, 80 + 20 * (k_ & 1)\n\trdw k_+1\n\t"
      "pm 0\n\tpm 1\n\tpm 2\n\tpm 3\n\tpm 4\n\t"
      ".set k_, k_ + 1\n\t.endr\n\t"
      "s_nop 7\n\t"
      ".purgem rdw\n\t.purgem rd1\n\t.purgem pm\n\t"
      : "=&{v[0:31]}"(acc)
      : "{v[32:63]}"(oa), "{v[64:71]}"(ob), "{v[120:123]}"(wa)
      : "memory", "v80", "v81", "v82", "v83", "v84", "v85", "v86", "v87", "v88", "v89", "v90", "v91", "v92", "v93", "v94", "v95",
        "v96", "v97", "v98", "v99", "v100", "v101", "v102", "v103", "v104", "v105", "v106", "v107", "v108", "v109", "v110", "v111",
        "v112", "v113", "v114", "v115", "v116", "v117", "v118", "v119");
}
__device__ __forceinline__ void proj_ld(f32x32& oa, f32x8& ob, const char* src, unsigned off) {
  asm volatile(
      ".set k_, 0\n\t.rept 10\n\t"
      "global_load_dwordx4 v[32+4*k_:35+4*k_], v72, %[src] sc1\n\t"
      ".if k_ < 9\n\tv_add_u32 v72, 0x20000, v72\n\t.endif\n\t"
      ".set k_, k_ + 1\n\t.endr\n\t"
      : "=&{v[32:63]}"(oa), "=&{v[64:71]}"(ob), "+{v72}"(off) : [src] "s"(src) : "memory");
}
typedef __attribute__((address_space(3))) f32x4* lds_f4;
__device__ __forceinline__ void proj_send(const f32x32& acc, char* lds, int lw, int grp, int lane) {
  lds_ptr p = (lds_ptr)lds + 143360 + lw * 5120 + lane * 16;
  if (grp) {
    *(lds_f4)(p) = f32x4{acc[0], acc[1], acc[2], acc[3]}; *(lds_f4)(p + 1024) = f32x4{acc[4], acc[5], acc[6], acc[7]};
    *(lds_f4)(p + 4096) = f32x4{acc[16], acc[17], acc[18], acc[19]};
  } else {
    *(lds_f4)(p + 2048) = f32x4{acc[8], acc[9], acc[10], acc[11]}; *(lds_f4)(p + 3072) = f32x4{acc[12], acc[13], acc[14], acc[15]};
  }
}
__device__ __forceinline__ void proj_finish(const f32x32& acc, char* lds, int lw, int grp, int lane, char* dst) {
  lds_ptr p = (lds_ptr)lds + 143360 + lw * 5120 + lane * 16 + grp * 2048;
  const f32x4 r0 = *(lds_f4)(p), r1 = *(lds_f4)(p + 1024);
  float a[8];
#pragma unroll
  for (int e = 0; e < 4; ++e) { a[e] = (grp ? acc[8 + e] : acc[e]) + r0[e]; a[4 + e] = (grp ? acc[12 + e] : acc[4 + e]) + r1[e]; }
  const auto s0 = __builtin_amdgcn_permlane16_swap(pk_bf16(a[0], a[1]), pk_bf16(a[4], a[5]), false, false);
  const auto s1 = __builtin_amdgcn_permlane16_swap(pk_bf16(a[2], a[3]), pk_bf16(a[6], a[7]), false, false);
  char* row = dst + (lane & 15) * 2560;
  *(u32x4*)(row + grp * 64 + ((lane >> 4) & 1) * 32 + (lane >> 5) * 16) = u32x4{s0[0], s1[0], s0[1], s1[1]};
  if (!grp) {
    const f32x4 r2 = *(lds_f4)(p + 4096);
    *(u32x2*)(row + 128 + (lane >> 4) * 8) = u32x2{pk_bf16(acc[16] + r2[0], acc[17] + r2[1]), pk_bf16(acc[18] + r2[2], acc[19] + r2[3])};
  }
}

__global__ void __launch_bounds__(512, 2)
k_fused(const char* __restrict__ xa, const char* __restrict__ wa, const char* __restrict__ woa, const char* __restrict__ xb,
        const char* __restrict__ wb, const char* __restrict__ wob, char* __restrict__ ws, char* __restrict__ out) {
  __shared__ __attribute__((aligned(16))) char lds[163840];
  STAMP_DECL STAMP(0);
  int lane = threadIdx.x & 63;
  const int w = __builtin_amdgcn_readfirstlane(threadIdx.x >> 6);
  const int grp = w >> 2, lw = w & 3;
  const int bid = blockIdx.x, xcd = bid & 7, slot = bid >> 3;
  const unsigned lds0 = (unsigned)(uintptr_t)lds;
  char* ctl = ws + CTL_WS;
  unsigned long long tag;
  f32x32 acc;
  f32x32 x3, x4, wf; u32x4 wa4;
  const int br = xcd >> 2, mt = (xcd & 3) * 4 + (slot & 3), u = slot >> 2;
  const int win = (5 * ((mt >> 1) & 3)) >> 1;
  int j2 = win + 3 + (u < 7 ? u : 6); j2 = j2 >= 10 ? j2 - 10 : j2;
  const int tok0 = mt * 128 + lw * 32;
  const int vt2 = __builtin_amdgcn_readfirstlane(u < 7 ? grp : 2);
  const int head = xcd * 4 + (slot & 3), qb = slot >> 2;
  auto tile_of = [&](int i) {
    int T = (i >= 8 ? (qb - 9 + i) : ((((head >> 3) + (i >> 1)) & 3) + 4 * (i & 1))) * 8 + w;
    return T < 0 ? 0 : T;
  };
  bool ready;
  unsigned pol;
  {
    const bool q1 = u < 5;
    const int j1 = win + u - 5;
#define ROT ((u + 3 * (slot & 3)) % 10)
    const int rot = ROT;
    const char* X = br ? xb : xa;
    const char* W = br ? wb : wa;
    unsigned o1[4], o2[4];
    {
      const unsigned row = lane >> 3, pos = lane & 7, gap1 = q1 ? 128 : 1280;
#pragma unroll
      for (int j = 0; j < 4; ++j) {
        const unsigned wcom = ((j >> 1) * 8 + row) * 3840 + ((pos ^ (4 * (j >> 1) + (row & 2))) * 16) + 1024 - 1024 * j;
        const unsigned xo = (8 * j + row) * 1280 + ((pos ^ (4 * (j & 1) + (row >> 1))) * 16) + 1024 - 1024 * j;
        o1[j] = grp ? wcom + (j & 1) * gap1 : xo;
        o2[j] = wcom + (j & 1) * 1280;
      }
    }
    const char* sA = (grp ? W + 16 * lw * 3840 + (q1 ? u * 256 : 1280 + j1 * 128) : X + (mt * 128 + 32 * lw) * 1280) - 1024;
    const char* sB = W + 16 * lw * 3840 + 1280 + j2 * 128 - 1024;
    const unsigned str1 = grp ? 64 * 3840 : 128;
    const unsigned m0v = lds0 + grp * 16384 + lw * 4096;
    int kt = rot;
    glds16x4(sA + kt * str1, o1, m0v);
    kt = kt == 9 ? 0 : kt + 1;
    glds16x4(sA + kt * str1, o1, m0v + 32768);
    kt = kt == 9 ? 0 : kt + 1;
    const int r16 = lane & 15, g = lane >> 4, q = r16 >> 2, p = lane & 3;
    const unsigned xa0 = lds0 + (lw * 32 + r16) * 128 + ((g ^ (r16 >> 1)) * 16);
    const unsigned wa0 = lds0 + 2048 * g + 1024 * grp + 128 * q + 16 * ((p >> 1) ^ (4 * (g & 1) + (q & 2))) + 8 * (p & 1);
    u32x16 ad = {xa0, xa0 ^ 64, wa0, wa0 ^ 32, wa0 ^ 64, wa0 ^ 96, o1[0], o1[1], o1[2], o1[3], o2[0], o2[1], o2[2], o2[3], 0, 0};
    PIN(ad);
    WAITV(4); BARRIER();
    STAMP(1);
    int k2 = rot + grp; k2 = k2 >= 10 ? k2 - 10 : k2;
    f32x32 x0, x1, x2;
    const int vt1 = grp & ((u + 3) >> 3);
    unsigned long long epoch;
    g1_pass1(vt1, acc, x0, x1, x2, x3, x4, ad, sA, sB, kt * str1, str1, k2 * (64 * 3840), m0v, ctl, epoch);
    STAMP(2);
    tag = (((unsigned long long)__builtin_amdgcn_readfirstlane((unsigned)(epoch >> 32)) << 32) | (unsigned)__builtin_amdgcn_readfirstlane((unsigned)epoch)) + 1;
    lane = lane_id();
    u32x16 pk; u32x4 so;
    const int cg1 = q1 ? 2 * u + grp : 10 + 10 * grp + j1;
    g1_pack(acc, vt1, q1, (vt1 ? cg1 - 20 : cg1) * 64, lane, pk, so);
    const unsigned jj = (20 * head + 19 * (lane & 1)) >> 6;
    pol = FQ_OFF + ((lane & 8) ? jj * 64 + qb * 8 + 2 * lw + ((lane >> 1) & 1)
                               : (10 + 10 * ((lane >> 1) & 1) + jj) * 64 + ((lane & 4) ? tile_of(1) : tile_of(0))) * 128;
    u32x16 ad2 = {ad[2], ad[3], ad[4], ad[5], ad[10], ad[11], ad[12], ad[13], so[0], so[1], so[2], so[3], pol, 0, 0, 0};
    k2 += 6; k2 = k2 >= 10 ? k2 - 10 : k2;
    STAMP(3);
    ad2[15] = stv;
    g1_pass2a(vt2, acc, x0, x1, x2, x3, x4, wf, ad2, pk, g1_out(ws, vt1, br, tok0), sB, m0v, k2 * (64 * 3840), tag,
              ctl + FQ_OFF + ((br * 30 + cg1) * 64 + (tok0 >> 5)) * 128, ctl);
    wa4 = u32x4{ad2[0], ad2[1], ad2[2], ad2[3]};
    stv = ad2[15];
    pol = ad2[12];
    ready = flags_ok(ad2[8], ad2[9], tag);
    STAMP(6);
  }
  if (!ready) poll_flags(ctl, pol, tag);
  STAMP(7);
  lane = lane_id();
  const int odd = head & 1, o2 = 5 * (head >> 1) + 2, o1 = odd ? o2 + 1 : o2 - 2;
  const int r32 = lane & 31, h = lane >> 5;
  const unsigned g1o = (o1 + h) * 32768 + r32 * 16;
  const unsigned g2o = o2 * 32768 + r32 * 16;
  const unsigned fmap = odd ? (r32 < 16 ? r32 + 4 : r32 - 16) : r32;
  const unsigned gvo = h * 1280 + ((head & 3) * 20 + fmap) * 16;
  const u32x4 go = {g1o, g2o - 1024, gvo, gvo + V_OFF};
  auto koff = [&](int i) { return (unsigned)(QK_WS + (i >= 8) * QK_BR + 80 * 32768) + tile_of(i) * 512; };
  auto voff = [&](int i) { return (unsigned)(V_WS + (i >= 8) * V_BR - V_OFF) + (head >> 2) * (256 * 1280) + tile_of(i) * 5120; };
  const unsigned mytile = lds0 + w * TILE_B;
  auto const_rows = [&](int sl) {
    if (lane < 48) {
      const unsigned grp4 = lane / 12, row = 20 + lane - grp4 * 12;
      const unsigned cv = row == 20 ? 0x3F803F80u : 0u;
      *(__attribute__((address_space(3))) u32x4*)((lds_ptr)lds + sl * CHUNK_B + w * TILE_B + V_OFF + grp4 * 512 + row * 16) = u32x4{cv, cv, cv, cv};
    }
  };
  const int qt = qb * 8 + 2 * lw + grp;
  bf16x8 q1a, q1b, q1B; s16x4 q2a, q2b, q2B;
  {
    const char* qA = ws + QK_WS + (qb * 8 + 2 * lw) * 512;
    const unsigned q2o = g2o + odd * 8;
    asm volatile("global_load_dwordx4 %0, %4, %6 sc1\n\tglobal_load_dwordx2 %1, %5, %6 sc1\n\t"
                 "global_load_dwordx4 %2, %4, %6 offset:512 sc1\n\tglobal_load_dwordx2 %3, %5, %6 offset:512 sc1"
                 : "=&v"(q1a), "=&v"(q2a), "=&v"(q1b), "=&v"(q2b) : "v"(g1o), "v"(q2o), "s"(qA) : "memory");
  }
  tile_dma(ws + koff(0), ws + voff(0), mytile, go);
  tile_dma(ws + koff(1), ws + voff(1), mytile + CHUNK_B, go);
  const_rows(0); const_rows(1); const_rows(2);
  g1_pass2b(vt2, acc, x3, x4, wf, wa4);
  STAMP(8);
  if (u < 7) {
    u32x16 pk; u32x4 so;
    g1_pack(acc, grp, false, grp ? j2 * 64 : 640 + j2 * 64, lane, pk, so);
    char* ob = g1_out(ws, grp, br, tok0);
#pragma unroll
    for (int i = 0; i < 4; ++i) st16(ob, so[i], u32x4{pk[4 * i], pk[4 * i + 1], pk[4 * i + 2], pk[4 * i + 3]});
    WAITV(4);
  } else WAITV(0);
  asm volatile("s_waitcnt lgkmcnt(0)\n\ts_barrier" : "+v"(q1a), "+v"(q2a), "+v"(q1b), "+v"(q2b) :: "memory");
  STAMP(9);
  const_rows(3); const_rows(4);
  const unsigned k1o = lane * 16;
  const unsigned k2o = h ? (V_OFF + 320) : (K2_OFF + r32 * 16 + odd * 8);
  const unsigned vo = V_OFF + h * 512 + (r32 <= 20 ? r32 : (r32 < 28 ? 21 : 28)) * 16;
  if (h) { q2a = s16x4{0, 0, 0, 0}; q2b = s16x4{0, 0, 0, 0}; }
  const f32x16 zero16 = {};
  {
    lds_cptr tp = (lds_cptr)lds;
    const bf16x8 k1 = lds_b128(tp + k1o); const s16x4 k2 = lds_b64(tp + k2o);
    f32x16 sa = MFMA32(k1, q1a, zero16), sb = MFMA32(k1, q1b, zero16);
    sa = MFMA32K8(k2, q2a, sa); sb = MFMA32K8(k2, q2b, sb);
    q2a = with_negmax(q2a, xhalf_max(max16(sa, sa[0])), h);
    q2b = with_negmax(q2b, xhalf_max(max16(sb, sb[0])), h);
  }
  f32x16 o;
  {
    f32x32 oab; f32x16 s0, pp;
    f32x32 fr;
    const unsigned ab = lds0 + grp * TILE_B;
    const unsigned jj = (20 * head + 19 * (lane & 1)) >> 6;
    {
      const int i = 2 + ((lane >> 2) & 7);
      int T = (i >= 8 ? (qb - 9 + i) : ((((head >> 3) + (i >> 1)) & 3) + 4 * (i & 1))) * 8 + w;
      T = T < 0 ? 0 : T;
      pol = FQ_OFF + (lane >= 32 ? (30 + jj) * 64 + qt : ((i >= 8 ? 40 : 10) + 10 * ((lane >> 1) & 1) + jj) * 64 + T) * 128;
    }
    u32x16 ad = {ab + k1o, ab + k2o, ab + vo, ab + 2 * CHUNK_B + k1o, ab + 2 * CHUNK_B + k2o, ab + 2 * CHUNK_B + vo,
                 ab + 4 * CHUNK_B + k1o, ab + 4 * CHUNK_B + k2o, ab + 4 * CHUNK_B + vo, go[0], go[1], go[2], go[3], pol, 0, 0};
    const u32x4 qa4 = __builtin_bit_cast(u32x4, q1a), qb4 = __builtin_bit_cast(u32x4, q1b);
    const u32x2 qa2 = __builtin_bit_cast(u32x2, q2a), qb2 = __builtin_bit_cast(u32x2, q2b);
    u32x16 qq = {qa4[0], qa4[1], qa4[2], qa4[3], qb4[0], qb4[1], qb4[2], qb4[3], qa2[0], qa2[1], qb2[0], qb2[1], 0xDDDDu, 0, 0, 0};
    asm volatile(
        "ds_read_b128 v[80:83], v128\n\tds_read_b64 v[84:85], v129\n\tds_read_b128 v[94:97], v128 offset:7168\n\tds_read_b64 v[98:99], v129 offset:7168\n\t"
        "ds_read_b128 v[86:89], v130\n\tds_read_b128 v[100:103], v130 offset:7168\n\tds_read_b128 v[90:93], v130 offset:1024\n\t"
        "s_waitcnt lgkmcnt(5)\n\t"
        "v_mfma_f32_32x32x16_bf16 v[32:47], v[80:83], v[64:67], 0\n\t"
        "v_mfma_f32_32x32x8_bf16 v[32:47], v[84:85], v[72:73], v[32:47]\n\t"
        ".set i_, 0\n\t.rept 16\n\tv_mov_b64 v[i_:i_+1], 0\n\t.set i_, i_ + 2\n\t.endr\n\t"
        "v_mov_b64 v[104:105], 0\n\tv_mov_b64 v[106:107], 0\n\t"
        "v_mov_b64 v[120:121], 0\n\tv_mov_b64 v[122:123], 0\n\tv_mov_b64 v[124:125], 0\n\tv_mov_b64 v[126:127], 0\n\t"
        "s_waitcnt vmcnt(0) lgkmcnt(0)"
        : "=&{v[0:31]}"(oab), "=&{v[112:127]}"(pp), "=&{v[32:47]}"(s0), "=&{v[80:111]}"(fr), "+{v[64:79]}"(qq), "+{v[128:143]}"(ad) :: "memory");
    if (u < 7) put_flag(ctl + FQ_OFF + ((br * 30 + 10 + 10 * grp + j2) * 64 + (tok0 >> 5)) * 128, tag, lane);
    STAMP(10);
    const u32x8 so0 = {koff(5), voff(5), koff(6), voff(6), koff(7), voff(7), koff(8), voff(8)};
    const u32x2 so1 = {koff(9), voff(9)};
    at_a<0, 4>(oab, s0, pp, fr, qq, ad, ws, so0, so1, mytile, ctl);
    if (!flags_ok(ad[14], ad[15], tag)) poll_flags(ctl, ad[13], tag);
    STAMP(11);
    if (bid == 0 && w == 4) {
#pragma unroll 1
      for (int i = 0; i < 4; ++i) {
        const unsigned e = i * 64 + lane, eu = e & 7, em = (e >> 3) & 15;
        poll_flags(ctl, FQ_OFF + (((e >> 7) * 30 + (eu < 5 ? 2 * eu : 5 + eu + ((5 * ((em >> 1) & 3)) >> 1))) * 64 + 4 * em) * 128, tag);
      }
      put_flag(ctl, tag, lane);
    }
#pragma unroll
    for (int i = 2; i < 5; ++i) tile_dma(ws + koff(i), ws + voff(i), mytile + i * CHUNK_B, u32x4{ad[9], ad[10], ad[11], ad[12]});
    {
      const unsigned L = (slot * 4 + lw) * 50 + (lane < 50 ? lane : 49);
      unsigned keep;
      asm volatile("s_mov_b32 %0, m0\n\ts_mov_b32 m0, %3\n\ts_nop 0\n\tglobal_load_lds_dword %1, %2\n\ts_mov_b32 m0, %0"
                   : "=&s"(keep) : "v"(L * 128), "s"(grp ? wob : woa), "s"(lds0 + 143360) : "memory");
    }
    at_a<4, 32>(oab, s0, pp, fr, qq, ad, ws, so0, so1, mytile, ctl);
    STAMP(12);
    {
      const char* qB = ws + QK_WS + QK_BR + qt * 512;
      const unsigned q2o = g2o + odd * 8;
      asm volatile("global_load_dwordx4 %0, %2, %4 sc1\n\tglobal_load_dwordx2 %1, %3, %4 sc1"
                   : "=&v"(q1B), "=&v"(q2B) : "v"(g1o), "v"(q2o), "s"(qB) : "memory");
    }
    if (qb == 0) {
      lds_ptr z = (lds_ptr)lds + 3 * CHUNK_B + w * TILE_B + lane * 16;
      *(lds_f4)(z) = f32x4{}; *(lds_f4)(z + 1024) = f32x4{}; *(lds_f4)(z + 2048) = f32x4{}; *(lds_f4)(z + 2560) = f32x4{};
    }
    lds_ptr xb = (lds_ptr)lds + lane * 16;
#pragma unroll
    for (int i = 0; i < 3; ++i) {
      f32x4 t;
#pragma unroll
      for (int e = 0; e < 4; ++e) { t[e] = grp ? oab[4 * i + e] : oab[16 + 4 * i + e]; o[4 * i + e] = grp ? oab[16 + 4 * i + e] : oab[4 * i + e]; }
      *(lds_f4)(xb + w * 3072 + i * 1024) = t;
    }
    asm volatile("s_waitcnt lgkmcnt(0)\n\ts_barrier" ::: "memory");
#pragma unroll
    for (int i = 0; i < 3; ++i) {
      const f32x4 r = *(lds_f4)(xb + (w ^ 4) * 3072 + i * 1024);
#pragma unroll
      for (int e = 0; e < 4; ++e) o[4 * i + e] += r[e];
    }
    PIN(o);
    asm volatile("s_waitcnt lgkmcnt(0)\n\ts_barrier" ::: "memory");
  }
  auto exact = [&](int b, bf16x8 q1, s16x4 q2, int t0, int t1, const f32x16& c0, const f32x16& c1) {
    __builtin_amdgcn_fence(__ATOMIC_ACQUIRE, "agent");
    const int ln = lane_id(), r32 = ln & 31, h = ln >> 5;
    if (h) q2 = s16x4{0, 0, 0, 0};
    const char* kg1 = ws + QK_WS + b * QK_BR + (size_t)(80 + o1 + h) * 32768 + r32 * 16;
    const char* kg2 = ws + QK_WS + b * QK_BR + (size_t)(80 + o2) * 32768 + r32 * 16 + odd * 8;
    const char* vg = ws + V_WS + b * V_BR + (head >> 2) * (256 * 1280) + h * 1280 +
                     ((head & 3) * 20 + (odd ? (r32 < 16 ? r32 + 4 : r32 - 16) : r32)) * 16;
    const u32x4 vc = r32 == 20 ? u32x4{0x3F803F80u, 0x3F803F80u, 0x3F803F80u, 0x3F803F80u} : u32x4{0u, 0u, 0u, 0u};
    float m = -INFINITY;
    f32x16 r = zero16;
#pragma unroll 1
    for (int ps = 0; ps < 2; ++ps) {
#pragma unroll 1
      for (int t = t0; t <= t1; ++t) {
        f32x16 s = MFMA32(*(const bf16x8*)(kg1 + t * 512), q1, t == t1 ? c1 : (t == t0 ? c0 : zero16));
        s = MFMA32K8(*(const s16x4*)(kg2 + t * 512), q2, s);
        if (ps == 0) { m = max16(s, m); continue; }
#pragma unroll
        for (int e = 0; e < 16; ++e) s[e] = __builtin_amdgcn_exp2f(s[e] - m);
        u32x4 va = vc, vb = vc;
        if (r32 < 20) { va = *(const u32x4*)(vg + t * 5120); vb = *(const u32x4*)(vg + t * 5120 + 2560); }
        r = smfmac(__builtin_bit_cast(bf16x8, va), lo8(s), r);
        r = smfmac(__builtin_bit_cast(bf16x8, vb), hi8(s), r);
      }
      m = xhalf_max(m);
    }
    return r;
  };
  auto unsafe = [&](const f32x16& r, float l) {
    float amax = 0.f;
#pragma unroll
    for (int e = 0; e < 12; ++e) amax = __builtin_fmaxf(amax, __builtin_fabsf(r[e]));
    return __any(!(l <= 1.2676506e30f) || !(l >= 5.4210109e-20f) || !(amax <= 3.0e38f));
  };
  char* oA = ws + O_WS + qt * 512;
  {
    float l = row_sum(o);
    if (__builtin_expect(unsafe(o, l), 0)) { o = exact(0, grp ? q1b : q1a, grp ? q2b : q2a, 0, 63, zero16, zero16); l = row_sum(o); }
    asm volatile("s_waitcnt vmcnt(0)" : "+v"(q1B), "+v"(q2B) :: "memory");
    store_o(o, l, oA, g1o, g2o + odd * 8, h);
  }
  STAMP(13);
  const int pm = xcd * 4 + (slot & 3), pn = slot >> 2;
  u32x8 wo;
#pragma unroll
  for (int i = 0; i < 5; ++i) {
    const unsigned v = 64 * i + lane, oc = v / 80, rr = (v - oc * 80) / 10, ch = v - oc * 80 - rr * 10;
    wo[i] = (8 * oc + (rr ^ (4 * (oc & 1)))) * 1280 + ch * 16 + 4096 - 1024 * (i & 3);
  }
  const unsigned polo = FO_OFF + ((grp * 16 + (lane & 15)) * 64 + pm * 2 + (lw >> 1)) * 128;
  wo[5] = 0; wo[6] = 0; wo[7] = 0;
  char* flagO = ctl + FO_OFF + (head * 64 + qt) * 128;
  u32x2 pa;
  {
    if (h) q2B = s16x4{0, 0, 0, 0};
    f32x16 maskD, maskF;
#pragma unroll
    for (int r = 0; r < 16; ++r) {
      const int key = (r & 3) + 8 * (r >> 2) + 4 * h;
      maskD[r] = key <= r32 ? 0.f : -INFINITY;
      maskF[r] = key > r32 ? 0.f : -INFINITY;
    }
    const unsigned d0 = lds0 + w * 5120;
    const unsigned tb = lds0 + 3 * CHUNK_B + (2 * lw + grp) * TILE_B;
    lds_cptr tp = (lds_cptr)lds + 3 * CHUNK_B + (2 * lw + grp + 7) * TILE_B;
    f32x16 s1 = MFMA32(lds_b128(tp + k1o), q1B, zero16), s2 = MFMA32(lds_b128(tp + TILE_B + k1o), q1B, maskD);
    s1 = MFMA32K8(lds_b64(tp + k2o), q2B, s1); s2 = MFMA32K8(lds_b64(tp + TILE_B + k2o), q2B, s2);
    const float m1 = max16(s1, s1[0]);
    const u32x2 qm = __builtin_bit_cast(u32x2, with_negmax(q2B, xhalf_max(max16(s2, qt ? m1 : -INFINITY)), h));
    const u32x4 q4 = __builtin_bit_cast(u32x4, q1B);
    const u32x16 qq = {q4[0], q4[1], q4[2], q4[3], 0, 0, 0, 0, qm[0], qm[1], 0, 0, 0xDDDDu, tb + k1o, tb + k2o, tb + vo};
    f32x16 ob;
    at_b(ob, maskF, maskD, qq, wo, woa + pn * 160 + w * (32 * 1280) - 4096, d0, d0 + 8 * 5120,
         __builtin_amdgcn_readfirstlane(grp ? 0x80000000u : d0 + 143360), tag, flagO);
    asm volatile("global_load_dwordx2 %0, %1, %2 sc1" : "=&v"(pa) : "v"(polo), "s"(ctl) : "memory");
    float l = row_sum(ob);
    if (__builtin_expect(unsafe(ob, l), 0)) {
      WAITV(0);
      ob = exact(1, q1B, q2B, qb ? qt - 8 : 0, qt, qb ? maskF : zero16, maskD); l = row_sum(ob);
    }
    STAMP(14);
    store_o(ob, l, oA + O_BR, g1o, g2o + odd * 8, h);
  }
  {
    const int r16 = lane & 15, g = lane >> 4, q = r16 >> 2, p = lane & 3;
    const unsigned wl0 = lds0 + g * 1280 + (q ^ (4 * (g & 1))) * 160 + p * 8, wl1 = lds0 + g * 1280 + ((4 + q) ^ (4 * (g & 1))) * 160 + p * 8;
    const unsigned ooff = (grp * 40 + g) * 32768 + (pm * 64 + lw * 16 + r16) * 16;
    char* dst = out + (size_t)(pm * 64 + lw * 16) * 2560 + pn * 160;
    f32x32 oa, pacc; f32x8 ob;
    asm volatile("s_waitcnt vmcnt(2)" : "+v"(pa) :: "memory");
    if (!flags_ok(pa[0], pa[1], tag)) poll_flags(ctl, polo, tag);
    proj_ld(oa, ob, ws + O_WS, ooff);
    BARRIER();
    WAITV(10);
    put_flag(flagO + 32 * 64 * 128, tag, lane);
    STAMP(15);
    const char* wsrc = wob + pn * 160 + w * (32 * 1280) - 4096;
    wo_dma(wsrc, lds0 + 86016 + w * 5120, wo);
    if (w < 2) wo_dma(wsrc + 8 * 32 * 1280, lds0 + 86016 + (8 + w) * 5120, wo);
    if (w < 2) asm volatile("s_waitcnt vmcnt(11)" ::: "memory"); else asm volatile("s_waitcnt vmcnt(6)" ::: "memory");
    asm volatile("" : "+{v[32:63]}"(oa), "+{v[64:71]}"(ob));
    {
      const unsigned b1 = grp * 51200, b2 = grp ? 143360 : 30720;
      proj_mm(pacc, oa, ob, u32x4{wl0 + b1, wl1 + b1, wl0 + b2, wl1 + b2});
    }
    STAMP(16);
    BARRIER();
    wo_dma(wsrc + 10 * 32 * 1280, lds0 + w * 5120, wo);
    if (w < 2) wo_dma(wsrc + 18 * 32 * 1280, lds0 + (8 + w) * 5120, wo);
    proj_send(pacc, lds, lw, grp, lane);
    asm volatile("s_waitcnt lgkmcnt(0)\n\ts_barrier" ::: "memory");
    proj_finish(pacc, lds, lw, grp, lane, dst);
    STAMP(17);
    poll_flags(ctl, polo + 32 * 64 * 128, tag);
    STAMP(18);
    proj_ld(oa, ob, ws + O_WS + O_BR, ooff);
    asm volatile("s_waitcnt lgkmcnt(0)\n\ts_barrier\n\ts_waitcnt vmcnt(0)" : "+{v[32:63]}"(oa), "+{v[64:71]}"(ob) :: "memory");
    STAMP(19);
    {
      const unsigned b1 = grp ? 0 : 86016;
      proj_mm(pacc, oa, ob, u32x4{wl0 + b1, wl1 + b1, wl0 + b1 + 30720, wl1 + b1 + 30720});
    }
    STAMP(20);
    proj_send(pacc, lds, lw, grp, lane);
    asm volatile("s_waitcnt lgkmcnt(0)\n\ts_barrier" ::: "memory");
    proj_finish(pacc, lds, lw, grp, lane, dst + 1280);
  }
  STAMP_OUT(ws, 0);
}

extern "C" void kernel_launch(void* const* d_in, const int* in_sizes, int n_in, void* d_out, int out_size, void* d_ws,
                              size_t ws_size, hipStream_t stream) {
  k_fused<<<256, 512, 0, stream>>>((const char*)d_in[0], (const char*)d_in[1], (const char*)d_in[2], (const char*)d_in[3],
                                   (const char*)d_in[4], (const char*)d_in[5], (char*)d_ws, (char*)d_out);
}
```

```cpp
#include <hip/hip_runtime.h>
#include <hip/hip_bf16.h>
#include <stdint.h>
#include <type_traits>

typedef __attribute__((ext_vector_type(8))) __bf16 bf16x8;
typedef __attribute__((ext_vector_type(16))) __bf16 bf16x16;
typedef __attribute__((ext_vector_type(4))) short s16x4;
typedef __attribute__((ext_vector_type(8))) short s16x8;
typedef __attribute__((ext_vector_type(2))) unsigned u32x2;
typedef __attribute__((ext_vector_type(4))) unsigned u32x4;
typedef __attribute__((ext_vector_type(4))) float f32x4;
typedef __attribute__((ext_vector_type(8))) float f32x8;
typedef __attribute__((ext_vector_type(16))) float f32x16;
typedef __attribute__((ext_vector_type(32))) float f32x32;
typedef __attribute__((address_space(3))) const char* lds_cptr;
typedef __attribute__((address_space(3))) char* lds_ptr;

constexpr size_t QK_WS = 0, QK_BR = (size_t)160 * 32768;
constexpr size_t V_WS = 2 * QK_BR, V_BR = (size_t)8 * 256 * 1280;
constexpr size_t O_WS = V_WS + 2 * V_BR, O_BR = (size_t)80 * 32768;
constexpr size_t CTL_WS = O_WS + 2 * O_BR;
constexpr unsigned FQ_OFF = CTL_WS + 4096, FO_OFF = FQ_OFF + 2 * 30 * 64 * 128;
constexpr float QSCALE = 0.22360679774997896f * 1.4426950408889634f;

#define STAMP_DECL
#define STV_PUT(x)
#define STV_GET(x)
#define STAMP(i) do {} while (0)
#define STAMP_OUT(ws_, kid) do {} while (0)
__device__ __forceinline__ int lane_id() {
  int l; asm volatile("v_mbcnt_lo_u32_b32 %0, -1, 0\n\tv_mbcnt_hi_u32_b32 %0, -1, %0" : "=v"(l)); return l;
}
#define SBAR() __builtin_amdgcn_sched_barrier(0)
#define PIN(x) asm volatile("" : "+v"(x))
#define PINS(x) asm volatile("" : "+s"(x))
#define WAITV(N) asm volatile("s_waitcnt vmcnt(" #N ")" ::: "memory")
#define BARRIER() asm volatile("s_barrier" ::: "memory")
#define WAITV_BAR(N) asm volatile("s_waitcnt vmcnt(" #N ") lgkmcnt(0)\n\ts_barrier" ::: "memory")

__device__ __forceinline__ void glds16(const void* sbase, unsigned voff, unsigned lds) {
  unsigned keep;
  asm volatile("s_mov_b32 %0, m0\n\ts_mov_b32 m0, %3\n\ts_nop 0\n\tglobal_load_lds_dwordx4 %1, %2\n\ts_mov_b32 m0, %0"
               : "=&s"(keep) : "v"(voff), "s"(sbase), "s"(lds) : "memory");
}
__device__ __forceinline__ void glds16x4(const void* sbase, const unsigned (&voff)[4], unsigned lds) {
  unsigned keep;
  asm volatile("s_mov_b32 %0, m0\n\ts_mov_b32 m0, %6\n\ts_nop 0\n\tglobal_load_lds_dwordx4 %1, %5\n\t"
               "global_load_lds_dwordx4 %2, %5 offset:1024\n\tglobal_load_lds_dwordx4 %3, %5 offset:2048\n\t"
               "global_load_lds_dwordx4 %4, %5 offset:3072\n\ts_mov_b32 m0, %0"
               : "=&s"(keep) : "v"(voff[0]), "v"(voff[1]), "v"(voff[2]), "v"(voff[3]), "s"(sbase), "s"(lds) : "memory");
}
__device__ __forceinline__ unsigned pk_bf16(float lo, float hi) {
  typedef __attribute__((ext_vector_type(2))) float f32x2_;
  typedef __attribute__((ext_vector_type(2))) __bf16 bf16x2_;
  const f32x2_ v = {lo, hi};
  return __builtin_bit_cast(unsigned, __builtin_convertvector(v, bf16x2_));
}
__device__ __forceinline__ void st16(char* sbase, unsigned voff, u32x4 v) {
  asm volatile("global_store_dwordx4 %0, %1, %2 sc1\n\ts_nop 1" :: "v"(voff), "v"(v), "s"(sbase) : "memory");
}
__device__ __forceinline__ void st8lo(char* sbase, unsigned voff, u32x2 v) {
  asm volatile("s_mov_b32 exec_hi, 0\n\tglobal_store_dwordx2 %0, %1, %2 sc1\n\ts_mov_b32 exec_hi, -1" :: "v"(voff), "v"(v), "s"(sbase) : "memory");
}
__device__ __forceinline__ bf16x8 lds_b128(lds_cptr p) { return *(const __attribute__((address_space(3))) bf16x8*)p; }
__device__ __forceinline__ s16x4 lds_b64(lds_cptr p) { return *(const __attribute__((address_space(3))) s16x4*)p; }
__device__ __forceinline__ s16x4 lds_tr(lds_cptr p) {
  return __builtin_amdgcn_ds_read_tr16_b64_v4i16((__attribute__((address_space(3))) s16x4*)p);
}
__device__ __forceinline__ bf16x8 cat8(s16x4 a, s16x4 b) {
  s16x8 v = {a[0], a[1], a[2], a[3], b[0], b[1], b[2], b[3]};
  return __builtin_bit_cast(bf16x8, v);
}
#define MFMA32(a, b, c) __builtin_amdgcn_mfma_f32_32x32x16_bf16(a, b, c, 0, 0, 0)
#define MFMA32K8(a, b, c) __builtin_amdgcn_mfma_f32_32x32x8bf16_1k(a, b, c, 0, 0, 0)
#define MFMA16(a, b, c) __builtin_amdgcn_mfma_f32_16x16x32_bf16(a, b, c, 0, 0, 0)

typedef __attribute__((ext_vector_type(8))) unsigned u32x8;
typedef __attribute__((ext_vector_type(16))) unsigned u32x16;
#define AWAIT(d, d1, off) \
    "s_mov_b32 s48, 0x200000\n\t5:\n\ts_waitcnt vmcnt(0)\n\tv_cmp_ne_u64 vcc, s[50:51], v[" #d ":" #d1 "]\n\ts_sub_u32 s48, s48, 1\n\t" \
    "s_cbranch_vccz 4f\n\ts_cbranch_scc1 4f\n\ts_sleep 2\n\tglobal_load_dwordx2 v[" #d ":" #d1 "], v" #off ", s[54:55] sc1\n\ts_branch 5b\n\t4:\n\t"
#define G1_ASM_MACROS \
    ".macro pack vt\n\t.set i_, 0\n\t.rept 4\n\t" \
    ".if \\vt\n\t.set e_, 8 * i_\n\t.set f_, 8 * i_ + 4\n\t.else\n\t.set e_, 4 * i_\n\t.set f_, 4 * i_ + 16\n\t.endif\n\t" \
    "v_cvt_pk_bf16_f32 v[80+4*i_], v[e_], v[e_+1]\n\tv_cvt_pk_bf16_f32 v[81+4*i_], v[e_+2], v[e_+3]\n\t" \
    "v_cvt_pk_bf16_f32 v[82+4*i_], v[f_], v[f_+1]\n\tv_cvt_pk_bf16_f32 v[83+4*i_], v[f_+2], v[f_+3]\n\t" \
    ".set i_, i_ + 1\n\t.endr\n\t.set i_, 0\n\t.rept 4\n\t" \
    "v_permlane16_swap_b32 v[80+4*i_], v[82+4*i_]\n\tv_permlane16_swap_b32 v[81+4*i_], v[83+4*i_]\n\t" \
    ".set i_, i_ + 1\n\t.endr\n\t.endm\n\t" \
    ".macro mm vt, c, x, w\n\t" \
    ".if (\\x) < 104\n\t"    \
    ".if \\vt\n\tv_mfma_f32_16x16x32_bf16 v[\\c:\\c+3], v[\\x:\\x+3], v[\\w:\\w+3], 0\n\t" \
    ".else\n\tv_mfma_f32_16x16x32_bf16 v[\\c:\\c+3], v[\\w:\\w+3], v[\\x:\\x+3], 0\n\t.endif\n\t" \
    ".elseif \\vt\n\tv_mfma_f32_16x16x32_bf16 v[\\c:\\c+3], v[\\x:\\x+3], v[\\w:\\w+3], v[\\c:\\c+3]\n\t" \
    ".else\n\tv_mfma_f32_16x16x32_bf16 v[\\c:\\c+3], v[\\w:\\w+3], v[\\x:\\x+3], v[\\c:\\c+3]\n\t.endif\n\t" \
    ".endm\n\t" \
    ".macro rx d, a, off\n\tds_read_b128 v[\\d:\\d+3], v\\a offset:\\off\n\tds_read_b128 v[\\d+4:\\d+7], v\\a offset:\\off+2048\n\t.endm\n\t" \
    ".macro rw d, a, off\n\tds_read_b64_tr_b16 v[\\d:\\d+1], v\\a offset:\\off\n\tds_read_b64_tr_b16 v[\\d+2:\\d+3], v\\a offset:\\off+512\n\t.endm\n\t" \
    ".macro bump lo, n, val\n\t.set bi_, \\lo\n\t.rept \\n\n\tv_add_u32 v[bi_], \\val, v[bi_]\n\t.set bi_, bi_+1\n\t.endr\n\t.endm\n\t"
#define G1_ASM_PURGE ".purgem pack\n\t.purgem mm\n\t.purgem rx\n\t.purgem rw\n\t.purgem bump\n\t"

__device__ __forceinline__ void g1_pass1(int vt, int kv, f32x32& x0, f32x32& x1, f32x32& x2, f32x32& x3, f32x32& x4, u32x8& adl, u32x8& adh, u32x16& pk,
                                         const char* sA, const char* sB, unsigned off1, unsigned str1, unsigned off2, unsigned m0v) {
  unsigned keep, tmp;
  const unsigned sAl = (unsigned)(uintptr_t)sA, sAh = (unsigned)((uintptr_t)sA >> 32);
  const unsigned sBl = (unsigned)(uintptr_t)sB, sBh = (unsigned)((uintptr_t)sB >> 32);
  asm volatile(
      G1_ASM_MACROS
      ".macro p1body vt\n\t"
      ".set st_, 0\n\t"
      ".rept 10\n\t"
      ".set xr_, 96 + 16 * st_\n\t"
      ".set o_, ((st_ %% 5) & 1) * 32768\n\t"
      ".set n_, (((st_ + 1) %% 5) & 1) * 32768\n\t"
      "s_waitcnt lgkmcnt(6)\n\t"
      "mm \\vt, 0, xr_, 32\n\trw 48, 64, o_+16384+8192\n\t"
      "mm \\vt, 16, xr_+4, 32\n\trx xr_+8, 78, o_\n\ts_waitcnt lgkmcnt(8)\n\t"
      "mm \\vt, 4, xr_, 36\n\trw 52, 65, o_+16384+8192\n\t"
      "mm \\vt, 20, xr_+4, 36\n\trw 56, 66, o_+16384+8192\n\ts_waitcnt lgkmcnt(10)\n\t"
      "mm \\vt, 8, xr_, 40\n\trw 60, 67, o_+16384+8192\n\t"
      "mm \\vt, 24, xr_+4, 40\n\ts_waitcnt lgkmcnt(10)\n\t"
      "mm \\vt, 12, xr_, 44\n\t"
      ".if st_ < 10 - 3\n\t"
      "s_add_u32 s40, %[sAl], %[off1]\n\ts_addc_u32 s41, %[sAh], 0\n\t"
      "s_add_u32 %[off1], %[off1], %[str1]\n\ts_cmp_eq_u32 %[off1], %[lim]\n\ts_cselect_b32 %[off1], 0, %[off1]\n\t"
      ".else\n\t"
      "s_add_u32 s40, %[sBl], %[off2]\n\ts_addc_u32 s41, %[sBh], 0\n\t"
      "s_add_u32 %[off2], %[off2], 2*64*3840\n\ts_cmp_ge_u32 %[off2], 640*3840\n\ts_cselect_b32 %[tmp], 640*3840, 0\n\ts_sub_u32 %[off2], %[off2], %[tmp]\n\t"
      ".endif\n\t"
      "mm \\vt, 28, xr_+4, 44\n\t"
      "s_add_u32 m0, %[m0v], ((st_ + 3) %% 5) * 32768\n\t"
      ".if (st_ %% 5) == 1 || (st_ %% 5) == 3\n\tbump 64, 4, 0x10000\n\tbump 77, 2, 0x10000\n\t.endif\n\t"
      ".if (st_ %% 5) == 4\n\tbump 64, 4, 0xfffe0000\n\tbump 77, 2, 0xfffe0000\n\t.endif\n\t"
      "s_waitcnt lgkmcnt(6)\n\t"
      "mm \\vt, 0, xr_+8, 48\n\t"
      "mm \\vt, 16, xr_+12, 48\n\t"
      "s_waitcnt vmcnt(4) lgkmcnt(0)\n\ts_barrier\n\t"
      ".set vo_, 72 - 4 * ((st_ + 3) / 10)\n\t"
      "mm \\vt, 4, xr_+8, 52\n\t"
      ".if st_ < 9\n\trw 32, 64, n_+16384\n\t.endif\n\t"
      "global_load_lds_dwordx4 v[vo_], s[40:41]\n\t"
      "mm \\vt, 20, xr_+12, 52\n\t"
      ".if st_ < 9\n\trx xr_+16, 77, n_\n\t.endif\n\t"
      "global_load_lds_dwordx4 v[vo_+1], s[40:41] offset:1024\n\t"
      "mm \\vt, 8, xr_+8, 56\n\t"
      ".if st_ < 9\n\trw 36, 65, n_+16384\n\t.endif\n\t"
      "global_load_lds_dwordx4 v[vo_+2], s[40:41] offset:2048\n\t"
      "mm \\vt, 24, xr_+12, 56\n\t"
      ".if st_ < 9\n\trw 40, 66, n_+16384\n\t.endif\n\t"
      "global_load_lds_dwordx4 v[vo_+3], s[40:41] offset:3072\n\t"
      "mm \\vt, 12, xr_+8, 60\n\t"
      ".if st_ < 9\n\trw 44, 67, n_+16384\n\t.endif\n\t"
      "mm \\vt, 28, xr_+12, 60\n\t"
      ".set st_, st_ + 1\n\t"
      ".endr\n\t"
      ".endm\n\t"
      "rw 32, 64, 16384\n\trx 96, 77, 0\n\trw 36, 65, 16384\n\trw 40, 66, 16384\n\trw 44, 67, 16384\n\t"
      "s_mov_b32 %[keep], m0\n\ts_add_u32 m0, %[m0v], 65536\n\t"
      "s_add_u32 s40, %[sAl], %[off1]\n\ts_addc_u32 s41, %[sAh], 0\n\t"
      "s_add_u32 %[off1], %[off1], %[str1]\n\ts_cmp_eq_u32 %[off1], %[lim]\n\ts_cselect_b32 %[off1], 0, %[off1]\n\t"
      "global_load_lds_dwordx4 v72, s[40:41]\n\tglobal_load_lds_dwordx4 v73, s[40:41] offset:1024\n\t"
      "global_load_lds_dwordx4 v74, s[40:41] offset:2048\n\tglobal_load_lds_dwordx4 v75, s[40:41] offset:3072\n\t"
      "s_cmp_lg_u32 %[vt], 0\n\ts_cbranch_scc1 1f\n\t"
      "p1body 0\n\t"
      "v_mov_b64 v[72:73], v[80:81]\n\tv_mov_b64 v[74:75], v[82:83]\n\ts_mov_b32 s40, %[sc]\n\ts_cmp_lg_u32 %[kv], 0\n\ts_nop 3\n\ts_cbranch_scc1 3f\n\t"
      ".set i_, 0\n\t.rept 16\n\tv_pk_mul_f32 v[i_:i_+1], v[i_:i_+1], s[40:41] op_sel_hi:[1,0]\n\t.set i_, i_ + 2\n\t.endr\n\t"
      "3:\n\tpack 0\n\ts_branch 2f\n\t"
      "1:\n\tp1body 1\n\t"
      "v_mov_b64 v[72:73], v[80:81]\n\tv_mov_b64 v[74:75], v[82:83]\n\ts_nop 5\n\tpack 1\n\t"
      "2:\n\ts_mov_b32 m0, %[keep]\n\t"
      ".purgem p1body\n\t" G1_ASM_PURGE
      : "=&{v[96:127]}"(x0), "=&{v[128:159]}"(x1), "=&{v[160:191]}"(x2), "=&{v[192:223]}"(x3), "=&{v[224:255]}"(x4),
        "+{v[64:71]}"(adl), "+{v[72:79]}"(adh), "+{v[80:95]}"(pk), [keep] "=&s"(keep), [tmp] "=&s"(tmp), [off1] "+s"(off1), [off2] "+s"(off2)
      : [sAl] "s"(sAl), [sAh] "s"(sAh), [sBl] "s"(sBl), [sBh] "s"(sBh), [str1] "s"(str1), [m0v] "s"(m0v), [vt] "s"(vt), [lim] "s"(10 * str1), [kv] "s"(kv),
        [sc] "n"(__builtin_bit_cast(unsigned, QSCALE))
      : "memory", "scc", "s40", "s41", "v0", "v1", "v2", "v3", "v4", "v5", "v6", "v7", "v8", "v9", "v10", "v11", "v12", "v13", "v14", "v15",
        "v16", "v17", "v18", "v19", "v20", "v21", "v22", "v23", "v24", "v25", "v26", "v27", "v28", "v29", "v30", "v31", "v32", "v33", "v34", "v35", "v36", "v37", "v38", "v39", "v40", "v41", "v42", "v43", "v44", "v45", "v46", "v47",
        "v48", "v49", "v50", "v51", "v52", "v53", "v54", "v55", "v56", "v57", "v58", "v59", "v60", "v61", "v62", "v63");
}

#define G1_P2_MACROS \
      G1_ASM_MACROS \
      ".macro slot n\n\t.set n_, \\n\n\t"    \
      ".if n_ < 4\n\tglobal_store_dwordx4 v[72+n_], v[80+4*n_:83+4*n_], s[54:55] sc1\n\t.endif\n\t" \
      ".if n_ >= 4 && n_ < 12\n\t" \
      ".if (n_ & 3) == 0\n\t" \
      "s_add_u32 m0, s46, (2 + n_ / 4) * 32768\n\ts_add_u32 s40, s44, s47\n\ts_addc_u32 s41, s45, 0\n\t" \
      "s_add_u32 s47, s47, 2*64*3840\n\ts_cmp_ge_u32 s47, 640*3840\n\ts_cselect_b32 s48, 640*3840, 0\n\ts_sub_u32 s47, s47, s48\n\t" \
      ".endif\n\t" \
      "global_load_lds_dwordx4 v[68+(n_&3)], s[40:41] offset:1024*(n_&3)\n\t" \
      ".endif\n\t" \
      ".if n_ == 14\n\tv_mov_b32 v72, s50\n\tv_mov_b32 v73, s51\n\tv_mov_b32 v74, s52\n\ts_mov_b64 exec, 1\n\ts_waitcnt vmcnt(8)\n\t" \
      "global_store_dwordx2 v74, v[72:73], s[54:55] sc1\n\ts_mov_b64 exec, -1\n\t.endif\n\t" \
      ".if n_ == 26\n\tglobal_load_dwordx2 v[72:73], v76, s[54:55] sc1\n\t.endif\n\t" \
      ".endm\n\t" \
      ".macro p2body vt, k0, k1\n\t" \
      ".set k_, \\k0\n\t" \
      ".rept \\k1 - \\k0\n\t" \
      ".set xr_, 96 + 8 * k_\n\t" \
      ".set c_, 32 + 16 * (k_ & 1)\n\t" \
      ".set d_, 48 - 16 * (k_ & 1)\n\t" \
      ".set o_, ((k_ + 1) & 7) * 8192\n\t" \
      ".if k_ == 3\n\ts_waitcnt vmcnt(6)\n\ts_barrier\n\t.endif\n\t" \
      ".if k_ == 11\n\ts_waitcnt vmcnt(1)\n\ts_barrier\n\t.endif\n\t" \
      ".if \\vt == 2\n\tslot 2*k_\n\tslot 2*k_+1\n\t" \
      ".elseif k_ < 19\n\t" \
      ".if ((k_ + 1) & 7) == 0\n\tbump 64, 4, 0x10000\n\t.endif\n\t" \
      "s_waitcnt lgkmcnt(6)\n\t" \
      "mm \\vt, 0, xr_, c_\n\trw d_, 64, o_\n\t" \
      "mm \\vt, 16, xr_+4, c_\n\tslot 2*k_\n\ts_waitcnt lgkmcnt(6)\n\t" \
      "mm \\vt, 4, xr_, c_+4\n\trw d_+4, 65, o_\n\t" \
      "mm \\vt, 20, xr_+4, c_+4\n\ts_waitcnt lgkmcnt(6)\n\t" \
      "mm \\vt, 8, xr_, c_+8\n\trw d_+8, 66, o_\n\t" \
      "mm \\vt, 24, xr_+4, c_+8\n\tslot 2*k_+1\n\ts_waitcnt lgkmcnt(6)\n\t" \
      "mm \\vt, 12, xr_, c_+12\n\trw d_+12, 67, o_\n\t" \
      "mm \\vt, 28, xr_+4, c_+12\n\t" \
      ".else\n\t" \
      "s_waitcnt lgkmcnt(0)\n\t" \
      "mm \\vt, 0, xr_, c_\n\tmm \\vt, 16, xr_+4, c_\n\tmm \\vt, 4, xr_, c_+4\n\tmm \\vt, 20, xr_+4, c_+4\n\t" \
      "mm \\vt, 8, xr_, c_+8\n\tmm \\vt, 24, xr_+4, c_+8\n\tmm \\vt, 12, xr_, c_+12\n\tmm \\vt, 28, xr_+4, c_+12\n\t" \
      "s_nop 7\n\t" \
      ".endif\n\t" \
      ".set k_, k_ + 1\n\t" \
      ".endr\n\t" \
      ".endm\n\t"
#define G1_P2_PURGE ".purgem p2body\n\t.purgem slot\n\t" G1_ASM_PURGE
__device__ __forceinline__ void g1_pass2a(int vt, f32x32& acc, const f32x32& x0, const f32x32& x1, const f32x32& x2, const f32x32& x3,
                                          const f32x32& x4, f32x32& wf, u32x8& adl, u32x8& adh, const u32x16& pk, const char* sB,
                                          unsigned m0v, unsigned off2, unsigned long long tag, unsigned flag, char* ws) {
  unsigned keep;
  asm volatile(G1_P2_MACROS
      "rw 32, 64, 0\n\trw 36, 65, 0\n\trw 40, 66, 0\n\trw 44, 67, 0\n\t"
      "s_mov_b32 %[keep], m0\n\t"
      "s_cmp_eq_u32 %[vt], 1\n\ts_cbranch_scc1 1f\n\ts_cmp_eq_u32 %[vt], 2\n\ts_cbranch_scc1 3f\n\t"
      "p2body 0, 0, 15\n\ts_branch 2f\n\t"
      "3:\n\tp2body 2, 0, 15\n\ts_branch 2f\n\t"
      "1:\n\tp2body 1, 0, 15\n\t"
      "2:\n\ts_mov_b32 m0, %[keep]\n\t" AWAIT(72, 73, 76)
      G1_P2_PURGE
      : "=&{v[0:31]}"(acc), "=&{v[32:63]}"(wf), "+{v[64:71]}"(adl), "+{v[72:79]}"(adh), [keep] "=&s"(keep), "+{s47}"(off2)
      : "{v[80:95]}"(pk), "{v[96:127]}"(x0), "{v[128:159]}"(x1), "{v[160:191]}"(x2), "{v[192:223]}"(x3), "{v[224:255]}"(x4), [vt] "s"(vt),
        "{s[44:45]}"(sB), "{s46}"(m0v), "{s[50:51]}"(tag), "{s52}"(flag), "{s[54:55]}"(ws)
      : "memory", "scc", "vcc", "s40", "s41", "s48");
}
__device__ __forceinline__ void g1_pass2b(int vt, f32x32& acc, const f32x32& x3, const f32x32& x4, f32x32& wf, u32x8& wa, char* ws) {
  asm volatile(G1_P2_MACROS
      "s_cmp_eq_u32 %[vt], 2\n\ts_cbranch_scc1 3f\n\ts_cmp_eq_u32 %[vt], 1\n\ts_cbranch_scc1 1f\n\t"
      "p2body 0, 15, 20\n\tpack 0\n\ts_branch 2f\n\t"
      "1:\n\tp2body 1, 15, 20\n\tpack 1\n\t"
      "2:\n\tglobal_store_dwordx4 v68, v[80:83], s[54:55] sc1\n\tglobal_store_dwordx4 v69, v[84:87], s[54:55] sc1\n\t"
      "global_store_dwordx4 v70, v[88:91], s[54:55] sc1\n\tglobal_store_dwordx4 v71, v[92:95], s[54:55] sc1\n\t"
      "s_waitcnt vmcnt(4)\n\t"
      "3:\n\ts_setprio 3\n\t"
      G1_P2_PURGE
      : "+{v[0:31]}"(acc), "+{v[32:63]}"(wf), "+{v[64:71]}"(wa)
      : "{v[192:223]}"(x3), "{v[224:255]}"(x4), [vt] "s"(vt), "{s[54:55]}"(ws)
      : "memory", "scc", "v80", "v81", "v82", "v83", "v84", "v85", "v86", "v87", "v88", "v89", "v90", "v91", "v92", "v93", "v94", "v95");
}

__device__ __forceinline__ u32x4 g1_offs(unsigned br, unsigned cg, unsigned T, unsigned lane) {
  const unsigned l16 = (lane & 31) * 16, h = lane >> 5;
  u32x4 so;
  if (cg < 20) {
    so[0] = h * 32768 + l16 + (br * (unsigned)QK_BR + cg * (8 * 32768) + T * 512);
    so[1] = so[0] + 65536; so[2] = so[0] + 2 * 65536; so[3] = so[0] + 3 * 65536;
  } else {
    const unsigned G = (cg - 20) * 4 + ((lane >> 4) & 1);
    const unsigned b = h * 1280 + l16 + ((unsigned)V_WS + br * (unsigned)V_BR + (cg - 20) * 1024 + T * 5120);
    so[0] = ((G * 52) >> 8) * (256 * 1280 - 1280) + b;
    so[1] = (((G + 2) * 52) >> 8) * (256 * 1280 - 1280) + b + 512;
    so[2] = so[0] + 2560; so[3] = so[1] + 2560;
  }
  return so;
}

constexpr int TILE_B = 3584, K2_OFF = 1024, V_OFF = 1536;
constexpr int CHUNK_B = 8 * TILE_B;

__device__ __forceinline__ f32x16 smfmac(bf16x8 a, f32x8 p, f32x16 c) {
  return __builtin_amdgcn_smfmac_f32_32x32x32_bf16(a, __builtin_bit_cast(bf16x16, p), c, 0xDDDD, 0, 0);
}
__device__ __forceinline__ f32x8 lo8(const f32x16& s) { return __builtin_shufflevector(s, s, 0, 1, 2, 3, 4, 5, 6, 7); }
__device__ __forceinline__ f32x8 hi8(const f32x16& s) { return __builtin_shufflevector(s, s, 8, 9, 10, 11, 12, 13, 14, 15); }
#define MX3(a, b, c) __builtin_fmaxf(__builtin_fmaxf((a), (b)), (c))
__device__ __forceinline__ float max16(const f32x16& s, float m) {
#pragma unroll
  for (int r = 0; r < 16; r += 2) m = MX3(m, s[r], s[r + 1]);
  return m;
}
__device__ __forceinline__ float xhalf_max(float m) {
  auto rr = __builtin_amdgcn_permlane32_swap(__float_as_uint(m), __float_as_uint(m), false, false);
  return __builtin_fmaxf(__uint_as_float(rr[0]), __uint_as_float(rr[1]));
}
__device__ __forceinline__ s16x4 with_negmax(s16x4 q2, float m, int h) {
  u32x2 v = __builtin_bit_cast(u32x2, q2);
  const unsigned nm = pk_bf16(-m, 0.0f);
  v[0] = h ? nm : v[0];
  return __builtin_bit_cast(s16x4, v);
}
__device__ __forceinline__ float row_sum(const f32x16& o) {
  auto rr = __builtin_amdgcn_permlane32_swap(__float_as_uint(o[8]), __float_as_uint(o[8]), false, false);
  return __uint_as_float(rr[1]);
}
template <bool MARK>
__device__ __forceinline__ void store_o(const f32x16& o, float l, char* base, unsigned lo1, unsigned lo2) {
  const float rl = __builtin_amdgcn_rcpf(l);
  const unsigned a0 = pk_bf16(o[0] * rl, o[1] * rl), a1 = pk_bf16(o[2] * rl, o[3] * rl);
  const unsigned b0 = pk_bf16(o[4] * rl, o[5] * rl), b1 = pk_bf16(o[6] * rl, o[7] * rl);
  const auto r0 = __builtin_amdgcn_permlane32_swap(a0, b0, false, false);
  const auto r1 = __builtin_amdgcn_permlane32_swap(a1, b1, false, false);
  u32x4 v = {r0[0], r1[0], r0[1], r1[1]};
  u32x2 c = {pk_bf16(o[8] * rl, o[9] * rl), pk_bf16(o[10] * rl, o[11] * rl)};
  if (MARK) {
    v = __builtin_elementwise_min(v, u32x4{~1u, ~1u, ~1u, ~1u}); c = __builtin_elementwise_min(c, u32x2{~1u, ~1u});
  }
  st16(base, lo1, v);
  st8lo(base, lo2, c);
}

__device__ __forceinline__ bool flags_ok(unsigned a, unsigned b, unsigned long long tag) {
  return __all(a == (unsigned)tag && b == (unsigned)(tag >> 32));
}
__device__ __forceinline__ void poll_flags(const char* ws, unsigned off, unsigned long long tag) {
  asm volatile("global_load_dwordx2 v[124:125], v126, s[54:55] sc1\n\t" AWAIT(124, 125, 126)
               :: "{v126}"(off), "{s[54:55]}"(ws), "{s[50:51]}"(tag) : "memory", "scc", "vcc", "s48", "v124", "v125");
}
__device__ __forceinline__ void put_flag(char* ws, unsigned off, unsigned long long tag) {
  asm volatile("s_mov_b64 exec, 1\n\tglobal_store_dwordx2 %0, %1, %2 sc1\n\ts_mov_b64 exec, -1" :: "v"(off), "v"(tag), "s"(ws) : "memory");
}
__device__ __forceinline__ void tile_dma(const char* ks, const char* vs, unsigned d, u32x4 go) {
  unsigned keep;
  asm volatile("s_mov_b32 %0, m0\n\ts_mov_b32 m0, %7\n\ts_nop 0\n\t"
               "global_load_lds_dwordx4 %1, %5 sc1\n\t"
               "s_mov_b32 exec_hi, 0\n\t"
               "global_load_lds_dwordx4 %2, %5 offset:1024 sc1\n\t"
               "s_mov_b32 exec_lo, 0xfffff\n\ts_mov_b32 exec_hi, 0xfffff\n\t"
               "global_load_lds_dwordx4 %3, %6 offset:1536 sc1\n\t"
               "global_load_lds_dwordx4 %4, %6 offset:2560 sc1\n\t"
               "s_mov_b64 exec, -1\n\ts_mov_b32 m0, %0"
               : "=&s"(keep) : "v"(go[0]), "v"(go[1]), "v"(go[2]), "v"(go[3]), "s"(ks), "s"(vs), "s"(d) : "memory");
}
__device__ __forceinline__ void wo_dma(const char* src, unsigned d, const u32x8& wo) {
  unsigned keep;
  asm volatile("s_mov_b32 %0, m0\n\ts_mov_b32 m0, %7\n\ts_nop 0\n\t"
               "global_load_lds_dwordx4 %1, %6\n\tglobal_load_lds_dwordx4 %2, %6 offset:1024\n\t"
               "global_load_lds_dwordx4 %3, %6 offset:2048\n\tglobal_load_lds_dwordx4 %4, %6 offset:3072\n\t"
               "s_add_u32 m0, m0, 4096\n\ts_nop 0\n\tglobal_load_lds_dwordx4 %5, %6\n\ts_mov_b32 m0, %0"
               : "=&s"(keep) : "v"(wo[0]), "v"(wo[1]), "v"(wo[2]), "v"(wo[3]), "v"(wo[4]), "s"(src), "s"(d) : "memory", "scc");
}

#define AT_ASM_MACROS \
    ".macro xp d, s\n\tv_exp_f32 v[\\d], v[\\s]\n\tv_exp_f32 v[\\d+1], v[\\s+1]\n\tv_exp_f32 v[\\d+2], v[\\s+2]\n\tv_exp_f32 v[\\d+3], v[\\s+3]\n\t.endm\n\t" \
    ".macro smf o, v, p\n\tv_smfmac_f32_32x32x32_bf16 v[\\o:\\o+15], v[\\v:\\v+3], v[\\p:\\p+7], v76\n\t.endm\n\t" \
    ".macro qk1 s, k, q\n\tv_mfma_f32_32x32x16_bf16 v[\\s:\\s+15], v[\\k:\\k+3], v[\\q:\\q+3], 0\n\t.endm\n\t" \
    ".macro qk1c s, k, q, c\n\tv_mfma_f32_32x32x16_bf16 v[\\s:\\s+15], v[\\k:\\k+3], v[\\q:\\q+3], v[\\c:\\c+15]\n\t.endm\n\t" \
    ".macro qk2 s, k, q\n\tv_mfma_f32_32x32x8_bf16 v[\\s:\\s+15], v[\\k:\\k+1], v[\\q:\\q+1], v[\\s:\\s+15]\n\t.endm\n\t"
#define AT_ASM_PURGE ".purgem xp\n\t.purgem smf\n\t.purgem qk1\n\t.purgem qk1c\n\t.purgem qk2\n\t"

#define AT_A_TEXT AT_ASM_MACROS \
      ".macro pos j\n\t.set sl_, (\\j) >> 2\n\t.if sl_ >= 3\n\t.set sl_, sl_ - 3\n\t.endif\n\t" \
      ".set ar_, 128 + 3 * (sl_ >> 1)\n\t.set of_, (sl_ & 1) * 28672 + ((\\j) & 3) * 7168\n\t.endm\n\t" \
      ".macro dma part\n\t" \
      ".if di_ >= 0\n\t" \
      ".if \\part == 0\n\t" \
      ".if di_ == 1\n\ts_mov_b32 m0, s73\n\t.else\n\ts_add_u32 m0, s72, ((di_ + 2) %% 5) * 28672\n\t.endif\n\t" \
      "s_add_u32 s40, s54, s[56+2*di_]\n\ts_addc_u32 s41, s55, 0\n\t" \
      "global_load_lds_dwordx4 v137, s[40:41] sc1\n\t" \
      ".elseif \\part == 1\n\t" \
      "s_mov_b32 exec_hi, 0\n\tglobal_load_lds_dwordx4 v138, s[40:41] offset:1024 sc1\n\ts_mov_b32 exec_hi, -1\n\t" \
      "s_add_u32 s40, s54, s[57+2*di_]\n\ts_addc_u32 s41, s55, 0\n\t" \
      ".else\n\t" \
      "s_mov_b32 exec_lo, 0xfffff\n\ts_mov_b32 exec_hi, 0xfffff\n\t" \
      "global_load_lds_dwordx4 v139, s[40:41] offset:1536 sc1\n\tglobal_load_lds_dwordx4 v140, s[40:41] offset:2560 sc1\n\t" \
      "s_mov_b64 exec, -1\n\t" \
      ".endif\n\t.endif\n\t" \
        \
      ".set n_, -1\n\t" \
      ".if j_ >= 22 && j_ < 26 && 3 * (j_ - 22) + \\part < 10\n\t.set n_, 3 * (j_ - 22) + \\part\n\t.set bk_, 2 * (n_ / 5)\n\t.endif\n\t" \
      ".if j_ >= 28 && 3 * (j_ - 28) + \\part < 5\n\t.set n_, 3 * (j_ - 28) + \\part\n\t.set bk_, 1\n\t.endif\n\t" \
      ".if n_ >= 0\n\t.set pc_, n_ %% 5\n\t" \
      ".if bk_ == 2\n\ts_cmp_lt_i32 s46, 0\n\ts_cbranch_scc1 9f\n\t.endif\n\t" \
      ".if pc_ == 0\n\ts_mov_b32 m0, s[44+bk_]\n\ts_add_u32 s40, s42, bk_ * 8 * 32 * 1280\n\ts_addc_u32 s41, s43, 0\n\t.endif\n\t" \
      ".if pc_ == 4\n\ts_add_u32 m0, m0, 4096\n\ts_nop 0\n\t.endif\n\t" \
      "global_load_lds_dwordx4 v[248+pc_], s[40:41] offset:1024*(pc_&3)\n\t" \
      ".if bk_ == 2\n\t9:\n\t.endif\n\t.endif\n\t" \
      ".endm\n\t" \
      ".macro flg msk\n\tv_mov_b32 v142, s50\n\tv_mov_b32 v143, s51\n\tv_mov_b32 v108, s52\n\ts_mov_b64 exec, \\msk\n\ts_cbranch_execz 8f\n\ts_waitcnt vmcnt(0)\n\t" \
      "global_store_dwordx2 v108, v[142:143], s[54:55] sc1\n\t8:\n\ts_mov_b64 exec, -1\n\t.endm\n\t" \
      ".macro tabl d, r\n\tglobal_load_dwordx4 v[\\d:\\d+3], v79, s[40:41] offset:1024*(\\r)\n\t.endm\n\t" \
      ".macro old q\n\t.if (\\q) >= 0 && (\\q) < 10\n\tglobal_load_dwordx4 v[184+4*(\\q):187+4*(\\q)], v108, s[54:55] sc1\n\tv_add_u32 v108, 0x20000, v108\n\t.endif\n\t.endm\n\t" \
      "s_mov_b32 %[keep], m0\n\t" \
      ".set j_, %c[j0]\n\t" \
      ".rept %c[j1] - %c[j0]\n\t" \
      ".set a_, 80 + 14 * (j_ & 1)\n\t.set b_, 94 - 14 * (j_ & 1)\n\t" \
      ".if j_ == 4\n\t" AWAIT(142, 143, 141) ".set di_, 0\n\tdma 0\n\tdma 1\n\tdma 2\n\t.endif\n\t" \
      ".if j_ == 6\n\ts_waitcnt vmcnt(10)\n\ts_barrier\n\t.endif\n\t" \
      ".if j_ == 10 || j_ == 14 || j_ == 22\n\ts_waitcnt vmcnt(0)\n\ts_barrier\n\t.endif\n\t" \
      ".if j_ == 18\n\ts_waitcnt vmcnt(1)\n\ts_barrier\n\t.endif\n\t" \
      ".if j_ == 28\n\ts_barrier\n\t.endif\n\t" \
      AT_STAMP \
      ".if j_ < 6\n\t.if (j_ & 1) == 0\n\ts_setprio 2 - j_ / 2\n\t.endif\n\t" \
      ".elseif j_ == 10 || j_ == 11\n\ts_setprio 3\n\t.elseif j_ == 12 || j_ == 13\n\ts_setprio 0\n\t" \
      ".elseif j_ < 22\n\ts_setprio 3 - ((j_ + 2) & 3)\n\t.elseif j_ >= 28\n\ts_setprio 31 - j_\n\t" \
      ".elseif j_ != 23 && j_ != 25\n\ts_setprio 3 - (j_ - 21) / 2\n\t.endif\n\t" \
      ".if j_ == 14\n\tflg 1\n\t" \
      "v_mov_b32 v108, 0x3f803f80\n\tv_mov_b32 v109, v108\n\tv_mov_b32 v110, v108\n\tv_mov_b32 v111, v108\n\ts_mov_b64 exec, 15\n\t" \
      "ds_write_b128 v77, v[108:111]\n\ts_mov_b64 exec, -1\n\t.endif\n\t" \
      ".if j_ == 28\n\t" AWAIT(142, 143, 253) "v_add_u32 v108, %c[obr], v254\n\t.endif\n\t" \
      ".set di_, -1\n\t" \
      ".if j_ == 4 || j_ == 5 || j_ == 6\n\t.set di_, j_ - 3\n\t.endif\n\t.if j_ == 10\n\t.set di_, 4\n\t.endif\n\t" \
      ".if j_ >= 14 && j_ < 17\n\t.set di_, j_ - 9\n\t.endif\n\t" \
        \
      "pos j_+2\n\t" \
      "qk1 48, a_, 68\n\t.if j_ < 30\n\tds_read_b128 v[a_:a_+3], v[ar_] offset:of_\n\t.endif\n\told 4*(j_-28)\n\txp 112, 32\n\t" \
      "qk2 48, a_+4, 74\n\t.if j_ < 30\n\tds_read_b64 v[a_+4:a_+5], v[ar_+1] offset:of_\n\t.endif\n\told 4*(j_-28)+1\n\txp 116, 36\n\t" \
      "pos j_+1\n\t" \
      "smf 16, b_+10, 120\n\t.if j_ < 31\n\tds_read_b128 v[b_+10:b_+13], v[ar_+2] offset:of_+1024\n\t.endif\n\told 4*(j_-28)+2\n\txp 120, 40\n\t" \
      "smf 0, a_+6, 112\n\t.if j_ < 30\n\ts_waitcnt lgkmcnt(3)\n\t.elseif j_ == 30\n\ts_waitcnt lgkmcnt(1)\n\t.else\n\ts_waitcnt lgkmcnt(0)\n\t.endif\n\t" \
      "old 4*(j_-28)+3\n\t" \
      ".if j_ == 5\n\tglobal_load_dwordx4 v[236:239], v137, s[42:43] sc1\n\tglobal_load_dwordx2 v[240:241], v78, s[42:43] sc1\n\t.endif\n\t" \
      "xp 124, 44\n\t" \
        \
      ".if j_ < 31\n\tqk1 32, b_, 64\n\t.endif\n\tdma 0\n\t" \
      ".if j_ == 0\n\tflg s[74:75]\n\t.endif\n\t" \
      ".if j_ == 3\n\tglobal_load_dwordx2 v[142:143], v141, s[54:55] sc1\n\t.endif\n\t" \
      ".if j_ == 7\n\ts_mov_b64 s[40:41], s[44:45]\n\ttabl 248, 0\n\ttabl 252, 1\n\t.endif\n\t" \
      ".if j_ == 8\n\ttabl 220, 2\n\ttabl 224, 3\n\t.endif\n\t" \
      ".if j_ == 17\n\ts_mov_b32 m0, 143360\n\ts_nop 0\n\tglobal_load_lds_dword v141, s[74:75]\n\t.endif\n\t" \
      ".if j_ == 26\n\tglobal_load_dwordx2 v[142:143], v253, s[54:55] sc1\n\t.endif\n\t" \
      "xp 112, 48\n\t" \
      ".if j_ < 31\n\tqk2 32, b_+4, 72\n\t.endif\n\tdma 1\n\t" \
      ".if j_ == 7\n\ttabl 160, 2\n\ttabl 164, 3\n\ts_add_u32 s40, s40, 4096\n\ts_addc_u32 s41, s41, 0\n\t.endif\n\t" \
      ".if j_ == 8\n\ts_add_u32 s40, s40, 4096\n\ts_addc_u32 s41, s41, 0\n\ttabl 228, 0\n\ttabl 232, 1\n\t.endif\n\t" \
      "xp 116, 52\n\t" \
      "smf 0, a_+10, 120\n\tdma 2\n\t" \
      ".if j_ == 7\n\ttabl 168, 0\n\ttabl 172, 1\n\t.endif\n\t" \
      "xp 120, 56\n\t" \
      "pos j_+2\n\t" \
      "smf 16, a_+6, 112\n\t.if j_ < 30\n\tds_read_b128 v[a_+6:a_+9], v[ar_+2] offset:of_\n\t.endif\n\txp 124, 60\n\t" \
      ".set j_, j_ + 1\n\t" \
      ".endr\n\t" \
      ".if %c[j1] == 32\n\ts_nop 1\n\tsmf 16, 104, 120\n\ts_nop 15\n\t.endif\n\t" \
      "s_mov_b32 m0, %[keep]\n\t" \
      ".purgem pos\n\t.purgem dma\n\t.purgem flg\n\t.purgem tabl\n\t.purgem old\n\t" AT_ASM_PURGE
#define AT_STAMP
#define AT_STV
#define AT_STV_ARG
#define AT_STV_PASS
#define AT_A_STATE "+{v[0:31]}"(o), "+{v[32:47]}"(s0), "+{v[112:127]}"(pp), "+{v[80:111]}"(fr), "+{v[128:143]}"(ad), [keep] "=&s"(keep)
#define AT_A_CLOB "memory", "scc", "vcc", "s40", "s41", "s48", "v48", "v49", "v50", "v51", "v52", "v53", "v54", "v55", "v56", "v57", "v58", "v59", "v60", "v61", "v62", "v63"
__device__ __forceinline__ void at_a1(f32x32& o, f32x16& s0, f32x16& pp, f32x32& fr, const u32x16& q, u32x16& ad, const char* ws, u32x16 so,
                                      unsigned m0v, unsigned m0b, unsigned long long tag, unsigned flag, unsigned long long fmask,
                                      const char* qB, const void* tab, f32x16& mF, f32x16& mD, u32x8& qv, u32x8& tw AT_STV_ARG) {
  unsigned keep;
  asm volatile(AT_A_TEXT
      : AT_A_STATE, "=&{v[160:175]}"(mF), "=&{v[220:235]}"(mD), "=&{v[236:243]}"(qv), "=&{v[248:255]}"(tw) AT_STV
      : "{v[64:79]}"(q), "{s[54:55]}"(ws), "{s[56:71]}"(so), "{s72}"(m0v), "{s73}"(m0b), "{s[50:51]}"(tag), "{s52}"(flag), "{s[74:75]}"(fmask),
        "{s[42:43]}"(qB), "{s[44:45]}"(tab), [j0] "n"(0), [j1] "n"(12), [obr] "n"(O_BR)
      : AT_A_CLOB);
}
__device__ __forceinline__ void at_a2(f32x32& o, f32x16& s0, f32x16& pp, f32x32& fr, const u32x16& q, u32x16& ad, const char* ws, u32x16 so,
                                      unsigned m0v, unsigned long long tag, unsigned flag, const char* woa, const u32x8& wo, const char* src,
                                      unsigned d0, unsigned d1, unsigned d2, f32x32& of, f32x8& of2) {
  unsigned keep;
  asm volatile(AT_A_TEXT
      : AT_A_STATE, "=&{v[184:215]}"(of), "=&{v[216:223]}"(of2)
      : "{v[64:79]}"(q), "{s[54:55]}"(ws), "{s[56:71]}"(so), "{s72}"(m0v), "{s[50:51]}"(tag), "{s52}"(flag), "{s[74:75]}"(woa),
        "{v[248:255]}"(wo), "{s[42:43]}"(src), "{s44}"(d0), "{s45}"(d1), "{s46}"(d2), [j0] "n"(12), [j1] "n"(32), [obr] "n"(O_BR)
      : AT_A_CLOB);
}

__device__ __forceinline__ void at_b(f32x16& o, f32x16& mF, const f32x16& mD, const u32x8& q, unsigned av, const u32x16& qa, f32x32& k0, f32x16& k1, f32x32& k2,
                                     f32x16& k3) {
  asm volatile(AT_ASM_MACROS
      "ds_read_b128 v[192:195], v242\n\tds_read_b64 v[196:197], v243\n\tds_read_b128 v[206:209], v242 offset:3584\n\tds_read_b64 v[210:211], v243 offset:3584\n\t"
      "ds_read_b128 v[198:201], v245\n\tds_read_b128 v[212:215], v245 offset:3584\n\tds_read_b128 v[202:205], v245 offset:1024\n\t"
      "s_waitcnt lgkmcnt(5)\n\t"
      "qk1c 160, 192, 236, 160\n\tds_read_b128 v[192:195], v242 offset:7168\n\tqk2 160, 196, 240\n\tds_read_b64 v[196:197], v243 offset:7168\n\t"
      ".set i_, 144\n\t.rept 8\n\tv_mov_b64 v[i_:i_+1], 0\n\t.set i_, i_ + 2\n\t.endr\n\t"
      "s_waitcnt lgkmcnt(2)\n\t"
      ".set t_, 0\n\t"
      ".rept 9\n\t"
      ".set a_, 192 + 14 * (t_ & 1)\n\t.set b_, 206 - 14 * (t_ & 1)\n\t.set c_, 160 - 112 * (t_ & 1)\n\t.set d_, 48 + 112 * (t_ & 1)\n\t"
      ".if t_ == 0 || t_ == 5\n\ts_setprio 2 - t_ / 5\n\t.endif\n\t"
      ".if t_ < 7\n\tqk1 d_, b_, 236\n\t.elseif t_ == 7\n\tqk1c d_, b_, 236, 220\n\t.endif\n\t"
      ".if t_ < 6\n\tds_read_b128 v[b_:b_+3], v242 offset:(t_+3)*3584\n\t.endif\n\t"
      "xp 176, c_\n\t"
      ".if t_ < 8\n\tqk2 d_, b_+4, 240\n\t.endif\n\t"
      ".if t_ < 6\n\tds_read_b64 v[b_+4:b_+5], v243 offset:(t_+3)*3584\n\t.endif\n\t"
      "xp 180, c_+4\n\t"
      ".if t_ > 0\n\tsmf 144, b_+10, 184\n\t.endif\n\t"
      ".if t_ < 8\n\tds_read_b128 v[b_+10:b_+13], v245 offset:(t_+1)*3584+1024\n\t.endif\n\t"
      "xp 184, c_+8\n\t"
      "smf 144, a_+6, 176\n\t"
      ".if t_ < 6\n\ts_waitcnt lgkmcnt(3)\n\t.elseif t_ < 8\n\ts_waitcnt lgkmcnt(1)\n\t.else\n\ts_waitcnt lgkmcnt(0)\n\t.endif\n\t"
      ".if t_ < 7\n\tds_read_b128 v[a_+6:a_+9], v245 offset:(t_+2)*3584\n\t.endif\n\t"
      "xp 188, c_+12\n\t"
      ".set t_, t_ + 1\n\t"
      ".endr\n\t"
      "s_nop 1\n\tsmf 144, 202, 184\n\ts_nop 11\n\t"
      AT_ASM_PURGE
      : "=&{v[144:159]}"(o), "+{v[160:175]}"(mF), "+{v[0:31]}"(k0), "+{v[32:47]}"(k1), "+{v[80:111]}"(k2), "+{v[112:127]}"(k3)
      : "{v[220:235]}"(mD), "{v[236:243]}"(q), "{v245}"(av), "{v[64:79]}"(qa)
      : "memory", "v48", "v49", "v50", "v51", "v52", "v53", "v54", "v55", "v56", "v57", "v58", "v59", "v60", "v61", "v62", "v63",
        "v176", "v177", "v178", "v179", "v180", "v181", "v182", "v183", "v184", "v185", "v186", "v187", "v188", "v189", "v190", "v191",
        "v192", "v193", "v194", "v195", "v196", "v197", "v198", "v199", "v200", "v201", "v202", "v203", "v204", "v205", "v206", "v207",
        "v208", "v209", "v210", "v211", "v212", "v213", "v214", "v215", "v216", "v217", "v218", "v219");
}

#define PJ_MACROS \
      ".macro rdw k, sp\n\t.set d_, 80 + 20 * ((\\k) & 1)\n\t" \
      ".if (\\k) < \\sp\n\t.set a_, 120\n\t.set o_, (\\k) * 5120\n\t.else\n\t.set a_, 122\n\t.set o_, ((\\k) - \\sp) * 5120\n\t.endif\n\t.endm\n\t" \
      ".macro rd1 i\n\tds_read_b64_tr_b16 v[d_+4*\\i:d_+4*\\i+1], v[a_] offset:o_+32*\\i\n\tds_read_b64_tr_b16 v[d_+4*\\i+2:d_+4*\\i+3], v[a_+1] offset:o_+32*\\i\n\t.endm\n\t" \
      ".macro pm i, ac, of, last\n\t" \
      ".if k_ == 0\n\tv_mfma_f32_16x16x32_bf16 v[\\ac+4*\\i:\\ac+4*\\i+3], v[c_+4*\\i:c_+4*\\i+3], v[\\of:\\of+3], 0\n\t" \
      ".else\n\tv_mfma_f32_16x16x32_bf16 v[\\ac+4*\\i:\\ac+4*\\i+3], v[c_+4*\\i:c_+4*\\i+3], v[\\of+4*k_:\\of+3+4*k_], v[\\ac+4*\\i:\\ac+4*\\i+3]\n\t.endif\n\t" \
      ".if k_ < \\last\n\trd1 \\i\n\ts_waitcnt lgkmcnt(8)\n\t.elseif \\i < 4\n\ts_waitcnt lgkmcnt(6 - 2 * \\i)\n\t.endif\n\t.endm\n\t" \
      ".macro ldo\n\tv_mov_b32 v72, v254\n\t.set q_, 0\n\t.rept 10\n\t" \
      "global_load_dwordx4 v[32+4*q_:35+4*q_], v72, s[54:55] sc1\n\t.if q_ < 9\n\tv_add_u32 v72, 0x20000, v72\n\t.endif\n\t.set q_, q_ + 1\n\t.endr\n\t.endm\n\t"
#define PJ_PURGE ".purgem rdw\n\t.purgem rd1\n\t.purgem pm\n\t.purgem ldo\n\t"
#define PJ_CLOB "v72", "v80", "v81", "v82", "v83", "v84", "v85", "v86", "v87", "v88", "v89", "v90", "v91", "v92", "v93", "v94", "v95", \
        "v96", "v97", "v98", "v99", "v100", "v101", "v102", "v103", "v104", "v105", "v106", "v107", "v108", "v109", "v110", "v111", \
        "v112", "v113", "v114", "v115", "v116", "v117", "v118", "v119"
__device__ __forceinline__ void proj_1(f32x32& acc, const f32x32& of, const f32x8& of2, f32x32& oa, f32x8& oa2, u32x4 wa, const u32x8& wo,
                                       const char* src0, const char* src1, unsigned d0, unsigned d1, const char* ws) {
  unsigned keep;
  asm volatile(PJ_MACROS
      ".macro wp n\n\t.set n_, \\n\n\t.set pc_, n_ %% 5\n\t"
      ".if n_ < 10\n\t"
      ".if n_ >= 5\n\ts_cmp_lt_i32 s45, 0\n\ts_cbranch_scc1 9f\n\t.endif\n\t"
      ".if pc_ == 0\n\ts_mov_b32 m0, s[44+n_/5]\n\ts_nop 0\n\t.endif\n\t"
      ".if pc_ == 4\n\ts_add_u32 m0, m0, 4096\n\ts_nop 0\n\t.endif\n\t"
      "global_load_lds_dwordx4 v[248+pc_], s[42+4*(n_/5):43+4*(n_/5)] offset:1024*(pc_&3)\n\t"
      ".if n_ >= 5\n\t9:\n\t.endif\n\t.endif\n\t"
      ".if n_ == 12\n\tldo\n\t.endif\n\t"
      ".endm\n\t"
      "rdw 0, 6\n\trd1 0\n\trd1 1\n\trd1 2\n\trd1 3\n\trd1 4\n\t"
      "s_mov_b32 %[keep], m0\n\ts_barrier\n\ts_waitcnt lgkmcnt(8)\n\t"
      ".set k_, 0\n\t.rept 10\n\t.set c_, 80 + 20 * (k_ & 1)\n\trdw k_+1, 6\n\t"
      ".if k_ == 0 || k_ == 3 || k_ == 6\n\ts_setprio 2 - k_ / 3\n\t.endif\n\t"
      "pm 0, 0, 184, 9\n\twp 2*k_\n\tpm 1, 0, 184, 9\n\tpm 2, 0, 184, 9\n\twp 2*k_+1\n\tpm 3, 0, 184, 9\n\tpm 4, 0, 184, 9\n\t"
      ".set k_, k_ + 1\n\t.endr\n\t"
      "s_setprio 3\n\ts_mov_b32 m0, %[keep]\n\ts_waitcnt vmcnt(10)\n\ts_barrier\n\ts_nop 4\n\t"
      ".purgem wp\n\t" PJ_PURGE
      : "=&{v[0:31]}"(acc), "=&{v[32:63]}"(oa), "=&{v[64:71]}"(oa2), [keep] "=&s"(keep)
      : "{v[184:215]}"(of), "{v[216:223]}"(of2), "{v[120:123]}"(wa), "{v[248:255]}"(wo), "{s[42:43]}"(src0), "{s[46:47]}"(src1), "{s44}"(d0),
        "{s45}"(d1), "{s[54:55]}"(ws)
      : "memory", "scc", PJ_CLOB);
}
template <int K0>
__device__ __forceinline__ void proj_2(f32x32& acc, f32x32& oa, f32x8& oa2, u32x4 wa, const u32x8& wo, const char* ws) {
  asm volatile(PJ_MACROS
      "rdw %c[k0], 5\n\trd1 0\n\trd1 1\n\trd1 2\n\trd1 3\n\trd1 4\n\t"
      ".if %c[k0] == 0\n\t"
      "s_mov_b32 s48, 0x40000\n\t5:\n\t"
      "v_max3_u32 v72, v32, v33, v34\n\t.set q_, 35\n\t.rept 18\n\tv_max3_u32 v72, v72, v[q_], v[q_+1]\n\t.set q_, q_ + 2\n\t.endr\n\t"
      "v_max_u32 v72, v72, v71\n\tv_cmp_eq_u32 vcc, -1, v72\n\ts_sub_u32 s48, s48, 1\n\t"
      "s_cbranch_vccz 4f\n\ts_cbranch_scc1 4f\n\tldo\n\ts_waitcnt vmcnt(0)\n\ts_branch 5b\n\t4:\n\t"
      ".endif\n\t"
      "s_waitcnt lgkmcnt(8)\n\t"
      ".set k_, %c[k0]\n\t.rept 5\n\t.set c_, 80 + 20 * (k_ & 1)\n\trdw k_+1, 5\n\t"
      "pm 0, 160, 32, %c[k0]+4\n\tpm 1, 160, 32, %c[k0]+4\n\tpm 2, 160, 32, %c[k0]+4\n\tpm 3, 160, 32, %c[k0]+4\n\tpm 4, 160, 32, %c[k0]+4\n\t"
      ".set k_, k_ + 1\n\t.endr\n\t"
      ".if %c[k0] == 0\n\ts_waitcnt vmcnt(0)\n\ts_barrier\n\t.else\n\ts_nop 7\n\t.endif\n\t"
      PJ_PURGE
      : "+{v[160:191]}"(acc), "+{v[32:63]}"(oa), "+{v[64:71]}"(oa2)
      : "{v[120:123]}"(wa), "{v[248:255]}"(wo), "{s[54:55]}"(ws), [k0] "n"(K0)
      : "memory", "scc", "vcc", "s48", PJ_CLOB);
}
typedef __attribute__((address_space(3))) f32x4* lds_f4;
__device__ __forceinline__ void proj_send(const f32x32& acc, char* lds, int lw, int grp, int lane) {
  lds_ptr p = (lds_ptr)lds + lw * 5120 + lane * 16;
  if (grp) {
    *(lds_f4)(p) = f32x4{acc[0], acc[1], acc[2], acc[3]}; *(lds_f4)(p + 1024) = f32x4{acc[4], acc[5], acc[6], acc[7]};
    *(lds_f4)(p + 4096) = f32x4{acc[16], acc[17], acc[18], acc[19]};
  } else {
    *(lds_f4)(p + 2048) = f32x4{acc[8], acc[9], acc[10], acc[11]}; *(lds_f4)(p + 3072) = f32x4{acc[12], acc[13], acc[14], acc[15]};
  }
}
#define OUT_POL ""
__device__ __forceinline__ void proj_out(const f32x4& a, const f32x4& b, char* p, unsigned off) {
  const auto s0 = __builtin_amdgcn_permlane16_swap(pk_bf16(a[0], a[1]), pk_bf16(b[0], b[1]), false, false);
  const auto s1 = __builtin_amdgcn_permlane16_swap(pk_bf16(a[2], a[3]), pk_bf16(b[2], b[3]), false, false);
  asm volatile("global_store_dwordx4 %0, %1, %2 " OUT_POL "\n\ts_nop 1" :: "v"(off), "v"(u32x4{s0[0], s1[0], s0[1], s1[1]}), "s"(p) : "memory");
}
__device__ __forceinline__ void proj_finish(const f32x32& acc, char* lds, int lw, int grp, int lane, char* dst) {
  lds_ptr p = (lds_ptr)lds + lw * 5120 + lane * 16;
  const unsigned row = (lane & 15) * 2560, o16 = row + ((lane >> 4) & 1) * 32 + (lane >> 5) * 16;
  if (grp) {
    proj_out(f32x4{acc[8], acc[9], acc[10], acc[11]} + *(lds_f4)(p + 2048), f32x4{acc[12], acc[13], acc[14], acc[15]} + *(lds_f4)(p + 3072), dst + 64, o16);
  } else {
    const f32x4 r2 = f32x4{acc[16], acc[17], acc[18], acc[19]} + *(lds_f4)(p + 4096);
    proj_out(f32x4{acc[0], acc[1], acc[2], acc[3]} + *(lds_f4)(p), f32x4{acc[4], acc[5], acc[6], acc[7]} + *(lds_f4)(p + 1024), dst, o16);
    asm volatile("global_store_dwordx2 %0, %1, %2 offset:128 " OUT_POL :: "v"(row + (lane >> 4) * 8), "v"(u32x2{pk_bf16(r2[0], r2[1]), pk_bf16(r2[2], r2[3])}), "s"(dst) : "memory");
  }
}

struct LaneTab { unsigned v[10][64][4]; };
constexpr LaneTab make_tab() {
  LaneTab t = {};
  for (unsigned l = 0; l < 64; ++l) {
    for (unsigned i = 0; i < 5; ++i) {
      const unsigned v = 64 * i + l, rw = v / 10;
      t.v[i >> 2][l][i & 3] = (rw ^ ((rw >> 1) & 4)) * 1280 + (v - rw * 10) * 16 + 4096 - 1024 * (i & 3);
    }
    const unsigned g = l >> 4, q = (l & 15) >> 2, p = l & 3;
    t.v[1][l][1] = (l & 15) * 64 * 128;
    t.v[1][l][2] = g * 32768 + (l & 15) * 16;
    t.v[1][l][3] = (g * 1280 + (q ^ (4 * (g & 1))) * 160 + p * 8) | ((g * 1280 + ((4 + q) ^ (4 * (g & 1))) * 160 + p * 8) << 16);
    for (unsigned r = 0; r < 16; ++r) {
      const unsigned key = (r & 3) + 8 * (r >> 2) + 4 * (l >> 5);
      t.v[2 + (r >> 2)][l][r & 3] = key > (l & 31) ? 0u : 0xFF800000u;
      t.v[6 + (r >> 2)][l][r & 3] = key <= (l & 31) ? 0u : 0xFF800000u;
    }
  }
  return t;
}
__device__ const LaneTab lane_tab = make_tab();

__global__ void __launch_bounds__(512, 2)
k_fused(const char* __restrict__ xa_, const char* __restrict__ wa_, const char* __restrict__ woa_, const char* __restrict__ xb_,
        const char* __restrict__ wb_, const char* __restrict__ wob_, char* __restrict__ ws_, char* __restrict__ out_) {
  __shared__ __attribute__((aligned(16))) char lds[163840];
  const unsigned bid = blockIdx.x, xcd = bid & 7, slot = bid >> 3, br = xcd >> 2;
  u32x4 ka; u32x2 kb; u32x8 kc;
  {
    const void* kp = (const void*)__builtin_amdgcn_kernarg_segment_ptr();
    asm volatile("s_load_dwordx4 %0, %3, %4\n\ts_load_dwordx8 %2, %3, 0x20\n\ts_load_dwordx2 %1, %3, 0x10"
                 : "=&s"(ka), "=&s"(kb), "=&s"(kc) : "s"(kp), "s"(br * 24));
  }
  STAMP_DECL STAMP(0);
  unsigned lane = threadIdx.x & 63;
  const unsigned w = __builtin_amdgcn_readfirstlane(threadIdx.x >> 6);
  const unsigned grp = w >> 2, lw = w & 3;
  const unsigned mt = (xcd & 3) * 4 + (slot & 3), u = slot >> 2;
  const unsigned win = (5 * ((mt >> 1) & 3)) >> 1;
  const unsigned kv1 = (u + 3) >> 3;
  const unsigned j1 = win + u - 5;
#define ROT ((5 * (u & 3)) / 2 + 5 * (slot & 1) + (xcd & 3))
  auto mod10 = [](unsigned x) { return __builtin_elementwise_min(x, x - 10); };
  const unsigned rot = mod10(ROT);
  const unsigned m0v = grp * 16384 + lw * 4096;
  const unsigned str1 = grp ? 64 * 3840 : 128;
  u32x8 adl, adh;
  unsigned o1[4];
  {
    const unsigned row = lane >> 3, pos = lane & 7;
    const unsigned tw = row * 3840 + ((pos ^ (row & 2)) * 16);
    const unsigned t1 = grp ? tw : row * 1280 + ((pos ^ (row >> 1)) * 16);
    const unsigned gap = kv1 ? 1280 : 128, m1 = grp ? 0 : 64;
    o1[0] = t1 + 1024;
    o1[1] = (t1 ^ m1) + (grp ? gap : 8 * 1280);
    o1[2] = (t1 ^ m1 ^ 64) + (grp ? 8 * 3840 - 1024 : 16 * 1280 - 1024);
    o1[3] = (t1 ^ 64) + (grp ? 8 * 3840 - 2048 + gap : 24 * 1280 - 2048);
    adl[4] = tw + 1024; adl[5] = tw + 1280; adl[6] = (tw ^ 64) + (8 * 3840 - 1024); adl[7] = (tw ^ 64) + (8 * 3840 - 2048 + 1280);
    adh[0] = o1[0]; adh[1] = o1[1]; adh[2] = o1[2]; adh[3] = o1[3];
  }
  const unsigned offA = grp ? 16 * lw * 3840 + (kv1 ? 1280 + j1 * 128 : u * 256) : (mt * 128 + 32 * lw) * 1280;
  asm volatile("s_waitcnt lgkmcnt(0)" : "+s"(ka), "+s"(kb), "+s"(kc));
  auto ptr = [](unsigned lo, unsigned hi) { return (char*)(((unsigned long long)hi << 32) | lo); };
  const char* sA = ptr(grp ? ka[2] : ka[0], grp ? ka[3] : ka[1]) + (offA - 1024ll);
  char* const ws = ptr(kc[4], kc[5]);
  glds16x4(sA + rot * str1, o1, m0v);
  glds16x4(sA + mod10(rot + 1) * str1, o1, m0v + 32768);
  u32x2 ep; unsigned tch;
  asm volatile("s_load_dwordx2 %0, %1, %2" : "=&s"(ep) : "s"(ws), "s"((unsigned)CTL_WS));
  const unsigned j2 = mod10(win + 3 + (u < 7 ? u : 6));
  const unsigned Tg = mt * 4 + lw;
  const unsigned vt1 = grp & kv1, vt2 = u < 7 ? grp : 2;
  const unsigned cg1 = kv1 ? 10 + 10 * grp + j1 : 2 * u + grp, cg2 = 10 + 10 * grp + j2;
  const unsigned head = xcd * 4 + (slot & 3), qb = u;
  const unsigned odd = head & 1, o2 = 5 * (head >> 1) + 2, o1h = odd ? o2 + 1 : o2 - 2;
  const unsigned qt = qb * 8 + 2 * lw + grp;
  u32x4 soA; u32x16 so;
  {
    const unsigned kb0 = 80 * 32768 + w * 512, vb0 = (unsigned)V_WS - V_OFF + xcd * (256 * 1280) + w * 5120;
    unsigned ko[10], vo_[10];
#pragma unroll
    for (int i = 0; i < 8; ++i) { const unsigned c = (((head >> 3) + (i >> 1)) & 3) + 4 * (i & 1); ko[i] = kb0 + c * 4096; vo_[i] = vb0 + c * 40960; }
    const unsigned c8 = qb ? qb - 1 : 0;
    ko[8] = kb0 + (unsigned)QK_BR + c8 * 4096; vo_[8] = vb0 + (unsigned)V_BR + c8 * 40960;
    ko[9] = kb0 + (unsigned)QK_BR + qb * 4096; vo_[9] = vb0 + (unsigned)V_BR + qb * 40960;
    soA = u32x4{ko[0], vo_[0], ko[1], vo_[1]};
    so = u32x16{ko[2], vo_[2], ko[8], vo_[8], ko[9], vo_[9], ko[3], vo_[3], ko[4], vo_[4], ko[5], vo_[5], ko[6], vo_[6], ko[7], vo_[7]};
    asm volatile("" : "+s"(soA), "+{s[56:71]}"(so));
  }
  {
    unsigned t;
    asm volatile("s_load_dword %0, %1, %2" : "=&s"(t) : "s"(&lane_tab), "s"(__builtin_elementwise_min(slot * 8 + w, 79u) * 128));
    tch = t;
  }
  unsigned flag1 = FQ_OFF + ((br * 30 + cg1) * 64 + Tg) * 128, flag2 = FQ_OFF + ((br * 30 + cg2) * 64 + Tg) * 128;
  unsigned flagO = FO_OFF + (head * 64 + qt) * 128;
  PINS(flag1); PINS(flag2); PINS(flagO);
  const char* sB = ptr(ka[2], ka[3]) + (16 * lw * 3840 + 256 + j2 * 128);
  u32x16 pk;
  { const u32x4 s4 = g1_offs(br, cg1, Tg, lane); pk[0] = s4[0]; pk[1] = s4[1]; pk[2] = s4[2]; pk[3] = s4[3]; }
  {
    const unsigned r16 = lane & 15, g = lane >> 4, q = r16 >> 2, p = lane & 3;
    adh[5] = (lw * 32 + r16) * 128 + ((g ^ (r16 >> 1)) * 16); adh[6] = adh[5] ^ 64;
    adl[0] = 2048 * g + 1024 * grp + 128 * q + 16 * ((p >> 1) ^ (4 * (g & 1) + (q & 2))) + 8 * (p & 1);
    adl[1] = adl[0] ^ 32; adl[2] = adl[0] ^ 64; adl[3] = adl[0] ^ 96;
    const unsigned jj = (20 * head + 19 * (lane & 1)) >> 6, b1 = (lane >> 1) & 1;
    adh[4] = FQ_OFF + ((lane & 8) ? jj * 64 + (qt ^ b1) : (10 + 10 * b1 + jj) * 64 + (head >> 3) * 8 + w + (lane & 4) * 8) * 128;
    adh[7] = 0;
  }
  asm volatile("s_waitcnt vmcnt(4) lgkmcnt(0)\n\ts_barrier" : "+{v[64:71]}"(adl), "+{v[72:79]}"(adh), "+{v[80:95]}"(pk) :: "memory");
  PINS(ep); PINS(tch);
  const unsigned long long tag = (((unsigned long long)ep[1] << 32) | ep[0]) + 1;
  STAMP(1);
  f32x32 acc, x3, x4, wf;
  {
    f32x32 x0, x1, x2;
    STV_PUT(adh[7]);
    g1_pass1(vt1, kv1, x0, x1, x2, x3, x4, adl, adh, pk, sA, sB, mod10(rot + 2) * str1, str1, mod10(rot + grp) * (64 * 3840), m0v);
    STV_GET(adh[7]); STAMP(3); STV_PUT(adh[7]);
    g1_pass2a(vt2, acc, x0, x1, x2, x3, x4, wf, adl, adh, pk, sB, m0v, mod10(rot + grp + 6) * (64 * 3840), tag, flag1, ws);
    STV_GET(adh[7]);
    STAMP(6);
  }
  lane = lane_id();
  const unsigned r32 = lane & 31, h = lane >> 5;
  const unsigned g1o = h * 32768 + r32 * 16 + o1h * 32768;
  const unsigned g2o = r32 * 16 + (o2 * 32768 + odd * 8);
  const unsigned gvo = h * 1280 + (__builtin_elementwise_min(r32 + 4 * odd, r32 + 4 * odd - 20) + (head & 3) * 20) * 16;
  const u32x4 go = {g1o, g2o - (1024 + odd * 8), gvo, gvo + V_OFF};
  const unsigned mytile = w * TILE_B;
  bf16x8 q1a, q1b; s16x4 q2a, q2b;
  asm volatile("global_load_dwordx4 %0, %4, %6 sc1\n\tglobal_load_dwordx2 %1, %5, %6 sc1\n\t"
               "global_load_dwordx4 %2, %4, %7 sc1\n\tglobal_load_dwordx2 %3, %5, %7 sc1"
               : "=&v"(q1a), "=&v"(q2a), "=&v"(q1b), "=&v"(q2b)
               : "v"(g1o), "v"(g2o), "s"(ws + qt * 512), "s"(ws + (qt ^ 1) * 512) : "memory");
  tile_dma(ws + soA[0], ws + soA[1], mytile, go);
  tile_dma(ws + soA[2], ws + soA[3], mytile + CHUNK_B, go);
  typedef __attribute__((address_space(3))) u32x4* lds_u4;
  lds_ptr crow = (lds_ptr)lds + (mytile + V_OFF + 320) + (lane >> 4) * 512 + __builtin_elementwise_min(lane & 15, 11u) * 16;
  const unsigned cv = (lane & 15) == 0 ? 0x3F803F80u : 0u;
  const u32x4 cv4 = {cv, cv, cv, cv};
  *(lds_u4)(crow) = cv4; *(lds_u4)(crow + CHUNK_B) = cv4; *(lds_u4)(crow + 2 * CHUNK_B) = cv4;
  u32x16 at;
  {
    const unsigned ab = grp * TILE_B;
    const unsigned a0 = lane * 16 + ab;
    const unsigned a1 = h ? (V_OFF + 320 + ab) : r32 * 16 + (K2_OFF + odd * 8 + ab);
    const unsigned a2 = h * 512 + (r32 < 28 ? __builtin_elementwise_min(r32, 21u) : 28) * 16 + (V_OFF + ab);
    const unsigned jj = (20 * head + 19 * (lane & 1)) >> 6, c = (lane >> 2) & 7;
    const unsigned cB = __builtin_elementwise_max((c & 1) + qb, 1u) + (30 * 8 - 1);
    const unsigned kvf = (lane & 2) * 320 + (h ? cB : c) * 8 + (10 * 64 + w);
    const unsigned pol = (jj * 64 + (lane >= 48 ? 30 * 64 + qt : kvf)) * 128 + FQ_OFF;
    at = u32x16{a0, a1, a2, a0 + 2 * CHUNK_B, a1 + 2 * CHUNK_B, a2 + 2 * CHUNK_B, a0 + 4 * CHUNK_B, a1 + 4 * CHUNK_B, a2 + 4 * CHUNK_B,
                go[0], go[1], go[2], go[3], pol, c, jj};
    asm volatile("" : "+{v[128:143]}"(at));
  }
  const u32x4 so2 = g1_offs(br, cg2, Tg, lane);
  adl[4] = so2[0]; adl[5] = so2[1]; adl[6] = so2[2]; adl[7] = so2[3];
  STAMP(7);
  g1_pass2b(vt2, acc, x3, x4, wf, adl, ws);
  STAMP(8);
  if (u == 7) {
    WAITV(0);
    if (bid == 255 && grp == 0) {
      const unsigned e = w * 64 + lane, eu = e & 7, em = (e >> 3) & 15;
      poll_flags(ws, FQ_OFF + (((e >> 7) * 30 + (eu < 5 ? 2 * eu : 5 + eu + ((5 * ((em >> 1) & 3)) >> 1))) * 64 + 4 * em) * 128, tag);
    }
  }
  asm volatile("s_waitcnt lgkmcnt(0)\n\ts_barrier" : "+v"(q1a), "+v"(q2a), "+v"(q1b), "+v"(q2b) :: "memory");
  STAMP(9);
  if (bid == 255 && w == 0) put_flag(ws, CTL_WS, tag);
  const f32x16 zero16 = {};
  {
    lds_cptr tp = (lds_cptr)lds - grp * TILE_B;
    const bf16x8 k1 = lds_b128(tp + at[0]); const s16x4 k2 = lds_b64(tp + at[1]);
    if (h) { q2a = s16x4{0, 0, 0, 0}; q2b = s16x4{0, 0, 0, 0}; }
    *(lds_u4)(crow + 3 * CHUNK_B) = cv4; *(lds_u4)(crow + 4 * CHUNK_B) = cv4;
    if (qb == 0) {
      lds_ptr z = (lds_ptr)lds + 3 * CHUNK_B + mytile + lane * 16;
      *(lds_f4)(z) = f32x4{}; *(lds_f4)(z + 1024) = f32x4{}; *(lds_f4)(z + 2048) = f32x4{}; *(lds_f4)(z + 2560) = f32x4{};
    }
    f32x16 sa = MFMA32(k1, q1a, zero16), sb = MFMA32(k1, q1b, zero16);
    sa = MFMA32K8(k2, q2a, sa); sb = MFMA32K8(k2, q2b, sb);
    q2a = with_negmax(q2a, xhalf_max(max16(sa, sa[0])), h);
    q2b = with_negmax(q2b, xhalf_max(max16(sb, sb[0])), h);
  }
  auto exact = [&](int b, bf16x8 q1, s16x4 q2, int t0, int t1) {
    __builtin_amdgcn_fence(__ATOMIC_ACQUIRE, "agent");
    const int ln = lane_id(), r32 = ln & 31, h = ln >> 5;
    if (h) q2 = s16x4{0, 0, 0, 0};
    typedef __attribute__((address_space(1))) const char* gptr;
    gptr kg1 = (gptr)ws + QK_WS + b * QK_BR + (size_t)(80 + o1h + h) * 32768 + r32 * 16;
    gptr kg2 = (gptr)ws + QK_WS + b * QK_BR + (size_t)(80 + o2) * 32768 + r32 * 16 + odd * 8;
    gptr vg = (gptr)ws + V_WS + b * V_BR + (head >> 2) * (256 * 1280) + h * 1280 +
                     ((head & 3) * 20 + (odd ? (r32 < 16 ? r32 + 4 : r32 - 16) : r32)) * 16;
    const u32x4 vc = r32 == 20 ? u32x4{0x3F803F80u, 0x3F803F80u, 0x3F803F80u, 0x3F803F80u} : u32x4{0u, 0u, 0u, 0u};
    float m = -INFINITY;
    f32x16 r = zero16;
#pragma unroll 1
    for (int ps = 0; ps < 2; ++ps) {
#pragma unroll 1
      for (int t = t0; t <= t1; ++t) {
        f32x16 s = MFMA32(*(__attribute__((address_space(1))) const bf16x8*)(kg1 + t * 512), q1, zero16);
        s = MFMA32K8(*(__attribute__((address_space(1))) const s16x4*)(kg2 + t * 512), q2, s);
        if (b) {
          const int d = (t1 - t) * 32 + r32 - 4 * h;
#pragma unroll
          for (int e = 0; e < 16; ++e) s[e] = (unsigned)(d - ((e & 3) + 8 * (e >> 2))) < 256u ? s[e] : -INFINITY;
        }
        if (ps == 0) { m = max16(s, m); continue; }
#pragma unroll
        for (int e = 0; e < 16; ++e) s[e] = __builtin_amdgcn_exp2f(s[e] - m);
        u32x4 va = vc, vb = vc;
        if (r32 < 20) { va = *(__attribute__((address_space(1))) const u32x4*)(vg + t * 5120); vb = *(__attribute__((address_space(1))) const u32x4*)(vg + t * 5120 + 2560); }
        r = smfmac(__builtin_bit_cast(bf16x8, va), lo8(s), r);
        r = smfmac(__builtin_bit_cast(bf16x8, vb), hi8(s), r);
      }
      m = xhalf_max(m);
    }
    return r;
  };
  auto unsafe = [&](const f32x16& r, float l) {
    float amax = 0.f;
#pragma unroll
    for (int e = 0; e < 12; ++e) amax = __builtin_fmaxf(amax, __builtin_fabsf(r[e]));
    return __any(!(l <= 1.2676506e30f) || !(l >= 5.4210109e-20f) || !(amax <= 3.0e38f));
  };
  const char* const woa = ptr(kb[0], kb[1]);
  const char* const wob = ptr(kc[2], kc[3]);
  char* const oA = ws + (O_WS + qt * 512);
  f32x32 oab, of; f32x8 of2;
  u32x8 wo;
  u32x16 qq;
  unsigned wl;
  {
    f32x16 s0, pp;
    f32x32 fr;
    {
      const u32x4 qa4 = __builtin_bit_cast(u32x4, q1a), qb4 = __builtin_bit_cast(u32x4, q1b);
      const u32x2 qa2 = __builtin_bit_cast(u32x2, q2a), qb2 = __builtin_bit_cast(u32x2, q2b);
      qq = u32x16{qa4[0], qa4[1], qa4[2], qa4[3], qb4[0], qb4[1], qb4[2], qb4[3], qa2[0], qa2[1], qb2[0], qb2[1], 0xDDDDu,
                  lane * 512 + (3 * CHUNK_B + V_OFF + 320 + mytile), at[10] + (1024 + odd * 8), lane * 16};
    }
    asm volatile(
        "ds_read_b128 v[80:83], v128\n\tds_read_b64 v[84:85], v129\n\tds_read_b128 v[94:97], v128 offset:7168\n\tds_read_b64 v[98:99], v129 offset:7168\n\t"
        "ds_read_b128 v[86:89], v130\n\tds_read_b128 v[100:103], v130 offset:7168\n\tds_read_b128 v[90:93], v130 offset:1024\n\t"
        ".set i_, 0\n\t.rept 10\n\tv_mov_b64 v[i_:i_+1], 0\n\t.set i_, i_ + 2\n\t.endr\n\t"
        "s_waitcnt lgkmcnt(5)\n\t"
        "v_mfma_f32_32x32x16_bf16 v[32:47], v[80:83], v[64:67], 0\n\t"
        "v_mfma_f32_32x32x8_bf16 v[32:47], v[84:85], v[72:73], v[32:47]\n\t"
        ".rept 6\n\tv_mov_b64 v[i_:i_+1], 0\n\t.set i_, i_ + 2\n\t.endr\n\t"
        "v_mov_b64 v[104:105], 0\n\tv_mov_b64 v[106:107], 0\n\t"
        "v_mov_b64 v[120:121], 0\n\tv_mov_b64 v[122:123], 0\n\tv_mov_b64 v[124:125], 0\n\tv_mov_b64 v[126:127], 0\n\t"
        "s_waitcnt lgkmcnt(0)"
        : "=&{v[0:31]}"(oab), "=&{v[112:127]}"(pp), "=&{v[32:47]}"(s0), "=&{v[80:111]}"(fr), "+{v[64:79]}"(qq), "+{v[128:143]}"(at) :: "memory");
    STAMP(10);
    f32x16 maskF, maskD; u32x8 qv;
    at_a1(oab, s0, pp, fr, qq, at, ws, so, mytile, mytile + (qb ? 3 : 4) * CHUNK_B, tag, flag2, (0x7fu >> u) & 1u, ws + (QK_BR + qt * 512),
          &lane_tab, maskF, maskD, qv, wo AT_STV_PASS);
    STAMP(11);
    {
      const unsigned tb = 3 * CHUNK_B + 2 * lw * TILE_B;
      const unsigned a0 = at[0] + tb, a1 = at[1] + tb;
      lds_cptr tp = (lds_cptr)lds + 7 * TILE_B;
      const bf16x8 ka1 = lds_b128(tp + a0), kb1 = lds_b128(tp + a0 + TILE_B);
      const s16x4 ka2 = lds_b64(tp + a1), kb2 = lds_b64(tp + a1 + TILE_B);
      const bf16x8 q1B = __builtin_bit_cast(bf16x8, u32x4{qv[0], qv[1], qv[2], qv[3]});
      const s16x4 q2B = __builtin_bit_cast(s16x4, h ? u32x2{0, 0} : u32x2{qv[4], qv[5]});
      f32x16 s1 = MFMA32(ka1, q1B, zero16), s2 = MFMA32(kb1, q1B, maskD);
      s1 = MFMA32K8(ka2, q2B, s1); s2 = MFMA32K8(kb2, q2B, s2);
      st16(oA, at[9], u32x4{~0u, ~0u, ~0u, ~0u}); st8lo(oA, qq[14], u32x2{~0u, ~0u});
      wl = wo[7];
      wo[5] += FO_OFF + (grp * 16 * 64 + head * 2 + (lw >> 1)) * 128;
      wo[6] += (unsigned)O_WS + grp * 40 * 32768 + (head * 64 + lw * 16) * 16;
      const float m1 = max16(s1, s1[0]);
      const u32x2 qm = __builtin_bit_cast(u32x2, with_negmax(q2B, xhalf_max(max16(s2, qt ? m1 : -INFINITY)), h));
      qv[4] = qm[0]; qv[5] = qm[1]; qv[6] = a0; qv[7] = a1;
      f32x16 ob;
      at_b(ob, maskF, maskD, qv, at[2] + tb, qq, oab, s0, fr, pp);
      float l = row_sum(ob);
      if (__builtin_expect(unsafe(ob, l), 0)) {
        ob = exact(1, __builtin_bit_cast(bf16x8, u32x4{qv[0], qv[1], qv[2], qv[3]}), __builtin_bit_cast(s16x4, u32x2{qv[4], qv[5]}),
                   qb ? qt - 8 : 0, qt);
        l = row_sum(ob);
      }
      store_o<false>(ob, l, oA + O_BR, at[9], qq[14]);
    }
    STAMP(13);
    at[13] = (__builtin_elementwise_min(qq[15], 24u * 16) + (slot * 8 + w) * (25 * 16)) * 8;
    at_a2(oab, s0, pp, fr, qq, at, ws, so, mytile, tag, flagO, woa, wo, wob + (qb * 160 + w * (32 * 1280) - 4096ll), w * 5120, (w + 8) * 5120,
          grp ? 0x80000000u : w * 5120 + 143360, of, of2);
  }
  STAMP(12);
  {
    lds_ptr xb = (lds_ptr)lds + 3 * CHUNK_B + qq[15];
#pragma unroll
    for (int i = 0; i < 3; ++i) *(lds_f4)(xb + w * 3072 + i * 1024) = f32x4{oab[16 + 4 * i], oab[17 + 4 * i], oab[18 + 4 * i], oab[19 + 4 * i]};
    asm volatile("s_setprio 3\n\ts_waitcnt vmcnt(0) lgkmcnt(0)\n\ts_barrier" : "+{v[184:215]}"(of), "+{v[216:223]}"(of2) :: "memory");
    f32x16 o = {};
#pragma unroll
    for (int i = 0; i < 3; ++i) {
      const f32x4 r = *(lds_f4)(xb + (w ^ 4) * 3072 + i * 1024);
#pragma unroll
      for (int e = 0; e < 4; ++e) o[4 * i + e] = oab[4 * i + e] + r[e];
    }
    float l = row_sum(o);
    if (__builtin_expect(unsafe(o, l), 0)) {
      o = exact(0, __builtin_bit_cast(bf16x8, u32x4{qq[0], qq[1], qq[2], qq[3]}), __builtin_bit_cast(s16x4, u32x2{qq[8], qq[9]}), 0, 63);
      l = row_sum(o);
    }
    store_o<true>(o, l, oA, at[9], qq[14]);
  }
  STAMP(14);
  {
    f32x32 p1, p2, oa; f32x8 oa2;
    const unsigned wl0 = wl & 0xffff, wl1 = wl >> 16;
    const char* ws0 = woa + (qb * 160 + (w < 5 ? w : w + 5) * (32 * 1280) - 4096ll);
    const char* ws1 = woa + (qb * 160 + (13 + (w & 1)) * (32 * 1280) - 4096ll);
    {
      const unsigned b1 = grp * 51200, b2 = grp ? 143360 : 30720;
      proj_1(p1, of, of2, oa, oa2, u32x4{wl0 + b1, wl1 + b1, wl0 + b2, wl1 + b2}, wo, ws0, ws1, 86016 + w * 5120,
             w < 2 ? 86016 + (8 + w) * 5120 : 0x80000000u, ws);
    }
    STAMP(16);
    lane = lane_id();
    proj_send(p1, lds + 143360, lw, grp, lane);
    wo_dma(ws0 + 5 * 32 * 1280, w * 5120, wo);
    if (w < 2) { wo_dma(ws1 + 5 * 32 * 1280, (8 + w) * 5120, wo); WAITV(10); } else WAITV(5);
    char* dst = ptr(kc[6], kc[7]) + ((head * 64 + lw * 16) * 2560 + qb * 160);
    STAMP(17);
    const unsigned b1 = 86016 + grp * 25600, b2 = grp * 25600;
    const u32x4 wab = {wl0 + b1, wl1 + b1, wl0 + b2, wl1 + b2};
    asm volatile("" : "={v[160:191]}"(p2));
    proj_2<0>(p2, oa, oa2, wab, wo, ws);
    STAMP(18);
    proj_finish(p1, lds + 143360, lw, grp, lane, dst + 1280);
    proj_2<5>(p2, oa, oa2, wab, wo, ws);
    STAMP(20);
    proj_send(p2, lds + 86016, lw, grp, lane);
    asm volatile("s_waitcnt lgkmcnt(0)\n\ts_barrier" ::: "memory");
    proj_finish(p2, lds + 86016, lw, grp, lane, dst);
  }
  STAMP_OUT(ws, 0);
}

extern "C" void kernel_launch(void* const* d_in, const int* in_sizes, int n_in, void* d_out, int out_size, void* d_ws,
                              size_t ws_size, hipStream_t stream) {
  k_fused<<<256, 512, 0, stream>>>((const char*)d_in[0], (const char*)d_in[1], (const char*)d_in[2], (const char*)d_in[3],
                                   (const char*)d_in[4], (const char*)d_in[5], (char*)d_ws, (char*)d_out);
}
```

```cpp
#include <hip/hip_runtime.h>
#include <hip/hip_bf16.h>
#include <stdint.h>
#include <type_traits>

typedef __attribute__((ext_vector_type(8))) __bf16 bf16x8;
typedef __attribute__((ext_vector_type(16))) __bf16 bf16x16;
typedef __attribute__((ext_vector_type(4))) short s16x4;
typedef __attribute__((ext_vector_type(8))) short s16x8;
typedef __attribute__((ext_vector_type(2))) unsigned u32x2;
typedef __attribute__((ext_vector_type(4))) unsigned u32x4;
typedef __attribute__((ext_vector_type(4))) float f32x4;
typedef __attribute__((ext_vector_type(8))) float f32x8;
typedef __attribute__((ext_vector_type(16))) float f32x16;
typedef __attribute__((ext_vector_type(32))) float f32x32;
typedef __attribute__((address_space(3))) const char* lds_cptr;
typedef __attribute__((address_space(3))) char* lds_ptr;

constexpr size_t QK_WS = 0, QK_BR = (size_t)160 * 32768;
constexpr size_t V_WS = 2 * QK_BR, V_BR = (size_t)8 * 256 * 1280;
constexpr size_t O_WS = V_WS + 2 * V_BR, O_BR = (size_t)80 * 32768;
constexpr size_t CTL_WS = O_WS + 2 * O_BR;
constexpr unsigned FQ_OFF = CTL_WS + 4096, FO_OFF = FQ_OFF + 2 * 30 * 64 * 128;
constexpr float QSCALE = 0.22360679774997896f * 1.4426950408889634f;

#define STAMP_DECL
#define STV_PUT(x)
#define STV_GET(x)
#define STAMP(i) do {} while (0)
#define STAMP_OUT(ws_, kid) do {} while (0)
__device__ __forceinline__ int lane_id() {
  int l; asm volatile("v_mbcnt_lo_u32_b32 %0, -1, 0\n\tv_mbcnt_hi_u32_b32 %0, -1, %0" : "=v"(l)); return l;
}
#define SBAR() __builtin_amdgcn_sched_barrier(0)
#define PIN(x) asm volatile("" : "+v"(x))
#define PINS(x) asm volatile("" : "+s"(x))
#define WAITV(N) asm volatile("s_waitcnt vmcnt(" #N ")" ::: "memory")
#define BARRIER() asm volatile("s_barrier" ::: "memory")
#define WAITV_BAR(N) asm volatile("s_waitcnt vmcnt(" #N ") lgkmcnt(0)\n\ts_barrier" ::: "memory")

__device__ __forceinline__ void glds16(const void* sbase, unsigned voff, unsigned lds) {
  unsigned keep;
  asm volatile("s_mov_b32 %0, m0\n\ts_mov_b32 m0, %3\n\ts_nop 0\n\tglobal_load_lds_dwordx4 %1, %2\n\ts_mov_b32 m0, %0"
               : "=&s"(keep) : "v"(voff), "s"(sbase), "s"(lds) : "memory");
}
__device__ __forceinline__ void glds16x4(const void* sbase, const unsigned (&voff)[4], unsigned lds) {
  unsigned keep;
  asm volatile("s_mov_b32 %0, m0\n\ts_mov_b32 m0, %6\n\ts_nop 0\n\tglobal_load_lds_dwordx4 %1, %5\n\t"
               "global_load_lds_dwordx4 %2, %5 offset:1024\n\tglobal_load_lds_dwordx4 %3, %5 offset:2048\n\t"
               "global_load_lds_dwordx4 %4, %5 offset:3072\n\ts_mov_b32 m0, %0"
               : "=&s"(keep) : "v"(voff[0]), "v"(voff[1]), "v"(voff[2]), "v"(voff[3]), "s"(sbase), "s"(lds) : "memory");
}
__device__ __forceinline__ unsigned pk_bf16(float lo, float hi) {
  typedef __attribute__((ext_vector_type(2))) float f32x2_;
  typedef __attribute__((ext_vector_type(2))) __bf16 bf16x2_;
  const f32x2_ v = {lo, hi};
  return __builtin_bit_cast(unsigned, __builtin_convertvector(v, bf16x2_));
}
__device__ __forceinline__ void st16(char* sbase, unsigned voff, u32x4 v) {
  asm volatile("global_store_dwordx4 %0, %1, %2 sc1\n\ts_nop 1" :: "v"(voff), "v"(v), "s"(sbase) : "memory");
}
__device__ __forceinline__ void st8lo(char* sbase, unsigned voff, u32x2 v) {
  asm volatile("s_mov_b32 exec_hi, 0\n\tglobal_store_dwordx2 %0, %1, %2 sc1\n\ts_mov_b32 exec_hi, -1" :: "v"(voff), "v"(v), "s"(sbase) : "memory");
}
__device__ __forceinline__ bf16x8 lds_b128(lds_cptr p) { return *(const __attribute__((address_space(3))) bf16x8*)p; }
__device__ __forceinline__ s16x4 lds_b64(lds_cptr p) { return *(const __attribute__((address_space(3))) s16x4*)p; }
__device__ __forceinline__ s16x4 lds_tr(lds_cptr p) {
  return __builtin_amdgcn_ds_read_tr16_b64_v4i16((__attribute__((address_space(3))) s16x4*)p);
}
__device__ __forceinline__ bf16x8 cat8(s16x4 a, s16x4 b) {
  s16x8 v = {a[0], a[1], a[2], a[3], b[0], b[1], b[2], b[3]};
  return __builtin_bit_cast(bf16x8, v);
}
#define MFMA32(a, b, c) __builtin_amdgcn_mfma_f32_32x32x16_bf16(a, b, c, 0, 0, 0)
#define MFMA32K8(a, b, c) __builtin_amdgcn_mfma_f32_32x32x8bf16_1k(a, b, c, 0, 0, 0)
#define MFMA16(a, b, c) __builtin_amdgcn_mfma_f32_16x16x32_bf16(a, b, c, 0, 0, 0)

typedef __attribute__((ext_vector_type(8))) unsigned u32x8;
typedef __attribute__((ext_vector_type(16))) unsigned u32x16;
#define AWAIT(d, d1, off) \
    "s_mov_b32 s48, 0x200000\n\t5:\n\ts_waitcnt vmcnt(0)\n\tv_cmp_ne_u64 vcc, s[50:51], v[" #d ":" #d1 "]\n\ts_sub_u32 s48, s48, 1\n\t" \
    "s_cbranch_vccz 4f\n\ts_cbranch_scc1 4f\n\ts_sleep 2\n\tglobal_load_dwordx2 v[" #d ":" #d1 "], v" #off ", s[54:55] sc1\n\ts_branch 5b\n\t4:\n\t"
#define G1_ASM_MACROS \
    ".macro pack vt\n\t.set i_, 0\n\t.rept 4\n\t" \
    ".if \\vt\n\t.set e_, 8 * i_\n\t.set f_, 8 * i_ + 4\n\t.else\n\t.set e_, 4 * i_\n\t.set f_, 4 * i_ + 16\n\t.endif\n\t" \
    "v_cvt_pk_bf16_f32 v[80+4*i_], v[e_], v[e_+1]\n\tv_cvt_pk_bf16_f32 v[81+4*i_], v[e_+2], v[e_+3]\n\t" \
    "v_cvt_pk_bf16_f32 v[82+4*i_], v[f_], v[f_+1]\n\tv_cvt_pk_bf16_f32 v[83+4*i_], v[f_+2], v[f_+3]\n\t" \
    ".set i_, i_ + 1\n\t.endr\n\t.set i_, 0\n\t.rept 4\n\t" \
    "v_permlane16_swap_b32 v[80+4*i_], v[82+4*i_]\n\tv_permlane16_swap_b32 v[81+4*i_], v[83+4*i_]\n\t" \
    ".set i_, i_ + 1\n\t.endr\n\t.endm\n\t" \
    ".macro mm vt, c, x, w\n\t" \
    ".if (\\x) < 104\n\t"    \
    ".if \\vt\n\tv_mfma_f32_16x16x32_bf16 v[\\c:\\c+3], v[\\x:\\x+3], v[\\w:\\w+3], 0\n\t" \
    ".else\n\tv_mfma_f32_16x16x32_bf16 v[\\c:\\c+3], v[\\w:\\w+3], v[\\x:\\x+3], 0\n\t.endif\n\t" \
    ".elseif \\vt\n\tv_mfma_f32_16x16x32_bf16 v[\\c:\\c+3], v[\\x:\\x+3], v[\\w:\\w+3], v[\\c:\\c+3]\n\t" \
    ".else\n\tv_mfma_f32_16x16x32_bf16 v[\\c:\\c+3], v[\\w:\\w+3], v[\\x:\\x+3], v[\\c:\\c+3]\n\t.endif\n\t" \
    ".endm\n\t" \
    ".macro rx d, a, off\n\tds_read_b128 v[\\d:\\d+3], v\\a offset:\\off\n\tds_read_b128 v[\\d+4:\\d+7], v\\a offset:\\off+2048\n\t.endm\n\t" \
    ".macro rw d, a, off\n\tds_read_b64_tr_b16 v[\\d:\\d+1], v\\a offset:\\off\n\tds_read_b64_tr_b16 v[\\d+2:\\d+3], v\\a offset:\\off+512\n\t.endm\n\t" \
    ".macro bump lo, n, val\n\t.set bi_, \\lo\n\t.rept \\n\n\tv_add_u32 v[bi_], \\val, v[bi_]\n\t.set bi_, bi_+1\n\t.endr\n\t.endm\n\t"
#define G1_ASM_PURGE ".purgem pack\n\t.purgem mm\n\t.purgem rx\n\t.purgem rw\n\t.purgem bump\n\t"

__device__ __forceinline__ void g1_pass1(int vt, int kv, f32x32& x0, f32x32& x1, f32x32& x2, f32x32& x3, f32x32& x4, u32x8& adl, u32x8& adh, u32x16& pk,
                                         const char* sA, const char* sB, unsigned off1, unsigned str1, unsigned off2, unsigned m0v) {
  unsigned keep, tmp;
  const unsigned sAl = (unsigned)(uintptr_t)sA, sAh = (unsigned)((uintptr_t)sA >> 32);
  const unsigned sBl = (unsigned)(uintptr_t)sB, sBh = (unsigned)((uintptr_t)sB >> 32);
  asm volatile(
      G1_ASM_MACROS
      ".macro p1body vt\n\t"
      ".set st_, 0\n\t"
      ".rept 10\n\t"
      ".set xr_, 96 + 16 * st_\n\t"
      ".set o_, ((st_ %% 5) & 1) * 32768\n\t"
      ".set n_, (((st_ + 1) %% 5) & 1) * 32768\n\t"
      "s_waitcnt lgkmcnt(6)\n\t"
      "mm \\vt, 0, xr_, 32\n\trw 48, 64, o_+16384+8192\n\t"
      "mm \\vt, 16, xr_+4, 32\n\trx xr_+8, 78, o_\n\ts_waitcnt lgkmcnt(8)\n\t"
      "mm \\vt, 4, xr_, 36\n\trw 52, 65, o_+16384+8192\n\t"
      "mm \\vt, 20, xr_+4, 36\n\trw 56, 66, o_+16384+8192\n\ts_waitcnt lgkmcnt(10)\n\t"
      "mm \\vt, 8, xr_, 40\n\trw 60, 67, o_+16384+8192\n\t"
      "mm \\vt, 24, xr_+4, 40\n\ts_waitcnt lgkmcnt(10)\n\t"
      "mm \\vt, 12, xr_, 44\n\t"
      ".if st_ < 10 - 3\n\t"
      "s_add_u32 s40, %[sAl], %[off1]\n\ts_addc_u32 s41, %[sAh], 0\n\t"
      "s_add_u32 %[off1], %[off1], %[str1]\n\ts_cmp_eq_u32 %[off1], %[lim]\n\ts_cselect_b32 %[off1], 0, %[off1]\n\t"
      ".else\n\t"
      "s_add_u32 s40, %[sBl], %[off2]\n\ts_addc_u32 s41, %[sBh], 0\n\t"
      "s_add_u32 %[off2], %[off2], 2*64*3840\n\ts_cmp_ge_u32 %[off2], 640*3840\n\ts_cselect_b32 %[tmp], 640*3840, 0\n\ts_sub_u32 %[off2], %[off2], %[tmp]\n\t"
      ".endif\n\t"
      "mm \\vt, 28, xr_+4, 44\n\t"
      "s_add_u32 m0, %[m0v], ((st_ + 3) %% 5) * 32768\n\t"
      ".if (st_ %% 5) == 1 || (st_ %% 5) == 3\n\tbump 64, 4, 0x10000\n\tbump 77, 2, 0x10000\n\t.endif\n\t"
      ".if (st_ %% 5) == 4\n\tbump 64, 4, 0xfffe0000\n\tbump 77, 2, 0xfffe0000\n\t.endif\n\t"
      "s_waitcnt lgkmcnt(6)\n\t"
      "mm \\vt, 0, xr_+8, 48\n\t"
      "mm \\vt, 16, xr_+12, 48\n\t"
      "s_waitcnt vmcnt(4) lgkmcnt(0)\n\ts_barrier\n\t"
      ".set vo_, 72 - 4 * ((st_ + 3) / 10)\n\t"
      "mm \\vt, 4, xr_+8, 52\n\t"
      ".if st_ < 9\n\trw 32, 64, n_+16384\n\t.endif\n\t"
      "global_load_lds_dwordx4 v[vo_], s[40:41]\n\t"
      "mm \\vt, 20, xr_+12, 52\n\t"
      ".if st_ < 9\n\trx xr_+16, 77, n_\n\t.endif\n\t"
      "global_load_lds_dwordx4 v[vo_+1], s[40:41] offset:1024\n\t"
      "mm \\vt, 8, xr_+8, 56\n\t"
      ".if st_ < 9\n\trw 36, 65, n_+16384\n\t.endif\n\t"
      "global_load_lds_dwordx4 v[vo_+2], s[40:41] offset:2048\n\t"
      "mm \\vt, 24, xr_+12, 56\n\t"
      ".if st_ < 9\n\trw 40, 66, n_+16384\n\t.endif\n\t"
      "global_load_lds_dwordx4 v[vo_+3], s[40:41] offset:3072\n\t"
      "mm \\vt, 12, xr_+8, 60\n\t"
      ".if st_ < 9\n\trw 44, 67, n_+16384\n\t.endif\n\t"
      "mm \\vt, 28, xr_+12, 60\n\t"
      ".set st_, st_ + 1\n\t"
      ".endr\n\t"
      ".endm\n\t"
      "rw 32, 64, 16384\n\trx 96, 77, 0\n\trw 36, 65, 16384\n\trw 40, 66, 16384\n\trw 44, 67, 16384\n\t"
      "s_mov_b32 %[keep], m0\n\ts_add_u32 m0, %[m0v], 65536\n\t"
      "s_add_u32 s40, %[sAl], %[off1]\n\ts_addc_u32 s41, %[sAh], 0\n\t"
      "s_add_u32 %[off1], %[off1], %[str1]\n\ts_cmp_eq_u32 %[off1], %[lim]\n\ts_cselect_b32 %[off1], 0, %[off1]\n\t"
      "global_load_lds_dwordx4 v72, s[40:41]\n\tglobal_load_lds_dwordx4 v73, s[40:41] offset:1024\n\t"
      "global_load_lds_dwordx4 v74, s[40:41] offset:2048\n\tglobal_load_lds_dwordx4 v75, s[40:41] offset:3072\n\t"
      "s_cmp_lg_u32 %[vt], 0\n\ts_cbranch_scc1 1f\n\t"
      "p1body 0\n\t"
      "v_mov_b64 v[72:73], v[80:81]\n\tv_mov_b64 v[74:75], v[82:83]\n\ts_mov_b32 s40, %[sc]\n\ts_cmp_lg_u32 %[kv], 0\n\ts_nop 3\n\ts_cbranch_scc1 3f\n\t"
      ".set i_, 0\n\t.rept 16\n\tv_pk_mul_f32 v[i_:i_+1], v[i_:i_+1], s[40:41] op_sel_hi:[1,0]\n\t.set i_, i_ + 2\n\t.endr\n\t"
      "3:\n\tpack 0\n\ts_branch 2f\n\t"
      "1:\n\tp1body 1\n\t"
      "v_mov_b64 v[72:73], v[80:81]\n\tv_mov_b64 v[74:75], v[82:83]\n\ts_nop 5\n\tpack 1\n\t"
      "2:\n\ts_mov_b32 m0, %[keep]\n\t"
      ".purgem p1body\n\t" G1_ASM_PURGE
      : "=&{v[96:127]}"(x0), "=&{v[128:159]}"(x1), "=&{v[160:191]}"(x2), "=&{v[192:223]}"(x3), "=&{v[224:255]}"(x4),
        "+{v[64:71]}"(adl), "+{v[72:79]}"(adh), "+{v[80:95]}"(pk), [keep] "=&s"(keep), [tmp] "=&s"(tmp), [off1] "+s"(off1), [off2] "+s"(off2)
      : [sAl] "s"(sAl), [sAh] "s"(sAh), [sBl] "s"(sBl), [sBh] "s"(sBh), [str1] "s"(str1), [m0v] "s"(m0v), [vt] "s"(vt), [lim] "s"(10 * str1), [kv] "s"(kv),
        [sc] "n"(__builtin_bit_cast(unsigned, QSCALE))
      : "memory", "scc", "s40", "s41", "v0", "v1", "v2", "v3", "v4", "v5", "v6", "v7", "v8", "v9", "v10", "v11", "v12", "v13", "v14", "v15",
        "v16", "v17", "v18", "v19", "v20", "v21", "v22", "v23", "v24", "v25", "v26", "v27", "v28", "v29", "v30", "v31", "v32", "v33", "v34", "v35", "v36", "v37", "v38", "v39", "v40", "v41", "v42", "v43", "v44", "v45", "v46", "v47",
        "v48", "v49", "v50", "v51", "v52", "v53", "v54", "v55", "v56", "v57", "v58", "v59", "v60", "v61", "v62", "v63");
}

#define G1_P2_MACROS \
      G1_ASM_MACROS \
      ".macro slot n\n\t.set n_, \\n\n\t"    \
      ".if n_ < 4\n\tglobal_store_dwordx4 v[72+n_], v[80+4*n_:83+4*n_], s[54:55] sc1\n\t.endif\n\t" \
      ".if n_ >= 4 && n_ < 12\n\t" \
      ".if (n_ & 3) == 0\n\t" \
      "s_add_u32 m0, s46, (2 + n_ / 4) * 32768\n\ts_add_u32 s40, s44, s47\n\ts_addc_u32 s41, s45, 0\n\t" \
      "s_add_u32 s47, s47, 2*64*3840\n\ts_cmp_ge_u32 s47, 640*3840\n\ts_cselect_b32 s48, 640*3840, 0\n\ts_sub_u32 s47, s47, s48\n\t" \
      ".endif\n\t" \
      "global_load_lds_dwordx4 v[68+(n_&3)], s[40:41] offset:1024*(n_&3)\n\t" \
      ".endif\n\t" \
      ".if n_ == 14\n\tv_mov_b32 v72, s50\n\tv_mov_b32 v73, s51\n\tv_mov_b32 v74, s52\n\ts_mov_b64 exec, 1\n\ts_waitcnt vmcnt(8)\n\t" \
      "global_store_dwordx2 v74, v[72:73], s[54:55] sc1\n\ts_mov_b64 exec, -1\n\t.endif\n\t" \
      ".if n_ == 26\n\tglobal_load_dwordx2 v[72:73], v76, s[54:55] sc1\n\t.endif\n\t" \
      ".endm\n\t" \
      ".macro p2body vt, k0, k1\n\t" \
      ".set k_, \\k0\n\t" \
      ".rept \\k1 - \\k0\n\t" \
      ".set xr_, 96 + 8 * k_\n\t" \
      ".set c_, 32 + 16 * (k_ & 1)\n\t" \
      ".set d_, 48 - 16 * (k_ & 1)\n\t" \
      ".set o_, ((k_ + 1) & 7) * 8192\n\t" \
      ".if k_ == 3\n\ts_waitcnt vmcnt(6)\n\ts_barrier\n\t.endif\n\t" \
      ".if k_ == 11\n\ts_waitcnt vmcnt(1)\n\ts_barrier\n\t.endif\n\t" \
      ".if \\vt == 2\n\tslot 2*k_\n\tslot 2*k_+1\n\t" \
      ".elseif k_ < 19\n\t" \
      ".if ((k_ + 1) & 7) == 0\n\tbump 64, 4, 0x10000\n\t.endif\n\t" \
      "s_waitcnt lgkmcnt(6)\n\t" \
      "mm \\vt, 0, xr_, c_\n\trw d_, 64, o_\n\t" \
      "mm \\vt, 16, xr_+4, c_\n\tslot 2*k_\n\ts_waitcnt lgkmcnt(6)\n\t" \
      "mm \\vt, 4, xr_, c_+4\n\trw d_+4, 65, o_\n\t" \
      "mm \\vt, 20, xr_+4, c_+4\n\ts_waitcnt lgkmcnt(6)\n\t" \
      "mm \\vt, 8, xr_, c_+8\n\trw d_+8, 66, o_\n\t" \
      "mm \\vt, 24, xr_+4, c_+8\n\tslot 2*k_+1\n\ts_waitcnt lgkmcnt(6)\n\t" \
      "mm \\vt, 12, xr_, c_+12\n\trw d_+12, 67, o_\n\t" \
      "mm \\vt, 28, xr_+4, c_+12\n\t" \
      ".else\n\t" \
      "s_waitcnt lgkmcnt(0)\n\t" \
      "mm \\vt, 0, xr_, c_\n\tmm \\vt, 16, xr_+4, c_\n\tmm \\vt, 4, xr_, c_+4\n\tmm \\vt, 20, xr_+4, c_+4\n\t" \
      "mm \\vt, 8, xr_, c_+8\n\tmm \\vt, 24, xr_+4, c_+8\n\tmm \\vt, 12, xr_, c_+12\n\tmm \\vt, 28, xr_+4, c_+12\n\t" \
      "s_nop 7\n\t" \
      ".endif\n\t" \
      ".set k_, k_ + 1\n\t" \
      ".endr\n\t" \
      ".endm\n\t"
#define G1_P2_PURGE ".purgem p2body\n\t.purgem slot\n\t" G1_ASM_PURGE
__device__ __forceinline__ void g1_pass2a(int vt, f32x32& acc, const f32x32& x0, const f32x32& x1, const f32x32& x2, const f32x32& x3,
                                          const f32x32& x4, f32x32& wf, u32x8& adl, u32x8& adh, const u32x16& pk, const char* sB,
                                          unsigned m0v, unsigned off2, unsigned long long tag, unsigned flag, char* ws) {
  unsigned keep;
  asm volatile(G1_P2_MACROS
      "rw 32, 64, 0\n\trw 36, 65, 0\n\trw 40, 66, 0\n\trw 44, 67, 0\n\t"
      "s_mov_b32 %[keep], m0\n\t"
      "s_cmp_eq_u32 %[vt], 1\n\ts_cbranch_scc1 1f\n\ts_cmp_eq_u32 %[vt], 2\n\ts_cbranch_scc1 3f\n\t"
      "p2body 0, 0, 15\n\ts_branch 2f\n\t"
      "3:\n\tp2body 2, 0, 15\n\ts_branch 2f\n\t"
      "1:\n\tp2body 1, 0, 15\n\t"
      "2:\n\ts_mov_b32 m0, %[keep]\n\t" AWAIT(72, 73, 76)
      G1_P2_PURGE
      : "=&{v[0:31]}"(acc), "=&{v[32:63]}"(wf), "+{v[64:71]}"(adl), "+{v[72:79]}"(adh), [keep] "=&s"(keep), "+{s47}"(off2)
      : "{v[80:95]}"(pk), "{v[96:127]}"(x0), "{v[128:159]}"(x1), "{v[160:191]}"(x2), "{v[192:223]}"(x3), "{v[224:255]}"(x4), [vt] "s"(vt),
        "{s[44:45]}"(sB), "{s46}"(m0v), "{s[50:51]}"(tag), "{s52}"(flag), "{s[54:55]}"(ws)
      : "memory", "scc", "vcc", "s40", "s41", "s48");
}
__device__ __forceinline__ void g1_pass2b(int vt, f32x32& acc, const f32x32& x3, const f32x32& x4, f32x32& wf, u32x8& wa, char* ws) {
  asm volatile(G1_P2_MACROS
      "s_cmp_eq_u32 %[vt], 2\n\ts_cbranch_scc1 3f\n\ts_cmp_eq_u32 %[vt], 1\n\ts_cbranch_scc1 1f\n\t"
      "p2body 0, 15, 20\n\tpack 0\n\ts_branch 2f\n\t"
      "1:\n\tp2body 1, 15, 20\n\tpack 1\n\t"
      "2:\n\tglobal_store_dwordx4 v68, v[80:83], s[54:55] sc1\n\tglobal_store_dwordx4 v69, v[84:87], s[54:55] sc1\n\t"
      "global_store_dwordx4 v70, v[88:91], s[54:55] sc1\n\tglobal_store_dwordx4 v71, v[92:95], s[54:55] sc1\n\t"
      "s_waitcnt vmcnt(4)\n\t"
      "3:\n\ts_setprio 3\n\t"
      G1_P2_PURGE
      : "+{v[0:31]}"(acc), "+{v[32:63]}"(wf), "+{v[64:71]}"(wa)
      : "{v[192:223]}"(x3), "{v[224:255]}"(x4), [vt] "s"(vt), "{s[54:55]}"(ws)
      : "memory", "scc", "v80", "v81", "v82", "v83", "v84", "v85", "v86", "v87", "v88", "v89", "v90", "v91", "v92", "v93", "v94", "v95");
}

__device__ __forceinline__ u32x4 g1_offs(unsigned br, unsigned cg, unsigned T, unsigned lane) {
  const unsigned l16 = (lane & 31) * 16, h = lane >> 5;
  u32x4 so;
  if (cg < 20) {
    so[0] = h * 32768 + l16 + (br * (unsigned)QK_BR + cg * (8 * 32768) + T * 512);
    so[1] = so[0] + 65536; so[2] = so[0] + 2 * 65536; so[3] = so[0] + 3 * 65536;
  } else {
    const unsigned G = (cg - 20) * 4 + ((lane >> 4) & 1);
    const unsigned b = h * 1280 + l16 + ((unsigned)V_WS + br * (unsigned)V_BR + (cg - 20) * 1024 + T * 5120);
    so[0] = ((G * 52) >> 8) * (256 * 1280 - 1280) + b;
    so[1] = (((G + 2) * 52) >> 8) * (256 * 1280 - 1280) + b + 512;
    so[2] = so[0] + 2560; so[3] = so[1] + 2560;
  }
  return so;
}

constexpr int TILE_B = 3584, K2_OFF = 1024, V_OFF = 1536;
constexpr int CHUNK_B = 8 * TILE_B;

__device__ __forceinline__ f32x16 smfmac(bf16x8 a, f32x8 p, f32x16 c) {
  return __builtin_amdgcn_smfmac_f32_32x32x32_bf16(a, __builtin_bit_cast(bf16x16, p), c, 0xDDDD, 0, 0);
}
__device__ __forceinline__ f32x8 lo8(const f32x16& s) { return __builtin_shufflevector(s, s, 0, 1, 2, 3, 4, 5, 6, 7); }
__device__ __forceinline__ f32x8 hi8(const f32x16& s) { return __builtin_shufflevector(s, s, 8, 9, 10, 11, 12, 13, 14, 15); }
#define MX3(a, b, c) __builtin_fmaxf(__builtin_fmaxf((a), (b)), (c))
__device__ __forceinline__ float max16(const f32x16& s, float m) {
#pragma unroll
  for (int r = 0; r < 16; r += 2) m = MX3(m, s[r], s[r + 1]);
  return m;
}
__device__ __forceinline__ float xhalf_max(float m) {
  auto rr = __builtin_amdgcn_permlane32_swap(__float_as_uint(m), __float_as_uint(m), false, false);
  return __builtin_fmaxf(__uint_as_float(rr[0]), __uint_as_float(rr[1]));
}
__device__ __forceinline__ s16x4 with_negmax(s16x4 q2, float m, int h) {
  u32x2 v = __builtin_bit_cast(u32x2, q2);
  const unsigned nm = pk_bf16(-m, 0.0f);
  v[0] = h ? nm : v[0];
  return __builtin_bit_cast(s16x4, v);
}
__device__ __forceinline__ float row_sum(const f32x16& o) {
  auto rr = __builtin_amdgcn_permlane32_swap(__float_as_uint(o[8]), __float_as_uint(o[8]), false, false);
  return __uint_as_float(rr[1]);
}
template <bool MARK>
__device__ __forceinline__ void store_o(const f32x16& o, float l, char* base, unsigned lo1, unsigned lo2) {
  const float rl = __builtin_amdgcn_rcpf(l);
  const unsigned a0 = pk_bf16(o[0] * rl, o[1] * rl), a1 = pk_bf16(o[2] * rl, o[3] * rl);
  const unsigned b0 = pk_bf16(o[4] * rl, o[5] * rl), b1 = pk_bf16(o[6] * rl, o[7] * rl);
  const auto r0 = __builtin_amdgcn_permlane32_swap(a0, b0, false, false);
  const auto r1 = __builtin_amdgcn_permlane32_swap(a1, b1, false, false);
  u32x4 v = {r0[0], r1[0], r0[1], r1[1]};
  u32x2 c = {pk_bf16(o[8] * rl, o[9] * rl), pk_bf16(o[10] * rl, o[11] * rl)};
  if (MARK) {
    v = __builtin_elementwise_min(v, u32x4{~1u, ~1u, ~1u, ~1u}); c = __builtin_elementwise_min(c, u32x2{~1u, ~1u});
  }
  st16(base, lo1, v);
  st8lo(base, lo2, c);
}

__device__ __forceinline__ bool flags_ok(unsigned a, unsigned b, unsigned long long tag) {
  return __all(a == (unsigned)tag && b == (unsigned)(tag >> 32));
}
__device__ __forceinline__ void poll_flags(const char* ws, unsigned off, unsigned long long tag) {
  asm volatile("global_load_dwordx2 v[124:125], v126, s[54:55] sc1\n\t" AWAIT(124, 125, 126)
               :: "{v126}"(off), "{s[54:55]}"(ws), "{s[50:51]}"(tag) : "memory", "scc", "vcc", "s48", "v124", "v125");
}
__device__ __forceinline__ void put_flag(char* ws, unsigned off, unsigned long long tag) {
  asm volatile("s_mov_b64 exec, 1\n\tglobal_store_dwordx2 %0, %1, %2 sc1\n\ts_mov_b64 exec, -1" :: "v"(off), "v"(tag), "s"(ws) : "memory");
}
__device__ __forceinline__ void tile_dma(const char* ks, const char* vs, unsigned d, u32x4 go) {
  unsigned keep;
  asm volatile("s_mov_b32 %0, m0\n\ts_mov_b32 m0, %7\n\ts_nop 0\n\t"
               "global_load_lds_dwordx4 %1, %5 sc1\n\t"
               "s_mov_b32 exec_hi, 0\n\t"
               "global_load_lds_dwordx4 %2, %5 offset:1024 sc1\n\t"
               "s_mov_b32 exec_lo, 0xfffff\n\ts_mov_b32 exec_hi, 0xfffff\n\t"
               "global_load_lds_dwordx4 %3, %6 offset:1536 sc1\n\t"
               "global_load_lds_dwordx4 %4, %6 offset:2560 sc1\n\t"
               "s_mov_b64 exec, -1\n\ts_mov_b32 m0, %0"
               : "=&s"(keep) : "v"(go[0]), "v"(go[1]), "v"(go[2]), "v"(go[3]), "s"(ks), "s"(vs), "s"(d) : "memory");
}
__device__ __forceinline__ void wo_dma(const char* src, unsigned d, const u32x8& wo) {
  unsigned keep;
  asm volatile("s_mov_b32 %0, m0\n\ts_mov_b32 m0, %7\n\ts_nop 0\n\t"
               "global_load_lds_dwordx4 %1, %6\n\tglobal_load_lds_dwordx4 %2, %6 offset:1024\n\t"
               "global_load_lds_dwordx4 %3, %6 offset:2048\n\tglobal_load_lds_dwordx4 %4, %6 offset:3072\n\t"
               "s_add_u32 m0, m0, 4096\n\ts_nop 0\n\tglobal_load_lds_dwordx4 %5, %6\n\ts_mov_b32 m0, %0"
               : "=&s"(keep) : "v"(wo[0]), "v"(wo[1]), "v"(wo[2]), "v"(wo[3]), "v"(wo[4]), "s"(src), "s"(d) : "memory", "scc");
}

#define AT_ASM_MACROS \
    ".macro xp d, s\n\tv_exp_f32 v[\\d], v[\\s]\n\tv_exp_f32 v[\\d+1], v[\\s+1]\n\tv_exp_f32 v[\\d+2], v[\\s+2]\n\tv_exp_f32 v[\\d+3], v[\\s+3]\n\t.endm\n\t" \
    ".macro smf o, v, p\n\tv_smfmac_f32_32x32x32_bf16 v[\\o:\\o+15], v[\\v:\\v+3], v[\\p:\\p+7], v76\n\t.endm\n\t" \
    ".macro qk1 s, k, q\n\tv_mfma_f32_32x32x16_bf16 v[\\s:\\s+15], v[\\k:\\k+3], v[\\q:\\q+3], 0\n\t.endm\n\t" \
    ".macro qk1c s, k, q, c\n\tv_mfma_f32_32x32x16_bf16 v[\\s:\\s+15], v[\\k:\\k+3], v[\\q:\\q+3], v[\\c:\\c+15]\n\t.endm\n\t" \
    ".macro qk2 s, k, q\n\tv_mfma_f32_32x32x8_bf16 v[\\s:\\s+15], v[\\k:\\k+1], v[\\q:\\q+1], v[\\s:\\s+15]\n\t.endm\n\t"
#define AT_ASM_PURGE ".purgem xp\n\t.purgem smf\n\t.purgem qk1\n\t.purgem qk1c\n\t.purgem qk2\n\t"

#define AT_A_TEXT AT_ASM_MACROS \
      ".macro pos j\n\t.set sl_, (\\j) >> 2\n\t.if sl_ >= 3\n\t.set sl_, sl_ - 3\n\t.endif\n\t" \
      ".set ar_, 128 + 3 * (sl_ >> 1)\n\t.set of_, (sl_ & 1) * 28672 + ((\\j) & 3) * 7168\n\t.endm\n\t" \
      ".macro dma part\n\t" \
      ".if di_ >= 0\n\t" \
      ".if \\part == 0\n\t" \
      ".if di_ == 1\n\ts_mov_b32 m0, s73\n\t.else\n\ts_add_u32 m0, s72, ((di_ + 2) %% 5) * 28672\n\t.endif\n\t" \
      "s_add_u32 s40, s54, s[56+2*di_]\n\ts_addc_u32 s41, s55, 0\n\t" \
      "global_load_lds_dwordx4 v137, s[40:41] sc1\n\t" \
      ".elseif \\part == 1\n\t" \
      "s_mov_b32 exec_hi, 0\n\tglobal_load_lds_dwordx4 v138, s[40:41] offset:1024 sc1\n\ts_mov_b32 exec_hi, -1\n\t" \
      "s_add_u32 s40, s54, s[57+2*di_]\n\ts_addc_u32 s41, s55, 0\n\t" \
      ".else\n\t" \
      "s_mov_b32 exec_lo, 0xfffff\n\ts_mov_b32 exec_hi, 0xfffff\n\t" \
      "global_load_lds_dwordx4 v139, s[40:41] offset:1536 sc1\n\tglobal_load_lds_dwordx4 v140, s[40:41] offset:2560 sc1\n\t" \
      "s_mov_b64 exec, -1\n\t" \
      ".endif\n\t.endif\n\t" \
        \
      ".set n_, -1\n\t" \
      ".if j_ >= 22 && j_ < 26 && 3 * (j_ - 22) + \\part < 10\n\t.set n_, 3 * (j_ - 22) + \\part\n\t.set bk_, 2 * (n_ / 5)\n\t.endif\n\t" \
      ".if j_ >= 28 && 3 * (j_ - 28) + \\part < 5\n\t.set n_, 3 * (j_ - 28) + \\part\n\t.set bk_, 1\n\t.endif\n\t" \
      ".if n_ >= 0\n\t.set pc_, n_ %% 5\n\t" \
      ".if bk_ == 2\n\ts_cmp_lt_i32 s46, 0\n\ts_cbranch_scc1 9f\n\t.endif\n\t" \
      ".if pc_ == 0\n\ts_mov_b32 m0, s[44+bk_]\n\ts_add_u32 s40, s42, bk_ * 8 * 32 * 1280\n\ts_addc_u32 s41, s43, 0\n\t.endif\n\t" \
      ".if pc_ == 4\n\ts_add_u32 m0, m0, 4096\n\ts_nop 0\n\t.endif\n\t" \
      "global_load_lds_dwordx4 v[248+pc_], s[40:41] offset:1024*(pc_&3)\n\t" \
      ".if bk_ == 2\n\t9:\n\t.endif\n\t.endif\n\t" \
      ".endm\n\t" \
      ".macro flg msk\n\tv_mov_b32 v142, s50\n\tv_mov_b32 v143, s51\n\tv_mov_b32 v108, s52\n\ts_mov_b64 exec, \\msk\n\ts_cbranch_execz 8f\n\ts_waitcnt vmcnt(0)\n\t" \
      "global_store_dwordx2 v108, v[142:143], s[54:55] sc1\n\t8:\n\ts_mov_b64 exec, -1\n\t.endm\n\t" \
      ".macro tabl d, r\n\tglobal_load_dwordx4 v[\\d:\\d+3], v79, s[40:41] offset:1024*(\\r)\n\t.endm\n\t" \
      ".macro old q\n\t.if (\\q) >= 0 && (\\q) < 10\n\tglobal_load_dwordx4 v[184+4*(\\q):187+4*(\\q)], v108, s[54:55] sc1\n\tv_add_u32 v108, 0x20000, v108\n\t.endif\n\t.endm\n\t" \
      "s_mov_b32 %[keep], m0\n\t" \
      ".set j_, %c[j0]\n\t" \
      ".rept %c[j1] - %c[j0]\n\t" \
      ".set a_, 80 + 14 * (j_ & 1)\n\t.set b_, 94 - 14 * (j_ & 1)\n\t" \
      ".if j_ == 4\n\t" AWAIT(142, 143, 141) ".set di_, 0\n\tdma 0\n\tdma 1\n\tdma 2\n\t.endif\n\t" \
      ".if j_ == 6\n\ts_waitcnt vmcnt(10)\n\ts_barrier\n\t.endif\n\t" \
      ".if j_ == 10 || j_ == 14 || j_ == 22\n\ts_waitcnt vmcnt(0)\n\ts_barrier\n\t.endif\n\t" \
      ".if j_ == 18\n\ts_waitcnt vmcnt(1)\n\ts_barrier\n\t.endif\n\t" \
      ".if j_ == 28\n\ts_barrier\n\t.endif\n\t" \
      AT_STAMP \
      ".if j_ < 6\n\t.if (j_ & 1) == 0\n\ts_setprio 2 - j_ / 2\n\t.endif\n\t" \
      ".elseif j_ == 10 || j_ == 11\n\ts_setprio 3\n\t.elseif j_ == 12 || j_ == 13\n\ts_setprio 0\n\t" \
      ".elseif j_ < 22\n\ts_setprio 3 - ((j_ + 2) & 3)\n\t.elseif j_ >= 28\n\ts_setprio 31 - j_\n\t" \
      ".elseif j_ != 23 && j_ != 25\n\ts_setprio 3 - (j_ - 21) / 2\n\t.endif\n\t" \
      ".if j_ == 14\n\tflg 1\n\t" \
      "v_mov_b32 v108, 0x3f803f80\n\tv_mov_b32 v109, v108\n\tv_mov_b32 v110, v108\n\tv_mov_b32 v111, v108\n\ts_mov_b64 exec, 15\n\t" \
      "ds_write_b128 v77, v[108:111]\n\ts_mov_b64 exec, -1\n\t.endif\n\t" \
      ".if j_ == 28\n\t" AWAIT(142, 143, 253) "v_add_u32 v108, %c[obr], v254\n\t.endif\n\t" \
      ".set di_, -1\n\t" \
      ".if j_ == 4 || j_ == 5 || j_ == 6\n\t.set di_, j_ - 3\n\t.endif\n\t.if j_ == 10\n\t.set di_, 4\n\t.endif\n\t" \
      ".if j_ >= 14 && j_ < 17\n\t.set di_, j_ - 9\n\t.endif\n\t" \
        \
      "pos j_+2\n\t" \
      "qk1 48, a_, 68\n\t.if j_ < 30\n\tds_read_b128 v[a_:a_+3], v[ar_] offset:of_\n\t.endif\n\told 4*(j_-28)\n\txp 112, 32\n\t" \
      "qk2 48, a_+4, 74\n\t.if j_ < 30\n\tds_read_b64 v[a_+4:a_+5], v[ar_+1] offset:of_\n\t.endif\n\told 4*(j_-28)+1\n\txp 116, 36\n\t" \
      "pos j_+1\n\t" \
      "smf 16, b_+10, 120\n\t.if j_ < 31\n\tds_read_b128 v[b_+10:b_+13], v[ar_+2] offset:of_+1024\n\t.endif\n\told 4*(j_-28)+2\n\txp 120, 40\n\t" \
      "smf 0, a_+6, 112\n\t.if j_ < 30\n\ts_waitcnt lgkmcnt(3)\n\t.elseif j_ == 30\n\ts_waitcnt lgkmcnt(1)\n\t.else\n\ts_waitcnt lgkmcnt(0)\n\t.endif\n\t" \
      "old 4*(j_-28)+3\n\t" \
      ".if j_ == 5\n\tglobal_load_dwordx4 v[236:239], v137, s[42:43] sc1\n\tglobal_load_dwordx2 v[240:241], v78, s[42:43] sc1\n\t.endif\n\t" \
      "xp 124, 44\n\t" \
        \
      ".if j_ < 31\n\tqk1 32, b_, 64\n\t.endif\n\tdma 0\n\t" \
      ".if j_ == 0\n\tflg s[74:75]\n\t.endif\n\t" \
      ".if j_ == 3\n\tglobal_load_dwordx2 v[142:143], v141, s[54:55] sc1\n\t.endif\n\t" \
      ".if j_ == 7\n\ts_mov_b64 s[40:41], s[44:45]\n\ttabl 248, 0\n\ttabl 252, 1\n\t.endif\n\t" \
      ".if j_ == 8\n\ttabl 220, 2\n\ttabl 224, 3\n\t.endif\n\t" \
      ".if j_ == 17\n\ts_mov_b32 m0, 143360\n\ts_nop 0\n\tglobal_load_lds_dword v141, s[74:75]\n\t.endif\n\t" \
      ".if j_ == 26\n\tglobal_load_dwordx2 v[142:143], v253, s[54:55] sc1\n\t.endif\n\t" \
      "xp 112, 48\n\t" \
      ".if j_ < 31\n\tqk2 32, b_+4, 72\n\t.endif\n\tdma 1\n\t" \
      ".if j_ == 7\n\ttabl 160, 2\n\ttabl 164, 3\n\ts_add_u32 s40, s40, 4096\n\ts_addc_u32 s41, s41, 0\n\t.endif\n\t" \
      ".if j_ == 8\n\ts_add_u32 s40, s40, 4096\n\ts_addc_u32 s41, s41, 0\n\ttabl 228, 0\n\ttabl 232, 1\n\t.endif\n\t" \
      "xp 116, 52\n\t" \
      "smf 0, a_+10, 120\n\tdma 2\n\t" \
      ".if j_ == 7\n\ttabl 168, 0\n\ttabl 172, 1\n\t.endif\n\t" \
      "xp 120, 56\n\t" \
      "pos j_+2\n\t" \
      "smf 16, a_+6, 112\n\t.if j_ < 30\n\tds_read_b128 v[a_+6:a_+9], v[ar_+2] offset:of_\n\t.endif\n\txp 124, 60\n\t" \
      ".set j_, j_ + 1\n\t" \
      ".endr\n\t" \
      ".if %c[j1] == 32\n\ts_nop 1\n\tsmf 16, 104, 120\n\ts_nop 15\n\t.endif\n\t" \
      "s_mov_b32 m0, %[keep]\n\t" \
      ".purgem pos\n\t.purgem dma\n\t.purgem flg\n\t.purgem tabl\n\t.purgem old\n\t" AT_ASM_PURGE
#define AT_STAMP
#define AT_STV
#define AT_STV_ARG
#define AT_STV_PASS
#define AT_A_STATE "+{v[0:31]}"(o), "+{v[32:47]}"(s0), "+{v[112:127]}"(pp), "+{v[80:111]}"(fr), "+{v[128:143]}"(ad), [keep] "=&s"(keep)
#define AT_A_CLOB "memory", "scc", "vcc", "s40", "s41", "s48", "v48", "v49", "v50", "v51", "v52", "v53", "v54", "v55", "v56", "v57", "v58", "v59", "v60", "v61", "v62", "v63"
__device__ __forceinline__ void at_a1(f32x32& o, f32x16& s0, f32x16& pp, f32x32& fr, const u32x16& q, u32x16& ad, const char* ws, u32x16 so,
                                      unsigned m0v, unsigned m0b, unsigned long long tag, unsigned flag, unsigned long long fmask,
                                      const char* qB, const void* tab, f32x16& mF, f32x16& mD, u32x8& qv, u32x8& tw AT_STV_ARG) {
  unsigned keep;
  asm volatile(AT_A_TEXT
      : AT_A_STATE, "=&{v[160:175]}"(mF), "=&{v[220:235]}"(mD), "=&{v[236:243]}"(qv), "=&{v[248:255]}"(tw) AT_STV
      : "{v[64:79]}"(q), "{s[54:55]}"(ws), "{s[56:71]}"(so), "{s72}"(m0v), "{s73}"(m0b), "{s[50:51]}"(tag), "{s52}"(flag), "{s[74:75]}"(fmask),
        "{s[42:43]}"(qB), "{s[44:45]}"(tab), [j0] "n"(0), [j1] "n"(12), [obr] "n"(O_BR)
      : AT_A_CLOB);
}
__device__ __forceinline__ void at_a2(f32x32& o, f32x16& s0, f32x16& pp, f32x32& fr, const u32x16& q, u32x16& ad, const char* ws, u32x16 so,
                                      unsigned m0v, unsigned long long tag, unsigned flag, const char* woa, const u32x8& wo, const char* src,
                                      unsigned d0, unsigned d1, unsigned d2, f32x32& of, f32x8& of2) {
  unsigned keep;
  asm volatile(AT_A_TEXT
      : AT_A_STATE, "=&{v[184:215]}"(of), "=&{v[216:223]}"(of2)
      : "{v[64:79]}"(q), "{s[54:55]}"(ws), "{s[56:71]}"(so), "{s72}"(m0v), "{s[50:51]}"(tag), "{s52}"(flag), "{s[74:75]}"(woa),
        "{v[248:255]}"(wo), "{s[42:43]}"(src), "{s44}"(d0), "{s45}"(d1), "{s46}"(d2), [j0] "n"(12), [j1] "n"(32), [obr] "n"(O_BR)
      : AT_A_CLOB);
}

__device__ __forceinline__ void at_b(f32x16& o, f32x16& mF, const f32x16& mD, const u32x8& q, unsigned av, const u32x16& qa, f32x32& k0, f32x16& k1, f32x32& k2,
                                     f32x16& k3) {
  asm volatile(AT_ASM_MACROS
      "ds_read_b128 v[192:195], v242\n\tds_read_b64 v[196:197], v243\n\tds_read_b128 v[206:209], v242 offset:3584\n\tds_read_b64 v[210:211], v243 offset:3584\n\t"
      "ds_read_b128 v[198:201], v245\n\tds_read_b128 v[212:215], v245 offset:3584\n\tds_read_b128 v[202:205], v245 offset:1024\n\t"
      "s_waitcnt lgkmcnt(5)\n\t"
      "qk1c 160, 192, 236, 160\n\tds_read_b128 v[192:195], v242 offset:7168\n\tqk2 160, 196, 240\n\tds_read_b64 v[196:197], v243 offset:7168\n\t"
      ".set i_, 144\n\t.rept 8\n\tv_mov_b64 v[i_:i_+1], 0\n\t.set i_, i_ + 2\n\t.endr\n\t"
      "s_waitcnt lgkmcnt(2)\n\t"
      ".set t_, 0\n\t"
      ".rept 9\n\t"
      ".set a_, 192 + 14 * (t_ & 1)\n\t.set b_, 206 - 14 * (t_ & 1)\n\t.set c_, 160 - 112 * (t_ & 1)\n\t.set d_, 48 + 112 * (t_ & 1)\n\t"
      ".if t_ == 0 || t_ == 5\n\ts_setprio 2 - t_ / 5\n\t.endif\n\t"
      ".if t_ < 7\n\tqk1 d_, b_, 236\n\t.elseif t_ == 7\n\tqk1c d_, b_, 236, 220\n\t.endif\n\t"
      ".if t_ < 6\n\tds_read_b128 v[b_:b_+3], v242 offset:(t_+3)*3584\n\t.endif\n\t"
      "xp 176, c_\n\t"
      ".if t_ < 8\n\tqk2 d_, b_+4, 240\n\t.endif\n\t"
      ".if t_ < 6\n\tds_read_b64 v[b_+4:b_+5], v243 offset:(t_+3)*3584\n\t.endif\n\t"
      "xp 180, c_+4\n\t"
      ".if t_ > 0\n\tsmf 144, b_+10, 184\n\t.endif\n\t"
      ".if t_ < 8\n\tds_read_b128 v[b_+10:b_+13], v245 offset:(t_+1)*3584+1024\n\t.endif\n\t"
      "xp 184, c_+8\n\t"
      "smf 144, a_+6, 176\n\t"
      ".if t_ < 6\n\ts_waitcnt lgkmcnt(3)\n\t.elseif t_ < 8\n\ts_waitcnt lgkmcnt(1)\n\t.else\n\ts_waitcnt lgkmcnt(0)\n\t.endif\n\t"
      ".if t_ < 7\n\tds_read_b128 v[a_+6:a_+9], v245 offset:(t_+2)*3584\n\t.endif\n\t"
      "xp 188, c_+12\n\t"
      ".set t_, t_ + 1\n\t"
      ".endr\n\t"
      "s_nop 1\n\tsmf 144, 202, 184\n\ts_nop 11\n\t"
      AT_ASM_PURGE
      : "=&{v[144:159]}"(o), "+{v[160:175]}"(mF), "+{v[0:31]}"(k0), "+{v[32:47]}"(k1), "+{v[80:111]}"(k2), "+{v[112:127]}"(k3)
      : "{v[220:235]}"(mD), "{v[236:243]}"(q), "{v245}"(av), "{v[64:79]}"(qa)
      : "memory", "v48", "v49", "v50", "v51", "v52", "v53", "v54", "v55", "v56", "v57", "v58", "v59", "v60", "v61", "v62", "v63",
        "v176", "v177", "v178", "v179", "v180", "v181", "v182", "v183", "v184", "v185", "v186", "v187", "v188", "v189", "v190", "v191",
        "v192", "v193", "v194", "v195", "v196", "v197", "v198", "v199", "v200", "v201", "v202", "v203", "v204", "v205", "v206", "v207",
        "v208", "v209", "v210", "v211", "v212", "v213", "v214", "v215", "v216", "v217", "v218", "v219");
}

#define PJ_MACROS \
      ".macro rdw k, sp\n\t.set d_, 80 + 20 * ((\\k) & 1)\n\t" \
      ".if (\\k) < \\sp\n\t.set a_, 120\n\t.set o_, (\\k) * 5120\n\t.else\n\t.set a_, 122\n\t.set o_, ((\\k) - \\sp) * 5120\n\t.endif\n\t.endm\n\t" \
      ".macro rd1 i\n\tds_read_b64_tr_b16 v[d_+4*\\i:d_+4*\\i+1], v[a_] offset:o_+32*\\i\n\tds_read_b64_tr_b16 v[d_+4*\\i+2:d_+4*\\i+3], v[a_+1] offset:o_+32*\\i\n\t.endm\n\t" \
      ".macro pm i, ac, of, last\n\t" \
      ".if k_ == 0\n\tv_mfma_f32_16x16x32_bf16 v[\\ac+4*\\i:\\ac+4*\\i+3], v[c_+4*\\i:c_+4*\\i+3], v[\\of:\\of+3], 0\n\t" \
      ".else\n\tv_mfma_f32_16x16x32_bf16 v[\\ac+4*\\i:\\ac+4*\\i+3], v[c_+4*\\i:c_+4*\\i+3], v[\\of+4*k_:\\of+3+4*k_], v[\\ac+4*\\i:\\ac+4*\\i+3]\n\t.endif\n\t" \
      ".if k_ < \\last\n\trd1 \\i\n\ts_waitcnt lgkmcnt(8)\n\t.elseif \\i < 4\n\ts_waitcnt lgkmcnt(6 - 2 * \\i)\n\t.endif\n\t.endm\n\t" \
      ".macro ldo\n\tv_mov_b32 v72, v254\n\t.set q_, 0\n\t.rept 10\n\t" \
      "global_load_dwordx4 v[32+4*q_:35+4*q_], v72, s[54:55] sc1\n\t.if q_ < 9\n\tv_add_u32 v72, 0x20000, v72\n\t.endif\n\t.set q_, q_ + 1\n\t.endr\n\t.endm\n\t"
#define PJ_PURGE ".purgem rdw\n\t.purgem rd1\n\t.purgem pm\n\t.purgem ldo\n\t"
#define PJ_CLOB "v72", "v80", "v81", "v82", "v83", "v84", "v85", "v86", "v87", "v88", "v89", "v90", "v91", "v92", "v93", "v94", "v95", \
        "v96", "v97", "v98", "v99", "v100", "v101", "v102", "v103", "v104", "v105", "v106", "v107", "v108", "v109", "v110", "v111", \
        "v112", "v113", "v114", "v115", "v116", "v117", "v118", "v119"
__device__ __forceinline__ void proj_1(f32x32& acc, const f32x32& of, const f32x8& of2, f32x32& oa, f32x8& oa2, u32x4 wa, const u32x8& wo,
                                       const char* src0, const char* src1, unsigned d0, unsigned d1, const char* ws) {
  unsigned keep;
  asm volatile(PJ_MACROS
      ".macro wp n\n\t.set n_, \\n\n\t.set pc_, n_ %% 5\n\t"
      ".if n_ < 10\n\t"
      ".if n_ >= 5\n\ts_cmp_lt_i32 s45, 0\n\ts_cbranch_scc1 9f\n\t.endif\n\t"
      ".if pc_ == 0\n\ts_mov_b32 m0, s[44+n_/5]\n\ts_nop 0\n\t.endif\n\t"
      ".if pc_ == 4\n\ts_add_u32 m0, m0, 4096\n\ts_nop 0\n\t.endif\n\t"
      "global_load_lds_dwordx4 v[248+pc_], s[42+4*(n_/5):43+4*(n_/5)] offset:1024*(pc_&3)\n\t"
      ".if n_ >= 5\n\t9:\n\t.endif\n\t.endif\n\t"
      ".if n_ == 12\n\tldo\n\t.endif\n\t"
      ".endm\n\t"
      "rdw 0, 6\n\trd1 0\n\trd1 1\n\trd1 2\n\trd1 3\n\trd1 4\n\t"
      "s_mov_b32 %[keep], m0\n\ts_barrier\n\ts_waitcnt lgkmcnt(8)\n\t"
      ".set k_, 0\n\t.rept 10\n\t.set c_, 80 + 20 * (k_ & 1)\n\trdw k_+1, 6\n\t"
      ".if k_ == 0 || k_ == 3 || k_ == 6\n\ts_setprio 2 - k_ / 3\n\t.endif\n\t"
      "pm 0, 0, 184, 9\n\twp 2*k_\n\tpm 1, 0, 184, 9\n\tpm 2, 0, 184, 9\n\twp 2*k_+1\n\tpm 3, 0, 184, 9\n\tpm 4, 0, 184, 9\n\t"
      ".set k_, k_ + 1\n\t.endr\n\t"
      "s_setprio 3\n\ts_mov_b32 m0, %[keep]\n\ts_waitcnt vmcnt(10)\n\ts_barrier\n\ts_nop 4\n\t"
      ".purgem wp\n\t" PJ_PURGE
      : "=&{v[0:31]}"(acc), "=&{v[32:63]}"(oa), "=&{v[64:71]}"(oa2), [keep] "=&s"(keep)
      : "{v[184:215]}"(of), "{v[216:223]}"(of2), "{v[120:123]}"(wa), "{v[248:255]}"(wo), "{s[42:43]}"(src0), "{s[46:47]}"(src1), "{s44}"(d0),
        "{s45}"(d1), "{s[54:55]}"(ws)
      : "memory", "scc", PJ_CLOB);
}
template <int K0>
__device__ __forceinline__ void proj_2(f32x32& acc, f32x32& oa, f32x8& oa2, u32x4 wa, const u32x8& wo, const char* ws) {
  asm volatile(PJ_MACROS
      "rdw %c[k0], 5\n\trd1 0\n\trd1 1\n\trd1 2\n\trd1 3\n\trd1 4\n\t"
      ".if %c[k0] == 0\n\t"
      "s_mov_b32 s48, 0x40000\n\t5:\n\t"
      "v_max3_u32 v72, v32, v33, v34\n\t.set q_, 35\n\t.rept 18\n\tv_max3_u32 v72, v72, v[q_], v[q_+1]\n\t.set q_, q_ + 2\n\t.endr\n\t"
      "v_max_u32 v72, v72, v71\n\tv_cmp_eq_u32 vcc, -1, v72\n\ts_sub_u32 s48, s48, 1\n\t"
      "s_cbranch_vccz 4f\n\ts_cbranch_scc1 4f\n\tldo\n\ts_waitcnt vmcnt(0)\n\ts_branch 5b\n\t4:\n\t"
      ".endif\n\t"
      "s_waitcnt lgkmcnt(8)\n\t"
      ".set k_, %c[k0]\n\t.rept 5\n\t.set c_, 80 + 20 * (k_ & 1)\n\trdw k_+1, 5\n\t"
      "pm 0, 160, 32, %c[k0]+4\n\tpm 1, 160, 32, %c[k0]+4\n\tpm 2, 160, 32, %c[k0]+4\n\tpm 3, 160, 32, %c[k0]+4\n\tpm 4, 160, 32, %c[k0]+4\n\t"
      ".set k_, k_ + 1\n\t.endr\n\t"
      ".if %c[k0] == 0\n\ts_waitcnt vmcnt(0)\n\ts_barrier\n\t.else\n\ts_nop 7\n\t.endif\n\t"
      PJ_PURGE
      : "+{v[160:191]}"(acc), "+{v[32:63]}"(oa), "+{v[64:71]}"(oa2)
      : "{v[120:123]}"(wa), "{v[248:255]}"(wo), "{s[54:55]}"(ws), [k0] "n"(K0)
      : "memory", "scc", "vcc", "s48", PJ_CLOB);
}
typedef __attribute__((address_space(3))) f32x4* lds_f4;
__device__ __forceinline__ void proj_send(const f32x32& acc, char* lds, int lw, int grp, int lane) {
  lds_ptr p = (lds_ptr)lds + lw * 5120 + lane * 16;
  if (grp) {
    *(lds_f4)(p) = f32x4{acc[0], acc[1], acc[2], acc[3]}; *(lds_f4)(p + 1024) = f32x4{acc[4], acc[5], acc[6], acc[7]};
    *(lds_f4)(p + 4096) = f32x4{acc[16], acc[17], acc[18], acc[19]};
  } else {
    *(lds_f4)(p + 2048) = f32x4{acc[8], acc[9], acc[10], acc[11]}; *(lds_f4)(p + 3072) = f32x4{acc[12], acc[13], acc[14], acc[15]};
  }
}
#define OUT_POL ""
__device__ __forceinline__ void proj_out(const f32x4& a, const f32x4& b, char* p, unsigned off) {
  const auto s0 = __builtin_amdgcn_permlane16_swap(pk_bf16(a[0], a[1]), pk_bf16(b[0], b[1]), false, false);
  const auto s1 = __builtin_amdgcn_permlane16_swap(pk_bf16(a[2], a[3]), pk_bf16(b[2], b[3]), false, false);
  asm volatile("global_store_dwordx4 %0, %1, %2 " OUT_POL "\n\ts_nop 1" :: "v"(off), "v"(u32x4{s0[0], s1[0], s0[1], s1[1]}), "s"(p) : "memory");
}
__device__ __forceinline__ void proj_finish(const f32x32& acc, char* lds, int lw, int grp, int lane, char* dst) {
  lds_ptr p = (lds_ptr)lds + lw * 5120 + lane * 16;
  const unsigned row = (lane & 15) * 2560, o16 = row + ((lane >> 4) & 1) * 32 + (lane >> 5) * 16;
  if (grp) {
    proj_out(f32x4{acc[8], acc[9], acc[10], acc[11]} + *(lds_f4)(p + 2048), f32x4{acc[12], acc[13], acc[14], acc[15]} + *(lds_f4)(p + 3072), dst + 64, o16);
  } else {
    const f32x4 r2 = f32x4{acc[16], acc[17], acc[18], acc[19]} + *(lds_f4)(p + 4096);
    proj_out(f32x4{acc[0], acc[1], acc[2], acc[3]} + *(lds_f4)(p), f32x4{acc[4], acc[5], acc[6], acc[7]} + *(lds_f4)(p + 1024), dst, o16);
    asm volatile("global_store_dwordx2 %0, %1, %2 offset:128 " OUT_POL :: "v"(row + (lane >> 4) * 8), "v"(u32x2{pk_bf16(r2[0], r2[1]), pk_bf16(r2[2], r2[3])}), "s"(dst) : "memory");
  }
}

struct LaneTab { unsigned v[10][64][4]; };
constexpr LaneTab make_tab() {
  LaneTab t = {};
  for (unsigned l = 0; l < 64; ++l) {
    for (unsigned i = 0; i < 5; ++i) {
      const unsigned v = 64 * i + l, rw = v / 10;
      t.v[i >> 2][l][i & 3] = (rw ^ ((rw >> 1) & 4)) * 1280 + (v - rw * 10) * 16 + 4096 - 1024 * (i & 3);
    }
    const unsigned g = l >> 4, q = (l & 15) >> 2, p = l & 3;
    t.v[1][l][1] = (l & 15) * 64 * 128;
    t.v[1][l][2] = g * 32768 + (l & 15) * 16;
    t.v[1][l][3] = (g * 1280 + (q ^ (4 * (g & 1))) * 160 + p * 8) | ((g * 1280 + ((4 + q) ^ (4 * (g & 1))) * 160 + p * 8) << 16);
    for (unsigned r = 0; r < 16; ++r) {
      const unsigned key = (r & 3) + 8 * (r >> 2) + 4 * (l >> 5);
      t.v[2 + (r >> 2)][l][r & 3] = key > (l & 31) ? 0u : 0xFF800000u;
      t.v[6 + (r >> 2)][l][r & 3] = key <= (l & 31) ? 0u : 0xFF800000u;
    }
  }
  return t;
}
__device__ const LaneTab lane_tab = make_tab();
constexpr unsigned CODE_B = 34816;

__global__ void __launch_bounds__(512, 2)
k_fused(const char* __restrict__ xa_, const char* __restrict__ wa_, const char* __restrict__ woa_, const char* __restrict__ xb_,
        const char* __restrict__ wb_, const char* __restrict__ wob_, char* __restrict__ ws_, char* __restrict__ out_) {
  __shared__ __attribute__((aligned(16))) char lds[163840];
  const unsigned bid = blockIdx.x, xcd = bid & 7, slot = bid >> 3, br = xcd >> 2;
  u32x4 ka; u32x2 kb; u32x8 kc;
  {
    const void* kp = (const void*)__builtin_amdgcn_kernarg_segment_ptr();
    asm volatile("s_load_dwordx4 %0, %3, %4\n\ts_load_dwordx8 %2, %3, 0x20\n\ts_load_dwordx2 %1, %3, 0x10"
                 : "=&s"(ka), "=&s"(kb), "=&s"(kc) : "s"(kp), "s"(br * 24));
  }
  STAMP_DECL STAMP(0);
  unsigned lane = threadIdx.x & 63;
  const unsigned w = __builtin_amdgcn_readfirstlane(threadIdx.x >> 6);
  const unsigned grp = w >> 2, lw = w & 3;
  const unsigned mt = (xcd & 3) * 4 + (slot & 3), u = slot >> 2;
  const unsigned win = (5 * ((mt >> 1) & 3)) >> 1;
  const unsigned kv1 = (u + 3) >> 3;
  const unsigned j1 = win + u - 5;
#define ROT ((5 * (u & 3)) / 2 + 5 * (slot & 1) + (xcd & 3))
  auto mod10 = [](unsigned x) { return __builtin_elementwise_min(x, x - 10); };
  const unsigned rot = mod10(ROT);
  const unsigned m0v = grp * 16384 + lw * 4096;
  const unsigned str1 = grp ? 64 * 3840 : 128;
  u32x8 adl, adh;
  unsigned o1[4];
  {
    const unsigned row = lane >> 3, pos = lane & 7;
    const unsigned tw = row * 3840 + ((pos ^ (row & 2)) * 16);
    const unsigned t1 = grp ? tw : row * 1280 + ((pos ^ (row >> 1)) * 16);
    const unsigned gap = kv1 ? 1280 : 128, m1 = grp ? 0 : 64;
    o1[0] = t1 + 1024;
    o1[1] = (t1 ^ m1) + (grp ? gap : 8 * 1280);
    o1[2] = (t1 ^ m1 ^ 64) + (grp ? 8 * 3840 - 1024 : 16 * 1280 - 1024);
    o1[3] = (t1 ^ 64) + (grp ? 8 * 3840 - 2048 + gap : 24 * 1280 - 2048);
    adl[4] = tw + 1024; adl[5] = tw + 1280; adl[6] = (tw ^ 64) + (8 * 3840 - 1024); adl[7] = (tw ^ 64) + (8 * 3840 - 2048 + 1280);
    adh[0] = o1[0]; adh[1] = o1[1]; adh[2] = o1[2]; adh[3] = o1[3];
  }
  const unsigned offA = grp ? 16 * lw * 3840 + (kv1 ? 1280 + j1 * 128 : u * 256) : (mt * 128 + 32 * lw) * 1280;
  asm volatile("s_waitcnt lgkmcnt(0)" : "+s"(ka), "+s"(kb), "+s"(kc));
  auto ptr = [](unsigned lo, unsigned hi) { return (char*)(((unsigned long long)hi << 32) | lo); };
  const char* sA = ptr(grp ? ka[2] : ka[0], grp ? ka[3] : ka[1]) + (offA - 1024ll);
  char* const ws = ptr(kc[4], kc[5]);
  glds16x4(sA + rot * str1, o1, m0v);
  glds16x4(sA + mod10(rot + 1) * str1, o1, m0v + 32768);
  u32x2 ep; unsigned tch[4];
  asm volatile("s_load_dwordx2 %0, %1, %2" : "=&s"(ep) : "s"(ws), "s"((unsigned)CTL_WS));
  {
    const unsigned i64 = (slot * 8 + w) * 64;
    unsigned long long pc;
    asm volatile("s_getpc_b64 %0\n\ts_load_dword %1, %0, %5\n\ts_load_dword %2, %0, %6\n\ts_load_dword %3, %0, %7\n\ts_load_dword %4, %8, %9"
                 : "=&s"(pc), "=&s"(tch[0]), "=&s"(tch[1]), "=&s"(tch[2]), "=&s"(tch[3])
                 : "s"(i64), "s"(i64 + 16384), "s"(__builtin_elementwise_min(i64 + 32768, CODE_B)), "s"(&lane_tab),
                   "s"(__builtin_elementwise_min(i64, 10176u)));
  }
  const unsigned j2 = mod10(win + 3 + (u < 7 ? u : 6));
  const unsigned Tg = mt * 4 + lw;
  const unsigned vt1 = grp & kv1, vt2 = u < 7 ? grp : 2;
  const unsigned cg1 = kv1 ? 10 + 10 * grp + j1 : 2 * u + grp, cg2 = 10 + 10 * grp + j2;
  const unsigned head = xcd * 4 + (slot & 3), qb = u;
  const unsigned odd = head & 1, o2 = 5 * (head >> 1) + 2, o1h = odd ? o2 + 1 : o2 - 2;
  const unsigned qt = qb * 8 + 2 * lw + grp;
  u32x4 soA; u32x16 so;
  {
    const unsigned kb0 = 80 * 32768 + w * 512, vb0 = (unsigned)V_WS - V_OFF + xcd * (256 * 1280) + w * 5120;
    unsigned ko[10], vo_[10];
#pragma unroll
    for (int i = 0; i < 8; ++i) { const unsigned c = (((head >> 3) + (i >> 1)) & 3) + 4 * (i & 1); ko[i] = kb0 + c * 4096; vo_[i] = vb0 + c * 40960; }
    const unsigned c8 = qb ? qb - 1 : 0;
    ko[8] = kb0 + (unsigned)QK_BR + c8 * 4096; vo_[8] = vb0 + (unsigned)V_BR + c8 * 40960;
    ko[9] = kb0 + (unsigned)QK_BR + qb * 4096; vo_[9] = vb0 + (unsigned)V_BR + qb * 40960;
    soA = u32x4{ko[0], vo_[0], ko[1], vo_[1]};
    so = u32x16{ko[2], vo_[2], ko[8], vo_[8], ko[9], vo_[9], ko[3], vo_[3], ko[4], vo_[4], ko[5], vo_[5], ko[6], vo_[6], ko[7], vo_[7]};
    asm volatile("" : "+s"(soA), "+{s[56:71]}"(so));
  }
  unsigned flag1 = FQ_OFF + ((br * 30 + cg1) * 64 + Tg) * 128, flag2 = FQ_OFF + ((br * 30 + cg2) * 64 + Tg) * 128;
  unsigned flagO = FO_OFF + (head * 64 + qt) * 128;
  PINS(flag1); PINS(flag2); PINS(flagO);
  const char* sB = ptr(ka[2], ka[3]) + (16 * lw * 3840 + 256 + j2 * 128);
  u32x16 pk;
  { const u32x4 s4 = g1_offs(br, cg1, Tg, lane); pk[0] = s4[0]; pk[1] = s4[1]; pk[2] = s4[2]; pk[3] = s4[3]; }
  {
    const unsigned r16 = lane & 15, g = lane >> 4, q = r16 >> 2, p = lane & 3;
    adh[5] = (lw * 32 + r16) * 128 + ((g ^ (r16 >> 1)) * 16); adh[6] = adh[5] ^ 64;
    adl[0] = 2048 * g + 1024 * grp + 128 * q + 16 * ((p >> 1) ^ (4 * (g & 1) + (q & 2))) + 8 * (p & 1);
    adl[1] = adl[0] ^ 32; adl[2] = adl[0] ^ 64; adl[3] = adl[0] ^ 96;
    const unsigned jj = (20 * head + 19 * (lane & 1)) >> 6, b1 = (lane >> 1) & 1;
    adh[4] = FQ_OFF + ((lane & 8) ? jj * 64 + (qt ^ b1) : (10 + 10 * b1 + jj) * 64 + (head >> 3) * 8 + w + (lane & 4) * 8) * 128;
    adh[7] = 0;
  }
  asm volatile("s_waitcnt vmcnt(4) lgkmcnt(0)\n\ts_barrier" : "+{v[64:71]}"(adl), "+{v[72:79]}"(adh), "+{v[80:95]}"(pk) :: "memory");
  PINS(ep); PINS(tch[0]); PINS(tch[1]); PINS(tch[2]); PINS(tch[3]);
  const unsigned long long tag = (((unsigned long long)ep[1] << 32) | ep[0]) + 1;
  STAMP(1);
  f32x32 acc, x3, x4, wf;
  {
    f32x32 x0, x1, x2;
    STV_PUT(adh[7]);
    g1_pass1(vt1, kv1, x0, x1, x2, x3, x4, adl, adh, pk, sA, sB, mod10(rot + 2) * str1, str1, mod10(rot + grp) * (64 * 3840), m0v);
    STV_GET(adh[7]); STAMP(3); STV_PUT(adh[7]);
    g1_pass2a(vt2, acc, x0, x1, x2, x3, x4, wf, adl, adh, pk, sB, m0v, mod10(rot + grp + 6) * (64 * 3840), tag, flag1, ws);
    STV_GET(adh[7]);
    STAMP(6);
  }
  lane = lane_id();
  const unsigned r32 = lane & 31, h = lane >> 5;
  const unsigned g1o = h * 32768 + r32 * 16 + o1h * 32768;
  const unsigned g2o = r32 * 16 + (o2 * 32768 + odd * 8);
  const unsigned gvo = h * 1280 + (__builtin_elementwise_min(r32 + 4 * odd, r32 + 4 * odd - 20) + (head & 3) * 20) * 16;
  const u32x4 go = {g1o, g2o - (1024 + odd * 8), gvo, gvo + V_OFF};
  const unsigned mytile = w * TILE_B;
  bf16x8 q1a, q1b; s16x4 q2a, q2b;
  asm volatile("global_load_dwordx4 %0, %4, %6 sc1\n\tglobal_load_dwordx2 %1, %5, %6 sc1\n\t"
               "global_load_dwordx4 %2, %4, %7 sc1\n\tglobal_load_dwordx2 %3, %5, %7 sc1"
               : "=&v"(q1a), "=&v"(q2a), "=&v"(q1b), "=&v"(q2b)
               : "v"(g1o), "v"(g2o), "s"(ws + qt * 512), "s"(ws + (qt ^ 1) * 512) : "memory");
  tile_dma(ws + soA[0], ws + soA[1], mytile, go);
  tile_dma(ws + soA[2], ws + soA[3], mytile + CHUNK_B, go);
  typedef __attribute__((address_space(3))) u32x4* lds_u4;
  lds_ptr crow = (lds_ptr)lds + (mytile + V_OFF + 320) + (lane >> 4) * 512 + __builtin_elementwise_min(lane & 15, 11u) * 16;
  const unsigned cv = (lane & 15) == 0 ? 0x3F803F80u : 0u;
  const u32x4 cv4 = {cv, cv, cv, cv};
  *(lds_u4)(crow) = cv4; *(lds_u4)(crow + CHUNK_B) = cv4; *(lds_u4)(crow + 2 * CHUNK_B) = cv4;
  u32x16 at;
  {
    const unsigned ab = grp * TILE_B;
    const unsigned a0 = lane * 16 + ab;
    const unsigned a1 = h ? (V_OFF + 320 + ab) : r32 * 16 + (K2_OFF + odd * 8 + ab);
    const unsigned a2 = h * 512 + (r32 < 28 ? __builtin_elementwise_min(r32, 21u) : 28) * 16 + (V_OFF + ab);
    const unsigned jj = (20 * head + 19 * (lane & 1)) >> 6, c = (lane >> 2) & 7;
    const unsigned cB = __builtin_elementwise_max((c & 1) + qb, 1u) + (30 * 8 - 1);
    const unsigned kvf = (lane & 2) * 320 + (h ? cB : c) * 8 + (10 * 64 + w);
    const unsigned pol = (jj * 64 + (lane >= 48 ? 30 * 64 + qt : kvf)) * 128 + FQ_OFF;
    at = u32x16{a0, a1, a2, a0 + 2 * CHUNK_B, a1 + 2 * CHUNK_B, a2 + 2 * CHUNK_B, a0 + 4 * CHUNK_B, a1 + 4 * CHUNK_B, a2 + 4 * CHUNK_B,
                go[0], go[1], go[2], go[3], pol, c, jj};
    asm volatile("" : "+{v[128:143]}"(at));
  }
  const u32x4 so2 = g1_offs(br, cg2, Tg, lane);
  adl[4] = so2[0]; adl[5] = so2[1]; adl[6] = so2[2]; adl[7] = so2[3];
  STAMP(7);
  g1_pass2b(vt2, acc, x3, x4, wf, adl, ws);
  STAMP(8);
  if (u == 7) {
    WAITV(0);
    if (bid == 255 && grp == 0) {
      const unsigned e = w * 64 + lane, eu = e & 7, em = (e >> 3) & 15;
      poll_flags(ws, FQ_OFF + (((e >> 7) * 30 + (eu < 5 ? 2 * eu : 5 + eu + ((5 * ((em >> 1) & 3)) >> 1))) * 64 + 4 * em) * 128, tag);
    }
  }
  asm volatile("s_waitcnt lgkmcnt(0)\n\ts_barrier" : "+v"(q1a), "+v"(q2a), "+v"(q1b), "+v"(q2b) :: "memory");
  STAMP(9);
  if (bid == 255 && w == 0) put_flag(ws, CTL_WS, tag);
  const f32x16 zero16 = {};
  {
    lds_cptr tp = (lds_cptr)lds - grp * TILE_B;
    const bf16x8 k1 = lds_b128(tp + at[0]); const s16x4 k2 = lds_b64(tp + at[1]);
    if (h) { q2a = s16x4{0, 0, 0, 0}; q2b = s16x4{0, 0, 0, 0}; }
    *(lds_u4)(crow + 3 * CHUNK_B) = cv4; *(lds_u4)(crow + 4 * CHUNK_B) = cv4;
    if (qb == 0) {
      lds_ptr z = (lds_ptr)lds + 3 * CHUNK_B + mytile + lane * 16;
      *(lds_f4)(z) = f32x4{}; *(lds_f4)(z + 1024) = f32x4{}; *(lds_f4)(z + 2048) = f32x4{}; *(lds_f4)(z + 2560) = f32x4{};
    }
    f32x16 sa = MFMA32(k1, q1a, zero16), sb = MFMA32(k1, q1b, zero16);
    sa = MFMA32K8(k2, q2a, sa); sb = MFMA32K8(k2, q2b, sb);
    q2a = with_negmax(q2a, xhalf_max(max16(sa, sa[0])), h);
    q2b = with_negmax(q2b, xhalf_max(max16(sb, sb[0])), h);
  }
  auto exact = [&](int b, bf16x8 q1, s16x4 q2, int t0, int t1) {
    __builtin_amdgcn_fence(__ATOMIC_ACQUIRE, "agent");
    const int ln = lane_id(), r32 = ln & 31, h = ln >> 5;
    if (h) q2 = s16x4{0, 0, 0, 0};
    typedef __attribute__((address_space(1))) const char* gptr;
    gptr kg1 = (gptr)ws + QK_WS + b * QK_BR + (size_t)(80 + o1h + h) * 32768 + r32 * 16;
    gptr kg2 = (gptr)ws + QK_WS + b * QK_BR + (size_t)(80 + o2) * 32768 + r32 * 16 + odd * 8;
    gptr vg = (gptr)ws + V_WS + b * V_BR + (head >> 2) * (256 * 1280) + h * 1280 +
                     ((head & 3) * 20 + (odd ? (r32 < 16 ? r32 + 4 : r32 - 16) : r32)) * 16;
    const u32x4 vc = r32 == 20 ? u32x4{0x3F803F80u, 0x3F803F80u, 0x3F803F80u, 0x3F803F80u} : u32x4{0u, 0u, 0u, 0u};
    float m = -INFINITY;
    f32x16 r = zero16;
#pragma unroll 1
    for (int ps = 0; ps < 2; ++ps) {
#pragma unroll 1
      for (int t = t0; t <= t1; ++t) {
        f32x16 s = MFMA32(*(__attribute__((address_space(1))) const bf16x8*)(kg1 + t * 512), q1, zero16);
        s = MFMA32K8(*(__attribute__((address_space(1))) const s16x4*)(kg2 + t * 512), q2, s);
        if (b) {
          const int d = (t1 - t) * 32 + r32 - 4 * h;
#pragma unroll
          for (int e = 0; e < 16; ++e) s[e] = (unsigned)(d - ((e & 3) + 8 * (e >> 2))) < 256u ? s[e] : -INFINITY;
        }
        if (ps == 0) { m = max16(s, m); continue; }
#pragma unroll
        for (int e = 0; e < 16; ++e) s[e] = __builtin_amdgcn_exp2f(s[e] - m);
        u32x4 va = vc, vb = vc;
        if (r32 < 20) { va = *(__attribute__((address_space(1))) const u32x4*)(vg + t * 5120); vb = *(__attribute__((address_space(1))) const u32x4*)(vg + t * 5120 + 2560); }
        r = smfmac(__builtin_bit_cast(bf16x8, va), lo8(s), r);
        r = smfmac(__builtin_bit_cast(bf16x8, vb), hi8(s), r);
      }
      m = xhalf_max(m);
    }
    return r;
  };
  auto unsafe = [&](const f32x16& r, float l) {
    float amax = 0.f;
#pragma unroll
    for (int e = 0; e < 12; ++e) amax = __builtin_fmaxf(amax, __builtin_fabsf(r[e]));
    return __any(!(l <= 1.2676506e30f) || !(l >= 5.4210109e-20f) || !(amax <= 3.0e38f));
  };
  const char* const woa = ptr(kb[0], kb[1]);
  const char* const wob = ptr(kc[2], kc[3]);
  char* const oA = ws + (O_WS + qt * 512);
  f32x32 oab, of; f32x8 of2;
  u32x8 wo;
  u32x16 qq;
  unsigned wl;
  {
    f32x16 s0, pp;
    f32x32 fr;
    {
      const u32x4 qa4 = __builtin_bit_cast(u32x4, q1a), qb4 = __builtin_bit_cast(u32x4, q1b);
      const u32x2 qa2 = __builtin_bit_cast(u32x2, q2a), qb2 = __builtin_bit_cast(u32x2, q2b);
      qq = u32x16{qa4[0], qa4[1], qa4[2], qa4[3], qb4[0], qb4[1], qb4[2], qb4[3], qa2[0], qa2[1], qb2[0], qb2[1], 0xDDDDu,
                  lane * 512 + (3 * CHUNK_B + V_OFF + 320 + mytile), at[10] + (1024 + odd * 8), lane * 16};
    }
    asm volatile(
        "ds_read_b128 v[80:83], v128\n\tds_read_b64 v[84:85], v129\n\tds_read_b128 v[94:97], v128 offset:7168\n\tds_read_b64 v[98:99], v129 offset:7168\n\t"
        "ds_read_b128 v[86:89], v130\n\tds_read_b128 v[100:103], v130 offset:7168\n\tds_read_b128 v[90:93], v130 offset:1024\n\t"
        ".set i_, 0\n\t.rept 10\n\tv_mov_b64 v[i_:i_+1], 0\n\t.set i_, i_ + 2\n\t.endr\n\t"
        "s_waitcnt lgkmcnt(5)\n\t"
        "v_mfma_f32_32x32x16_bf16 v[32:47], v[80:83], v[64:67], 0\n\t"
        "v_mfma_f32_32x32x8_bf16 v[32:47], v[84:85], v[72:73], v[32:47]\n\t"
        ".rept 6\n\tv_mov_b64 v[i_:i_+1], 0\n\t.set i_, i_ + 2\n\t.endr\n\t"
        "v_mov_b64 v[104:105], 0\n\tv_mov_b64 v[106:107], 0\n\t"
        "v_mov_b64 v[120:121], 0\n\tv_mov_b64 v[122:123], 0\n\tv_mov_b64 v[124:125], 0\n\tv_mov_b64 v[126:127], 0\n\t"
        "s_waitcnt lgkmcnt(0)"
        : "=&{v[0:31]}"(oab), "=&{v[112:127]}"(pp), "=&{v[32:47]}"(s0), "=&{v[80:111]}"(fr), "+{v[64:79]}"(qq), "+{v[128:143]}"(at) :: "memory");
    STAMP(10);
    f32x16 maskF, maskD; u32x8 qv;
    at_a1(oab, s0, pp, fr, qq, at, ws, so, mytile, mytile + (qb ? 3 : 4) * CHUNK_B, tag, flag2, (0x7fu >> u) & 1u, ws + (QK_BR + qt * 512),
          &lane_tab, maskF, maskD, qv, wo AT_STV_PASS);
    STAMP(11);
    {
      const unsigned tb = 3 * CHUNK_B + 2 * lw * TILE_B;
      const unsigned a0 = at[0] + tb, a1 = at[1] + tb;
      lds_cptr tp = (lds_cptr)lds + 7 * TILE_B;
      const bf16x8 ka1 = lds_b128(tp + a0), kb1 = lds_b128(tp + a0 + TILE_B);
      const s16x4 ka2 = lds_b64(tp + a1), kb2 = lds_b64(tp + a1 + TILE_B);
      const bf16x8 q1B = __builtin_bit_cast(bf16x8, u32x4{qv[0], qv[1], qv[2], qv[3]});
      const s16x4 q2B = __builtin_bit_cast(s16x4, h ? u32x2{0, 0} : u32x2{qv[4], qv[5]});
      f32x16 s1 = MFMA32(ka1, q1B, zero16), s2 = MFMA32(kb1, q1B, maskD);
      s1 = MFMA32K8(ka2, q2B, s1); s2 = MFMA32K8(kb2, q2B, s2);
      st16(oA, at[9], u32x4{~0u, ~0u, ~0u, ~0u}); st8lo(oA, qq[14], u32x2{~0u, ~0u});
      wl = wo[7];
      wo[5] += FO_OFF + (grp * 16 * 64 + head * 2 + (lw >> 1)) * 128;
      wo[6] += (unsigned)O_WS + grp * 40 * 32768 + (head * 64 + lw * 16) * 16;
      const float m1 = max16(s1, s1[0]);
      const u32x2 qm = __builtin_bit_cast(u32x2, with_negmax(q2B, xhalf_max(max16(s2, qt ? m1 : -INFINITY)), h));
      qv[4] = qm[0]; qv[5] = qm[1]; qv[6] = a0; qv[7] = a1;
      f32x16 ob;
      at_b(ob, maskF, maskD, qv, at[2] + tb, qq, oab, s0, fr, pp);
      float l = row_sum(ob);
      if (__builtin_expect(unsafe(ob, l), 0)) {
        ob = exact(1, __builtin_bit_cast(bf16x8, u32x4{qv[0], qv[1], qv[2], qv[3]}), __builtin_bit_cast(s16x4, u32x2{qv[4], qv[5]}),
                   qb ? qt - 8 : 0, qt);
        l = row_sum(ob);
      }
      store_o<false>(ob, l, oA + O_BR, at[9], qq[14]);
    }
    STAMP(13);
    at[13] = (__builtin_elementwise_min(qq[15], 24u * 16) + (slot * 8 + w) * (25 * 16)) * 8;
    at_a2(oab, s0, pp, fr, qq, at, ws, so, mytile, tag, flagO, woa, wo, wob + (qb * 160 + w * (32 * 1280) - 4096ll), w * 5120, (w + 8) * 5120,
          grp ? 0x80000000u : w * 5120 + 143360, of, of2);
  }
  STAMP(12);
  {
    lds_ptr xb = (lds_ptr)lds + 3 * CHUNK_B + qq[15];
#pragma unroll
    for (int i = 0; i < 3; ++i) *(lds_f4)(xb + w * 3072 + i * 1024) = f32x4{oab[16 + 4 * i], oab[17 + 4 * i], oab[18 + 4 * i], oab[19 + 4 * i]};
    asm volatile("s_setprio 3\n\ts_waitcnt vmcnt(0) lgkmcnt(0)\n\ts_barrier" : "+{v[184:215]}"(of), "+{v[216:223]}"(of2) :: "memory");
    f32x16 o = {};
#pragma unroll
    for (int i = 0; i < 3; ++i) {
      const f32x4 r = *(lds_f4)(xb + (w ^ 4) * 3072 + i * 1024);
#pragma unroll
      for (int e = 0; e < 4; ++e) o[4 * i + e] = oab[4 * i + e] + r[e];
    }
    float l = row_sum(o);
    if (__builtin_expect(unsafe(o, l), 0)) {
      o = exact(0, __builtin_bit_cast(bf16x8, u32x4{qq[0], qq[1], qq[2], qq[3]}), __builtin_bit_cast(s16x4, u32x2{qq[8], qq[9]}), 0, 63);
      l = row_sum(o);
    }
    store_o<true>(o, l, oA, at[9], qq[14]);
  }
  STAMP(14);
  {
    f32x32 p1, p2, oa; f32x8 oa2;
    const unsigned wl0 = wl & 0xffff, wl1 = wl >> 16;
    const char* ws0 = woa + (qb * 160 + (w < 5 ? w : w + 5) * (32 * 1280) - 4096ll);
    const char* ws1 = woa + (qb * 160 + (13 + (w & 1)) * (32 * 1280) - 4096ll);
    {
      const unsigned b1 = grp * 51200, b2 = grp ? 143360 : 30720;
      proj_1(p1, of, of2, oa, oa2, u32x4{wl0 + b1, wl1 + b1, wl0 + b2, wl1 + b2}, wo, ws0, ws1, 86016 + w * 5120,
             w < 2 ? 86016 + (8 + w) * 5120 : 0x80000000u, ws);
    }
    STAMP(16);
    lane = lane_id();
    proj_send(p1, lds + 143360, lw, grp, lane);
    wo_dma(ws0 + 5 * 32 * 1280, w * 5120, wo);
    if (w < 2) { wo_dma(ws1 + 5 * 32 * 1280, (8 + w) * 5120, wo); WAITV(10); } else WAITV(5);
    char* dst = ptr(kc[6], kc[7]) + ((head * 64 + lw * 16) * 2560 + qb * 160);
    STAMP(17);
    const unsigned b1 = 86016 + grp * 25600, b2 = grp * 25600;
    const u32x4 wab = {wl0 + b1, wl1 + b1, wl0 + b2, wl1 + b2};
    asm volatile("" : "={v[160:191]}"(p2));
    proj_2<0>(p2, oa, oa2, wab, wo, ws);
    STAMP(18);
    proj_finish(p1, lds + 143360, lw, grp, lane, dst + 1280);
    proj_2<5>(p2, oa, oa2, wab, wo, ws);
    STAMP(20);
    proj_send(p2, lds + 86016, lw, grp, lane);
    asm volatile("s_waitcnt lgkmcnt(0)\n\ts_barrier" ::: "memory");
    proj_finish(p2, lds + 86016, lw, grp, lane, dst);
  }
  STAMP_OUT(ws, 0);
}

extern "C" void kernel_launch(void* const* d_in, const int* in_sizes, int n_in, void* d_out, int out_size, void* d_ws,
                              size_t ws_size, hipStream_t stream) {
  k_fused<<<256, 512, 0, stream>>>((const char*)d_in[0], (const char*)d_in[1], (const char*)d_in[2], (const char*)d_in[3],
                                   (const char*)d_in[4], (const char*)d_in[5], (char*)d_ws, (char*)d_out);
}
```

```cpp
#include <hip/hip_runtime.h>
#include <hip/hip_bf16.h>
#include <stdint.h>
#include <type_traits>

typedef __attribute__((ext_vector_type(8))) __bf16 bf16x8;
typedef __attribute__((ext_vector_type(16))) __bf16 bf16x16;
typedef __attribute__((ext_vector_type(4))) short s16x4;
typedef __attribute__((ext_vector_type(8))) short s16x8;
typedef __attribute__((ext_vector_type(2))) unsigned u32x2;
typedef __attribute__((ext_vector_type(4))) unsigned u32x4;
typedef __attribute__((ext_vector_type(4))) float f32x4;
typedef __attribute__((ext_vector_type(8))) float f32x8;
typedef __attribute__((ext_vector_type(16))) float f32x16;
typedef __attribute__((ext_vector_type(32))) float f32x32;
typedef __attribute__((address_space(3))) const char* lds_cptr;
typedef __attribute__((address_space(3))) char* lds_ptr;

constexpr size_t QK_WS = 0, QK_BR = (size_t)160 * 32768;
constexpr size_t V_WS = 2 * QK_BR, V_BR = (size_t)8 * 256 * 1280;
constexpr size_t O_WS = V_WS + 2 * V_BR, O_BR = (size_t)80 * 32768;
constexpr size_t CTL_WS = O_WS + 2 * O_BR;
constexpr unsigned FQ_OFF = CTL_WS + 4096, FO_OFF = FQ_OFF + 2 * 30 * 64 * 128;
constexpr float QSCALE = 0.22360679774997896f * 1.4426950408889634f;

#define STAMP_DECL
#define STV_PUT(x)
#define STV_GET(x)
#define STAMP(i) do {} while (0)
#define STAMP_OUT(ws_, kid) do {} while (0)
__device__ __forceinline__ int lane_id() {
  int l; asm volatile("v_mbcnt_lo_u32_b32 %0, -1, 0\n\tv_mbcnt_hi_u32_b32 %0, -1, %0" : "=v"(l)); return l;
}
#define SBAR() __builtin_amdgcn_sched_barrier(0)
#define PIN(x) asm volatile("" : "+v"(x))
#define PINS(x) asm volatile("" : "+s"(x))
#define WAITV(N) asm volatile("s_waitcnt vmcnt(" #N ")" ::: "memory")
#define BARRIER() asm volatile("s_barrier" ::: "memory")
#define WAITV_BAR(N) asm volatile("s_waitcnt vmcnt(" #N ") lgkmcnt(0)\n\ts_barrier" ::: "memory")

__device__ __forceinline__ void glds16(const void* sbase, unsigned voff, unsigned lds) {
  unsigned keep;
  asm volatile("s_mov_b32 %0, m0\n\ts_mov_b32 m0, %3\n\ts_nop 0\n\tglobal_load_lds_dwordx4 %1, %2\n\ts_mov_b32 m0, %0"
               : "=&s"(keep) : "v"(voff), "s"(sbase), "s"(lds) : "memory");
}
__device__ __forceinline__ void glds16x4(const void* sbase, const unsigned (&voff)[4], unsigned lds) {
  unsigned keep;
  asm volatile("s_mov_b32 %0, m0\n\ts_mov_b32 m0, %6\n\ts_nop 0\n\tglobal_load_lds_dwordx4 %1, %5\n\t"
               "global_load_lds_dwordx4 %2, %5 offset:1024\n\tglobal_load_lds_dwordx4 %3, %5 offset:2048\n\t"
               "global_load_lds_dwordx4 %4, %5 offset:3072\n\ts_mov_b32 m0, %0"
               : "=&s"(keep) : "v"(voff[0]), "v"(voff[1]), "v"(voff[2]), "v"(voff[3]), "s"(sbase), "s"(lds) : "memory");
}
__device__ __forceinline__ unsigned pk_bf16(float lo, float hi) {
  typedef __attribute__((ext_vector_type(2))) float f32x2_;
  typedef __attribute__((ext_vector_type(2))) __bf16 bf16x2_;
  const f32x2_ v = {lo, hi};
  return __builtin_bit_cast(unsigned, __builtin_convertvector(v, bf16x2_));
}
__device__ __forceinline__ void st16(char* sbase, unsigned voff, u32x4 v) {
  asm volatile("global_store_dwordx4 %0, %1, %2 sc1\n\ts_nop 1" :: "v"(voff), "v"(v), "s"(sbase) : "memory");
}
__device__ __forceinline__ void st8lo(char* sbase, unsigned voff, u32x2 v) {
  asm volatile("s_mov_b32 exec_hi, 0\n\tglobal_store_dwordx2 %0, %1, %2 sc1\n\ts_mov_b32 exec_hi, -1" :: "v"(voff), "v"(v), "s"(sbase) : "memory");
}
__device__ __forceinline__ bf16x8 lds_b128(lds_cptr p) { return *(const __attribute__((address_space(3))) bf16x8*)p; }
__device__ __forceinline__ s16x4 lds_b64(lds_cptr p) { return *(const __attribute__((address_space(3))) s16x4*)p; }
__device__ __forceinline__ s16x4 lds_tr(lds_cptr p) {
  return __builtin_amdgcn_ds_read_tr16_b64_v4i16((__attribute__((address_space(3))) s16x4*)p);
}
__device__ __forceinline__ bf16x8 cat8(s16x4 a, s16x4 b) {
  s16x8 v = {a[0], a[1], a[2], a[3], b[0], b[1], b[2], b[3]};
  return __builtin_bit_cast(bf16x8, v);
}
#define MFMA32(a, b, c) __builtin_amdgcn_mfma_f32_32x32x16_bf16(a, b, c, 0, 0, 0)
#define MFMA32K8(a, b, c) __builtin_amdgcn_mfma_f32_32x32x8bf16_1k(a, b, c, 0, 0, 0)
#define MFMA16(a, b, c) __builtin_amdgcn_mfma_f32_16x16x32_bf16(a, b, c, 0, 0, 0)

typedef __attribute__((ext_vector_type(8))) unsigned u32x8;
typedef __attribute__((ext_vector_type(16))) unsigned u32x16;
#define AWAIT(d, d1, off) \
    "s_mov_b32 s48, 0x200000\n\t5:\n\ts_waitcnt vmcnt(0)\n\tv_cmp_ne_u64 vcc, s[50:51], v[" #d ":" #d1 "]\n\ts_sub_u32 s48, s48, 1\n\t" \
    "s_cbranch_vccz 4f\n\ts_cbranch_scc1 4f\n\ts_sleep 2\n\tglobal_load_dwordx2 v[" #d ":" #d1 "], v" #off ", s[54:55] sc1\n\ts_branch 5b\n\t4:\n\t"
#define G1_STAMP(i)
#define NPOLL 22
#define NFLAG 6
#define NAW 28
#define KBAR 11
__device__ __forceinline__ void g1_all(unsigned vt1, unsigned kv1, unsigned vt2, u32x16& ad, u32x16& pk, u32x16& at, u32x4& cv4,
                                       const char* sA, unsigned off1, unsigned str1, const char* sB, unsigned off2, unsigned m0v,
                                       unsigned long long tag, unsigned flag, char* ws, unsigned tile, u32x4 so, unsigned qoff) {
  unsigned keep;
  f32x32 d0, d1, d2, d3, d4; f32x16 d5, d6; f32x8 d7; f32x4 d8;
  asm volatile(
      ".macro pack vt\n\t.set i_, 0\n\t.rept 4\n\t"
      ".if \\vt\n\t.set e_, 8 * i_\n\t.set f_, 8 * i_ + 4\n\t.else\n\t.set e_, 4 * i_\n\t.set f_, 4 * i_ + 16\n\t.endif\n\t"
      "v_cvt_pk_bf16_f32 v[48+4*i_], v[e_], v[e_+1]\n\tv_cvt_pk_bf16_f32 v[49+4*i_], v[e_+2], v[e_+3]\n\t"
      "v_cvt_pk_bf16_f32 v[50+4*i_], v[f_], v[f_+1]\n\tv_cvt_pk_bf16_f32 v[51+4*i_], v[f_+2], v[f_+3]\n\t"
      ".set i_, i_ + 1\n\t.endr\n\t.set i_, 0\n\t.rept 4\n\t"
      "v_permlane16_swap_b32 v[48+4*i_], v[50+4*i_]\n\tv_permlane16_swap_b32 v[49+4*i_], v[51+4*i_]\n\t"
      ".set i_, i_ + 1\n\t.endr\n\t.endm\n\t"
      ".macro mm vt, c, x, w\n\t"
      ".if (\\x) < 104\n\t"
      ".if \\vt\n\tv_mfma_f32_16x16x32_bf16 v[\\c:\\c+3], v[\\x:\\x+3], v[\\w:\\w+3], 0\n\t"
      ".else\n\tv_mfma_f32_16x16x32_bf16 v[\\c:\\c+3], v[\\w:\\w+3], v[\\x:\\x+3], 0\n\t.endif\n\t"
      ".elseif \\vt\n\tv_mfma_f32_16x16x32_bf16 v[\\c:\\c+3], v[\\x:\\x+3], v[\\w:\\w+3], v[\\c:\\c+3]\n\t"
      ".else\n\tv_mfma_f32_16x16x32_bf16 v[\\c:\\c+3], v[\\w:\\w+3], v[\\x:\\x+3], v[\\c:\\c+3]\n\t.endif\n\t"
      ".endm\n\t"
      ".macro rx d, a, off\n\tds_read_b128 v[\\d:\\d+3], v\\a offset:\\off\n\tds_read_b128 v[\\d+4:\\d+7], v\\a offset:\\off+2048\n\t.endm\n\t"
      ".macro rw d, a, off\n\tds_read_b64_tr_b16 v[\\d:\\d+1], v\\a offset:\\off\n\tds_read_b64_tr_b16 v[\\d+2:\\d+3], v\\a offset:\\off+512\n\t.endm\n\t"
      ".macro bump lo, n, val\n\t.set bi_, \\lo\n\t.rept \\n\n\tv_add_u32 v[bi_], \\val, v[bi_]\n\t.set bi_, bi_+1\n\t.endr\n\t.endm\n\t"
      ".macro src2\n\ts_add_u32 s40, s44, s47\n\ts_addc_u32 s41, s45, 0\n\t"
      "s_add_u32 s47, s47, 2*64*3840\n\ts_cmp_ge_u32 s47, 640*3840\n\ts_cselect_b32 s48, 640*3840, 0\n\ts_sub_u32 s47, s47, s48\n\t.endm\n\t"
      ".macro p1body vt\n\t"
      ".set st_, 0\n\t"
      ".rept 10\n\t"
      ".set xr_, 96 + 16 * st_\n\t"
      ".set o_, ((st_ %% 5) & 1) * 32768\n\t"
      ".set n_, (((st_ + 1) %% 5) & 1) * 32768 + 16384\n\t.if st_ == 9\n\t.set n_, 0\n\t.endif\n\t"
      "s_waitcnt lgkmcnt(6)\n\t"
      "mm \\vt, 0, xr_, 32\n\trw 48, 76, o_+16384+8192\n\t"
      "mm \\vt, 16, xr_+4, 32\n\trx xr_+8, 74, o_\n\ts_waitcnt lgkmcnt(8)\n\t"
      "mm \\vt, 4, xr_, 36\n\trw 52, 77, o_+16384+8192\n\t"
      "mm \\vt, 20, xr_+4, 36\n\trw 56, 78, o_+16384+8192\n\ts_waitcnt lgkmcnt(10)\n\t"
      "mm \\vt, 8, xr_, 40\n\trw 60, 79, o_+16384+8192\n\t"
      "mm \\vt, 24, xr_+4, 40\n\ts_waitcnt lgkmcnt(10)\n\t"
      "mm \\vt, 12, xr_, 44\n\t"
      ".if st_ < 7\n\t"
      "s_add_u32 s40, s42, s36\n\ts_addc_u32 s41, s43, 0\n\ts_add_u32 s36, s36, s37\n\ts_cmp_eq_u32 s36, s38\n\ts_cselect_b32 s36, 0, s36\n\t"
      ".else\n\tsrc2\n\t.endif\n\t"
      "mm \\vt, 28, xr_+4, 44\n\t"
      "s_add_u32 m0, s46, ((st_ + 3) %% 5) * 32768\n\t"
      ".if (st_ %% 5) == 1 || (st_ %% 5) == 3\n\tbump 76, 4, 0x10000\n\tbump 73, 2, 0x10000\n\t.endif\n\t"
      ".if (st_ %% 5) == 4\n\tbump 76, 4, 0xfffe0000\n\t.if st_ < 9\n\tbump 73, 2, 0xfffe0000\n\t.endif\n\t.endif\n\t"
      "s_waitcnt lgkmcnt(6)\n\t"
      "mm \\vt, 0, xr_+8, 48\n\t"
      "mm \\vt, 16, xr_+12, 48\n\t"
      "s_waitcnt vmcnt(4) lgkmcnt(0)\n\ts_barrier\n\t"
      ".set vo_, 64 + 4 * ((st_ + 3) / 10)\n\t"
      "mm \\vt, 4, xr_+8, 52\n\trw 32, 76, n_\n\t"
      "global_load_lds_dwordx4 v[vo_], s[40:41]\n\t"
      "mm \\vt, 20, xr_+12, 52\n\t"
      ".if st_ < 9\n\trx xr_+16, 73, n_-16384\n\t.endif\n\t"
      "global_load_lds_dwordx4 v[vo_+1], s[40:41] offset:1024\n\t"
      "mm \\vt, 8, xr_+8, 56\n\trw 36, 77, n_\n\t"
      "global_load_lds_dwordx4 v[vo_+2], s[40:41] offset:2048\n\t"
      "mm \\vt, 24, xr_+12, 56\n\trw 40, 78, n_\n\t"
      "global_load_lds_dwordx4 v[vo_+3], s[40:41] offset:3072\n\t"
      "mm \\vt, 12, xr_+8, 60\n\trw 44, 79, n_\n\t"
      "mm \\vt, 28, xr_+12, 60\n\t"
      ".set st_, st_ + 1\n\t"
      ".endr\n\t"
      "v_mov_b64 v[64:65], v[80:81]\n\tv_add_u32 v66, 131072 - 128512 * \\vt, v80\n\tv_add_u32 v67, 131072 - 128512 * \\vt, v81\n\t"
      ".endm\n\t"
      ".macro tdma i\n\t"
      "s_add_u32 m0, s72, \\i * 28672\n\ts_add_u32 s40, s54, s[76+2*\\i]\n\ts_addc_u32 s41, s55, 0\n\t"
      "global_load_lds_dwordx4 v137, s[40:41] sc1\n\ts_mov_b32 exec_hi, 0\n\tglobal_load_lds_dwordx4 v138, s[40:41] offset:1024 sc1\n\t"
      "s_add_u32 s40, s54, s[77+2*\\i]\n\ts_addc_u32 s41, s55, 0\n\ts_mov_b32 exec_lo, 0xfffff\n\ts_mov_b32 exec_hi, 0xfffff\n\t"
      "global_load_lds_dwordx4 v139, s[40:41] offset:1536 sc1\n\tglobal_load_lds_dwordx4 v140, s[40:41] offset:2560 sc1\n\ts_mov_b64 exec, -1\n\t"
      ".endm\n\t"
      ".macro slot n\n\t.set n_, \\n\n\t"
      ".if n_ >= 2 && n_ < 10\n\t"
      ".if ((n_ - 2) & 3) == 0\n\ts_add_u32 m0, s46, (3 + (n_ - 2) / 4) * 32768\n\tsrc2\n\t.endif\n\t"
      "global_load_lds_dwordx4 v[68+((n_-2)&3)], s[40:41] offset:1024*((n_-2)&3)\n\t"
      ".endif\n\t"
      ".if n_ == %c[nflag]\n\tv_mov_b32 v96, s50\n\tv_mov_b32 v97, s51\n\tv_mov_b32 v98, s52\n\ts_mov_b64 exec, 1\n\ts_waitcnt vmcnt(%c[nflag] - 1)\n\t"
      "global_store_dwordx2 v98, v[96:97], s[54:55] sc1\n\ts_mov_b64 exec, -1\n\t.endif\n\t"
      ".if n_ == 12\n\tv_mov_b64 v[128:129], v[84:85]\n\tv_mov_b32 v130, v86\n\tv_add_u32 v131, 57344, v84\n\tv_add_u32 v132, 57344, v85\n\t.endif\n\t"
      ".if n_ == 13\n\tv_add_u32 v133, 57344, v86\n\tv_add_u32 v134, 114688, v84\n\tv_add_u32 v135, 114688, v85\n\tv_add_u32 v136, 114688, v86\n\t.endif\n\t"
      ".if n_ == 14\n\tv_mov_b32 v137, v87\n\tv_mov_b64 v[138:139], v[88:89]\n\tv_add_u32 v140, 1536, v89\n\tv_mov_b32 v141, v90\n\t.endif\n\t"
      ".if n_ == 15\n\tv_mov_b32 v104, v92\n\tv_mov_b32 v105, v92\n\tv_mov_b32 v106, v92\n\tv_mov_b32 v107, v92\n\t.endif\n\t"
      ".if n_ == %c[naw] + 3\n\tds_write_b128 v91, v[104:107]\n\tds_write_b128 v91, v[104:107] offset:28672\n\t.endif\n\t"
      ".if n_ == %c[npoll]\n\tglobal_load_dwordx2 v[100:101], v72, s[54:55] sc1\n\t.endif\n\t"
      ".if n_ == %c[naw]\n\t" AWAIT(100, 101, 72) G1_STAMP(6) "tdma 0\n\t.endif\n\t"
      ".if n_ == %c[naw] + 1\n\ts_add_u32 s40, s54, s80\n\ts_addc_u32 s41, s55, 0\n\ts_xor_b32 s48, s80, 512\n\t"
      "global_load_dwordx4 v[64:67], v137, s[40:41] sc1\n\tglobal_load_dwordx2 v[72:73], v93, s[40:41] sc1\n\t"
      "s_add_u32 s40, s54, s48\n\ts_addc_u32 s41, s55, 0\n\t"
      "global_load_dwordx4 v[68:71], v137, s[40:41] sc1\n\tglobal_load_dwordx2 v[74:75], v93, s[40:41] sc1\n\t.endif\n\t"
      ".if n_ == %c[naw] + 2\n\ttdma 1\n\t.endif\n\t"
      ".endm\n\t"
      ".macro st1 i\n\t.if k_ == 0\n\tglobal_store_dwordx4 v[64+\\i], v[48+4*\\i:51+4*\\i], s[54:55] sc1\n\t.endif\n\t.endm\n\t"
      ".macro p2body vt\n\t"
      ".set k_, 0\n\t"
      ".rept 20\n\t"
      ".set xr_, 96 + 8 * k_\n\t"
      ".set c_, 32 + 16 * (k_ & 1)\n\t"
      ".set d_, 48 - 16 * (k_ & 1)\n\t"
      ".set o_, ((k_ + 1) & 7) * 8192\n\t"
      ".if k_ == 3\n\ts_waitcnt vmcnt(8)\n\ts_barrier\n\t.endif\n\t"
      ".if k_ == %c[kbar]\n\ts_waitcnt vmcnt(0)\n\ts_barrier\n\t.endif\n\t"
      ".if \\vt == 2\n\tst1 0\n\tst1 1\n\tst1 2\n\tst1 3\n\tslot 2*k_\n\tslot 2*k_+1\n\t"
      ".elseif k_ < 19\n\t"
      ".if ((k_ + 1) & 7) == 0\n\tbump 76, 4, 0x10000\n\t.endif\n\t"
      "s_waitcnt lgkmcnt(6)\n\t"
      "mm \\vt, 0, xr_, c_\n\tst1 0\n\trw d_, 76, o_\n\t"
      "mm \\vt, 16, xr_+4, c_\n\tslot 2*k_\n\ts_waitcnt lgkmcnt(6)\n\t"
      "mm \\vt, 4, xr_, c_+4\n\tst1 1\n\trw d_+4, 77, o_\n\t"
      "mm \\vt, 20, xr_+4, c_+4\n\ts_waitcnt lgkmcnt(6)\n\t"
      "mm \\vt, 8, xr_, c_+8\n\tst1 2\n\trw d_+8, 78, o_\n\t"
      "mm \\vt, 24, xr_+4, c_+8\n\tslot 2*k_+1\n\ts_waitcnt lgkmcnt(6)\n\t"
      "mm \\vt, 12, xr_, c_+12\n\tst1 3\n\trw d_+12, 79, o_\n\t"
      "mm \\vt, 28, xr_+4, c_+12\n\t"
      ".else\n\t"
      "s_waitcnt lgkmcnt(0)\n\t"
      "mm \\vt, 0, xr_, c_\n\tmm \\vt, 16, xr_+4, c_\n\tmm \\vt, 4, xr_, c_+4\n\tmm \\vt, 20, xr_+4, c_+4\n\t"
      "mm \\vt, 8, xr_, c_+8\n\tmm \\vt, 24, xr_+4, c_+8\n\tmm \\vt, 12, xr_, c_+12\n\tmm \\vt, 28, xr_+4, c_+12\n\t"
      "s_setprio 3\n\tv_add_u32 v80, 131072 - 128512 * \\vt, v82\n\tv_add_u32 v81, 131072 - 128512 * \\vt, v83\n\ts_nop 4\n\tpack \\vt\n\t"
      ".endif\n\t"
      ".set k_, k_ + 1\n\t"
      ".endr\n\t"
      ".endm\n\t"
      "rw 32, 76, 16384\n\trx 96, 73, 0\n\trw 36, 77, 16384\n\trw 40, 78, 16384\n\trw 44, 79, 16384\n\t"
      "s_mov_b32 %[keep], m0\n\t"
      "s_cmp_lg_u32 %[vt1], 0\n\ts_cbranch_scc1 1f\n\t"
      "p1body 0\n\t"
      "s_mov_b32 s40, %[sc]\n\ts_cmp_lg_u32 %[kv1], 0\n\ts_nop 2\n\ts_cbranch_scc1 3f\n\t"
      ".set i_, 0\n\t.rept 16\n\tv_pk_mul_f32 v[i_:i_+1], v[i_:i_+1], s[40:41] op_sel_hi:[1,0]\n\t.set i_, i_ + 2\n\t.endr\n\t"
      "3:\n\tpack 0\n\ts_branch 2f\n\t"
      "1:\n\tp1body 1\n\ts_nop 4\n\tpack 1\n\t"
      "2:\n\t" G1_STAMP(3)
      "s_cmp_eq_u32 %[vt2], 1\n\ts_cbranch_scc1 1f\n\ts_cmp_eq_u32 %[vt2], 2\n\ts_cbranch_scc0 6f\n\t"
      "p2body 2\n\ts_waitcnt vmcnt(0)\n\ts_setprio 3\n\ts_branch 3f\n\t"
      "6:\n\tp2body 0\n\ts_branch 2f\n\t"
      "1:\n\tp2body 1\n\t"
      "2:\n\tglobal_store_dwordx4 v82, v[48:51], s[54:55] sc1\n\tglobal_store_dwordx4 v83, v[52:55], s[54:55] sc1\n\t"
      "global_store_dwordx4 v80, v[56:59], s[54:55] sc1\n\tglobal_store_dwordx4 v81, v[60:63], s[54:55] sc1\n\t"
      "3:\n\ts_mov_b32 m0, %[keep]\n\tv_mov_b32 v76, 0xdddd\n\tv_mov_b32 v77, v94\n\tv_mov_b32 v78, v93\n\t"
      "v_mbcnt_lo_u32_b32 v79, -1, 0\n\tv_mbcnt_hi_u32_b32 v79, -1, v79\n\tv_lshlrev_b32 v79, 4, v79\n\t"
      "s_waitcnt vmcnt(8) lgkmcnt(0)\n\t"
      ".purgem pack\n\t.purgem mm\n\t.purgem rx\n\t.purgem rw\n\t.purgem bump\n\t.purgem src2\n\t.purgem p1body\n\t.purgem tdma\n\t"
      ".purgem slot\n\t.purgem st1\n\t.purgem p2body\n\t"
      : "+{v[64:79]}"(ad), "+{v[80:95]}"(pk), "=&{v[128:143]}"(at), "=&{v[104:107]}"(cv4), [keep] "=&s"(keep), "+{s36}"(off1), "+{s47}"(off2),
        "=&{v[0:31]}"(d0), "=&{v[32:63]}"(d1), "=&{v[160:191]}"(d2), "=&{v[192:223]}"(d3), "=&{v[224:255]}"(d4), "=&{v[112:127]}"(d5),
        "=&{v[144:159]}"(d6), "=&{v[96:103]}"(d7), "=&{v[108:111]}"(d8)
      : [vt1] "s"(vt1), [kv1] "s"(kv1), [vt2] "s"(vt2), "{s[42:43]}"(sA), "{s37}"(str1), "{s38}"(10 * str1), "{s[44:45]}"(sB), "{s46}"(m0v),
        "{s[50:51]}"(tag), "{s52}"(flag), "{s[54:55]}"(ws), "{s72}"(tile), "{s[76:79]}"(so), "{s80}"(qoff),
        [sc] "n"(__builtin_bit_cast(unsigned, QSCALE)), [npoll] "n"(NPOLL), [nflag] "n"(NFLAG), [naw] "n"(NAW), [kbar] "n"(KBAR)
      : "memory", "scc", "vcc", "s40", "s41", "s48");
}

__device__ __forceinline__ u32x2 g1_offs(unsigned br, unsigned cg, unsigned T, unsigned lane) {
  const unsigned l16 = (lane & 31) * 16, h = lane >> 5;
  const unsigned a = h * 32768 + l16 + (br * (unsigned)QK_BR + cg * (8 * 32768) + T * 512);
  const unsigned G = (cg - 20) * 4 + ((lane >> 4) & 1);
  const unsigned b = h * 1280 + l16 + ((unsigned)V_WS + br * (unsigned)V_BR + (cg - 20) * 1024 + T * 5120);
  return cg < 20 ? u32x2{a, a + 65536}
                 : u32x2{((G * 52) >> 8) * (256 * 1280 - 1280) + b, (((G + 2) * 52) >> 8) * (256 * 1280 - 1280) + b + 512};
}

constexpr int TILE_B = 3584, K2_OFF = 1024, V_OFF = 1536;
constexpr int CHUNK_B = 8 * TILE_B;

__device__ __forceinline__ f32x16 smfmac(bf16x8 a, f32x8 p, f32x16 c) {
  return __builtin_amdgcn_smfmac_f32_32x32x32_bf16(a, __builtin_bit_cast(bf16x16, p), c, 0xDDDD, 0, 0);
}
__device__ __forceinline__ f32x8 lo8(const f32x16& s) { return __builtin_shufflevector(s, s, 0, 1, 2, 3, 4, 5, 6, 7); }
__device__ __forceinline__ f32x8 hi8(const f32x16& s) { return __builtin_shufflevector(s, s, 8, 9, 10, 11, 12, 13, 14, 15); }
#define MX3(a, b, c) __builtin_fmaxf(__builtin_fmaxf((a), (b)), (c))
__device__ __forceinline__ float max16(const f32x16& s, float m) {
#pragma unroll
  for (int r = 0; r < 16; r += 2) m = MX3(m, s[r], s[r + 1]);
  return m;
}
__device__ __forceinline__ float xhalf_max(float m) {
  auto rr = __builtin_amdgcn_permlane32_swap(__float_as_uint(m), __float_as_uint(m), false, false);
  return __builtin_fmaxf(__uint_as_float(rr[0]), __uint_as_float(rr[1]));
}
__device__ __forceinline__ float row_sum(const f32x16& o) {
  auto rr = __builtin_amdgcn_permlane32_swap(__float_as_uint(o[8]), __float_as_uint(o[8]), false, false);
  return __uint_as_float(rr[1]);
}
template <bool MARK>
__device__ __forceinline__ void store_o(const f32x16& o, float l, char* base, unsigned lo1, unsigned lo2) {
  const float rl = __builtin_amdgcn_rcpf(l);
  const unsigned a0 = pk_bf16(o[0] * rl, o[1] * rl), a1 = pk_bf16(o[2] * rl, o[3] * rl);
  const unsigned b0 = pk_bf16(o[4] * rl, o[5] * rl), b1 = pk_bf16(o[6] * rl, o[7] * rl);
  const auto r0 = __builtin_amdgcn_permlane32_swap(a0, b0, false, false);
  const auto r1 = __builtin_amdgcn_permlane32_swap(a1, b1, false, false);
  u32x4 v = {r0[0], r1[0], r0[1], r1[1]};
  u32x2 c = {pk_bf16(o[8] * rl, o[9] * rl), pk_bf16(o[10] * rl, o[11] * rl)};
  if (MARK) {
    v = __builtin_elementwise_min(v, u32x4{~1u, ~1u, ~1u, ~1u}); c = __builtin_elementwise_min(c, u32x2{~1u, ~1u});
  }
  st16(base, lo1, v);
  st8lo(base, lo2, c);
}

__device__ __forceinline__ void poll_flags(const char* ws, unsigned off, unsigned long long tag) {
  asm volatile("global_load_dwordx2 v[124:125], v126, s[54:55] sc1\n\t" AWAIT(124, 125, 126)
               :: "{v126}"(off), "{s[54:55]}"(ws), "{s[50:51]}"(tag) : "memory", "scc", "vcc", "s48", "v124", "v125");
}
__device__ __forceinline__ void put_flag(char* ws, unsigned off, unsigned long long tag) {
  asm volatile("s_mov_b64 exec, 1\n\tglobal_store_dwordx2 %0, %1, %2 sc1\n\ts_mov_b64 exec, -1" :: "v"(off), "v"(tag), "s"(ws) : "memory");
}
__device__ __forceinline__ void wo_dma(const char* src, unsigned d, const u32x8& wo) {
  unsigned keep;
  asm volatile("s_mov_b32 %0, m0\n\ts_mov_b32 m0, %7\n\ts_nop 0\n\t"
               "global_load_lds_dwordx4 %1, %6\n\tglobal_load_lds_dwordx4 %2, %6 offset:1024\n\t"
               "global_load_lds_dwordx4 %3, %6 offset:2048\n\tglobal_load_lds_dwordx4 %4, %6 offset:3072\n\t"
               "s_add_u32 m0, m0, 4096\n\ts_nop 0\n\tglobal_load_lds_dwordx4 %5, %6\n\ts_mov_b32 m0, %0"
               : "=&s"(keep) : "v"(wo[0]), "v"(wo[1]), "v"(wo[2]), "v"(wo[3]), "v"(wo[4]), "s"(src), "s"(d) : "memory", "scc");
}

#define AT_ASM_MACROS \
    ".macro mx d, s\n\tv_max3_f32 v[\\d], v[\\s], v[\\s+1], v[\\s+2]\n\t.set i_, \\s + 3\n\t.rept 6\n\tv_max3_f32 v[\\d], v[\\d], v[i_], v[i_+1]\n\t" \
    ".set i_, i_ + 2\n\t.endr\n\tv_max_f32 v[\\d], v[\\d], v[\\s+15]\n\t.endm\n\t" \
    ".macro xp d, s\n\tv_exp_f32 v[\\d], v[\\s]\n\tv_exp_f32 v[\\d+1], v[\\s+1]\n\tv_exp_f32 v[\\d+2], v[\\s+2]\n\tv_exp_f32 v[\\d+3], v[\\s+3]\n\t.endm\n\t" \
    ".macro smf o, v, p\n\tv_smfmac_f32_32x32x32_bf16 v[\\o:\\o+15], v[\\v:\\v+3], v[\\p:\\p+7], v76\n\t.endm\n\t" \
    ".macro qk1 s, k, q\n\tv_mfma_f32_32x32x16_bf16 v[\\s:\\s+15], v[\\k:\\k+3], v[\\q:\\q+3], 0\n\t.endm\n\t" \
    ".macro qk1c s, k, q, c\n\tv_mfma_f32_32x32x16_bf16 v[\\s:\\s+15], v[\\k:\\k+3], v[\\q:\\q+3], v[\\c:\\c+15]\n\t.endm\n\t" \
    ".macro qk2 s, k, q\n\tv_mfma_f32_32x32x8_bf16 v[\\s:\\s+15], v[\\k:\\k+1], v[\\q:\\q+1], v[\\s:\\s+15]\n\t.endm\n\t"
#define AT_ASM_PURGE ".purgem mx\n\t.purgem xp\n\t.purgem smf\n\t.purgem qk1\n\t.purgem qk1c\n\t.purgem qk2\n\t"

#define AT_A_TEXT AT_ASM_MACROS \
      ".macro pos j\n\t.set sl_, (\\j) >> 2\n\t.if sl_ >= 3\n\t.set sl_, sl_ - 3\n\t.endif\n\t" \
      ".set ar_, 128 + 3 * (sl_ >> 1)\n\t.set of_, (sl_ & 1) * 28672 + ((\\j) & 3) * 7168\n\t.endm\n\t" \
      ".macro dma part\n\t" \
      ".if di_ >= 0\n\t" \
      ".if \\part == 0\n\t" \
      ".if di_ == 1\n\ts_mov_b32 m0, s73\n\t.else\n\ts_add_u32 m0, s72, ((di_ + 2) %% 5) * 28672\n\t.endif\n\t" \
      "s_add_u32 s40, s54, s[56+2*di_]\n\ts_addc_u32 s41, s55, 0\n\t" \
      "global_load_lds_dwordx4 v137, s[40:41] sc1\n\t" \
      ".elseif \\part == 1\n\t" \
      "s_mov_b32 exec_hi, 0\n\tglobal_load_lds_dwordx4 v138, s[40:41] offset:1024 sc1\n\ts_mov_b32 exec_hi, -1\n\t" \
      "s_add_u32 s40, s54, s[57+2*di_]\n\ts_addc_u32 s41, s55, 0\n\t" \
      ".else\n\t" \
      "s_mov_b32 exec_lo, 0xfffff\n\ts_mov_b32 exec_hi, 0xfffff\n\t" \
      "global_load_lds_dwordx4 v139, s[40:41] offset:1536 sc1\n\tglobal_load_lds_dwordx4 v140, s[40:41] offset:2560 sc1\n\t" \
      "s_mov_b64 exec, -1\n\t" \
      ".endif\n\t.endif\n\t" \
        \
      ".set n_, -1\n\t" \
      ".if j_ >= 22 && j_ < 26 && 3 * (j_ - 22) + \\part < 10\n\t.set n_, 3 * (j_ - 22) + \\part\n\t.set bk_, 2 * (n_ / 5)\n\t.endif\n\t" \
      ".if j_ >= 28 && 3 * (j_ - 28) + \\part < 5\n\t.set n_, 3 * (j_ - 28) + \\part\n\t.set bk_, 1\n\t.endif\n\t" \
      ".if n_ >= 0\n\t.set pc_, n_ %% 5\n\t" \
      ".if bk_ == 2\n\ts_cmp_lt_i32 s46, 0\n\ts_cbranch_scc1 9f\n\t.endif\n\t" \
      ".if pc_ == 0\n\ts_mov_b32 m0, s[44+bk_]\n\ts_add_u32 s40, s42, bk_ * 8 * 32 * 1280\n\ts_addc_u32 s41, s43, 0\n\t.endif\n\t" \
      ".if pc_ == 4\n\ts_add_u32 m0, m0, 4096\n\ts_nop 0\n\t.endif\n\t" \
      "global_load_lds_dwordx4 v[248+pc_], s[40:41] offset:1024*(pc_&3)\n\t" \
      ".if bk_ == 2\n\t9:\n\t.endif\n\t.endif\n\t" \
      ".endm\n\t" \
      ".macro flg msk\n\tv_mov_b32 v142, s50\n\tv_mov_b32 v143, s51\n\tv_mov_b32 v108, s52\n\ts_mov_b64 exec, \\msk\n\ts_cbranch_execz 8f\n\ts_waitcnt vmcnt(0)\n\t" \
      "global_store_dwordx2 v108, v[142:143], s[54:55] sc1\n\t8:\n\ts_mov_b64 exec, -1\n\t.endm\n\t" \
      ".macro tabl d, r\n\tglobal_load_dwordx4 v[\\d:\\d+3], v79, s[40:41] offset:1024*(\\r)\n\t.endm\n\t" \
      ".macro old q\n\t.if (\\q) >= 0 && (\\q) < 10\n\tglobal_load_dwordx4 v[184+4*(\\q):187+4*(\\q)], v108, s[54:55] sc1\n\tv_add_u32 v108, 0x20000, v108\n\t.endif\n\t.endm\n\t" \
      "s_mov_b32 %[keep], m0\n\t" \
      ".set j_, %c[j0]\n\t" \
      ".rept %c[j1] - %c[j0]\n\t" \
      ".set a_, 80 + 14 * (j_ & 1)\n\t.set b_, 94 - 14 * (j_ & 1)\n\t" \
      ".if j_ == 4\n\t" AWAIT(142, 143, 141) ".set di_, 0\n\tdma 0\n\tdma 1\n\tdma 2\n\t.endif\n\t" \
      ".if j_ == 6\n\ts_waitcnt vmcnt(10)\n\ts_barrier\n\t.endif\n\t" \
      ".if j_ == 10 || j_ == 14 || j_ == 22\n\ts_waitcnt vmcnt(0)\n\ts_barrier\n\t.endif\n\t" \
      ".if j_ == 18\n\ts_waitcnt vmcnt(1)\n\ts_barrier\n\t.endif\n\t" \
      ".if j_ == 28 || j_ == 2\n\ts_barrier\n\t.endif\n\t" \
      AT_STAMP \
      ".if j_ < 2\n\ts_setprio 1 - j_\n\t" \
      ".elseif j_ == 10 || j_ == 11\n\ts_setprio 3\n\t.elseif j_ == 12 || j_ == 13\n\ts_setprio 0\n\t" \
      ".elseif j_ < 22\n\ts_setprio 3 - ((j_ + 2) & 3)\n\t.elseif j_ >= 28\n\ts_setprio 31 - j_\n\t" \
      ".elseif j_ != 23 && j_ != 25\n\ts_setprio 3 - (j_ - 21) / 2\n\t.endif\n\t" \
      ".if j_ == 14\n\tflg 1\n\t" \
      "v_mov_b32 v108, 0x3f803f80\n\tv_mov_b32 v109, v108\n\tv_mov_b32 v110, v108\n\tv_mov_b32 v111, v108\n\ts_mov_b64 exec, 15\n\t" \
      "ds_write_b128 v77, v[108:111]\n\ts_mov_b64 exec, -1\n\t.endif\n\t" \
      ".if j_ == 28\n\t" AWAIT(142, 143, 253) "v_add_u32 v108, %c[obr], v254\n\t.endif\n\t" \
      ".set di_, -1\n\t" \
      ".if j_ == 4 || j_ == 5 || j_ == 6\n\t.set di_, j_ - 3\n\t.endif\n\t.if j_ == 10\n\t.set di_, 4\n\t.endif\n\t" \
      ".if j_ >= 14 && j_ < 17\n\t.set di_, j_ - 9\n\t.endif\n\t" \
        \
      "pos j_+2\n\t" \
      "qk1 48, a_, 68\n\t.if j_ < 30\n\tds_read_b128 v[a_:a_+3], v[ar_] offset:of_\n\t.endif\n\told 4*(j_-28)\n\txp 112, 32\n\t" \
      "qk2 48, a_+4, 74\n\t.if j_ < 30\n\tds_read_b64 v[a_+4:a_+5], v[ar_+1] offset:of_\n\t.endif\n\told 4*(j_-28)+1\n\txp 116, 36\n\t" \
      "pos j_+1\n\t" \
      "smf 16, b_+10, 120\n\t.if j_ < 31\n\tds_read_b128 v[b_+10:b_+13], v[ar_+2] offset:of_+1024\n\t.endif\n\told 4*(j_-28)+2\n\txp 120, 40\n\t" \
      "smf 0, a_+6, 112\n\t.if j_ < 30\n\ts_waitcnt lgkmcnt(3)\n\t.elseif j_ == 30\n\ts_waitcnt lgkmcnt(1)\n\t.else\n\ts_waitcnt lgkmcnt(0)\n\t.endif\n\t" \
      "old 4*(j_-28)+3\n\t" \
      ".if j_ == 5\n\tglobal_load_dwordx4 v[236:239], v137, s[42:43] sc1\n\tglobal_load_dwordx2 v[240:241], v78, s[42:43] sc1\n\t.endif\n\t" \
      "xp 124, 44\n\t" \
        \
      ".if j_ < 31\n\tqk1 32, b_, 64\n\t.endif\n\tdma 0\n\t" \
      ".if j_ == 0\n\tflg s[74:75]\n\t.endif\n\t" \
      ".if j_ == 3\n\tglobal_load_dwordx2 v[142:143], v141, s[54:55] sc1\n\t.endif\n\t" \
      ".if j_ == 7\n\ts_mov_b64 s[40:41], s[44:45]\n\ttabl 248, 0\n\ttabl 252, 1\n\t.endif\n\t" \
      ".if j_ == 8\n\ttabl 220, 2\n\ttabl 224, 3\n\t.endif\n\t" \
      ".if j_ == 17\n\ts_mov_b32 m0, 143360\n\ts_nop 0\n\tglobal_load_lds_dword v141, s[74:75]\n\t.endif\n\t" \
      ".if j_ == 26\n\tglobal_load_dwordx2 v[142:143], v253, s[54:55] sc1\n\t.endif\n\t" \
      "xp 112, 48\n\t" \
      ".if j_ < 31\n\tqk2 32, b_+4, 72\n\t.endif\n\tdma 1\n\t" \
      ".if j_ == 7\n\ttabl 160, 2\n\ttabl 164, 3\n\ts_add_u32 s40, s40, 4096\n\ts_addc_u32 s41, s41, 0\n\t.endif\n\t" \
      ".if j_ == 8\n\ts_add_u32 s40, s40, 4096\n\ts_addc_u32 s41, s41, 0\n\ttabl 228, 0\n\ttabl 232, 1\n\t.endif\n\t" \
      "xp 116, 52\n\t" \
      "smf 0, a_+10, 120\n\tdma 2\n\t" \
      ".if j_ == 7\n\ttabl 168, 0\n\ttabl 172, 1\n\t.endif\n\t" \
      "xp 120, 56\n\t" \
      "pos j_+2\n\t" \
      "smf 16, a_+6, 112\n\t.if j_ < 30\n\tds_read_b128 v[a_+6:a_+9], v[ar_+2] offset:of_\n\t.endif\n\txp 124, 60\n\t" \
      ".set j_, j_ + 1\n\t" \
      ".endr\n\t" \
      ".if %c[j1] == 32\n\ts_nop 1\n\tsmf 16, 104, 120\n\ts_nop 15\n\t.endif\n\t" \
      "s_mov_b32 m0, %[keep]\n\t" \
      ".purgem pos\n\t.purgem dma\n\t.purgem flg\n\t.purgem tabl\n\t.purgem old\n\t" AT_ASM_PURGE
#define AT_STAMP
#define AT_STV
#define AT_STV_ARG
#define AT_STV_PASS
#define AT_A_STATE "+{v[0:31]}"(o), "+{v[32:47]}"(s0), "+{v[112:127]}"(pp), "+{v[80:111]}"(fr), "+{v[128:143]}"(ad), [keep] "=&s"(keep)
#define AT_A_CLOB "memory", "scc", "vcc", "s40", "s41", "s48", "v48", "v49", "v50", "v51", "v52", "v53", "v54", "v55", "v56", "v57", "v58", "v59", "v60", "v61", "v62", "v63"
__device__ __forceinline__ void at_a1(f32x32& o, f32x16& s0, f32x16& pp, f32x32& fr, const u32x16& q, u32x16& ad, const char* ws, u32x16 so,
                                      unsigned m0v, unsigned m0b, unsigned long long tag, unsigned flag, unsigned long long fmask,
                                      const char* qB, const void* tab, f32x16& mF, f32x16& mD, u32x8& qv, u32x8& tw AT_STV_ARG) {
  unsigned keep;
  asm volatile(AT_A_TEXT
      : AT_A_STATE, "=&{v[160:175]}"(mF), "=&{v[220:235]}"(mD), "=&{v[236:243]}"(qv), "=&{v[248:255]}"(tw) AT_STV
      : "{v[64:79]}"(q), "{s[54:55]}"(ws), "{s[56:71]}"(so), "{s72}"(m0v), "{s73}"(m0b), "{s[50:51]}"(tag), "{s52}"(flag), "{s[74:75]}"(fmask),
        "{s[42:43]}"(qB), "{s[44:45]}"(tab), [j0] "n"(0), [j1] "n"(12), [obr] "n"(O_BR)
      : AT_A_CLOB);
}
__device__ __forceinline__ void at_a2(f32x32& o, f32x16& s0, f32x16& pp, f32x32& fr, const u32x16& q, u32x16& ad, const char* ws, u32x16 so,
                                      unsigned m0v, unsigned long long tag, unsigned flag, const char* woa, const u32x8& wo, const char* src,
                                      unsigned d0, unsigned d1, unsigned d2, f32x32& of, f32x8& of2) {
  unsigned keep;
  asm volatile(AT_A_TEXT
      : AT_A_STATE, "=&{v[184:215]}"(of), "=&{v[216:223]}"(of2)
      : "{v[64:79]}"(q), "{s[54:55]}"(ws), "{s[56:71]}"(so), "{s72}"(m0v), "{s[50:51]}"(tag), "{s52}"(flag), "{s[74:75]}"(woa),
        "{v[248:255]}"(wo), "{s[42:43]}"(src), "{s44}"(d0), "{s45}"(d1), "{s46}"(d2), [j0] "n"(12), [j1] "n"(32), [obr] "n"(O_BR)
      : AT_A_CLOB);
}

__device__ __forceinline__ void at_b(f32x16& o, f32x16& mF, const f32x16& mD, u32x8& q, unsigned tb, unsigned qt, char* oA, const u32x16& qa,
                                     u32x16& ad, f32x32& k0, f32x16& k1, f32x32& k2, f32x16& k3) {
  asm volatile(AT_ASM_MACROS
      "v_add_u32 v242, %[tb], v128\n\tv_add_u32 v243, %[tb], v129\n\tv_add_u32 v245, %[tb], v130\n\t"
      "ds_read_b128 v[176:179], v242 offset:7*3584\n\tds_read_b64 v[180:181], v243 offset:7*3584\n\t"
      "ds_read_b128 v[184:187], v242 offset:8*3584\n\tds_read_b64 v[182:183], v243 offset:8*3584\n\t"
      "ds_read_b128 v[192:195], v242\n\tds_read_b64 v[196:197], v243\n\tds_read_b128 v[206:209], v242 offset:3584\n\tds_read_b64 v[210:211], v243 offset:3584\n\t"
      "ds_read_b128 v[198:201], v245\n\tds_read_b128 v[212:215], v245 offset:3584\n\tds_read_b128 v[202:205], v245 offset:1024\n\t"
      "s_mov_b32 exec_lo, 0\n\tv_mov_b64 v[240:241], 0\n\ts_mov_b32 exec_lo, -1\n\tv_mov_b64 v[188:189], -1\n\tv_mov_b64 v[190:191], -1\n\t"
      "s_waitcnt lgkmcnt(7)\n\t"
      "qk1 144, 176, 236\n\tqk2 144, 180, 240\n\tqk1c 48, 184, 236, 220\n\tqk2 48, 182, 240\n\t"
      "global_store_dwordx4 v137, v[188:191], s[42:43] sc1\n\ts_mov_b32 exec_hi, 0\n\tglobal_store_dwordx2 v78, v[188:189], s[42:43] sc1\n\ts_mov_b32 exec_hi, -1\n\t"
      "s_waitcnt lgkmcnt(5)\n\tqk1c 160, 192, 236, 160\n\tds_read_b128 v[192:195], v242 offset:7168\n\ts_cmp_eq_u32 %[qt], 0\n\ts_nop 1\n\t"
      "mx 216, 144\n\tmx 218, 48\n\ts_cbranch_scc1 1f\n\tv_max_f32 v218, v218, v216\n\t1:\n\tv_mov_b32 v219, v218\n\t"
      "v_mov_b64 v[144:145], 0\n\tv_mov_b64 v[146:147], 0\n\tv_permlane32_swap_b32 v218, v219\n\tv_max_f32 v218, v218, v219\n\t"
      "s_mov_b32 exec_lo, 0\n\tv_cvt_pk_bf16_f32 v240, -v218, 0\n\ts_mov_b32 exec_lo, -1\n\tv_mov_b64 v[148:149], 0\n\t"
      "qk2 160, 196, 240\n\tds_read_b64 v[196:197], v243 offset:7168\n\t"
      ".set i_, 150\n\t.rept 5\n\tv_mov_b64 v[i_:i_+1], 0\n\t.set i_, i_ + 2\n\t.endr\n\t"
      "s_waitcnt lgkmcnt(2)\n\ts_nop 1\n\t"
      ".set t_, 0\n\t"
      ".rept 9\n\t"
      ".set a_, 192 + 14 * (t_ & 1)\n\t.set b_, 206 - 14 * (t_ & 1)\n\t.set c_, 160 - 112 * (t_ & 1)\n\t.set d_, 48 + 112 * (t_ & 1)\n\t"
      ".if t_ == 0 || t_ == 5\n\ts_setprio 2 - t_ / 5\n\t.endif\n\t"
      ".if t_ < 7\n\tqk1 d_, b_, 236\n\t.elseif t_ == 7\n\tqk1c d_, b_, 236, 220\n\t.endif\n\t"
      ".if t_ < 6\n\tds_read_b128 v[b_:b_+3], v242 offset:(t_+3)*3584\n\t.endif\n\t"
      "xp 176, c_\n\t"
      ".if t_ < 8\n\tqk2 d_, b_+4, 240\n\t.endif\n\t"
      ".if t_ < 6\n\tds_read_b64 v[b_+4:b_+5], v243 offset:(t_+3)*3584\n\t.endif\n\t"
      "xp 180, c_+4\n\t"
      ".if t_ > 0\n\tsmf 144, b_+10, 184\n\t.endif\n\t"
      ".if t_ < 8\n\tds_read_b128 v[b_+10:b_+13], v245 offset:(t_+1)*3584+1024\n\t.endif\n\t"
      "xp 184, c_+8\n\t"
      "smf 144, a_+6, 176\n\t"
      ".if t_ < 6\n\ts_waitcnt lgkmcnt(3)\n\t.elseif t_ < 8\n\ts_waitcnt lgkmcnt(1)\n\t.else\n\ts_waitcnt lgkmcnt(0)\n\t.endif\n\t"
      ".if t_ < 7\n\tds_read_b128 v[a_+6:a_+9], v245 offset:(t_+2)*3584\n\t.endif\n\t"
      "xp 188, c_+12\n\t"
      ".set t_, t_ + 1\n\t"
      ".endr\n\t"
      "s_nop 1\n\tsmf 144, 202, 184\n\ts_nop 11\n\t"
      AT_ASM_PURGE
      : "=&{v[144:159]}"(o), "+{v[160:175]}"(mF), "+{v[0:31]}"(k0), "+{v[32:47]}"(k1), "+{v[80:111]}"(k2), "+{v[112:127]}"(k3), "+{v[128:143]}"(ad),
        "+{v[236:243]}"(q)
      : "{v[220:235]}"(mD), "{v[64:79]}"(qa), [tb] "s"(tb), [qt] "s"(qt), "{s[42:43]}"(oA)
      : "memory", "scc", "v245", "v48", "v49", "v50", "v51", "v52", "v53", "v54", "v55", "v56", "v57", "v58", "v59", "v60", "v61", "v62", "v63",
        "v176", "v177", "v178", "v179", "v180", "v181", "v182", "v183", "v184", "v185", "v186", "v187", "v188", "v189", "v190", "v191",
        "v192", "v193", "v194", "v195", "v196", "v197", "v198", "v199", "v200", "v201", "v202", "v203", "v204", "v205", "v206", "v207",
        "v208", "v209", "v210", "v211", "v212", "v213", "v214", "v215", "v216", "v217", "v218", "v219");
}

#define PJ_MACROS \
      ".macro rdw k, sp\n\t.set d_, 80 + 20 * ((\\k) & 1)\n\t" \
      ".if (\\k) < \\sp\n\t.set a_, 120\n\t.set o_, (\\k) * 5120\n\t.else\n\t.set a_, 122\n\t.set o_, ((\\k) - \\sp) * 5120\n\t.endif\n\t.endm\n\t" \
      ".macro rd1 i\n\tds_read_b64_tr_b16 v[d_+4*\\i:d_+4*\\i+1], v[a_] offset:o_+32*\\i\n\tds_read_b64_tr_b16 v[d_+4*\\i+2:d_+4*\\i+3], v[a_+1] offset:o_+32*\\i\n\t.endm\n\t" \
      ".macro pm i, ac, of, last\n\t" \
      ".if k_ == 0\n\tv_mfma_f32_16x16x32_bf16 v[\\ac+4*\\i:\\ac+4*\\i+3], v[c_+4*\\i:c_+4*\\i+3], v[\\of:\\of+3], 0\n\t" \
      ".else\n\tv_mfma_f32_16x16x32_bf16 v[\\ac+4*\\i:\\ac+4*\\i+3], v[c_+4*\\i:c_+4*\\i+3], v[\\of+4*k_:\\of+3+4*k_], v[\\ac+4*\\i:\\ac+4*\\i+3]\n\t.endif\n\t" \
      ".if k_ < \\last\n\trd1 \\i\n\ts_waitcnt lgkmcnt(8)\n\t.elseif \\i < 4\n\ts_waitcnt lgkmcnt(6 - 2 * \\i)\n\t.endif\n\t.endm\n\t" \
      ".macro ldo\n\tv_mov_b32 v72, v254\n\t.set q_, 0\n\t.rept 10\n\t" \
      "global_load_dwordx4 v[32+4*q_:35+4*q_], v72, s[54:55] sc1\n\t.if q_ < 9\n\tv_add_u32 v72, 0x20000, v72\n\t.endif\n\t.set q_, q_ + 1\n\t.endr\n\t.endm\n\t"
#define PJ_PURGE ".purgem rdw\n\t.purgem rd1\n\t.purgem pm\n\t.purgem ldo\n\t"
#define PJ_CLOB "v72", "v80", "v81", "v82", "v83", "v84", "v85", "v86", "v87", "v88", "v89", "v90", "v91", "v92", "v93", "v94", "v95", \
        "v96", "v97", "v98", "v99", "v100", "v101", "v102", "v103", "v104", "v105", "v106", "v107", "v108", "v109", "v110", "v111", \
        "v112", "v113", "v114", "v115", "v116", "v117", "v118", "v119"
__device__ __forceinline__ void proj_1(f32x32& acc, const f32x32& of, const f32x8& of2, f32x32& oa, f32x8& oa2, u32x4 wa, const u32x8& wo,
                                       const char* src0, const char* src1, unsigned d0, unsigned d1, const char* ws) {
  unsigned keep;
  asm volatile(PJ_MACROS
      ".macro wp n\n\t.set n_, \\n\n\t.set pc_, n_ %% 5\n\t"
      ".if n_ < 10\n\t"
      ".if n_ >= 5\n\ts_cmp_lt_i32 s45, 0\n\ts_cbranch_scc1 9f\n\t.endif\n\t"
      ".if pc_ == 0\n\ts_mov_b32 m0, s[44+n_/5]\n\ts_nop 0\n\t.endif\n\t"
      ".if pc_ == 4\n\ts_add_u32 m0, m0, 4096\n\ts_nop 0\n\t.endif\n\t"
      "global_load_lds_dwordx4 v[248+pc_], s[42+4*(n_/5):43+4*(n_/5)] offset:1024*(pc_&3)\n\t"
      ".if n_ >= 5\n\t9:\n\t.endif\n\t.endif\n\t"
      ".if n_ == 12\n\tldo\n\t.endif\n\t"
      ".endm\n\t"
      "rdw 0, 6\n\trd1 0\n\trd1 1\n\trd1 2\n\trd1 3\n\trd1 4\n\t"
      "s_mov_b32 %[keep], m0\n\ts_barrier\n\ts_waitcnt lgkmcnt(8)\n\t"
      ".set k_, 0\n\t.rept 10\n\t.set c_, 80 + 20 * (k_ & 1)\n\trdw k_+1, 6\n\t"
      ".if k_ == 0 || k_ == 3 || k_ == 6\n\ts_setprio 2 - k_ / 3\n\t.endif\n\t"
      "pm 0, 0, 184, 9\n\twp 2*k_\n\tpm 1, 0, 184, 9\n\tpm 2, 0, 184, 9\n\twp 2*k_+1\n\tpm 3, 0, 184, 9\n\tpm 4, 0, 184, 9\n\t"
      ".set k_, k_ + 1\n\t.endr\n\t"
      "s_setprio 3\n\ts_mov_b32 m0, %[keep]\n\ts_waitcnt vmcnt(10)\n\ts_barrier\n\ts_nop 4\n\t"
      ".purgem wp\n\t" PJ_PURGE
      : "=&{v[0:31]}"(acc), "=&{v[32:63]}"(oa), "=&{v[64:71]}"(oa2), [keep] "=&s"(keep)
      : "{v[184:215]}"(of), "{v[216:223]}"(of2), "{v[120:123]}"(wa), "{v[248:255]}"(wo), "{s[42:43]}"(src0), "{s[46:47]}"(src1), "{s44}"(d0),
        "{s45}"(d1), "{s[54:55]}"(ws)
      : "memory", "scc", PJ_CLOB);
}
template <int K0>
__device__ __forceinline__ void proj_2(f32x32& acc, f32x32& oa, f32x8& oa2, u32x4 wa, const u32x8& wo, const char* ws) {
  asm volatile(PJ_MACROS
      "rdw %c[k0], 5\n\trd1 0\n\trd1 1\n\trd1 2\n\trd1 3\n\trd1 4\n\t"
      ".if %c[k0] == 0\n\t"
      "s_mov_b32 s48, 0x40000\n\t5:\n\t"
      "v_max3_u32 v72, v32, v33, v34\n\t.set q_, 35\n\t.rept 18\n\tv_max3_u32 v72, v72, v[q_], v[q_+1]\n\t.set q_, q_ + 2\n\t.endr\n\t"
      "v_max_u32 v72, v72, v71\n\tv_cmp_eq_u32 vcc, -1, v72\n\ts_sub_u32 s48, s48, 1\n\t"
      "s_cbranch_vccz 4f\n\ts_cbranch_scc1 4f\n\tldo\n\ts_waitcnt vmcnt(0)\n\ts_branch 5b\n\t4:\n\t"
      ".endif\n\t"
      "s_waitcnt lgkmcnt(8)\n\t"
      ".set k_, %c[k0]\n\t.rept 5\n\t.set c_, 80 + 20 * (k_ & 1)\n\trdw k_+1, 5\n\t"
      "pm 0, 160, 32, %c[k0]+4\n\tpm 1, 160, 32, %c[k0]+4\n\tpm 2, 160, 32, %c[k0]+4\n\tpm 3, 160, 32, %c[k0]+4\n\tpm 4, 160, 32, %c[k0]+4\n\t"
      ".set k_, k_ + 1\n\t.endr\n\t"
      ".if %c[k0] == 0\n\ts_waitcnt vmcnt(0)\n\ts_barrier\n\t.else\n\ts_nop 7\n\t.endif\n\t"
      PJ_PURGE
      : "+{v[160:191]}"(acc), "+{v[32:63]}"(oa), "+{v[64:71]}"(oa2)
      : "{v[120:123]}"(wa), "{v[248:255]}"(wo), "{s[54:55]}"(ws), [k0] "n"(K0)
      : "memory", "scc", "vcc", "s48", PJ_CLOB);
}
typedef __attribute__((address_space(3))) f32x4* lds_f4;
__device__ __forceinline__ void proj_send(const f32x32& acc, char* lds, int lw, int grp, int lane) {
  lds_ptr p = (lds_ptr)lds + lw * 5120 + lane * 16;
  if (grp) {
    *(lds_f4)(p) = f32x4{acc[0], acc[1], acc[2], acc[3]}; *(lds_f4)(p + 1024) = f32x4{acc[4], acc[5], acc[6], acc[7]};
    *(lds_f4)(p + 4096) = f32x4{acc[16], acc[17], acc[18], acc[19]};
  } else {
    *(lds_f4)(p + 2048) = f32x4{acc[8], acc[9], acc[10], acc[11]}; *(lds_f4)(p + 3072) = f32x4{acc[12], acc[13], acc[14], acc[15]};
  }
}
#define OUT_POL ""
__device__ __forceinline__ void proj_out(const f32x4& a, const f32x4& b, char* p, unsigned off) {
  const auto s0 = __builtin_amdgcn_permlane16_swap(pk_bf16(a[0], a[1]), pk_bf16(b[0], b[1]), false, false);
  const auto s1 = __builtin_amdgcn_permlane16_swap(pk_bf16(a[2], a[3]), pk_bf16(b[2], b[3]), false, false);
  asm volatile("global_store_dwordx4 %0, %1, %2 " OUT_POL "\n\ts_nop 1" :: "v"(off), "v"(u32x4{s0[0], s1[0], s0[1], s1[1]}), "s"(p) : "memory");
}
__device__ __forceinline__ void proj_finish(const f32x32& acc, char* lds, int lw, int grp, int lane, char* dst) {
  lds_ptr p = (lds_ptr)lds + lw * 5120 + lane * 16;
  const unsigned row = (lane & 15) * 2560, o16 = row + ((lane >> 4) & 1) * 32 + (lane >> 5) * 16;
  if (grp) {
    proj_out(f32x4{acc[8], acc[9], acc[10], acc[11]} + *(lds_f4)(p + 2048), f32x4{acc[12], acc[13], acc[14], acc[15]} + *(lds_f4)(p + 3072), dst + 64, o16);
  } else {
    const f32x4 r2 = f32x4{acc[16], acc[17], acc[18], acc[19]} + *(lds_f4)(p + 4096);
    proj_out(f32x4{acc[0], acc[1], acc[2], acc[3]} + *(lds_f4)(p), f32x4{acc[4], acc[5], acc[6], acc[7]} + *(lds_f4)(p + 1024), dst, o16);
    asm volatile("global_store_dwordx2 %0, %1, %2 offset:128 " OUT_POL :: "v"(row + (lane >> 4) * 8), "v"(u32x2{pk_bf16(r2[0], r2[1]), pk_bf16(r2[2], r2[3])}), "s"(dst) : "memory");
  }
}

struct LaneTab { unsigned v[10][64][4]; };
constexpr LaneTab make_tab() {
  LaneTab t = {};
  for (unsigned l = 0; l < 64; ++l) {
    for (unsigned i = 0; i < 5; ++i) {
      const unsigned v = 64 * i + l, rw = v / 10;
      t.v[i >> 2][l][i & 3] = (rw ^ ((rw >> 1) & 4)) * 1280 + (v - rw * 10) * 16 + 4096 - 1024 * (i & 3);
    }
    const unsigned g = l >> 4, q = (l & 15) >> 2, p = l & 3;
    t.v[1][l][1] = (l & 15) * 64 * 128;
    t.v[1][l][2] = g * 32768 + (l & 15) * 16;
    t.v[1][l][3] = (g * 1280 + (q ^ (4 * (g & 1))) * 160 + p * 8) | ((g * 1280 + ((4 + q) ^ (4 * (g & 1))) * 160 + p * 8) << 16);
    for (unsigned r = 0; r < 16; ++r) {
      const unsigned key = (r & 3) + 8 * (r >> 2) + 4 * (l >> 5);
      t.v[2 + (r >> 2)][l][r & 3] = key > (l & 31) ? 0u : 0xFF800000u;
      t.v[6 + (r >> 2)][l][r & 3] = key <= (l & 31) ? 0u : 0xFF800000u;
    }
  }
  return t;
}
__device__ const LaneTab lane_tab = make_tab();
constexpr unsigned CODE_B = 34816;

__global__ void __launch_bounds__(512, 2)
k_fused(const char* __restrict__ xa_, const char* __restrict__ wa_, const char* __restrict__ woa_, const char* __restrict__ xb_,
        const char* __restrict__ wb_, const char* __restrict__ wob_, char* __restrict__ ws_, char* __restrict__ out_) {
  __shared__ __attribute__((aligned(16))) char lds[163840];
  const unsigned bid = blockIdx.x, xcd = bid & 7, slot = bid >> 3, br = xcd >> 2;
  u32x4 ka; u32x2 kb; u32x8 kc;
  {
    const void* kp = (const void*)__builtin_amdgcn_kernarg_segment_ptr();
    asm volatile("s_load_dwordx4 %0, %3, %4\n\ts_load_dwordx8 %2, %3, 0x20\n\ts_load_dwordx2 %1, %3, 0x10"
                 : "=&s"(ka), "=&s"(kb), "=&s"(kc) : "s"(kp), "s"(br * 24));
  }
  STAMP_DECL STAMP(0);
  unsigned lane = threadIdx.x & 63;
  const unsigned w = __builtin_amdgcn_readfirstlane(threadIdx.x >> 6);
  const unsigned grp = w >> 2, lw = w & 3;
  const unsigned mt = (xcd & 3) * 4 + (slot & 3), u = slot >> 2;
  const unsigned win = (5 * ((mt >> 1) & 3)) >> 1;
  const unsigned kv1 = (u + 3) >> 3;
  const unsigned j1 = win + u - 5;
#define ROT (5 * (u & 1) + (5 * (xcd & 3)) / 2)
  auto mod10 = [](unsigned x) { return __builtin_elementwise_min(x, x - 10); };
  const unsigned rot = mod10(ROT);
  const unsigned m0v = grp * 16384 + lw * 4096;
  const unsigned str1 = grp ? 64 * 3840 : 128;
  unsigned o1[4], tw;
  {
    const unsigned row = lane >> 3, pos = lane & 7;
    tw = row * 3840 + ((pos ^ (row & 2)) * 16);
    const unsigned t1 = grp ? tw : row * 1280 + ((pos ^ (row >> 1)) * 16);
    const unsigned gap = kv1 ? 1280 : 128, m1 = grp ? 0 : 64;
    o1[0] = t1 + 1024;
    o1[1] = (t1 ^ m1) + (grp ? gap : 8 * 1280);
    o1[2] = (t1 ^ m1 ^ 64) + (grp ? 8 * 3840 - 1024 : 16 * 1280 - 1024);
    o1[3] = (t1 ^ 64) + (grp ? 8 * 3840 - 2048 + gap : 24 * 1280 - 2048);
  }
  const unsigned offA = grp ? 16 * lw * 3840 + (kv1 ? 1280 + j1 * 128 : u * 256) : (mt * 128 + 32 * lw) * 1280;
  asm volatile("s_waitcnt lgkmcnt(0)" : "+s"(ka), "+s"(kb), "+s"(kc));
  auto ptr = [](unsigned lo, unsigned hi) { return (char*)(((unsigned long long)hi << 32) | lo); };
  const char* sA = ptr(grp ? ka[2] : ka[0], grp ? ka[3] : ka[1]) + (offA - 1024ll);
  char* const ws = ptr(kc[4], kc[5]);
  glds16x4(sA + rot * str1, o1, m0v);
  glds16x4(sA + mod10(rot + 1) * str1, o1, m0v + 32768);
  u32x2 ep; unsigned tch[4];
  asm volatile("s_load_dwordx2 %0, %1, %2" : "=&s"(ep) : "s"(ws), "s"((unsigned)CTL_WS));
  {
    const unsigned i64 = (slot * 8 + w) * 64;
    unsigned long long pc;
    asm volatile("s_getpc_b64 %0\n\ts_load_dword %1, %0, %5\n\ts_load_dword %2, %0, %6\n\ts_load_dword %3, %0, %7\n\ts_load_dword %4, %8, %9"
                 : "=&s"(pc), "=&s"(tch[0]), "=&s"(tch[1]), "=&s"(tch[2]), "=&s"(tch[3])
                 : "s"(i64), "s"(i64 + 16384), "s"(__builtin_elementwise_min(i64 + 32768, CODE_B)), "s"(&lane_tab),
                   "s"(__builtin_elementwise_min(i64, 10176u)));
  }
  const unsigned j2 = mod10(win + 3 + (u < 7 ? u : 6));
  const unsigned Tg = mt * 4 + lw;
  const unsigned vt1 = grp & kv1, vt2 = u < 7 ? grp : 2;
  const unsigned cg1 = kv1 ? 10 + 10 * grp + j1 : 2 * u + grp, cg2 = 10 + 10 * grp + j2;
  const unsigned head = xcd * 4 + (slot & 3), qb = u;
  const unsigned odd = head & 1, o2 = 5 * (head >> 1) + 2, o1h = odd ? o2 + 1 : o2 - 2;
  const unsigned qt = qb * 8 + 2 * lw + grp;
  u32x4 soA; u32x16 so;
  {
    const unsigned kb0 = 80 * 32768 + w * 512, vb0 = (unsigned)V_WS - V_OFF + xcd * (256 * 1280) + w * 5120;
    unsigned ko[10], vo_[10];
#pragma unroll
    for (int i = 0; i < 8; ++i) { const unsigned c = (((head >> 3) + (i >> 1)) & 3) + 4 * (i & 1); ko[i] = kb0 + c * 4096; vo_[i] = vb0 + c * 40960; }
    const unsigned c8 = qb ? qb - 1 : 0;
    ko[8] = kb0 + (unsigned)QK_BR + c8 * 4096; vo_[8] = vb0 + (unsigned)V_BR + c8 * 40960;
    ko[9] = kb0 + (unsigned)QK_BR + qb * 4096; vo_[9] = vb0 + (unsigned)V_BR + qb * 40960;
    soA = u32x4{ko[0], vo_[0], ko[1], vo_[1]};
    so = u32x16{ko[2], vo_[2], ko[8], vo_[8], ko[9], vo_[9], ko[3], vo_[3], ko[4], vo_[4], ko[5], vo_[5], ko[6], vo_[6], ko[7], vo_[7]};
    asm volatile("" : "+{s[76:79]}"(soA), "+{s[56:71]}"(so));
  }
  unsigned flag1 = FQ_OFF + ((br * 30 + cg1) * 64 + Tg) * 128, flag2 = FQ_OFF + ((br * 30 + cg2) * 64 + Tg) * 128;
  unsigned flagO = FO_OFF + (head * 64 + qt) * 128;
  PINS(flag1); PINS(flag2); PINS(flagO);
  const char* sB = ptr(ka[2], ka[3]) + (16 * lw * 3840 + 256 + j2 * 128);
  const unsigned mytile = w * TILE_B;
  u32x16 qq, pk, at;
  {
    const unsigned r16 = lane & 15, g = lane >> 4, q = r16 >> 2, p = lane & 3, r32 = lane & 31, h = lane >> 5;
    const unsigned xa0 = (lw * 32 + r16) * 128 + ((g ^ (r16 >> 1)) * 16);
    const unsigned wa0 = 2048 * g + 1024 * grp + 128 * q + 16 * ((p >> 1) ^ (4 * (g & 1) + (q & 2))) + 8 * (p & 1);
    const unsigned jj = (20 * head + 19 * (lane & 1)) >> 6, b1 = (lane >> 1) & 1, c = (lane >> 2) & 7;
    const unsigned pol1 = FQ_OFF + (jj * 64 + ((lane & 8) ? (qt ^ b1) : b1 * 640 + (lane & 4) * 8 + (640 + (head >> 3) * 8 + w))) * 128;
    qq = u32x16{o1[0], o1[1], o1[2], o1[3], tw + 1024, tw + 1280, (tw ^ 64) + (8 * 3840 - 1024), (tw ^ 64) + (8 * 3840 - 2048 + 1280),
                pol1, xa0, xa0 ^ 64, xa0, wa0, wa0 ^ 32, wa0 ^ 64, wa0 ^ 96};
    const unsigned cB = __builtin_elementwise_max((c & 1) + qb, 1u) + (30 * 8 - 1);
    const unsigned pol2 = (jj * 64 + (lane >= 48 ? 30 * 64 + qt : b1 * 640 + (h ? cB : c) * 8 + (640 + w))) * 128 + FQ_OFF;
    const u32x2 s1 = g1_offs(br, cg1, Tg, lane), s2 = g1_offs(br, cg2, Tg, lane);
    const unsigned ab = grp * TILE_B;
    pk = u32x16{s1[0], s1[1], s2[0], s2[1],
                lane * 16 + ab,
                h ? (V_OFF + 320 + ab) : r32 * 16 + (K2_OFF + odd * 8 + ab),
                h * 512 + (r32 < 28 ? __builtin_elementwise_min(r32, 21u) : 28) * 16 + (V_OFF + ab),
                h * 32768 + r32 * 16 + o1h * 32768,
                r32 * 16 + (o2 * 32768 - 1024),
                h * 1280 + (__builtin_elementwise_min(r32 + 4 * odd, r32 + 4 * odd - 20) + (head & 3) * 20) * 16,
                pol2,
                g * 512 + __builtin_elementwise_min(r16, 11u) * 16 + (mytile + V_OFF + 320),
                r16 == 0 ? 0x3F803F80u : 0u,
                r32 * 16 + (o2 * 32768 + odd * 8),
                lane * 512 + (3 * CHUNK_B + V_OFF + 320 + mytile),
                lane};
  }
  asm volatile("" : "+{v[64:79]}"(qq), "+{v[80:95]}"(pk) :: "memory");
  {
    const unsigned oo[4] = {qq[0], qq[1], qq[2], qq[3]};
    glds16x4(sA + mod10(rot + 2) * str1, oo, m0v + 65536);
  }
  asm volatile("s_waitcnt vmcnt(8) lgkmcnt(0)\n\ts_barrier" ::: "memory");
  PINS(ep); PINS(tch[0]); PINS(tch[1]); PINS(tch[2]); PINS(tch[3]);
  const unsigned long long tag = (((unsigned long long)ep[1] << 32) | ep[0]) + 1;
  STAMP(1);
  u32x4 cv4;
  STV_PUT(pk[15]);
  g1_all(vt1, kv1, vt2, qq, pk, at, cv4, sA, mod10(rot + 3) * str1, str1, sB, mod10(rot + grp) * (64 * 3840), m0v, tag, flag1, ws, mytile, soA,
         qt * 512);
  STV_GET(pk[15]);
  STAMP(8);
  if (bid == 255 && grp == 0) {
    const unsigned e = w * 64 + lane_id(), eu = e & 7, em = (e >> 3) & 15;
    poll_flags(ws, FQ_OFF + (((e >> 7) * 30 + (eu < 5 ? 2 * eu : 5 + eu + ((5 * ((em >> 1) & 3)) >> 1))) * 64 + 4 * em) * 128, tag);
  }
  BARRIER();
  STAMP(9);
  if (bid == 255 && w == 0) put_flag(ws, CTL_WS, tag);
  typedef __attribute__((address_space(3))) u32x4* lds_u4;
  *(lds_u4)((lds_ptr)lds + pk[11] + 2 * CHUNK_B) = cv4; *(lds_u4)((lds_ptr)lds + pk[11] + 3 * CHUNK_B) = cv4; *(lds_u4)((lds_ptr)lds + pk[11] + 4 * CHUNK_B) = cv4;
  if (qb == 0) {
    lds_ptr z = (lds_ptr)lds + 3 * CHUNK_B + mytile + qq[15];
    *(lds_f4)(z) = f32x4{}; *(lds_f4)(z + 1024) = f32x4{}; *(lds_f4)(z + 2048) = f32x4{}; *(lds_f4)(z + 2560) = f32x4{};
  }
  const f32x16 zero16 = {};
  auto exact = [&](int b, bf16x8 q1, s16x4 q2, int t0, int t1) {
    __builtin_amdgcn_fence(__ATOMIC_ACQUIRE, "agent");
    const int ln = lane_id(), r32 = ln & 31, h = ln >> 5;
    unsigned head = xcd * 4 + (slot & 3);
    PINS(head);
    const unsigned odd = head & 1, o2 = 5 * (head >> 1) + 2, o1h = odd ? o2 + 1 : o2 - 2;
    if (h) q2 = s16x4{0, 0, 0, 0};
    typedef __attribute__((address_space(1))) const char* gptr;
    gptr kg1 = (gptr)ws + QK_WS + b * QK_BR + (size_t)(80 + o1h + h) * 32768 + r32 * 16;
    gptr kg2 = (gptr)ws + QK_WS + b * QK_BR + (size_t)(80 + o2) * 32768 + r32 * 16 + odd * 8;
    gptr vg = (gptr)ws + V_WS + b * V_BR + (head >> 2) * (256 * 1280) + h * 1280 +
                     ((head & 3) * 20 + (odd ? (r32 < 16 ? r32 + 4 : r32 - 16) : r32)) * 16;
    const u32x4 vc = r32 == 20 ? u32x4{0x3F803F80u, 0x3F803F80u, 0x3F803F80u, 0x3F803F80u} : u32x4{0u, 0u, 0u, 0u};
    float m = -INFINITY;
    f32x16 r = zero16;
#pragma unroll 1
    for (int ps = 0; ps < 2; ++ps) {
#pragma unroll 1
      for (int t = t0; t <= t1; ++t) {
        f32x16 s = MFMA32(*(__attribute__((address_space(1))) const bf16x8*)(kg1 + t * 512), q1, zero16);
        s = MFMA32K8(*(__attribute__((address_space(1))) const s16x4*)(kg2 + t * 512), q2, s);
        if (b) {
          const int d = (t1 - t) * 32 + r32 - 4 * h;
#pragma unroll
          for (int e = 0; e < 16; ++e) s[e] = (unsigned)(d - ((e & 3) + 8 * (e >> 2))) < 256u ? s[e] : -INFINITY;
        }
        if (ps == 0) { m = max16(s, m); continue; }
#pragma unroll
        for (int e = 0; e < 16; ++e) s[e] = __builtin_amdgcn_exp2f(s[e] - m);
        u32x4 va = vc, vb = vc;
        if (r32 < 20) { va = *(__attribute__((address_space(1))) const u32x4*)(vg + t * 5120); vb = *(__attribute__((address_space(1))) const u32x4*)(vg + t * 5120 + 2560); }
        r = smfmac(__builtin_bit_cast(bf16x8, va), lo8(s), r);
        r = smfmac(__builtin_bit_cast(bf16x8, vb), hi8(s), r);
      }
      m = xhalf_max(m);
    }
    return r;
  };
  auto unsafe = [&](const f32x16& r, float l) {
    float amax = 0.f;
#pragma unroll
    for (int e = 0; e < 12; ++e) amax = __builtin_fmaxf(amax, __builtin_fabsf(r[e]));
    return __any(!(l <= 1.2676506e30f) || !(l >= 5.4210109e-20f) || !(amax <= 3.0e38f));
  };
  const char* const woa = ptr(kb[0], kb[1]);
  const char* const wob = ptr(kc[2], kc[3]);
  char* const oA = ws + (O_WS + qt * 512);
  f32x32 oab, of; f32x8 of2;
  u32x8 wo;
  unsigned wl;
  {
    f32x16 s0, pp;
    f32x32 fr;
    asm volatile(AT_ASM_MACROS
        "v_subrev_u32 v108, %[ab], v129\n\tds_read_b128 v[112:115], v79\n\tds_read_b64 v[116:117], v108\n\t"
        "ds_read_b128 v[80:83], v128\n\tds_read_b64 v[84:85], v129\n\tds_read_b128 v[94:97], v128 offset:7168\n\tds_read_b64 v[98:99], v129 offset:7168\n\t"
        "ds_read_b128 v[86:89], v130\n\tds_read_b128 v[100:103], v130 offset:7168\n\tds_read_b128 v[90:93], v130 offset:1024\n\t"
        "s_mov_b32 exec_lo, 0\n\tv_mov_b64 v[72:73], 0\n\tv_mov_b64 v[74:75], 0\n\ts_mov_b32 exec_lo, -1\n\t"
        "s_waitcnt lgkmcnt(7)\n\t"
        "qk1 32, 112, 64\n\tqk2 32, 116, 72\n\tqk1 48, 112, 68\n\tqk2 48, 116, 74\n\t"
        ".set i_, 0\n\t.rept 10\n\tv_mov_b64 v[i_:i_+1], 0\n\t.set i_, i_ + 2\n\t.endr\n\t"
        "mx 108, 32\n\tv_mov_b32 v109, v108\n\tmx 110, 48\n\tv_mov_b32 v111, v110\n\t"
        "v_permlane32_swap_b32 v108, v109\n\ts_waitcnt lgkmcnt(0)\n\tqk1 32, 80, 64\n\tv_permlane32_swap_b32 v110, v111\n\t"
        "v_max_f32 v108, v108, v109\n\tv_max_f32 v110, v110, v111\n\t"
        "s_mov_b32 exec_lo, 0\n\tv_cvt_pk_bf16_f32 v72, -v108, 0\n\tv_cvt_pk_bf16_f32 v74, -v110, 0\n\ts_mov_b32 exec_lo, -1\n\ts_nop 0\n\t"
        "qk2 32, 84, 72\n\t"
        ".set i_, 20\n\t.rept 6\n\tv_mov_b64 v[i_:i_+1], 0\n\t.set i_, i_ + 2\n\t.endr\n\t"
        "v_mov_b64 v[104:105], 0\n\tv_mov_b64 v[106:107], 0\n\t"
        "v_mov_b64 v[120:121], 0\n\tv_mov_b64 v[122:123], 0\n\tv_mov_b64 v[124:125], 0\n\tv_mov_b64 v[126:127], 0\n\t"
        AT_ASM_PURGE
        : "=&{v[0:31]}"(oab), "=&{v[112:127]}"(pp), "=&{v[32:47]}"(s0), "=&{v[80:111]}"(fr), "+{v[64:79]}"(qq), "+{v[128:143]}"(at)
        : [ab] "s"(grp * TILE_B)
        : "memory", "v48", "v49", "v50", "v51", "v52", "v53", "v54", "v55", "v56", "v57", "v58", "v59", "v60", "v61", "v62", "v63");
    STAMP(10);
    f32x16 maskF, maskD; u32x8 qv;
    at_a1(oab, s0, pp, fr, qq, at, ws, so, mytile, mytile + (qb ? 3 : 4) * CHUNK_B, tag, flag2, (0x7fu >> u) & 1u, ws + (QK_BR + qt * 512),
          &lane_tab, maskF, maskD, qv, wo AT_STV_PASS);
    STAMP(11);
    {
      f32x16 ob;
      at_b(ob, maskF, maskD, qv, 3 * CHUNK_B + 2 * lw * TILE_B, qt, oA, qq, at, oab, s0, fr, pp);
      wl = wo[7];
      wo[5] += FO_OFF + (grp * 16 * 64 + head * 2 + (lw >> 1)) * 128;
      wo[6] += (unsigned)O_WS + grp * 40 * 32768 + (head * 64 + lw * 16) * 16;
      float l = row_sum(ob);
      if (__builtin_expect(unsafe(ob, l), 0)) {
        ob = exact(1, __builtin_bit_cast(bf16x8, u32x4{qv[0], qv[1], qv[2], qv[3]}), __builtin_bit_cast(s16x4, u32x2{qv[4], qv[5]}),
                   qb ? qt - 8 : 0, qt);
        l = row_sum(ob);
      }
      store_o<false>(ob, l, oA + O_BR, at[9], qq[14]);
    }
    STAMP(13);
    at[13] = (__builtin_elementwise_min(qq[15], 24u * 16) + (slot * 8 + w) * (25 * 16)) * 8;
    at_a2(oab, s0, pp, fr, qq, at, ws, so, mytile, tag, flagO, woa, wo, wob + (qb * 160 + w * (32 * 1280) - 4096ll), w * 5120, (w + 8) * 5120,
          grp ? 0x80000000u : w * 5120 + 143360, of, of2);
  }
  STAMP(12);
  {
    lds_ptr xb = (lds_ptr)lds + 3 * CHUNK_B + qq[15];
#pragma unroll
    for (int i = 0; i < 3; ++i) *(lds_f4)(xb + w * 3072 + i * 1024) = f32x4{oab[16 + 4 * i], oab[17 + 4 * i], oab[18 + 4 * i], oab[19 + 4 * i]};
    asm volatile("s_setprio 3\n\ts_waitcnt vmcnt(0) lgkmcnt(0)\n\ts_barrier" : "+{v[184:215]}"(of), "+{v[216:223]}"(of2) :: "memory");
    f32x16 o = {};
#pragma unroll
    for (int i = 0; i < 3; ++i) {
      const f32x4 r = *(lds_f4)(xb + (w ^ 4) * 3072 + i * 1024);
#pragma unroll
      for (int e = 0; e < 4; ++e) o[4 * i + e] = oab[4 * i + e] + r[e];
    }
    float l = row_sum(o);
    if (__builtin_expect(unsafe(o, l), 0)) {
      o = exact(0, __builtin_bit_cast(bf16x8, u32x4{qq[0], qq[1], qq[2], qq[3]}), __builtin_bit_cast(s16x4, u32x2{qq[8], qq[9]}), 0, 63);
      l = row_sum(o);
    }
    store_o<true>(o, l, oA, at[9], qq[14]);
  }
  STAMP(14);
  {
    f32x32 p1, p2, oa; f32x8 oa2;
    const unsigned wl0 = wl & 0xffff, wl1 = wl >> 16;
    const char* ws0 = woa + (qb * 160 + (w < 5 ? w : w + 5) * (32 * 1280) - 4096ll);
    const char* ws1 = woa + (qb * 160 + (13 + (w & 1)) * (32 * 1280) - 4096ll);
    {
      const unsigned b1 = grp * 51200, b2 = grp ? 143360 : 30720;
      proj_1(p1, of, of2, oa, oa2, u32x4{wl0 + b1, wl1 + b1, wl0 + b2, wl1 + b2}, wo, ws0, ws1, 86016 + w * 5120,
             w < 2 ? 86016 + (8 + w) * 5120 : 0x80000000u, ws);
    }
    STAMP(16);
    lane = lane_id();
    proj_send(p1, lds + 143360, lw, grp, lane);
    wo_dma(ws0 + 5 * 32 * 1280, w * 5120, wo);
    if (w < 2) { wo_dma(ws1 + 5 * 32 * 1280, (8 + w) * 5120, wo); WAITV(10); } else WAITV(5);
    char* dst = ptr(kc[6], kc[7]) + ((head * 64 + lw * 16) * 2560 + qb * 160);
    STAMP(17);
    const unsigned b1 = 86016 + grp * 25600, b2 = grp * 25600;
    const u32x4 wab = {wl0 + b1, wl1 + b1, wl0 + b2, wl1 + b2};
    asm volatile("" : "={v[160:191]}"(p2));
    proj_2<0>(p2, oa, oa2, wab, wo, ws);
    STAMP(18);
    proj_finish(p1, lds + 143360, lw, grp, lane, dst + 1280);
    proj_2<5>(p2, oa, oa2, wab, wo, ws);
    STAMP(20);
    proj_send(p2, lds + 86016, lw, grp, lane);
    asm volatile("s_waitcnt lgkmcnt(0)\n\ts_barrier" ::: "memory");
    proj_finish(p2, lds + 86016, lw, grp, lane, dst);
  }
  STAMP_OUT(ws, 0);
}

extern "C" void kernel_launch(void* const* d_in, const int* in_sizes, int n_in, void* d_out, int out_size, void* d_ws,
                              size_t ws_size, hipStream_t stream) {
  k_fused<<<256, 512, 0, stream>>>((const char*)d_in[0], (const char*)d_in[1], (const char*)d_in[2], (const char*)d_in[3],
                                   (const char*)d_in[4], (const char*)d_in[5], (char*)d_ws, (char*)d_out);
}
```

```cpp
#include <hip/hip_runtime.h>
#include <hip/hip_bf16.h>
#include <stdint.h>
#include <type_traits>

typedef __attribute__((ext_vector_type(8))) __bf16 bf16x8;
typedef __attribute__((ext_vector_type(16))) __bf16 bf16x16;
typedef __attribute__((ext_vector_type(4))) short s16x4;
typedef __attribute__((ext_vector_type(8))) short s16x8;
typedef __attribute__((ext_vector_type(2))) unsigned u32x2;
typedef __attribute__((ext_vector_type(4))) unsigned u32x4;
typedef __attribute__((ext_vector_type(4))) float f32x4;
typedef __attribute__((ext_vector_type(8))) float f32x8;
typedef __attribute__((ext_vector_type(16))) float f32x16;
typedef __attribute__((ext_vector_type(32))) float f32x32;
typedef __attribute__((address_space(3))) const char* lds_cptr;
typedef __attribute__((address_space(3))) char* lds_ptr;

constexpr size_t QK_WS = 0, QK_BR = (size_t)160 * 32768;
constexpr size_t V_WS = 2 * QK_BR, V_BR = (size_t)8 * 256 * 1280;
constexpr size_t O_WS = V_WS + 2 * V_BR, O_BR = (size_t)80 * 32768;
constexpr size_t CTL_WS = O_WS + 2 * O_BR;
constexpr unsigned FQ_OFF = CTL_WS + 4096, FO_OFF = FQ_OFF + 2 * 30 * 64 * 128;
constexpr float QSCALE = 0.22360679774997896f * 1.4426950408889634f;

#define STAMP_DECL
#define STV_PUT(x)
#define STV_GET(x)
#define STAMP(i) do {} while (0)
#define STAMP_OUT(ws_, kid) do {} while (0)
__device__ __forceinline__ int lane_id() {
  int l; asm volatile("v_mbcnt_lo_u32_b32 %0, -1, 0\n\tv_mbcnt_hi_u32_b32 %0, -1, %0" : "=v"(l)); return l;
}
#define SBAR() __builtin_amdgcn_sched_barrier(0)
#define PIN(x) asm volatile("" : "+v"(x))
#define PINS(x) asm volatile("" : "+s"(x))
#define WAITV(N) asm volatile("s_waitcnt vmcnt(" #N ")" ::: "memory")
#define BARRIER() asm volatile("s_barrier" ::: "memory")
#define WAITV_BAR(N) asm volatile("s_waitcnt vmcnt(" #N ") lgkmcnt(0)\n\ts_barrier" ::: "memory")

__device__ __forceinline__ void glds16(const void* sbase, unsigned voff, unsigned lds) {
  unsigned keep;
  asm volatile("s_mov_b32 %0, m0\n\ts_mov_b32 m0, %3\n\ts_nop 0\n\tglobal_load_lds_dwordx4 %1, %2\n\ts_mov_b32 m0, %0"
               : "=&s"(keep) : "v"(voff), "s"(sbase), "s"(lds) : "memory");
}
__device__ __forceinline__ void glds16x4(const void* sbase, const unsigned (&voff)[4], unsigned lds) {
  unsigned keep;
  asm volatile("s_mov_b32 %0, m0\n\ts_mov_b32 m0, %6\n\ts_nop 0\n\tglobal_load_lds_dwordx4 %1, %5\n\t"
               "global_load_lds_dwordx4 %2, %5 offset:1024\n\tglobal_load_lds_dwordx4 %3, %5 offset:2048\n\t"
               "global_load_lds_dwordx4 %4, %5 offset:3072\n\ts_mov_b32 m0, %0"
               : "=&s"(keep) : "v"(voff[0]), "v"(voff[1]), "v"(voff[2]), "v"(voff[3]), "s"(sbase), "s"(lds) : "memory");
}
__device__ __forceinline__ unsigned pk_bf16(float lo, float hi) {
  typedef __attribute__((ext_vector_type(2))) float f32x2_;
  typedef __attribute__((ext_vector_type(2))) __bf16 bf16x2_;
  const f32x2_ v = {lo, hi};
  return __builtin_bit_cast(unsigned, __builtin_convertvector(v, bf16x2_));
}
__device__ __forceinline__ void st16(char* sbase, unsigned voff, u32x4 v) {
  asm volatile("global_store_dwordx4 %0, %1, %2 sc1\n\ts_nop 1" :: "v"(voff), "v"(v), "s"(sbase) : "memory");
}
__device__ __forceinline__ void st8lo(char* sbase, unsigned voff, u32x2 v) {
  asm volatile("s_mov_b32 exec_hi, 0\n\tglobal_store_dwordx2 %0, %1, %2 sc1\n\ts_mov_b32 exec_hi, -1" :: "v"(voff), "v"(v), "s"(sbase) : "memory");
}
__device__ __forceinline__ bf16x8 lds_b128(lds_cptr p) { return *(const __attribute__((address_space(3))) bf16x8*)p; }
__device__ __forceinline__ s16x4 lds_b64(lds_cptr p) { return *(const __attribute__((address_space(3))) s16x4*)p; }
__device__ __forceinline__ s16x4 lds_tr(lds_cptr p) {
  return __builtin_amdgcn_ds_read_tr16_b64_v4i16((__attribute__((address_space(3))) s16x4*)p);
}
__device__ __forceinline__ bf16x8 cat8(s16x4 a, s16x4 b) {
  s16x8 v = {a[0], a[1], a[2], a[3], b[0], b[1], b[2], b[3]};
  return __builtin_bit_cast(bf16x8, v);
}
#define MFMA32(a, b, c) __builtin_amdgcn_mfma_f32_32x32x16_bf16(a, b, c, 0, 0, 0)
#define MFMA32K8(a, b, c) __builtin_amdgcn_mfma_f32_32x32x8bf16_1k(a, b, c, 0, 0, 0)
#define MFMA16(a, b, c) __builtin_amdgcn_mfma_f32_16x16x32_bf16(a, b, c, 0, 0, 0)

typedef __attribute__((ext_vector_type(8))) unsigned u32x8;
typedef __attribute__((ext_vector_type(16))) unsigned u32x16;
#define AWAIT(d, d1, off) \
    "s_mov_b32 s48, 0x200000\n\t5:\n\ts_waitcnt vmcnt(0)\n\tv_cmp_ne_u64 vcc, s[50:51], v[" #d ":" #d1 "]\n\ts_sub_u32 s48, s48, 1\n\t" \
    "s_cbranch_vccz 4f\n\ts_cbranch_scc1 4f\n\ts_sleep 2\n\tglobal_load_dwordx2 v[" #d ":" #d1 "], v" #off ", s[54:55] sc1\n\ts_branch 5b\n\t4:\n\t"
#define G1_STAMP(i)
#define NPOLL 22
#define NFLAG 6
#define NAW 28
#define KBAR 11
__device__ __forceinline__ void g1_all(unsigned vt1, unsigned kv1, unsigned vt2, u32x16& ad, u32x16& pk, u32x16& at, u32x4& cv4,
                                       const char* sA, unsigned off1, unsigned str1, const char* sB, unsigned off2, unsigned m0v,
                                       unsigned long long tag, unsigned flag, char* ws, unsigned tile, u32x4 so, unsigned qoff) {
  unsigned keep;
  f32x32 d0, d1, d2, d3, d4; f32x16 d5, d6; f32x8 d7; f32x4 d8;
  asm volatile(
      ".macro pack vt\n\t.set i_, 0\n\t.rept 4\n\t"
      ".if \\vt\n\t.set e_, 8 * i_\n\t.set f_, 8 * i_ + 4\n\t.else\n\t.set e_, 4 * i_\n\t.set f_, 4 * i_ + 16\n\t.endif\n\t"
      "v_cvt_pk_bf16_f32 v[48+4*i_], v[e_], v[e_+1]\n\tv_cvt_pk_bf16_f32 v[49+4*i_], v[e_+2], v[e_+3]\n\t"
      "v_cvt_pk_bf16_f32 v[50+4*i_], v[f_], v[f_+1]\n\tv_cvt_pk_bf16_f32 v[51+4*i_], v[f_+2], v[f_+3]\n\t"
      ".set i_, i_ + 1\n\t.endr\n\t.set i_, 0\n\t.rept 4\n\t"
      "v_permlane16_swap_b32 v[48+4*i_], v[50+4*i_]\n\tv_permlane16_swap_b32 v[49+4*i_], v[51+4*i_]\n\t"
      ".set i_, i_ + 1\n\t.endr\n\t.endm\n\t"
      ".macro mm vt, c, x, w\n\t"
      ".if (\\x) < 104\n\t"
      ".if \\vt\n\tv_mfma_f32_16x16x32_bf16 v[\\c:\\c+3], v[\\x:\\x+3], v[\\w:\\w+3], 0\n\t"
      ".else\n\tv_mfma_f32_16x16x32_bf16 v[\\c:\\c+3], v[\\w:\\w+3], v[\\x:\\x+3], 0\n\t.endif\n\t"
      ".elseif \\vt\n\tv_mfma_f32_16x16x32_bf16 v[\\c:\\c+3], v[\\x:\\x+3], v[\\w:\\w+3], v[\\c:\\c+3]\n\t"
      ".else\n\tv_mfma_f32_16x16x32_bf16 v[\\c:\\c+3], v[\\w:\\w+3], v[\\x:\\x+3], v[\\c:\\c+3]\n\t.endif\n\t"
      ".endm\n\t"
      ".macro rx d, a, off\n\tds_read_b128 v[\\d:\\d+3], v\\a offset:\\off\n\tds_read_b128 v[\\d+4:\\d+7], v\\a offset:\\off+2048\n\t.endm\n\t"
      ".macro rw d, a, off\n\tds_read_b64_tr_b16 v[\\d:\\d+1], v\\a offset:\\off\n\tds_read_b64_tr_b16 v[\\d+2:\\d+3], v\\a offset:\\off+512\n\t.endm\n\t"
      ".macro bump lo, n, val\n\t.set bi_, \\lo\n\t.rept \\n\n\tv_add_u32 v[bi_], \\val, v[bi_]\n\t.set bi_, bi_+1\n\t.endr\n\t.endm\n\t"
      ".macro src2\n\ts_add_u32 s40, s44, s47\n\ts_addc_u32 s41, s45, 0\n\t"
      "s_add_u32 s47, s47, 2*64*3840\n\ts_cmp_ge_u32 s47, 640*3840\n\ts_cselect_b32 s48, 640*3840, 0\n\ts_sub_u32 s47, s47, s48\n\t.endm\n\t"
      ".macro p1body vt\n\t"
      ".set st_, 0\n\t"
      ".rept 10\n\t"
      ".set xr_, 96 + 16 * st_\n\t"
      ".set o_, ((st_ %% 5) & 1) * 32768\n\t"
      ".set n_, (((st_ + 1) %% 5) & 1) * 32768 + 16384\n\t.if st_ == 9\n\t.set n_, 0\n\t.endif\n\t"
      "s_waitcnt lgkmcnt(6)\n\t"
      "mm \\vt, 0, xr_, 32\n\trw 48, 76, o_+16384+8192\n\t"
      "mm \\vt, 16, xr_+4, 32\n\trx xr_+8, 74, o_\n\ts_waitcnt lgkmcnt(8)\n\t"
      "mm \\vt, 4, xr_, 36\n\trw 52, 77, o_+16384+8192\n\t"
      "mm \\vt, 20, xr_+4, 36\n\trw 56, 78, o_+16384+8192\n\ts_waitcnt lgkmcnt(10)\n\t"
      "mm \\vt, 8, xr_, 40\n\trw 60, 79, o_+16384+8192\n\t"
      "mm \\vt, 24, xr_+4, 40\n\ts_waitcnt lgkmcnt(10)\n\t"
      "mm \\vt, 12, xr_, 44\n\t"
      ".if st_ < 7\n\t"
      "s_add_u32 s40, s42, s36\n\ts_addc_u32 s41, s43, 0\n\ts_add_u32 s36, s36, s37\n\ts_cmp_eq_u32 s36, s38\n\ts_cselect_b32 s36, 0, s36\n\t"
      ".else\n\tsrc2\n\t.endif\n\t"
      "mm \\vt, 28, xr_+4, 44\n\t"
      "s_add_u32 m0, s46, ((st_ + 3) %% 5) * 32768\n\t"
      ".if (st_ %% 5) == 1 || (st_ %% 5) == 3\n\tbump 76, 4, 0x10000\n\tbump 73, 2, 0x10000\n\t.endif\n\t"
      ".if (st_ %% 5) == 4\n\tbump 76, 4, 0xfffe0000\n\t.if st_ < 9\n\tbump 73, 2, 0xfffe0000\n\t.endif\n\t.endif\n\t"
      "s_waitcnt lgkmcnt(6)\n\t"
      "mm \\vt, 0, xr_+8, 48\n\t"
      "mm \\vt, 16, xr_+12, 48\n\t"
      "s_waitcnt vmcnt(4) lgkmcnt(0)\n\ts_barrier\n\t"
      ".set vo_, 64 + 4 * ((st_ + 3) / 10)\n\t"
      "mm \\vt, 4, xr_+8, 52\n\trw 32, 76, n_\n\t"
      "global_load_lds_dwordx4 v[vo_], s[40:41]\n\t"
      "mm \\vt, 20, xr_+12, 52\n\t"
      ".if st_ < 9\n\trx xr_+16, 73, n_-16384\n\t.endif\n\t"
      "global_load_lds_dwordx4 v[vo_+1], s[40:41] offset:1024\n\t"
      "mm \\vt, 8, xr_+8, 56\n\trw 36, 77, n_\n\t"
      "global_load_lds_dwordx4 v[vo_+2], s[40:41] offset:2048\n\t"
      "mm \\vt, 24, xr_+12, 56\n\trw 40, 78, n_\n\t"
      "global_load_lds_dwordx4 v[vo_+3], s[40:41] offset:3072\n\t"
      "mm \\vt, 12, xr_+8, 60\n\trw 44, 79, n_\n\t"
      "mm \\vt, 28, xr_+12, 60\n\t"
      ".set st_, st_ + 1\n\t"
      ".endr\n\t"
      "v_mov_b64 v[64:65], v[80:81]\n\tv_add_u32 v66, 131072 - 128512 * \\vt, v80\n\tv_add_u32 v67, 131072 - 128512 * \\vt, v81\n\t"
      ".endm\n\t"
      ".macro tdma i\n\t"
      "s_add_u32 m0, s72, \\i * 28672\n\ts_add_u32 s40, s54, s[76+2*\\i]\n\ts_addc_u32 s41, s55, 0\n\t"
      "global_load_lds_dwordx4 v137, s[40:41] sc1\n\ts_mov_b32 exec_hi, 0\n\tglobal_load_lds_dwordx4 v138, s[40:41] offset:1024 sc1\n\t"
      "s_add_u32 s40, s54, s[77+2*\\i]\n\ts_addc_u32 s41, s55, 0\n\ts_mov_b32 exec_lo, 0xfffff\n\ts_mov_b32 exec_hi, 0xfffff\n\t"
      "global_load_lds_dwordx4 v139, s[40:41] offset:1536 sc1\n\tglobal_load_lds_dwordx4 v140, s[40:41] offset:2560 sc1\n\ts_mov_b64 exec, -1\n\t"
      ".endm\n\t"
      ".macro slot n\n\t.set n_, \\n\n\t"
      ".if n_ >= 2 && n_ < 10\n\t"
      ".if ((n_ - 2) & 3) == 0\n\ts_add_u32 m0, s46, (3 + (n_ - 2) / 4) * 32768\n\tsrc2\n\t.endif\n\t"
      "global_load_lds_dwordx4 v[68+((n_-2)&3)], s[40:41] offset:1024*((n_-2)&3)\n\t"
      ".endif\n\t"
      ".if n_ == %c[nflag]\n\tv_mov_b32 v96, s50\n\tv_mov_b32 v97, s51\n\tv_mov_b32 v98, s52\n\ts_mov_b64 exec, 1\n\ts_waitcnt vmcnt(%c[nflag] - 1)\n\t"
      "global_store_dwordx2 v98, v[96:97], s[54:55] sc1\n\ts_mov_b64 exec, -1\n\t.endif\n\t"
      ".if n_ == 12\n\tv_mov_b64 v[128:129], v[84:85]\n\tv_mov_b32 v130, v86\n\tv_add_u32 v131, 57344, v84\n\tv_add_u32 v132, 57344, v85\n\t.endif\n\t"
      ".if n_ == 13\n\tv_add_u32 v133, 57344, v86\n\tv_add_u32 v134, 114688, v84\n\tv_add_u32 v135, 114688, v85\n\tv_add_u32 v136, 114688, v86\n\t.endif\n\t"
      ".if n_ == 14\n\tv_mov_b32 v137, v87\n\tv_mov_b64 v[138:139], v[88:89]\n\tv_add_u32 v140, 1536, v89\n\tv_mov_b32 v141, v90\n\t.endif\n\t"
      ".if n_ == 15\n\tv_mov_b32 v104, v92\n\tv_mov_b32 v105, v92\n\tv_mov_b32 v106, v92\n\tv_mov_b32 v107, v92\n\t.endif\n\t"
      ".if n_ == %c[naw] + 3\n\tds_write_b128 v91, v[104:107]\n\tds_write_b128 v91, v[104:107] offset:28672\n\t.endif\n\t"
      ".if n_ == %c[npoll]\n\tglobal_load_dwordx2 v[100:101], v72, s[54:55] sc1\n\t.endif\n\t"
      ".if n_ == %c[naw]\n\t" AWAIT(100, 101, 72) G1_STAMP(6) "tdma 0\n\t.endif\n\t"
      ".if n_ == %c[naw] + 1\n\ts_add_u32 s40, s54, s80\n\ts_addc_u32 s41, s55, 0\n\ts_xor_b32 s48, s80, 512\n\t"
      "global_load_dwordx4 v[64:67], v137, s[40:41] sc1\n\tglobal_load_dwordx2 v[72:73], v93, s[40:41] sc1\n\t"
      "s_add_u32 s40, s54, s48\n\ts_addc_u32 s41, s55, 0\n\t"
      "global_load_dwordx4 v[68:71], v137, s[40:41] sc1\n\tglobal_load_dwordx2 v[74:75], v93, s[40:41] sc1\n\t.endif\n\t"
      ".if n_ == %c[naw] + 2\n\ttdma 1\n\t.endif\n\t"
      ".endm\n\t"
      ".macro st1 i\n\t.if k_ == 0\n\tglobal_store_dwordx4 v[64+\\i], v[48+4*\\i:51+4*\\i], s[54:55] sc1\n\t.endif\n\t.endm\n\t"
      ".macro p2body vt\n\t"
      ".set k_, 0\n\t"
      ".rept 20\n\t"
      ".set xr_, 96 + 8 * k_\n\t"
      ".set c_, 32 + 16 * (k_ & 1)\n\t"
      ".set d_, 48 - 16 * (k_ & 1)\n\t"
      ".set o_, ((k_ + 1) & 7) * 8192\n\t"
      ".if k_ == 3\n\ts_waitcnt vmcnt(8)\n\ts_barrier\n\t.endif\n\t"
      ".if k_ == %c[kbar]\n\ts_waitcnt vmcnt(0)\n\ts_barrier\n\t.endif\n\t"
      ".if \\vt == 2\n\tst1 0\n\tst1 1\n\tst1 2\n\tst1 3\n\tslot 2*k_\n\tslot 2*k_+1\n\t"
      ".elseif k_ < 19\n\t"
      ".if ((k_ + 1) & 7) == 0\n\tbump 76, 4, 0x10000\n\t.endif\n\t"
      "s_waitcnt lgkmcnt(6)\n\t"
      "mm \\vt, 0, xr_, c_\n\tst1 0\n\trw d_, 76, o_\n\t"
      "mm \\vt, 16, xr_+4, c_\n\tslot 2*k_\n\ts_waitcnt lgkmcnt(6)\n\t"
      "mm \\vt, 4, xr_, c_+4\n\tst1 1\n\trw d_+4, 77, o_\n\t"
      "mm \\vt, 20, xr_+4, c_+4\n\ts_waitcnt lgkmcnt(6)\n\t"
      "mm \\vt, 8, xr_, c_+8\n\tst1 2\n\trw d_+8, 78, o_\n\t"
      "mm \\vt, 24, xr_+4, c_+8\n\tslot 2*k_+1\n\ts_waitcnt lgkmcnt(6)\n\t"
      "mm \\vt, 12, xr_, c_+12\n\tst1 3\n\trw d_+12, 79, o_\n\t"
      "mm \\vt, 28, xr_+4, c_+12\n\t"
      ".else\n\t"
      "s_waitcnt lgkmcnt(0)\n\t"
      "mm \\vt, 0, xr_, c_\n\tmm \\vt, 16, xr_+4, c_\n\tmm \\vt, 4, xr_, c_+4\n\tmm \\vt, 20, xr_+4, c_+4\n\t"
      "mm \\vt, 8, xr_, c_+8\n\tmm \\vt, 24, xr_+4, c_+8\n\tmm \\vt, 12, xr_, c_+12\n\tmm \\vt, 28, xr_+4, c_+12\n\t"
      "s_setprio 3\n\tv_add_u32 v80, 131072 - 128512 * \\vt, v82\n\tv_add_u32 v81, 131072 - 128512 * \\vt, v83\n\ts_nop 4\n\tpack \\vt\n\t"
      ".endif\n\t"
      ".set k_, k_ + 1\n\t"
      ".endr\n\t"
      ".endm\n\t"
      "rw 32, 76, 16384\n\trx 96, 73, 0\n\trw 36, 77, 16384\n\trw 40, 78, 16384\n\trw 44, 79, 16384\n\t"
      "s_mov_b32 %[keep], m0\n\t"
      "s_cmp_lg_u32 %[vt1], 0\n\ts_cbranch_scc1 1f\n\t"
      "p1body 0\n\t"
      "s_mov_b32 s40, %[sc]\n\ts_cmp_lg_u32 %[kv1], 0\n\ts_nop 2\n\ts_cbranch_scc1 3f\n\t"
      ".set i_, 0\n\t.rept 16\n\tv_pk_mul_f32 v[i_:i_+1], v[i_:i_+1], s[40:41] op_sel_hi:[1,0]\n\t.set i_, i_ + 2\n\t.endr\n\t"
      "3:\n\tpack 0\n\ts_branch 2f\n\t"
      "1:\n\tp1body 1\n\ts_nop 4\n\tpack 1\n\t"
      "2:\n\t" G1_STAMP(3)
      "s_cmp_eq_u32 %[vt2], 1\n\ts_cbranch_scc1 1f\n\ts_cmp_eq_u32 %[vt2], 2\n\ts_cbranch_scc0 6f\n\t"
      "p2body 2\n\ts_waitcnt vmcnt(0)\n\ts_setprio 3\n\ts_branch 3f\n\t"
      "6:\n\tp2body 0\n\ts_branch 2f\n\t"
      "1:\n\tp2body 1\n\t"
      "2:\n\tglobal_store_dwordx4 v82, v[48:51], s[54:55] sc1\n\tglobal_store_dwordx4 v83, v[52:55], s[54:55] sc1\n\t"
      "global_store_dwordx4 v80, v[56:59], s[54:55] sc1\n\tglobal_store_dwordx4 v81, v[60:63], s[54:55] sc1\n\t"
      "3:\n\ts_mov_b32 m0, %[keep]\n\tv_mov_b32 v76, 0xdddd\n\tv_mov_b32 v77, v94\n\tv_mov_b32 v78, v93\n\t"
      "v_mbcnt_lo_u32_b32 v79, -1, 0\n\tv_mbcnt_hi_u32_b32 v79, -1, v79\n\tv_lshlrev_b32 v79, 4, v79\n\t"
      "s_waitcnt vmcnt(8) lgkmcnt(0)\n\t"
      ".purgem pack\n\t.purgem mm\n\t.purgem rx\n\t.purgem rw\n\t.purgem bump\n\t.purgem src2\n\t.purgem p1body\n\t.purgem tdma\n\t"
      ".purgem slot\n\t.purgem st1\n\t.purgem p2body\n\t"
      : "+{v[64:79]}"(ad), "+{v[80:95]}"(pk), "=&{v[128:143]}"(at), "=&{v[104:107]}"(cv4), [keep] "=&s"(keep), "+{s36}"(off1), "+{s47}"(off2),
        "=&{v[0:31]}"(d0), "=&{v[32:63]}"(d1), "=&{v[160:191]}"(d2), "=&{v[192:223]}"(d3), "=&{v[224:255]}"(d4), "=&{v[112:127]}"(d5),
        "=&{v[144:159]}"(d6), "=&{v[96:103]}"(d7), "=&{v[108:111]}"(d8)
      : [vt1] "s"(vt1), [kv1] "s"(kv1), [vt2] "s"(vt2), "{s[42:43]}"(sA), "{s37}"(str1), "{s38}"(10 * str1), "{s[44:45]}"(sB), "{s46}"(m0v),
        "{s[50:51]}"(tag), "{s52}"(flag), "{s[54:55]}"(ws), "{s72}"(tile), "{s[76:79]}"(so), "{s80}"(qoff),
        [sc] "n"(__builtin_bit_cast(unsigned, QSCALE)), [npoll] "n"(NPOLL), [nflag] "n"(NFLAG), [naw] "n"(NAW), [kbar] "n"(KBAR)
      : "memory", "scc", "vcc", "s40", "s41", "s48");
}

__device__ __forceinline__ u32x2 g1_offs(unsigned br, unsigned cg, unsigned T, unsigned lane) {
  const unsigned l16 = (lane & 31) * 16, h = lane >> 5;
  const unsigned a = h * 32768 + l16 + (br * (unsigned)QK_BR + cg * (8 * 32768) + T * 512);
  const unsigned G = (cg - 20) * 4 + ((lane >> 4) & 1);
  const unsigned b = h * 1280 + l16 + ((unsigned)V_WS + br * (unsigned)V_BR + (cg - 20) * 1024 + T * 5120);
  return cg < 20 ? u32x2{a, a + 65536}
                 : u32x2{((G * 52) >> 8) * (256 * 1280 - 1280) + b, (((G + 2) * 52) >> 8) * (256 * 1280 - 1280) + b + 512};
}

constexpr int TILE_B = 3584, K2_OFF = 1024, V_OFF = 1536;
constexpr int CHUNK_B = 8 * TILE_B;

__device__ __forceinline__ f32x16 smfmac(bf16x8 a, f32x8 p, f32x16 c) {
  return __builtin_amdgcn_smfmac_f32_32x32x32_bf16(a, __builtin_bit_cast(bf16x16, p), c, 0xDDDD, 0, 0);
}
__device__ __forceinline__ f32x8 lo8(const f32x16& s) { return __builtin_shufflevector(s, s, 0, 1, 2, 3, 4, 5, 6, 7); }
__device__ __forceinline__ f32x8 hi8(const f32x16& s) { return __builtin_shufflevector(s, s, 8, 9, 10, 11, 12, 13, 14, 15); }
#define MX3(a, b, c) __builtin_fmaxf(__builtin_fmaxf((a), (b)), (c))
__device__ __forceinline__ float max16(const f32x16& s, float m) {
#pragma unroll
  for (int r = 0; r < 16; r += 2) m = MX3(m, s[r], s[r + 1]);
  return m;
}
__device__ __forceinline__ float xhalf_max(float m) {
  auto rr = __builtin_amdgcn_permlane32_swap(__float_as_uint(m), __float_as_uint(m), false, false);
  return __builtin_fmaxf(__uint_as_float(rr[0]), __uint_as_float(rr[1]));
}
__device__ __forceinline__ float row_sum(const f32x16& o) {
  auto rr = __builtin_amdgcn_permlane32_swap(__float_as_uint(o[8]), __float_as_uint(o[8]), false, false);
  return __uint_as_float(rr[1]);
}
template <bool MARK>
__device__ __forceinline__ void store_o(const f32x16& o, float l, char* base, unsigned lo1, unsigned lo2) {
  const float rl = __builtin_amdgcn_rcpf(l);
  const unsigned a0 = pk_bf16(o[0] * rl, o[1] * rl), a1 = pk_bf16(o[2] * rl, o[3] * rl);
  const unsigned b0 = pk_bf16(o[4] * rl, o[5] * rl), b1 = pk_bf16(o[6] * rl, o[7] * rl);
  const auto r0 = __builtin_amdgcn_permlane32_swap(a0, b0, false, false);
  const auto r1 = __builtin_amdgcn_permlane32_swap(a1, b1, false, false);
  u32x4 v = {r0[0], r1[0], r0[1], r1[1]};
  u32x2 c = {pk_bf16(o[8] * rl, o[9] * rl), pk_bf16(o[10] * rl, o[11] * rl)};
  if (MARK) {
    v = __builtin_elementwise_min(v, u32x4{~1u, ~1u, ~1u, ~1u}); c = __builtin_elementwise_min(c, u32x2{~1u, ~1u});
  }
  st16(base, lo1, v);
  st8lo(base, lo2, c);
}

__device__ __forceinline__ void poll_flags(const char* ws, unsigned off, unsigned long long tag) {
  asm volatile("global_load_dwordx2 v[124:125], v126, s[54:55] sc1\n\t" AWAIT(124, 125, 126)
               :: "{v126}"(off), "{s[54:55]}"(ws), "{s[50:51]}"(tag) : "memory", "scc", "vcc", "s48", "v124", "v125");
}
__device__ __forceinline__ void put_flag(char* ws, unsigned off, unsigned long long tag) {
  asm volatile("s_mov_b64 exec, 1\n\tglobal_store_dwordx2 %0, %1, %2 sc1\n\ts_mov_b64 exec, -1" :: "v"(off), "v"(tag), "s"(ws) : "memory");
}
__device__ __forceinline__ void wo_dma(const char* src, unsigned d, const u32x8& wo) {
  unsigned keep;
  asm volatile("s_mov_b32 %0, m0\n\ts_mov_b32 m0, %7\n\ts_nop 0\n\t"
               "global_load_lds_dwordx4 %1, %6\n\tglobal_load_lds_dwordx4 %2, %6 offset:1024\n\t"
               "global_load_lds_dwordx4 %3, %6 offset:2048\n\tglobal_load_lds_dwordx4 %4, %6 offset:3072\n\t"
               "s_add_u32 m0, m0, 4096\n\ts_nop 0\n\tglobal_load_lds_dwordx4 %5, %6\n\ts_mov_b32 m0, %0"
               : "=&s"(keep) : "v"(wo[0]), "v"(wo[1]), "v"(wo[2]), "v"(wo[3]), "v"(wo[4]), "s"(src), "s"(d) : "memory", "scc");
}

#define AT_ASM_MACROS \
    ".macro mx d, s\n\tv_max3_f32 v[\\d], v[\\s], v[\\s+1], v[\\s+2]\n\t.set i_, \\s + 3\n\t.rept 6\n\tv_max3_f32 v[\\d], v[\\d], v[i_], v[i_+1]\n\t" \
    ".set i_, i_ + 2\n\t.endr\n\tv_max_f32 v[\\d], v[\\d], v[\\s+15]\n\t.endm\n\t" \
    ".macro xp d, s\n\tv_exp_f32 v[\\d], v[\\s]\n\tv_exp_f32 v[\\d+1], v[\\s+1]\n\tv_exp_f32 v[\\d+2], v[\\s+2]\n\tv_exp_f32 v[\\d+3], v[\\s+3]\n\t.endm\n\t" \
    ".macro smf o, v, p\n\tv_smfmac_f32_32x32x32_bf16 v[\\o:\\o+15], v[\\v:\\v+3], v[\\p:\\p+7], v76\n\t.endm\n\t" \
    ".macro qk1 s, k, q\n\tv_mfma_f32_32x32x16_bf16 v[\\s:\\s+15], v[\\k:\\k+3], v[\\q:\\q+3], 0\n\t.endm\n\t" \
    ".macro qk1c s, k, q, c\n\tv_mfma_f32_32x32x16_bf16 v[\\s:\\s+15], v[\\k:\\k+3], v[\\q:\\q+3], v[\\c:\\c+15]\n\t.endm\n\t" \
    ".macro qk2 s, k, q\n\tv_mfma_f32_32x32x8_bf16 v[\\s:\\s+15], v[\\k:\\k+1], v[\\q:\\q+1], v[\\s:\\s+15]\n\t.endm\n\t"
#define AT_ASM_PURGE ".purgem mx\n\t.purgem xp\n\t.purgem smf\n\t.purgem qk1\n\t.purgem qk1c\n\t.purgem qk2\n\t"

#define AT_A_TEXT AT_ASM_MACROS \
      ".macro pos j\n\t.set sl_, (\\j) >> 2\n\t.if sl_ >= 3\n\t.set sl_, sl_ - 3\n\t.endif\n\t" \
      ".set ar_, 128 + 3 * (sl_ >> 1)\n\t.set of_, (sl_ & 1) * 28672 + ((\\j) & 3) * 7168\n\t.endm\n\t" \
      ".macro dma part\n\t" \
      ".if di_ >= 0\n\t" \
      ".if \\part == 0\n\t" \
      ".if di_ == 1\n\ts_mov_b32 m0, s73\n\t.else\n\ts_add_u32 m0, s72, ((di_ + 2) %% 5) * 28672\n\t.endif\n\t" \
      "s_add_u32 s40, s54, s[56+2*di_]\n\ts_addc_u32 s41, s55, 0\n\t" \
      "global_load_lds_dwordx4 v137, s[40:41] sc1\n\t" \
      ".elseif \\part == 1\n\t" \
      "s_mov_b32 exec_hi, 0\n\tglobal_load_lds_dwordx4 v138, s[40:41] offset:1024 sc1\n\ts_mov_b32 exec_hi, -1\n\t" \
      "s_add_u32 s40, s54, s[57+2*di_]\n\ts_addc_u32 s41, s55, 0\n\t" \
      ".else\n\t" \
      "s_mov_b32 exec_lo, 0xfffff\n\ts_mov_b32 exec_hi, 0xfffff\n\t" \
      "global_load_lds_dwordx4 v139, s[40:41] offset:1536 sc1\n\tglobal_load_lds_dwordx4 v140, s[40:41] offset:2560 sc1\n\t" \
      "s_mov_b64 exec, -1\n\t" \
      ".endif\n\t.endif\n\t" \
        \
      ".set n_, -1\n\t" \
      ".if j_ >= 22 && j_ < 26 && 3 * (j_ - 22) + \\part < 10\n\t.set n_, 3 * (j_ - 22) + \\part\n\t.set bk_, 2 * (n_ / 5)\n\t.endif\n\t" \
      ".if j_ >= 28 && 3 * (j_ - 28) + \\part < 5\n\t.set n_, 3 * (j_ - 28) + \\part\n\t.set bk_, 1\n\t.endif\n\t" \
      ".if n_ >= 0\n\t.set pc_, n_ %% 5\n\t" \
      ".if bk_ == 2\n\ts_cmp_lt_i32 s46, 0\n\ts_cbranch_scc1 9f\n\t.endif\n\t" \
      ".if pc_ == 0\n\ts_mov_b32 m0, s[44+bk_]\n\ts_add_u32 s40, s42, bk_ * 8 * 32 * 1280\n\ts_addc_u32 s41, s43, 0\n\t.endif\n\t" \
      ".if pc_ == 4\n\ts_add_u32 m0, m0, 4096\n\ts_nop 0\n\t.endif\n\t" \
      "global_load_lds_dwordx4 v[248+pc_], s[40:41] offset:1024*(pc_&3)\n\t" \
      ".if bk_ == 2\n\t9:\n\t.endif\n\t.endif\n\t" \
      ".endm\n\t" \
      ".macro flg msk\n\tv_mov_b32 v142, s50\n\tv_mov_b32 v143, s51\n\tv_mov_b32 v108, s52\n\ts_mov_b64 exec, \\msk\n\ts_cbranch_execz 8f\n\ts_waitcnt vmcnt(0)\n\t" \
      "global_store_dwordx2 v108, v[142:143], s[54:55] sc1\n\t8:\n\ts_mov_b64 exec, -1\n\t.endm\n\t" \
      ".macro tabl d, r\n\tglobal_load_dwordx4 v[\\d:\\d+3], v79, s[40:41] offset:1024*(\\r)\n\t.endm\n\t" \
      ".macro old q\n\t.if (\\q) >= 0 && (\\q) < 10\n\tglobal_load_dwordx4 v[184+4*(\\q):187+4*(\\q)], v108, s[54:55] sc1\n\tv_add_u32 v108, 0x20000, v108\n\t.endif\n\t.endm\n\t" \
      "s_mov_b32 %[keep], m0\n\t" \
      ".set j_, %c[j0]\n\t" \
      ".rept %c[j1] - %c[j0]\n\t" \
      ".set a_, 80 + 14 * (j_ & 1)\n\t.set b_, 94 - 14 * (j_ & 1)\n\t" \
      ".if j_ == 4\n\t" AWAIT(142, 143, 141) ".set di_, 0\n\tdma 0\n\tdma 1\n\tdma 2\n\t.endif\n\t" \
      ".if j_ == 6\n\ts_waitcnt vmcnt(10)\n\ts_barrier\n\t.endif\n\t" \
      ".if j_ == 10 || j_ == 14 || j_ == 22\n\ts_waitcnt vmcnt(0)\n\ts_barrier\n\t.endif\n\t" \
      ".if j_ == 18\n\ts_waitcnt vmcnt(1)\n\ts_barrier\n\t.endif\n\t" \
      ".if j_ == 28 || j_ == 2\n\ts_barrier\n\t.endif\n\t" \
      AT_STAMP \
      ".if j_ < 2\n\ts_setprio 1 - j_\n\t" \
      ".elseif j_ == 10 || j_ == 11\n\ts_setprio 3\n\t.elseif j_ == 12 || j_ == 13\n\ts_setprio 0\n\t" \
      ".elseif j_ < 22\n\ts_setprio 3 - ((j_ + 2) & 3)\n\t.elseif j_ >= 28\n\ts_setprio 31 - j_\n\t" \
      ".elseif j_ != 23 && j_ != 25\n\ts_setprio 3 - (j_ - 21) / 2\n\t.endif\n\t" \
      ".if j_ == 14\n\tflg 1\n\t" \
      "v_mov_b32 v108, 0x3f803f80\n\tv_mov_b32 v109, v108\n\tv_mov_b32 v110, v108\n\tv_mov_b32 v111, v108\n\ts_mov_b64 exec, 15\n\t" \
      "ds_write_b128 v77, v[108:111]\n\ts_mov_b64 exec, -1\n\t.endif\n\t" \
      ".if j_ == 28\n\t" AWAIT(142, 143, 253) "v_add_u32 v108, %c[obr], v254\n\t.endif\n\t" \
      ".set di_, -1\n\t" \
      ".if j_ == 4 || j_ == 5 || j_ == 6\n\t.set di_, j_ - 3\n\t.endif\n\t.if j_ == 10\n\t.set di_, 4\n\t.endif\n\t" \
      ".if j_ >= 14 && j_ < 17\n\t.set di_, j_ - 9\n\t.endif\n\t" \
        \
      "pos j_+2\n\t" \
      "qk1 48, a_, 68\n\t.if j_ < 30\n\tds_read_b128 v[a_:a_+3], v[ar_] offset:of_\n\t.endif\n\told 4*(j_-28)\n\txp 112, 32\n\t" \
      "qk2 48, a_+4, 74\n\t.if j_ < 30\n\tds_read_b64 v[a_+4:a_+5], v[ar_+1] offset:of_\n\t.endif\n\told 4*(j_-28)+1\n\txp 116, 36\n\t" \
      "pos j_+1\n\t" \
      "smf 16, b_+10, 120\n\t.if j_ < 31\n\tds_read_b128 v[b_+10:b_+13], v[ar_+2] offset:of_+1024\n\t.endif\n\told 4*(j_-28)+2\n\txp 120, 40\n\t" \
      "smf 0, a_+6, 112\n\t.if j_ < 30\n\ts_waitcnt lgkmcnt(3)\n\t.elseif j_ == 30\n\ts_waitcnt lgkmcnt(1)\n\t.else\n\ts_waitcnt lgkmcnt(0)\n\t.endif\n\t" \
      "old 4*(j_-28)+3\n\t" \
      ".if j_ == 5\n\tglobal_load_dwordx4 v[236:239], v137, s[42:43] sc1\n\tglobal_load_dwordx2 v[240:241], v78, s[42:43] sc1\n\t.endif\n\t" \
      "xp 124, 44\n\t" \
        \
      ".if j_ < 31\n\tqk1 32, b_, 64\n\t.endif\n\tdma 0\n\t" \
      ".if j_ == 0\n\tflg s[74:75]\n\t.endif\n\t" \
      ".if j_ == 3\n\tglobal_load_dwordx2 v[142:143], v141, s[54:55] sc1\n\t.endif\n\t" \
      ".if j_ == 7\n\ts_mov_b64 s[40:41], s[44:45]\n\ttabl 248, 0\n\ttabl 252, 1\n\t.endif\n\t" \
      ".if j_ == 8\n\ttabl 220, 2\n\ttabl 224, 3\n\t.endif\n\t" \
      ".if j_ == 17\n\ts_mov_b32 m0, 143360\n\ts_nop 0\n\tglobal_load_lds_dword v141, s[74:75]\n\t.endif\n\t" \
      ".if j_ == 26\n\tglobal_load_dwordx2 v[142:143], v253, s[54:55] sc1\n\t.endif\n\t" \
      "xp 112, 48\n\t" \
      ".if j_ < 31\n\tqk2 32, b_+4, 72\n\t.endif\n\tdma 1\n\t" \
      ".if j_ == 7\n\ttabl 160, 2\n\ttabl 164, 3\n\ts_add_u32 s40, s40, 4096\n\ts_addc_u32 s41, s41, 0\n\t.endif\n\t" \
      ".if j_ == 8\n\ts_add_u32 s40, s40, 4096\n\ts_addc_u32 s41, s41, 0\n\ttabl 228, 0\n\ttabl 232, 1\n\t.endif\n\t" \
      "xp 116, 52\n\t" \
      "smf 0, a_+10, 120\n\tdma 2\n\t" \
      ".if j_ == 7\n\ttabl 168, 0\n\ttabl 172, 1\n\t.endif\n\t" \
      "xp 120, 56\n\t" \
      "pos j_+2\n\t" \
      "smf 16, a_+6, 112\n\t.if j_ < 30\n\tds_read_b128 v[a_+6:a_+9], v[ar_+2] offset:of_\n\t.endif\n\txp 124, 60\n\t" \
      ".set j_, j_ + 1\n\t" \
      ".endr\n\t" \
      ".if %c[j1] == 32\n\ts_nop 1\n\tsmf 16, 104, 120\n\ts_nop 15\n\t.endif\n\t" \
      "s_mov_b32 m0, %[keep]\n\t" \
      ".purgem pos\n\t.purgem dma\n\t.purgem flg\n\t.purgem tabl\n\t.purgem old\n\t" AT_ASM_PURGE
#define AT_STAMP
#define AT_STV
#define AT_STV_ARG
#define AT_STV_PASS
#define AT_A_STATE "+{v[0:31]}"(o), "+{v[32:47]}"(s0), "+{v[112:127]}"(pp), "+{v[80:111]}"(fr), "+{v[128:143]}"(ad), [keep] "=&s"(keep)
#define AT_A_CLOB "memory", "scc", "vcc", "s40", "s41", "s48", "v48", "v49", "v50", "v51", "v52", "v53", "v54", "v55", "v56", "v57", "v58", "v59", "v60", "v61", "v62", "v63"
__device__ __forceinline__ void at_a1(f32x32& o, f32x16& s0, f32x16& pp, f32x32& fr, const u32x16& q, u32x16& ad, const char* ws, u32x16 so,
                                      unsigned m0v, unsigned m0b, unsigned long long tag, unsigned flag, unsigned long long fmask,
                                      const char* qB, const void* tab, f32x16& mF, f32x16& mD, u32x8& qv, u32x8& tw AT_STV_ARG) {
  unsigned keep;
  asm volatile(AT_A_TEXT
      : AT_A_STATE, "=&{v[160:175]}"(mF), "=&{v[220:235]}"(mD), "=&{v[236:243]}"(qv), "=&{v[248:255]}"(tw) AT_STV
      : "{v[64:79]}"(q), "{s[54:55]}"(ws), "{s[56:71]}"(so), "{s72}"(m0v), "{s73}"(m0b), "{s[50:51]}"(tag), "{s52}"(flag), "{s[74:75]}"(fmask),
        "{s[42:43]}"(qB), "{s[44:45]}"(tab), [j0] "n"(0), [j1] "n"(12), [obr] "n"(O_BR)
      : AT_A_CLOB);
}
__device__ __forceinline__ void at_a2(f32x32& o, f32x16& s0, f32x16& pp, f32x32& fr, const u32x16& q, u32x16& ad, const char* ws, u32x16 so,
                                      unsigned m0v, unsigned long long tag, unsigned flag, const char* woa, const u32x8& wo, const char* src,
                                      unsigned d0, unsigned d1, unsigned d2, f32x32& of, f32x8& of2) {
  unsigned keep;
  asm volatile(AT_A_TEXT
      : AT_A_STATE, "=&{v[184:215]}"(of), "=&{v[216:223]}"(of2)
      : "{v[64:79]}"(q), "{s[54:55]}"(ws), "{s[56:71]}"(so), "{s72}"(m0v), "{s[50:51]}"(tag), "{s52}"(flag), "{s[74:75]}"(woa),
        "{v[248:255]}"(wo), "{s[42:43]}"(src), "{s44}"(d0), "{s45}"(d1), "{s46}"(d2), [j0] "n"(12), [j1] "n"(32), [obr] "n"(O_BR)
      : AT_A_CLOB);
}

__device__ __forceinline__ void at_b(f32x16& o, f32x16& mF, const f32x16& mD, u32x8& q, unsigned tb, unsigned qt, char* oA, const u32x16& qa,
                                     u32x16& ad, f32x32& k0, f32x16& k1, f32x32& k2, f32x16& k3) {
  asm volatile(AT_ASM_MACROS
      "v_add_u32 v242, %[tb], v128\n\tv_add_u32 v243, %[tb], v129\n\tv_add_u32 v245, %[tb], v130\n\t"
      "ds_read_b128 v[176:179], v242 offset:7*3584\n\tds_read_b64 v[180:181], v243 offset:7*3584\n\t"
      "ds_read_b128 v[184:187], v242 offset:8*3584\n\tds_read_b64 v[182:183], v243 offset:8*3584\n\t"
      "ds_read_b128 v[192:195], v242\n\tds_read_b64 v[196:197], v243\n\tds_read_b128 v[206:209], v242 offset:3584\n\tds_read_b64 v[210:211], v243 offset:3584\n\t"
      "ds_read_b128 v[198:201], v245\n\tds_read_b128 v[212:215], v245 offset:3584\n\tds_read_b128 v[202:205], v245 offset:1024\n\t"
      "s_mov_b32 exec_lo, 0\n\tv_mov_b64 v[240:241], 0\n\ts_mov_b32 exec_lo, -1\n\tv_mov_b64 v[188:189], -1\n\tv_mov_b64 v[190:191], -1\n\t"
      "s_waitcnt lgkmcnt(7)\n\t"
      "qk1 144, 176, 236\n\tqk2 144, 180, 240\n\tqk1c 48, 184, 236, 220\n\tqk2 48, 182, 240\n\t"
      "global_store_dwordx4 v137, v[188:191], s[42:43] sc1\n\ts_mov_b32 exec_hi, 0\n\tglobal_store_dwordx2 v78, v[188:189], s[42:43] sc1\n\ts_mov_b32 exec_hi, -1\n\t"
      "s_waitcnt lgkmcnt(5)\n\tqk1c 160, 192, 236, 160\n\tds_read_b128 v[192:195], v242 offset:7168\n\ts_cmp_eq_u32 %[qt], 0\n\ts_nop 1\n\t"
      "mx 216, 144\n\tmx 218, 48\n\ts_cbranch_scc1 1f\n\tv_max_f32 v218, v218, v216\n\t1:\n\tv_mov_b32 v219, v218\n\t"
      "v_mov_b64 v[144:145], 0\n\tv_mov_b64 v[146:147], 0\n\tv_permlane32_swap_b32 v218, v219\n\tv_max_f32 v218, v218, v219\n\t"
      "s_mov_b32 exec_lo, 0\n\tv_cvt_pk_bf16_f32 v240, -v218, 0\n\ts_mov_b32 exec_lo, -1\n\tv_mov_b64 v[148:149], 0\n\t"
      "qk2 160, 196, 240\n\tds_read_b64 v[196:197], v243 offset:7168\n\t"
      ".set i_, 150\n\t.rept 5\n\tv_mov_b64 v[i_:i_+1], 0\n\t.set i_, i_ + 2\n\t.endr\n\t"
      "s_waitcnt lgkmcnt(2)\n\ts_nop 1\n\t"
      ".set t_, 0\n\t"
      ".rept 9\n\t"
      ".set a_, 192 + 14 * (t_ & 1)\n\t.set b_, 206 - 14 * (t_ & 1)\n\t.set c_, 160 - 112 * (t_ & 1)\n\t.set d_, 48 + 112 * (t_ & 1)\n\t"
      ".if t_ == 0 || t_ == 5\n\ts_setprio 2 - t_ / 5\n\t.endif\n\t"
      ".if t_ < 7\n\tqk1 d_, b_, 236\n\t.elseif t_ == 7\n\tqk1c d_, b_, 236, 220\n\t.endif\n\t"
      ".if t_ < 6\n\tds_read_b128 v[b_:b_+3], v242 offset:(t_+3)*3584\n\t.endif\n\t"
      "xp 176, c_\n\t"
      ".if t_ < 8\n\tqk2 d_, b_+4, 240\n\t.endif\n\t"
      ".if t_ < 6\n\tds_read_b64 v[b_+4:b_+5], v243 offset:(t_+3)*3584\n\t.endif\n\t"
      "xp 180, c_+4\n\t"
      ".if t_ > 0\n\tsmf 144, b_+10, 184\n\t.endif\n\t"
      ".if t_ < 8\n\tds_read_b128 v[b_+10:b_+13], v245 offset:(t_+1)*3584+1024\n\t.endif\n\t"
      "xp 184, c_+8\n\t"
      "smf 144, a_+6, 176\n\t"
      ".if t_ < 6\n\ts_waitcnt lgkmcnt(3)\n\t.elseif t_ < 8\n\ts_waitcnt lgkmcnt(1)\n\t.else\n\ts_waitcnt lgkmcnt(0)\n\t.endif\n\t"
      ".if t_ < 7\n\tds_read_b128 v[a_+6:a_+9], v245 offset:(t_+2)*3584\n\t.endif\n\t"
      "xp 188, c_+12\n\t"
      ".set t_, t_ + 1\n\t"
      ".endr\n\t"
      "s_nop 1\n\tsmf 144, 202, 184\n\ts_nop 11\n\t"
      AT_ASM_PURGE
      : "=&{v[144:159]}"(o), "+{v[160:175]}"(mF), "+{v[0:31]}"(k0), "+{v[32:47]}"(k1), "+{v[80:111]}"(k2), "+{v[112:127]}"(k3), "+{v[128:143]}"(ad),
        "+{v[236:243]}"(q)
      : "{v[220:235]}"(mD), "{v[64:79]}"(qa), [tb] "s"(tb), [qt] "s"(qt), "{s[42:43]}"(oA)
      : "memory", "scc", "v245", "v48", "v49", "v50", "v51", "v52", "v53", "v54", "v55", "v56", "v57", "v58", "v59", "v60", "v61", "v62", "v63",
        "v176", "v177", "v178", "v179", "v180", "v181", "v182", "v183", "v184", "v185", "v186", "v187", "v188", "v189", "v190", "v191",
        "v192", "v193", "v194", "v195", "v196", "v197", "v198", "v199", "v200", "v201", "v202", "v203", "v204", "v205", "v206", "v207",
        "v208", "v209", "v210", "v211", "v212", "v213", "v214", "v215", "v216", "v217", "v218", "v219");
}

#define PJ_MACROS \
      ".macro rdw k, sp\n\t.set d_, 80 + 20 * ((\\k) & 1)\n\t" \
      ".if (\\k) < \\sp\n\t.set a_, 120\n\t.set o_, (\\k) * 5120\n\t.else\n\t.set a_, 122\n\t.set o_, ((\\k) - \\sp) * 5120\n\t.endif\n\t.endm\n\t" \
      ".macro rd1 i\n\tds_read_b64_tr_b16 v[d_+4*\\i:d_+4*\\i+1], v[a_] offset:o_+32*\\i\n\tds_read_b64_tr_b16 v[d_+4*\\i+2:d_+4*\\i+3], v[a_+1] offset:o_+32*\\i\n\t.endm\n\t" \
      ".macro pm i, ac, of, last\n\t" \
      ".if k_ == 0\n\tv_mfma_f32_16x16x32_bf16 v[\\ac+4*\\i:\\ac+4*\\i+3], v[c_+4*\\i:c_+4*\\i+3], v[\\of:\\of+3], 0\n\t" \
      ".else\n\tv_mfma_f32_16x16x32_bf16 v[\\ac+4*\\i:\\ac+4*\\i+3], v[c_+4*\\i:c_+4*\\i+3], v[\\of+4*k_:\\of+3+4*k_], v[\\ac+4*\\i:\\ac+4*\\i+3]\n\t.endif\n\t" \
      ".if k_ < \\last\n\trd1 \\i\n\ts_waitcnt lgkmcnt(8)\n\t.elseif \\i < 4\n\ts_waitcnt lgkmcnt(6 - 2 * \\i)\n\t.endif\n\t.endm\n\t" \
      ".macro ldo\n\tv_mov_b32 v72, v254\n\t.set q_, 0\n\t.rept 10\n\t" \
      "global_load_dwordx4 v[32+4*q_:35+4*q_], v72, s[54:55] sc1\n\t.if q_ < 9\n\tv_add_u32 v72, 0x20000, v72\n\t.endif\n\t.set q_, q_ + 1\n\t.endr\n\t.endm\n\t"
#define PJ_PURGE ".purgem rdw\n\t.purgem rd1\n\t.purgem pm\n\t.purgem ldo\n\t"
#define PJ_CLOB "v72", "v80", "v81", "v82", "v83", "v84", "v85", "v86", "v87", "v88", "v89", "v90", "v91", "v92", "v93", "v94", "v95", \
        "v96", "v97", "v98", "v99", "v100", "v101", "v102", "v103", "v104", "v105", "v106", "v107", "v108", "v109", "v110", "v111", \
        "v112", "v113", "v114", "v115", "v116", "v117", "v118", "v119"
__device__ __forceinline__ void proj_1(f32x32& acc, const f32x32& of, const f32x8& of2, f32x32& oa, f32x8& oa2, u32x4 wa, const u32x8& wo,
                                       const char* src0, const char* src1, unsigned d0, unsigned d1, const char* ws) {
  unsigned keep;
  asm volatile(PJ_MACROS
      ".macro wp n\n\t.set n_, \\n\n\t.set pc_, n_ %% 5\n\t"
      ".if n_ < 10\n\t"
      ".if n_ >= 5\n\ts_cmp_lt_i32 s45, 0\n\ts_cbranch_scc1 9f\n\t.endif\n\t"
      ".if pc_ == 0\n\ts_mov_b32 m0, s[44+n_/5]\n\ts_nop 0\n\t.endif\n\t"
      ".if pc_ == 4\n\ts_add_u32 m0, m0, 4096\n\ts_nop 0\n\t.endif\n\t"
      "global_load_lds_dwordx4 v[248+pc_], s[42+4*(n_/5):43+4*(n_/5)] offset:1024*(pc_&3)\n\t"
      ".if n_ >= 5\n\t9:\n\t.endif\n\t.endif\n\t"
      ".if n_ == 12\n\tldo\n\t.endif\n\t"
      ".endm\n\t"
      "rdw 0, 6\n\trd1 0\n\trd1 1\n\trd1 2\n\trd1 3\n\trd1 4\n\t"
      "s_mov_b32 %[keep], m0\n\ts_barrier\n\ts_waitcnt lgkmcnt(8)\n\t"
      ".set k_, 0\n\t.rept 10\n\t.set c_, 80 + 20 * (k_ & 1)\n\trdw k_+1, 6\n\t"
      ".if k_ == 0 || k_ == 3 || k_ == 6\n\ts_setprio 2 - k_ / 3\n\t.endif\n\t"
      "pm 0, 0, 184, 9\n\twp 2*k_\n\tpm 1, 0, 184, 9\n\tpm 2, 0, 184, 9\n\twp 2*k_+1\n\tpm 3, 0, 184, 9\n\tpm 4, 0, 184, 9\n\t"
      ".set k_, k_ + 1\n\t.endr\n\t"
      "s_setprio 3\n\ts_mov_b32 m0, %[keep]\n\ts_waitcnt vmcnt(10)\n\ts_barrier\n\ts_nop 4\n\t"
      ".purgem wp\n\t" PJ_PURGE
      : "=&{v[0:31]}"(acc), "=&{v[32:63]}"(oa), "=&{v[64:71]}"(oa2), [keep] "=&s"(keep)
      : "{v[184:215]}"(of), "{v[216:223]}"(of2), "{v[120:123]}"(wa), "{v[248:255]}"(wo), "{s[42:43]}"(src0), "{s[46:47]}"(src1), "{s44}"(d0),
        "{s45}"(d1), "{s[54:55]}"(ws)
      : "memory", "scc", PJ_CLOB);
}
template <int K0>
__device__ __forceinline__ void proj_2(f32x32& acc, f32x32& oa, f32x8& oa2, u32x4 wa, const u32x8& wo, const char* ws) {
  asm volatile(PJ_MACROS
      "rdw %c[k0], 5\n\trd1 0\n\trd1 1\n\trd1 2\n\trd1 3\n\trd1 4\n\t"
      ".if %c[k0] == 0\n\t"
      "s_mov_b32 s48, 0x40000\n\t5:\n\t"
      "v_max3_u32 v72, v32, v33, v34\n\t.set q_, 35\n\t.rept 18\n\tv_max3_u32 v72, v72, v[q_], v[q_+1]\n\t.set q_, q_ + 2\n\t.endr\n\t"
      "v_max_u32 v72, v72, v71\n\tv_cmp_eq_u32 vcc, -1, v72\n\ts_sub_u32 s48, s48, 1\n\t"
      "s_cbranch_vccz 4f\n\ts_cbranch_scc1 4f\n\tldo\n\ts_waitcnt vmcnt(0)\n\ts_branch 5b\n\t4:\n\t"
      ".endif\n\t"
      "s_waitcnt lgkmcnt(8)\n\t"
      ".set k_, %c[k0]\n\t.rept 5\n\t.set c_, 80 + 20 * (k_ & 1)\n\trdw k_+1, 5\n\t"
      "pm 0, 160, 32, %c[k0]+4\n\tpm 1, 160, 32, %c[k0]+4\n\tpm 2, 160, 32, %c[k0]+4\n\tpm 3, 160, 32, %c[k0]+4\n\tpm 4, 160, 32, %c[k0]+4\n\t"
      ".set k_, k_ + 1\n\t.endr\n\t"
      ".if %c[k0] == 0\n\ts_waitcnt vmcnt(0)\n\ts_barrier\n\t.else\n\ts_nop 7\n\t.endif\n\t"
      PJ_PURGE
      : "+{v[160:191]}"(acc), "+{v[32:63]}"(oa), "+{v[64:71]}"(oa2)
      : "{v[120:123]}"(wa), "{v[248:255]}"(wo), "{s[54:55]}"(ws), [k0] "n"(K0)
      : "memory", "scc", "vcc", "s48", PJ_CLOB);
}
typedef __attribute__((address_space(3))) f32x4* lds_f4;
__device__ __forceinline__ void proj_send(const f32x32& acc, char* lds, int lw, int grp, int lane) {
  lds_ptr p = (lds_ptr)lds + lw * 5120 + lane * 16;
  if (grp) {
    *(lds_f4)(p) = f32x4{acc[0], acc[1], acc[2], acc[3]}; *(lds_f4)(p + 1024) = f32x4{acc[4], acc[5], acc[6], acc[7]};
    *(lds_f4)(p + 4096) = f32x4{acc[16], acc[17], acc[18], acc[19]};
  } else {
    *(lds_f4)(p + 2048) = f32x4{acc[8], acc[9], acc[10], acc[11]}; *(lds_f4)(p + 3072) = f32x4{acc[12], acc[13], acc[14], acc[15]};
  }
}
#define OUT_WT 1
template <bool WT>
__device__ __forceinline__ void proj_out(const f32x4& a, const f32x4& b, char* p, unsigned off) {
  const auto s0 = __builtin_amdgcn_permlane16_swap(pk_bf16(a[0], a[1]), pk_bf16(b[0], b[1]), false, false);
  const auto s1 = __builtin_amdgcn_permlane16_swap(pk_bf16(a[2], a[3]), pk_bf16(b[2], b[3]), false, false);
  const u32x4 v = {s0[0], s1[0], s0[1], s1[1]};
  if (WT) asm volatile("global_store_dwordx4 %0, %1, %2 sc1\n\ts_nop 1" :: "v"(off), "v"(v), "s"(p) : "memory");
  else asm volatile("global_store_dwordx4 %0, %1, %2\n\ts_nop 1" :: "v"(off), "v"(v), "s"(p) : "memory");
}
template <bool WT>
__device__ __forceinline__ void proj_finish(const f32x32& acc, char* lds, int lw, int grp, int lane, char* dst) {
  lds_ptr p = (lds_ptr)lds + lw * 5120 + lane * 16;
  const unsigned row = (lane & 15) * 2560, o16 = row + ((lane >> 4) & 1) * 32 + (lane >> 5) * 16;
  if (grp) {
    proj_out<WT>(f32x4{acc[8], acc[9], acc[10], acc[11]} + *(lds_f4)(p + 2048), f32x4{acc[12], acc[13], acc[14], acc[15]} + *(lds_f4)(p + 3072), dst + 64, o16);
  } else {
    const f32x4 r2 = f32x4{acc[16], acc[17], acc[18], acc[19]} + *(lds_f4)(p + 4096);
    proj_out<WT>(f32x4{acc[0], acc[1], acc[2], acc[3]} + *(lds_f4)(p), f32x4{acc[4], acc[5], acc[6], acc[7]} + *(lds_f4)(p + 1024), dst, o16);
    const u32x2 v = {pk_bf16(r2[0], r2[1]), pk_bf16(r2[2], r2[3])};
    if (WT) asm volatile("global_store_dwordx2 %0, %1, %2 offset:128 sc1" :: "v"(row + (lane >> 4) * 8), "v"(v), "s"(dst) : "memory");
    else asm volatile("global_store_dwordx2 %0, %1, %2 offset:128" :: "v"(row + (lane >> 4) * 8), "v"(v), "s"(dst) : "memory");
  }
}

struct LaneTab { unsigned v[10][64][4]; };
constexpr LaneTab make_tab() {
  LaneTab t = {};
  for (unsigned l = 0; l < 64; ++l) {
    for (unsigned i = 0; i < 5; ++i) {
      const unsigned v = 64 * i + l, rw = v / 10;
      t.v[i >> 2][l][i & 3] = (rw ^ ((rw >> 1) & 4)) * 1280 + (v - rw * 10) * 16 + 4096 - 1024 * (i & 3);
    }
    const unsigned g = l >> 4, q = (l & 15) >> 2, p = l & 3;
    t.v[1][l][1] = (l & 15) * 64 * 128;
    t.v[1][l][2] = g * 32768 + (l & 15) * 16;
    t.v[1][l][3] = (g * 1280 + (q ^ (4 * (g & 1))) * 160 + p * 8) | ((g * 1280 + ((4 + q) ^ (4 * (g & 1))) * 160 + p * 8) << 16);
    for (unsigned r = 0; r < 16; ++r) {
      const unsigned key = (r & 3) + 8 * (r >> 2) + 4 * (l >> 5);
      t.v[2 + (r >> 2)][l][r & 3] = key > (l & 31) ? 0u : 0xFF800000u;
      t.v[6 + (r >> 2)][l][r & 3] = key <= (l & 31) ? 0u : 0xFF800000u;
    }
  }
  return t;
}
__device__ const LaneTab lane_tab = make_tab();
constexpr unsigned CODE_B = 34816;

__global__ void __launch_bounds__(512, 2)
k_fused(const char* __restrict__ xa_, const char* __restrict__ wa_, const char* __restrict__ woa_, const char* __restrict__ xb_,
        const char* __restrict__ wb_, const char* __restrict__ wob_, char* __restrict__ ws_, char* __restrict__ out_) {
  __shared__ __attribute__((aligned(16))) char lds[163840];
  const unsigned bid = blockIdx.x, xcd = bid & 7, slot = bid >> 3, br = xcd >> 2;
  u32x4 ka; u32x2 kb; u32x8 kc;
  {
    const void* kp = (const void*)__builtin_amdgcn_kernarg_segment_ptr();
    asm volatile("s_load_dwordx4 %0, %3, %4\n\ts_load_dwordx8 %2, %3, 0x20\n\ts_load_dwordx2 %1, %3, 0x10"
                 : "=&s"(ka), "=&s"(kb), "=&s"(kc) : "s"(kp), "s"(br * 24));
  }
  STAMP_DECL STAMP(0);
  unsigned lane = threadIdx.x & 63;
  const unsigned w = __builtin_amdgcn_readfirstlane(threadIdx.x >> 6);
  const unsigned grp = w >> 2, lw = w & 3;
  const unsigned mt = (xcd & 3) * 4 + (slot & 3), u = slot >> 2;
  const unsigned win = (5 * ((mt >> 1) & 3)) >> 1;
  const unsigned kv1 = (u + 3) >> 3;
  const unsigned j1 = win + u - 5;
#define ROT (5 * (u & 1) + (5 * (xcd & 3)) / 2)
  auto mod10 = [](unsigned x) { return __builtin_elementwise_min(x, x - 10); };
  const unsigned rot = mod10(ROT);
  const unsigned m0v = grp * 16384 + lw * 4096;
  const unsigned str1 = grp ? 64 * 3840 : 128;
  unsigned o1[4], tw;
  {
    const unsigned row = lane >> 3, pos = lane & 7;
    tw = row * 3840 + ((pos ^ (row & 2)) * 16);
    const unsigned t1 = grp ? tw : row * 1280 + ((pos ^ (row >> 1)) * 16);
    const unsigned gap = kv1 ? 1280 : 128, m1 = grp ? 0 : 64;
    o1[0] = t1 + 1024;
    o1[1] = (t1 ^ m1) + (grp ? gap : 8 * 1280);
    o1[2] = (t1 ^ m1 ^ 64) + (grp ? 8 * 3840 - 1024 : 16 * 1280 - 1024);
    o1[3] = (t1 ^ 64) + (grp ? 8 * 3840 - 2048 + gap : 24 * 1280 - 2048);
  }
  const unsigned offA = grp ? 16 * lw * 3840 + (kv1 ? 1280 + j1 * 128 : u * 256) : (mt * 128 + 32 * lw) * 1280;
  asm volatile("s_waitcnt lgkmcnt(0)" : "+s"(ka), "+s"(kb), "+s"(kc));
  auto ptr = [](unsigned lo, unsigned hi) { return (char*)(((unsigned long long)hi << 32) | lo); };
  const char* sA = ptr(grp ? ka[2] : ka[0], grp ? ka[3] : ka[1]) + (offA - 1024ll);
  char* const ws = ptr(kc[4], kc[5]);
  glds16x4(sA + rot * str1, o1, m0v);
  glds16x4(sA + mod10(rot + 1) * str1, o1, m0v + 32768);
  u32x2 ep; unsigned tch[4];
  asm volatile("s_load_dwordx2 %0, %1, %2" : "=&s"(ep) : "s"(ws), "s"((unsigned)CTL_WS));
  {
    const unsigned i64 = (slot * 8 + w) * 64;
    unsigned long long pc;
    asm volatile("s_getpc_b64 %0\n\ts_load_dword %1, %0, %5\n\ts_load_dword %2, %0, %6\n\ts_load_dword %3, %0, %7\n\ts_load_dword %4, %8, %9"
                 : "=&s"(pc), "=&s"(tch[0]), "=&s"(tch[1]), "=&s"(tch[2]), "=&s"(tch[3])
                 : "s"(i64), "s"(i64 + 16384), "s"(__builtin_elementwise_min(i64 + 32768, CODE_B)), "s"(&lane_tab),
                   "s"(__builtin_elementwise_min(i64, 10176u)));
  }
  const unsigned j2 = mod10(win + 3 + (u < 7 ? u : 6));
  const unsigned Tg = mt * 4 + lw;
  const unsigned vt1 = grp & kv1, vt2 = u < 7 ? grp : 2;
  const unsigned cg1 = kv1 ? 10 + 10 * grp + j1 : 2 * u + grp, cg2 = 10 + 10 * grp + j2;
  const unsigned head = xcd * 4 + (slot & 3), qb = u;
  const unsigned odd = head & 1, o2 = 5 * (head >> 1) + 2, o1h = odd ? o2 + 1 : o2 - 2;
  const unsigned qt = qb * 8 + 2 * lw + grp;
  u32x4 soA; u32x16 so;
  {
    const unsigned kb0 = 80 * 32768 + w * 512, vb0 = (unsigned)V_WS - V_OFF + xcd * (256 * 1280) + w * 5120;
    unsigned ko[10], vo_[10];
#pragma unroll
    for (int i = 0; i < 8; ++i) { const unsigned c = (((head >> 3) + (i >> 1)) & 3) + 4 * (i & 1); ko[i] = kb0 + c * 4096; vo_[i] = vb0 + c * 40960; }
    const unsigned c8 = qb ? qb - 1 : 0;
    ko[8] = kb0 + (unsigned)QK_BR + c8 * 4096; vo_[8] = vb0 + (unsigned)V_BR + c8 * 40960;
    ko[9] = kb0 + (unsigned)QK_BR + qb * 4096; vo_[9] = vb0 + (unsigned)V_BR + qb * 40960;
    soA = u32x4{ko[0], vo_[0], ko[1], vo_[1]};
    so = u32x16{ko[2], vo_[2], ko[8], vo_[8], ko[9], vo_[9], ko[3], vo_[3], ko[4], vo_[4], ko[5], vo_[5], ko[6], vo_[6], ko[7], vo_[7]};
    asm volatile("" : "+{s[76:79]}"(soA), "+{s[56:71]}"(so));
  }
  unsigned flag1 = FQ_OFF + ((br * 30 + cg1) * 64 + Tg) * 128, flag2 = FQ_OFF + ((br * 30 + cg2) * 64 + Tg) * 128;
  unsigned flagO = FO_OFF + (head * 64 + qt) * 128;
  PINS(flag1); PINS(flag2); PINS(flagO);
  const char* sB = ptr(ka[2], ka[3]) + (16 * lw * 3840 + 256 + j2 * 128);
  const unsigned mytile = w * TILE_B;
  u32x16 qq, pk, at;
  {
    const unsigned r16 = lane & 15, g = lane >> 4, q = r16 >> 2, p = lane & 3, r32 = lane & 31, h = lane >> 5;
    const unsigned xa0 = (lw * 32 + r16) * 128 + ((g ^ (r16 >> 1)) * 16);
    const unsigned wa0 = 2048 * g + 1024 * grp + 128 * q + 16 * ((p >> 1) ^ (4 * (g & 1) + (q & 2))) + 8 * (p & 1);
    const unsigned jj = (20 * head + 19 * (lane & 1)) >> 6, b1 = (lane >> 1) & 1, c = (lane >> 2) & 7;
    const unsigned pol1 = FQ_OFF + (jj * 64 + ((lane & 8) ? (qt ^ b1) : b1 * 640 + (lane & 4) * 8 + (640 + (head >> 3) * 8 + w))) * 128;
    qq = u32x16{o1[0], o1[1], o1[2], o1[3], tw + 1024, tw + 1280, (tw ^ 64) + (8 * 3840 - 1024), (tw ^ 64) + (8 * 3840 - 2048 + 1280),
                pol1, xa0, xa0 ^ 64, xa0, wa0, wa0 ^ 32, wa0 ^ 64, wa0 ^ 96};
    const unsigned cB = __builtin_elementwise_max((c & 1) + qb, 1u) + (30 * 8 - 1);
    const unsigned pol2 = (jj * 64 + (lane >= 48 ? 30 * 64 + qt : b1 * 640 + (h ? cB : c) * 8 + (640 + w))) * 128 + FQ_OFF;
    const u32x2 s1 = g1_offs(br, cg1, Tg, lane), s2 = g1_offs(br, cg2, Tg, lane);
    const unsigned ab = grp * TILE_B;
    pk = u32x16{s1[0], s1[1], s2[0], s2[1],
                lane * 16 + ab,
                h ? (V_OFF + 320 + ab) : r32 * 16 + (K2_OFF + odd * 8 + ab),
                h * 512 + (r32 < 28 ? __builtin_elementwise_min(r32, 21u) : 28) * 16 + (V_OFF + ab),
                h * 32768 + r32 * 16 + o1h * 32768,
                r32 * 16 + (o2 * 32768 - 1024),
                h * 1280 + (__builtin_elementwise_min(r32 + 4 * odd, r32 + 4 * odd - 20) + (head & 3) * 20) * 16,
                pol2,
                g * 512 + __builtin_elementwise_min(r16, 11u) * 16 + (mytile + V_OFF + 320),
                r16 == 0 ? 0x3F803F80u : 0u,
                r32 * 16 + (o2 * 32768 + odd * 8),
                lane * 512 + (3 * CHUNK_B + V_OFF + 320 + mytile),
                lane};
  }
  asm volatile("" : "+{v[64:79]}"(qq), "+{v[80:95]}"(pk) :: "memory");
  {
    const unsigned oo[4] = {qq[0], qq[1], qq[2], qq[3]};
    glds16x4(sA + mod10(rot + 2) * str1, oo, m0v + 65536);
  }
  asm volatile("s_waitcnt vmcnt(8) lgkmcnt(0)\n\ts_barrier" ::: "memory");
  PINS(ep); PINS(tch[0]); PINS(tch[1]); PINS(tch[2]); PINS(tch[3]);
  const unsigned long long tag = (((unsigned long long)ep[1] << 32) | ep[0]) + 1;
  STAMP(1);
  u32x4 cv4;
  STV_PUT(pk[15]);
  g1_all(vt1, kv1, vt2, qq, pk, at, cv4, sA, mod10(rot + 3) * str1, str1, sB, mod10(rot + grp) * (64 * 3840), m0v, tag, flag1, ws, mytile, soA,
         qt * 512);
  STV_GET(pk[15]);
  STAMP(8);
  if (bid == 255 && grp == 0) {
    const unsigned e = w * 64 + lane_id(), eu = e & 7, em = (e >> 3) & 15;
    poll_flags(ws, FQ_OFF + (((e >> 7) * 30 + (eu < 5 ? 2 * eu : 5 + eu + ((5 * ((em >> 1) & 3)) >> 1))) * 64 + 4 * em) * 128, tag);
  }
  BARRIER();
  STAMP(9);
  if (bid == 255 && w == 0) put_flag(ws, CTL_WS, tag);
  typedef __attribute__((address_space(3))) u32x4* lds_u4;
  *(lds_u4)((lds_ptr)lds + pk[11] + 2 * CHUNK_B) = cv4; *(lds_u4)((lds_ptr)lds + pk[11] + 3 * CHUNK_B) = cv4; *(lds_u4)((lds_ptr)lds + pk[11] + 4 * CHUNK_B) = cv4;
  if (qb == 0) {
    lds_ptr z = (lds_ptr)lds + 3 * CHUNK_B + mytile + qq[15];
    *(lds_f4)(z) = f32x4{}; *(lds_f4)(z + 1024) = f32x4{}; *(lds_f4)(z + 2048) = f32x4{}; *(lds_f4)(z + 2560) = f32x4{};
  }
  const f32x16 zero16 = {};
  auto exact = [&](int b, bf16x8 q1, s16x4 q2, int t0, int t1) {
    __builtin_amdgcn_fence(__ATOMIC_ACQUIRE, "agent");
    const int ln = lane_id(), r32 = ln & 31, h = ln >> 5;
    unsigned head = xcd * 4 + (slot & 3);
    PINS(head);
    const unsigned odd = head & 1, o2 = 5 * (head >> 1) + 2, o1h = odd ? o2 + 1 : o2 - 2;
    if (h) q2 = s16x4{0, 0, 0, 0};
    typedef __attribute__((address_space(1))) const char* gptr;
    gptr kg1 = (gptr)ws + QK_WS + b * QK_BR + (size_t)(80 + o1h + h) * 32768 + r32 * 16;
    gptr kg2 = (gptr)ws + QK_WS + b * QK_BR + (size_t)(80 + o2) * 32768 + r32 * 16 + odd * 8;
    gptr vg = (gptr)ws + V_WS + b * V_BR + (head >> 2) * (256 * 1280) + h * 1280 +
                     ((head & 3) * 20 + (odd ? (r32 < 16 ? r32 + 4 : r32 - 16) : r32)) * 16;
    const u32x4 vc = r32 == 20 ? u32x4{0x3F803F80u, 0x3F803F80u, 0x3F803F80u, 0x3F803F80u} : u32x4{0u, 0u, 0u, 0u};
    float m = -INFINITY;
    f32x16 r = zero16;
#pragma unroll 1
    for (int ps = 0; ps < 2; ++ps) {
#pragma unroll 1
      for (int t = t0; t <= t1; ++t) {
        f32x16 s = MFMA32(*(__attribute__((address_space(1))) const bf16x8*)(kg1 + t * 512), q1, zero16);
        s = MFMA32K8(*(__attribute__((address_space(1))) const s16x4*)(kg2 + t * 512), q2, s);
        if (b) {
          const int d = (t1 - t) * 32 + r32 - 4 * h;
#pragma unroll
          for (int e = 0; e < 16; ++e) s[e] = (unsigned)(d - ((e & 3) + 8 * (e >> 2))) < 256u ? s[e] : -INFINITY;
        }
        if (ps == 0) { m = max16(s, m); continue; }
#pragma unroll
        for (int e = 0; e < 16; ++e) s[e] = __builtin_amdgcn_exp2f(s[e] - m);
        u32x4 va = vc, vb = vc;
        if (r32 < 20) { va = *(__attribute__((address_space(1))) const u32x4*)(vg + t * 5120); vb = *(__attribute__((address_space(1))) const u32x4*)(vg + t * 5120 + 2560); }
        r = smfmac(__builtin_bit_cast(bf16x8, va), lo8(s), r);
        r = smfmac(__builtin_bit_cast(bf16x8, vb), hi8(s), r);
      }
      m = xhalf_max(m);
    }
    return r;
  };
  auto unsafe = [&](const f32x16& r, float l) {
    float amax = 0.f;
#pragma unroll
    for (int e = 0; e < 12; ++e) amax = __builtin_fmaxf(amax, __builtin_fabsf(r[e]));
    return __any(!(l <= 1.2676506e30f) || !(l >= 5.4210109e-20f) || !(amax <= 3.0e38f));
  };
  const char* const woa = ptr(kb[0], kb[1]);
  const char* const wob = ptr(kc[2], kc[3]);
  char* const oA = ws + (O_WS + qt * 512);
  f32x32 oab, of; f32x8 of2;
  u32x8 wo;
  unsigned wl;
  {
    f32x16 s0, pp;
    f32x32 fr;
    asm volatile(AT_ASM_MACROS
        "v_subrev_u32 v108, %[ab], v129\n\tds_read_b128 v[112:115], v79\n\tds_read_b64 v[116:117], v108\n\t"
        "ds_read_b128 v[80:83], v128\n\tds_read_b64 v[84:85], v129\n\tds_read_b128 v[94:97], v128 offset:7168\n\tds_read_b64 v[98:99], v129 offset:7168\n\t"
        "ds_read_b128 v[86:89], v130\n\tds_read_b128 v[100:103], v130 offset:7168\n\tds_read_b128 v[90:93], v130 offset:1024\n\t"
        "s_mov_b32 exec_lo, 0\n\tv_mov_b64 v[72:73], 0\n\tv_mov_b64 v[74:75], 0\n\ts_mov_b32 exec_lo, -1\n\t"
        "s_waitcnt lgkmcnt(7)\n\t"
        "qk1 32, 112, 64\n\tqk2 32, 116, 72\n\tqk1 48, 112, 68\n\tqk2 48, 116, 74\n\t"
        ".set i_, 0\n\t.rept 10\n\tv_mov_b64 v[i_:i_+1], 0\n\t.set i_, i_ + 2\n\t.endr\n\t"
        "mx 108, 32\n\tv_mov_b32 v109, v108\n\tmx 110, 48\n\tv_mov_b32 v111, v110\n\t"
        "v_permlane32_swap_b32 v108, v109\n\ts_waitcnt lgkmcnt(0)\n\tqk1 32, 80, 64\n\tv_permlane32_swap_b32 v110, v111\n\t"
        "v_max_f32 v108, v108, v109\n\tv_max_f32 v110, v110, v111\n\t"
        "s_mov_b32 exec_lo, 0\n\tv_cvt_pk_bf16_f32 v72, -v108, 0\n\tv_cvt_pk_bf16_f32 v74, -v110, 0\n\ts_mov_b32 exec_lo, -1\n\ts_nop 0\n\t"
        "qk2 32, 84, 72\n\t"
        ".set i_, 20\n\t.rept 6\n\tv_mov_b64 v[i_:i_+1], 0\n\t.set i_, i_ + 2\n\t.endr\n\t"
        "v_mov_b64 v[104:105], 0\n\tv_mov_b64 v[106:107], 0\n\t"
        "v_mov_b64 v[120:121], 0\n\tv_mov_b64 v[122:123], 0\n\tv_mov_b64 v[124:125], 0\n\tv_mov_b64 v[126:127], 0\n\t"
        AT_ASM_PURGE
        : "=&{v[0:31]}"(oab), "=&{v[112:127]}"(pp), "=&{v[32:47]}"(s0), "=&{v[80:111]}"(fr), "+{v[64:79]}"(qq), "+{v[128:143]}"(at)
        : [ab] "s"(grp * TILE_B)
        : "memory", "v48", "v49", "v50", "v51", "v52", "v53", "v54", "v55", "v56", "v57", "v58", "v59", "v60", "v61", "v62", "v63");
    STAMP(10);
    f32x16 maskF, maskD; u32x8 qv;
    at_a1(oab, s0, pp, fr, qq, at, ws, so, mytile, mytile + (qb ? 3 : 4) * CHUNK_B, tag, flag2, (0x7fu >> u) & 1u, ws + (QK_BR + qt * 512),
          &lane_tab, maskF, maskD, qv, wo AT_STV_PASS);
    STAMP(11);
    {
      f32x16 ob;
      at_b(ob, maskF, maskD, qv, 3 * CHUNK_B + 2 * lw * TILE_B, qt, oA, qq, at, oab, s0, fr, pp);
      wl = wo[7];
      wo[5] += FO_OFF + (grp * 16 * 64 + head * 2 + (lw >> 1)) * 128;
      wo[6] += (unsigned)O_WS + grp * 40 * 32768 + (head * 64 + lw * 16) * 16;
      float l = row_sum(ob);
      if (__builtin_expect(unsafe(ob, l), 0)) {
        ob = exact(1, __builtin_bit_cast(bf16x8, u32x4{qv[0], qv[1], qv[2], qv[3]}), __builtin_bit_cast(s16x4, u32x2{qv[4], qv[5]}),
                   qb ? qt - 8 : 0, qt);
        l = row_sum(ob);
      }
      store_o<false>(ob, l, oA + O_BR, at[9], qq[14]);
    }
    STAMP(13);
    at[13] = (__builtin_elementwise_min(qq[15], 24u * 16) + (slot * 8 + w) * (25 * 16)) * 8;
    at_a2(oab, s0, pp, fr, qq, at, ws, so, mytile, tag, flagO, woa, wo, wob + (qb * 160 + w * (32 * 1280) - 4096ll), w * 5120, (w + 8) * 5120,
          grp ? 0x80000000u : w * 5120 + 143360, of, of2);
  }
  STAMP(12);
  {
    lds_ptr xb = (lds_ptr)lds + 3 * CHUNK_B + qq[15];
#pragma unroll
    for (int i = 0; i < 3; ++i) *(lds_f4)(xb + w * 3072 + i * 1024) = f32x4{oab[16 + 4 * i], oab[17 + 4 * i], oab[18 + 4 * i], oab[19 + 4 * i]};
    asm volatile("s_setprio 3\n\ts_waitcnt vmcnt(0) lgkmcnt(0)\n\ts_barrier" : "+{v[184:215]}"(of), "+{v[216:223]}"(of2) :: "memory");
    f32x16 o = {};
#pragma unroll
    for (int i = 0; i < 3; ++i) {
      const f32x4 r = *(lds_f4)(xb + (w ^ 4) * 3072 + i * 1024);
#pragma unroll
      for (int e = 0; e < 4; ++e) o[4 * i + e] = oab[4 * i + e] + r[e];
    }
    float l = row_sum(o);
    if (__builtin_expect(unsafe(o, l), 0)) {
      o = exact(0, __builtin_bit_cast(bf16x8, u32x4{qq[0], qq[1], qq[2], qq[3]}), __builtin_bit_cast(s16x4, u32x2{qq[8], qq[9]}), 0, 63);
      l = row_sum(o);
    }
    store_o<true>(o, l, oA, at[9], qq[14]);
  }
  STAMP(14);
  {
    f32x32 p1, p2, oa; f32x8 oa2;
    const unsigned wl0 = wl & 0xffff, wl1 = wl >> 16;
    const char* ws0 = woa + (qb * 160 + (w < 5 ? w : w + 5) * (32 * 1280) - 4096ll);
    const char* ws1 = woa + (qb * 160 + (13 + (w & 1)) * (32 * 1280) - 4096ll);
    {
      const unsigned b1 = grp * 51200, b2 = grp ? 143360 : 30720;
      proj_1(p1, of, of2, oa, oa2, u32x4{wl0 + b1, wl1 + b1, wl0 + b2, wl1 + b2}, wo, ws0, ws1, 86016 + w * 5120,
             w < 2 ? 86016 + (8 + w) * 5120 : 0x80000000u, ws);
    }
    STAMP(16);
    lane = lane_id();
    proj_send(p1, lds + 143360, lw, grp, lane);
    wo_dma(ws0 + 5 * 32 * 1280, w * 5120, wo);
    if (w < 2) { wo_dma(ws1 + 5 * 32 * 1280, (8 + w) * 5120, wo); WAITV(10); } else WAITV(5);
    char* dst = ptr(kc[6], kc[7]) + ((head * 64 + lw * 16) * 2560 + qb * 160);
    STAMP(17);
    const unsigned b1 = 86016 + grp * 25600, b2 = grp * 25600;
    const u32x4 wab = {wl0 + b1, wl1 + b1, wl0 + b2, wl1 + b2};
    asm volatile("" : "={v[160:191]}"(p2));
    proj_2<0>(p2, oa, oa2, wab, wo, ws);
    STAMP(18);
    proj_finish<(OUT_WT & 1) != 0>(p1, lds + 143360, lw, grp, lane, dst + 1280);
    proj_2<5>(p2, oa, oa2, wab, wo, ws);
    STAMP(20);
    proj_send(p2, lds + 86016, lw, grp, lane);
    asm volatile("s_waitcnt lgkmcnt(0)\n\ts_barrier" ::: "memory");
    proj_finish<(OUT_WT & 2) != 0>(p2, lds + 86016, lw, grp, lane, dst);
  }
  STAMP_OUT(ws, 0);
}

extern "C" void kernel_launch(void* const* d_in, const int* in_sizes, int n_in, void* d_out, int out_size, void* d_ws,
                              size_t ws_size, hipStream_t stream) {
  k_fused<<<256, 512, 0, stream>>>((const char*)d_in[0], (const char*)d_in[1], (const char*)d_in[2], (const char*)d_in[3],
                                   (const char*)d_in[4], (const char*)d_in[5], (char*)d_ws, (char*)d_out);
}
```

```cpp
#include <hip/hip_runtime.h>
#include <hip/hip_bf16.h>
#include <stdint.h>
#include <type_traits>

typedef __attribute__((ext_vector_type(8))) __bf16 bf16x8;
typedef __attribute__((ext_vector_type(16))) __bf16 bf16x16;
typedef __attribute__((ext_vector_type(4))) short s16x4;
typedef __attribute__((ext_vector_type(8))) short s16x8;
typedef __attribute__((ext_vector_type(2))) unsigned u32x2;
typedef __attribute__((ext_vector_type(4))) unsigned u32x4;
typedef __attribute__((ext_vector_type(4))) float f32x4;
typedef __attribute__((ext_vector_type(8))) float f32x8;
typedef __attribute__((ext_vector_type(16))) float f32x16;
typedef __attribute__((ext_vector_type(32))) float f32x32;
typedef __attribute__((address_space(3))) const char* lds_cptr;
typedef __attribute__((address_space(3))) char* lds_ptr;

#define OCT 33280
#define STR_(x) #x
#define STR(x) STR_(x)
constexpr size_t QK_WS = 0, QK_BR = (size_t)160 * OCT;
constexpr size_t V_WS = 2 * QK_BR, V_BR = (size_t)8 * 256 * 1280;
constexpr size_t O_WS = V_WS + 2 * V_BR, O_BR = (size_t)80 * OCT;
constexpr size_t CTL_WS = O_WS + 2 * O_BR;
constexpr unsigned FQ_OFF = CTL_WS + 4096, FO_OFF = FQ_OFF + 2 * 30 * 64 * 128;
constexpr float QSCALE = 0.22360679774997896f * 1.4426950408889634f;

#define STAMP_DECL
#define STV_PUT(x)
#define STV_GET(x)
#define STAMP(i) do {} while (0)
#define STAMP_OUT(ws_, kid) do {} while (0)
__device__ __forceinline__ int lane_id() {
  int l; asm volatile("v_mbcnt_lo_u32_b32 %0, -1, 0\n\tv_mbcnt_hi_u32_b32 %0, -1, %0" : "=v"(l)); return l;
}
#define SBAR() __builtin_amdgcn_sched_barrier(0)
#define PIN(x) asm volatile("" : "+v"(x))
#define PINS(x) asm volatile("" : "+s"(x))
#define WAITV(N) asm volatile("s_waitcnt vmcnt(" #N ")" ::: "memory")
#define BARRIER() asm volatile("s_barrier" ::: "memory")
#define WAITV_BAR(N) asm volatile("s_waitcnt vmcnt(" #N ") lgkmcnt(0)\n\ts_barrier" ::: "memory")

__device__ __forceinline__ void glds16(const void* sbase, unsigned voff, unsigned lds) {
  unsigned keep;
  asm volatile("s_mov_b32 %0, m0\n\ts_mov_b32 m0, %3\n\ts_nop 0\n\tglobal_load_lds_dwordx4 %1, %2\n\ts_mov_b32 m0, %0"
               : "=&s"(keep) : "v"(voff), "s"(sbase), "s"(lds) : "memory");
}
__device__ __forceinline__ void glds16x4(const void* sbase, const unsigned (&voff)[4], unsigned lds) {
  unsigned keep;
  asm volatile("s_mov_b32 %0, m0\n\ts_mov_b32 m0, %6\n\ts_nop 0\n\tglobal_load_lds_dwordx4 %1, %5\n\t"
               "global_load_lds_dwordx4 %2, %5 offset:1024\n\tglobal_load_lds_dwordx4 %3, %5 offset:2048\n\t"
               "global_load_lds_dwordx4 %4, %5 offset:3072\n\ts_mov_b32 m0, %0"
               : "=&s"(keep) : "v"(voff[0]), "v"(voff[1]), "v"(voff[2]), "v"(voff[3]), "s"(sbase), "s"(lds) : "memory");
}
__device__ __forceinline__ unsigned pk_bf16(float lo, float hi) {
  typedef __attribute__((ext_vector_type(2))) float f32x2_;
  typedef __attribute__((ext_vector_type(2))) __bf16 bf16x2_;
  const f32x2_ v = {lo, hi};
  return __builtin_bit_cast(unsigned, __builtin_convertvector(v, bf16x2_));
}
__device__ __forceinline__ void st16(char* sbase, unsigned voff, u32x4 v) {
  asm volatile("global_store_dwordx4 %0, %1, %2 sc1\n\ts_nop 1" :: "v"(voff), "v"(v), "s"(sbase) : "memory");
}
__device__ __forceinline__ void st8lo(char* sbase, unsigned voff, u32x2 v) {
  asm volatile("s_mov_b32 exec_hi, 0\n\tglobal_store_dwordx2 %0, %1, %2 sc1\n\ts_mov_b32 exec_hi, -1" :: "v"(voff), "v"(v), "s"(sbase) : "memory");
}
__device__ __forceinline__ bf16x8 lds_b128(lds_cptr p) { return *(const __attribute__((address_space(3))) bf16x8*)p; }
__device__ __forceinline__ s16x4 lds_b64(lds_cptr p) { return *(const __attribute__((address_space(3))) s16x4*)p; }
__device__ __forceinline__ s16x4 lds_tr(lds_cptr p) {
  return __builtin_amdgcn_ds_read_tr16_b64_v4i16((__attribute__((address_space(3))) s16x4*)p);
}
__device__ __forceinline__ bf16x8 cat8(s16x4 a, s16x4 b) {
  s16x8 v = {a[0], a[1], a[2], a[3], b[0], b[1], b[2], b[3]};
  return __builtin_bit_cast(bf16x8, v);
}
#define MFMA32(a, b, c) __builtin_amdgcn_mfma_f32_32x32x16_bf16(a, b, c, 0, 0, 0)
#define MFMA32K8(a, b, c) __builtin_amdgcn_mfma_f32_32x32x8bf16_1k(a, b, c, 0, 0, 0)
#define MFMA16(a, b, c) __builtin_amdgcn_mfma_f32_16x16x32_bf16(a, b, c, 0, 0, 0)

typedef __attribute__((ext_vector_type(8))) unsigned u32x8;
typedef __attribute__((ext_vector_type(16))) unsigned u32x16;
#define AWAIT(d, d1, off) \
    "s_mov_b32 s48, 0x200000\n\t5:\n\ts_waitcnt vmcnt(0)\n\tv_cmp_ne_u64 vcc, s[50:51], v[" #d ":" #d1 "]\n\ts_sub_u32 s48, s48, 1\n\t" \
    "s_cbranch_vccz 4f\n\ts_cbranch_scc1 4f\n\ts_sleep 2\n\tglobal_load_dwordx2 v[" #d ":" #d1 "], v" #off ", s[54:55] sc1\n\ts_branch 5b\n\t4:\n\t"
#define G1_STAMP(i)
#define NPOLL 22
#define NFLAG 6
#define NAW 28
#define KBAR 11
__device__ __forceinline__ void g1_all(unsigned vt1, unsigned kv1, unsigned vt2, u32x16& ad, u32x16& pk, u32x16& at, u32x4& cv4,
                                       const char* sA, unsigned off1, unsigned str1, const char* sB, unsigned off2, unsigned m0v,
                                       unsigned long long tag, unsigned flag, char* ws, unsigned tile, u32x4 so, unsigned qoff) {
  unsigned keep;
  f32x32 d0, d1, d2, d3, d4; f32x16 d5, d6; f32x8 d7; f32x4 d8;
  asm volatile(
      ".macro pack vt\n\t.set i_, 0\n\t.rept 4\n\t"
      ".if \\vt\n\t.set e_, 8 * i_\n\t.set f_, 8 * i_ + 4\n\t.else\n\t.set e_, 4 * i_\n\t.set f_, 4 * i_ + 16\n\t.endif\n\t"
      "v_cvt_pk_bf16_f32 v[48+4*i_], v[e_], v[e_+1]\n\tv_cvt_pk_bf16_f32 v[49+4*i_], v[e_+2], v[e_+3]\n\t"
      "v_cvt_pk_bf16_f32 v[50+4*i_], v[f_], v[f_+1]\n\tv_cvt_pk_bf16_f32 v[51+4*i_], v[f_+2], v[f_+3]\n\t"
      ".set i_, i_ + 1\n\t.endr\n\t.set i_, 0\n\t.rept 4\n\t"
      "v_permlane16_swap_b32 v[48+4*i_], v[50+4*i_]\n\tv_permlane16_swap_b32 v[49+4*i_], v[51+4*i_]\n\t"
      ".set i_, i_ + 1\n\t.endr\n\t.endm\n\t"
      ".macro mm vt, c, x, w\n\t"
      ".if (\\x) < 104\n\t"
      ".if \\vt\n\tv_mfma_f32_16x16x32_bf16 v[\\c:\\c+3], v[\\x:\\x+3], v[\\w:\\w+3], 0\n\t"
      ".else\n\tv_mfma_f32_16x16x32_bf16 v[\\c:\\c+3], v[\\w:\\w+3], v[\\x:\\x+3], 0\n\t.endif\n\t"
      ".elseif \\vt\n\tv_mfma_f32_16x16x32_bf16 v[\\c:\\c+3], v[\\x:\\x+3], v[\\w:\\w+3], v[\\c:\\c+3]\n\t"
      ".else\n\tv_mfma_f32_16x16x32_bf16 v[\\c:\\c+3], v[\\w:\\w+3], v[\\x:\\x+3], v[\\c:\\c+3]\n\t.endif\n\t"
      ".endm\n\t"
      ".macro rx d, a, off\n\tds_read_b128 v[\\d:\\d+3], v\\a offset:\\off\n\tds_read_b128 v[\\d+4:\\d+7], v\\a offset:\\off+2048\n\t.endm\n\t"
      ".macro rw d, a, off\n\tds_read_b64_tr_b16 v[\\d:\\d+1], v\\a offset:\\off\n\tds_read_b64_tr_b16 v[\\d+2:\\d+3], v\\a offset:\\off+512\n\t.endm\n\t"
      ".macro bump lo, n, val\n\t.set bi_, \\lo\n\t.rept \\n\n\tv_add_u32 v[bi_], \\val, v[bi_]\n\t.set bi_, bi_+1\n\t.endr\n\t.endm\n\t"
      ".macro src2\n\ts_add_u32 s40, s44, s47\n\ts_addc_u32 s41, s45, 0\n\t"
      "s_add_u32 s47, s47, 2*64*3840\n\ts_cmp_ge_u32 s47, 640*3840\n\ts_cselect_b32 s48, 640*3840, 0\n\ts_sub_u32 s47, s47, s48\n\t.endm\n\t"
      ".macro p1body vt\n\t"
      ".set st_, 0\n\t"
      ".rept 10\n\t"
      ".set xr_, 96 + 16 * st_\n\t"
      ".set o_, ((st_ %% 5) & 1) * 32768\n\t"
      ".set n_, (((st_ + 1) %% 5) & 1) * 32768 + 16384\n\t.if st_ == 9\n\t.set n_, 0\n\t.endif\n\t"
      "s_waitcnt lgkmcnt(6)\n\t"
      "mm \\vt, 0, xr_, 32\n\trw 48, 76, o_+16384+8192\n\t"
      "mm \\vt, 16, xr_+4, 32\n\trx xr_+8, 74, o_\n\ts_waitcnt lgkmcnt(8)\n\t"
      "mm \\vt, 4, xr_, 36\n\trw 52, 77, o_+16384+8192\n\t"
      "mm \\vt, 20, xr_+4, 36\n\trw 56, 78, o_+16384+8192\n\ts_waitcnt lgkmcnt(10)\n\t"
      "mm \\vt, 8, xr_, 40\n\trw 60, 79, o_+16384+8192\n\t"
      "mm \\vt, 24, xr_+4, 40\n\ts_waitcnt lgkmcnt(10)\n\t"
      "mm \\vt, 12, xr_, 44\n\t"
      ".if st_ < 7\n\t"
      "s_add_u32 s40, s42, s36\n\ts_addc_u32 s41, s43, 0\n\ts_add_u32 s36, s36, s37\n\ts_cmp_eq_u32 s36, s38\n\ts_cselect_b32 s36, 0, s36\n\t"
      ".else\n\tsrc2\n\t.endif\n\t"
      "mm \\vt, 28, xr_+4, 44\n\t"
      "s_add_u32 m0, s46, ((st_ + 3) %% 5) * 32768\n\t"
      ".if (st_ %% 5) == 1 || (st_ %% 5) == 3\n\tbump 76, 4, 0x10000\n\tbump 73, 2, 0x10000\n\t.endif\n\t"
      ".if (st_ %% 5) == 4\n\tbump 76, 4, 0xfffe0000\n\t.if st_ < 9\n\tbump 73, 2, 0xfffe0000\n\t.endif\n\t.endif\n\t"
      "s_waitcnt lgkmcnt(6)\n\t"
      "mm \\vt, 0, xr_+8, 48\n\t"
      "mm \\vt, 16, xr_+12, 48\n\t"
      "s_waitcnt vmcnt(4) lgkmcnt(0)\n\ts_barrier\n\t"
      ".set vo_, 64 + 4 * ((st_ + 3) / 10)\n\t"
      "mm \\vt, 4, xr_+8, 52\n\trw 32, 76, n_\n\t"
      "global_load_lds_dwordx4 v[vo_], s[40:41]\n\t"
      "mm \\vt, 20, xr_+12, 52\n\t"
      ".if st_ < 9\n\trx xr_+16, 73, n_-16384\n\t.endif\n\t"
      "global_load_lds_dwordx4 v[vo_+1], s[40:41] offset:1024\n\t"
      "mm \\vt, 8, xr_+8, 56\n\trw 36, 77, n_\n\t"
      "global_load_lds_dwordx4 v[vo_+2], s[40:41] offset:2048\n\t"
      "mm \\vt, 24, xr_+12, 56\n\trw 40, 78, n_\n\t"
      "global_load_lds_dwordx4 v[vo_+3], s[40:41] offset:3072\n\t"
      "mm \\vt, 12, xr_+8, 60\n\trw 44, 79, n_\n\t"
      "mm \\vt, 28, xr_+12, 60\n\t"
      ".set st_, st_ + 1\n\t"
      ".endr\n\t"
      "v_mov_b64 v[64:65], v[80:81]\n\tv_add_u32 v66, " STR(OCT) "*4 - (" STR(OCT) "*4 - 2560) * \\vt, v80\n\tv_add_u32 v67, " STR(OCT) "*4 - (" STR(OCT) "*4 - 2560) * \\vt, v81\n\t"
      ".endm\n\t"
      ".macro tdma i\n\t"
      "s_add_u32 m0, s72, \\i * 28672\n\ts_add_u32 s40, s54, s[76+2*\\i]\n\ts_addc_u32 s41, s55, 0\n\t"
      "global_load_lds_dwordx4 v137, s[40:41] sc1\n\ts_mov_b32 exec_hi, 0\n\tglobal_load_lds_dwordx4 v138, s[40:41] offset:1024 sc1\n\t"
      "s_add_u32 s40, s54, s[77+2*\\i]\n\ts_addc_u32 s41, s55, 0\n\ts_mov_b32 exec_lo, 0xfffff\n\ts_mov_b32 exec_hi, 0xfffff\n\t"
      "global_load_lds_dwordx4 v139, s[40:41] offset:1536 sc1\n\tglobal_load_lds_dwordx4 v140, s[40:41] offset:2560 sc1\n\ts_mov_b64 exec, -1\n\t"
      ".endm\n\t"
      ".macro slot n\n\t.set n_, \\n\n\t"
      ".if n_ >= 2 && n_ < 10\n\t"
      ".if ((n_ - 2) & 3) == 0\n\ts_add_u32 m0, s46, (3 + (n_ - 2) / 4) * 32768\n\tsrc2\n\t.endif\n\t"
      "global_load_lds_dwordx4 v[68+((n_-2)&3)], s[40:41] offset:1024*((n_-2)&3)\n\t"
      ".endif\n\t"
      ".if n_ == %c[nflag]\n\tv_mov_b32 v96, s50\n\tv_mov_b32 v97, s51\n\tv_mov_b32 v98, s52\n\ts_mov_b64 exec, 1\n\ts_waitcnt vmcnt(%c[nflag] - 1)\n\t"
      "global_store_dwordx2 v98, v[96:97], s[54:55] sc1\n\ts_mov_b64 exec, -1\n\t.endif\n\t"
      ".if n_ == 12\n\tv_mov_b64 v[128:129], v[84:85]\n\tv_mov_b32 v130, v86\n\tv_add_u32 v131, 57344, v84\n\tv_add_u32 v132, 57344, v85\n\t.endif\n\t"
      ".if n_ == 13\n\tv_add_u32 v133, 57344, v86\n\tv_add_u32 v134, 114688, v84\n\tv_add_u32 v135, 114688, v85\n\tv_add_u32 v136, 114688, v86\n\t.endif\n\t"
      ".if n_ == 14\n\tv_mov_b32 v137, v87\n\tv_mov_b64 v[138:139], v[88:89]\n\tv_add_u32 v140, 1536, v89\n\tv_mov_b32 v141, v90\n\t.endif\n\t"
      ".if n_ == 15\n\tv_mov_b32 v104, v92\n\tv_mov_b32 v105, v92\n\tv_mov_b32 v106, v92\n\tv_mov_b32 v107, v92\n\t.endif\n\t"
      ".if n_ == %c[naw] + 3\n\tds_write_b128 v91, v[104:107]\n\tds_write_b128 v91, v[104:107] offset:28672\n\t.endif\n\t"
      ".if n_ == %c[npoll]\n\tglobal_load_dwordx2 v[100:101], v72, s[54:55] sc1\n\t.endif\n\t"
      ".if n_ == %c[naw]\n\t" AWAIT(100, 101, 72) G1_STAMP(6) "tdma 0\n\t.endif\n\t"
      ".if n_ == %c[naw] + 1\n\ts_add_u32 s40, s54, s80\n\ts_addc_u32 s41, s55, 0\n\ts_xor_b32 s48, s80, 512\n\t"
      "global_load_dwordx4 v[64:67], v137, s[40:41] sc1\n\tglobal_load_dwordx2 v[72:73], v93, s[40:41] sc1\n\t"
      "s_add_u32 s40, s54, s48\n\ts_addc_u32 s41, s55, 0\n\t"
      "global_load_dwordx4 v[68:71], v137, s[40:41] sc1\n\tglobal_load_dwordx2 v[74:75], v93, s[40:41] sc1\n\t.endif\n\t"
      ".if n_ == %c[naw] + 2\n\ttdma 1\n\t.endif\n\t"
      ".endm\n\t"
      ".macro st1 i\n\t.if k_ == 0\n\tglobal_store_dwordx4 v[64+\\i], v[48+4*\\i:51+4*\\i], s[54:55] sc1\n\t.endif\n\t.endm\n\t"
      ".macro p2body vt\n\t"
      ".set k_, 0\n\t"
      ".rept 20\n\t"
      ".set xr_, 96 + 8 * k_\n\t"
      ".set c_, 32 + 16 * (k_ & 1)\n\t"
      ".set d_, 48 - 16 * (k_ & 1)\n\t"
      ".set o_, ((k_ + 1) & 7) * 8192\n\t"
      ".if k_ == 3\n\ts_waitcnt vmcnt(8)\n\ts_barrier\n\t.endif\n\t"
      ".if k_ == %c[kbar]\n\ts_waitcnt vmcnt(0)\n\ts_barrier\n\t.endif\n\t"
      ".if \\vt == 2\n\tst1 0\n\tst1 1\n\tst1 2\n\tst1 3\n\tslot 2*k_\n\tslot 2*k_+1\n\t"
      ".elseif k_ < 19\n\t"
      ".if ((k_ + 1) & 7) == 0\n\tbump 76, 4, 0x10000\n\t.endif\n\t"
      "s_waitcnt lgkmcnt(6)\n\t"
      "mm \\vt, 0, xr_, c_\n\tst1 0\n\trw d_, 76, o_\n\t"
      "mm \\vt, 16, xr_+4, c_\n\tslot 2*k_\n\ts_waitcnt lgkmcnt(6)\n\t"
      "mm \\vt, 4, xr_, c_+4\n\tst1 1\n\trw d_+4, 77, o_\n\t"
      "mm \\vt, 20, xr_+4, c_+4\n\ts_waitcnt lgkmcnt(6)\n\t"
      "mm \\vt, 8, xr_, c_+8\n\tst1 2\n\trw d_+8, 78, o_\n\t"
      "mm \\vt, 24, xr_+4, c_+8\n\tslot 2*k_+1\n\ts_waitcnt lgkmcnt(6)\n\t"
      "mm \\vt, 12, xr_, c_+12\n\tst1 3\n\trw d_+12, 79, o_\n\t"
      "mm \\vt, 28, xr_+4, c_+12\n\t"
      ".else\n\t"
      "s_waitcnt lgkmcnt(0)\n\t"
      "mm \\vt, 0, xr_, c_\n\tmm \\vt, 16, xr_+4, c_\n\tmm \\vt, 4, xr_, c_+4\n\tmm \\vt, 20, xr_+4, c_+4\n\t"
      "mm \\vt, 8, xr_, c_+8\n\tmm \\vt, 24, xr_+4, c_+8\n\tmm \\vt, 12, xr_, c_+12\n\tmm \\vt, 28, xr_+4, c_+12\n\t"
      "s_setprio 3\n\tv_add_u32 v80, " STR(OCT) "*4 - (" STR(OCT) "*4 - 2560) * \\vt, v82\n\tv_add_u32 v81, " STR(OCT) "*4 - (" STR(OCT) "*4 - 2560) * \\vt, v83\n\ts_nop 4\n\tpack \\vt\n\t"
      ".endif\n\t"
      ".set k_, k_ + 1\n\t"
      ".endr\n\t"
      ".endm\n\t"
      "rw 32, 76, 16384\n\trx 96, 73, 0\n\trw 36, 77, 16384\n\trw 40, 78, 16384\n\trw 44, 79, 16384\n\t"
      "s_mov_b32 %[keep], m0\n\t"
      "s_cmp_lg_u32 %[vt1], 0\n\ts_cbranch_scc1 1f\n\t"
      "p1body 0\n\t"
      "s_mov_b32 s40, %[sc]\n\ts_cmp_lg_u32 %[kv1], 0\n\ts_nop 2\n\ts_cbranch_scc1 3f\n\t"
      ".set i_, 0\n\t.rept 16\n\tv_pk_mul_f32 v[i_:i_+1], v[i_:i_+1], s[40:41] op_sel_hi:[1,0]\n\t.set i_, i_ + 2\n\t.endr\n\t"
      "3:\n\tpack 0\n\ts_branch 2f\n\t"
      "1:\n\tp1body 1\n\ts_nop 4\n\tpack 1\n\t"
      "2:\n\t" G1_STAMP(3)
      "s_cmp_eq_u32 %[vt2], 1\n\ts_cbranch_scc1 1f\n\ts_cmp_eq_u32 %[vt2], 2\n\ts_cbranch_scc0 6f\n\t"
      "p2body 2\n\ts_waitcnt vmcnt(0)\n\ts_setprio 3\n\ts_branch 3f\n\t"
      "6:\n\tp2body 0\n\ts_branch 2f\n\t"
      "1:\n\tp2body 1\n\t"
      "2:\n\tglobal_store_dwordx4 v82, v[48:51], s[54:55] sc1\n\tglobal_store_dwordx4 v83, v[52:55], s[54:55] sc1\n\t"
      "global_store_dwordx4 v80, v[56:59], s[54:55] sc1\n\tglobal_store_dwordx4 v81, v[60:63], s[54:55] sc1\n\t"
      "3:\n\ts_mov_b32 m0, %[keep]\n\tv_mov_b32 v76, 0xdddd\n\tv_mov_b32 v77, v94\n\tv_mov_b32 v78, v93\n\t"
      "v_mbcnt_lo_u32_b32 v79, -1, 0\n\tv_mbcnt_hi_u32_b32 v79, -1, v79\n\tv_lshlrev_b32 v79, 4, v79\n\t"
      "s_waitcnt vmcnt(8) lgkmcnt(0)\n\t"
      ".purgem pack\n\t.purgem mm\n\t.purgem rx\n\t.purgem rw\n\t.purgem bump\n\t.purgem src2\n\t.purgem p1body\n\t.purgem tdma\n\t"
      ".purgem slot\n\t.purgem st1\n\t.purgem p2body\n\t"
      : "+{v[64:79]}"(ad), "+{v[80:95]}"(pk), "=&{v[128:143]}"(at), "=&{v[104:107]}"(cv4), [keep] "=&s"(keep), "+{s36}"(off1), "+{s47}"(off2),
        "=&{v[0:31]}"(d0), "=&{v[32:63]}"(d1), "=&{v[160:191]}"(d2), "=&{v[192:223]}"(d3), "=&{v[224:255]}"(d4), "=&{v[112:127]}"(d5),
        "=&{v[144:159]}"(d6), "=&{v[96:103]}"(d7), "=&{v[108:111]}"(d8)
      : [vt1] "s"(vt1), [kv1] "s"(kv1), [vt2] "s"(vt2), "{s[42:43]}"(sA), "{s37}"(str1), "{s38}"(10 * str1), "{s[44:45]}"(sB), "{s46}"(m0v),
        "{s[50:51]}"(tag), "{s52}"(flag), "{s[54:55]}"(ws), "{s72}"(tile), "{s[76:79]}"(so), "{s80}"(qoff),
        [sc] "n"(__builtin_bit_cast(unsigned, QSCALE)), [npoll] "n"(NPOLL), [nflag] "n"(NFLAG), [naw] "n"(NAW), [kbar] "n"(KBAR)
      : "memory", "scc", "vcc", "s40", "s41", "s48");
}

__device__ __forceinline__ u32x2 g1_offs(unsigned br, unsigned cg, unsigned T, unsigned lane) {
  const unsigned l16 = (lane & 31) * 16, h = lane >> 5;
  const unsigned a = h * OCT + l16 + (br * (unsigned)QK_BR + cg * (8 * OCT) + T * 512);
  const unsigned G = (cg - 20) * 4 + ((lane >> 4) & 1);
  const unsigned b = h * 1280 + l16 + ((unsigned)V_WS + br * (unsigned)V_BR + (cg - 20) * 1024 + T * 5120);
  return cg < 20 ? u32x2{a, a + 2 * OCT}
                 : u32x2{((G * 52) >> 8) * (256 * 1280 - 1280) + b, (((G + 2) * 52) >> 8) * (256 * 1280 - 1280) + b + 512};
}

constexpr int TILE_B = 3584, K2_OFF = 1024, V_OFF = 1536;
constexpr int CHUNK_B = 8 * TILE_B;

__device__ __forceinline__ f32x16 smfmac(bf16x8 a, f32x8 p, f32x16 c) {
  return __builtin_amdgcn_smfmac_f32_32x32x32_bf16(a, __builtin_bit_cast(bf16x16, p), c, 0xDDDD, 0, 0);
}
__device__ __forceinline__ f32x8 lo8(const f32x16& s) { return __builtin_shufflevector(s, s, 0, 1, 2, 3, 4, 5, 6, 7); }
__device__ __forceinline__ f32x8 hi8(const f32x16& s) { return __builtin_shufflevector(s, s, 8, 9, 10, 11, 12, 13, 14, 15); }
#define MX3(a, b, c) __builtin_fmaxf(__builtin_fmaxf((a), (b)), (c))
__device__ __forceinline__ float max16(const f32x16& s, float m) {
#pragma unroll
  for (int r = 0; r < 16; r += 2) m = MX3(m, s[r], s[r + 1]);
  return m;
}
__device__ __forceinline__ float xhalf_max(float m) {
  auto rr = __builtin_amdgcn_permlane32_swap(__float_as_uint(m), __float_as_uint(m), false, false);
  return __builtin_fmaxf(__uint_as_float(rr[0]), __uint_as_float(rr[1]));
}
__device__ __forceinline__ float row_sum(const f32x16& o) {
  auto rr = __builtin_amdgcn_permlane32_swap(__float_as_uint(o[8]), __float_as_uint(o[8]), false, false);
  return __uint_as_float(rr[1]);
}
template <bool MARK>
__device__ __forceinline__ void store_o(const f32x16& o, float l, char* base, unsigned lo1, unsigned lo2) {
  const float rl = __builtin_amdgcn_rcpf(l);
  const unsigned a0 = pk_bf16(o[0] * rl, o[1] * rl), a1 = pk_bf16(o[2] * rl, o[3] * rl);
  const unsigned b0 = pk_bf16(o[4] * rl, o[5] * rl), b1 = pk_bf16(o[6] * rl, o[7] * rl);
  const auto r0 = __builtin_amdgcn_permlane32_swap(a0, b0, false, false);
  const auto r1 = __builtin_amdgcn_permlane32_swap(a1, b1, false, false);
  u32x4 v = {r0[0], r1[0], r0[1], r1[1]};
  u32x2 c = {pk_bf16(o[8] * rl, o[9] * rl), pk_bf16(o[10] * rl, o[11] * rl)};
  if (MARK) {
    v = __builtin_elementwise_min(v, u32x4{~1u, ~1u, ~1u, ~1u}); c = __builtin_elementwise_min(c, u32x2{~1u, ~1u});
  }
  st16(base, lo1, v);
  st8lo(base, lo2, c);
}

__device__ __forceinline__ void poll_flags(const char* ws, unsigned off, unsigned long long tag) {
  asm volatile("global_load_dwordx2 v[124:125], v126, s[54:55] sc1\n\t" AWAIT(124, 125, 126)
               :: "{v126}"(off), "{s[54:55]}"(ws), "{s[50:51]}"(tag) : "memory", "scc", "vcc", "s48", "v124", "v125");
}
__device__ __forceinline__ void put_flag(char* ws, unsigned off, unsigned long long tag) {
  asm volatile("s_mov_b64 exec, 1\n\tglobal_store_dwordx2 %0, %1, %2 sc1\n\ts_mov_b64 exec, -1" :: "v"(off), "v"(tag), "s"(ws) : "memory");
}
__device__ __forceinline__ void wo_dma(const char* src, unsigned d, const u32x8& wo) {
  unsigned keep;
  asm volatile("s_mov_b32 %0, m0\n\ts_mov_b32 m0, %7\n\ts_nop 0\n\t"
               "global_load_lds_dwordx4 %1, %6\n\tglobal_load_lds_dwordx4 %2, %6 offset:1024\n\t"
               "global_load_lds_dwordx4 %3, %6 offset:2048\n\tglobal_load_lds_dwordx4 %4, %6 offset:3072\n\t"
               "s_add_u32 m0, m0, 4096\n\ts_nop 0\n\tglobal_load_lds_dwordx4 %5, %6\n\ts_mov_b32 m0, %0"
               : "=&s"(keep) : "v"(wo[0]), "v"(wo[1]), "v"(wo[2]), "v"(wo[3]), "v"(wo[4]), "s"(src), "s"(d) : "memory", "scc");
}

#define AT_ASM_MACROS \
    ".macro mx d, s\n\tv_max3_f32 v[\\d], v[\\s], v[\\s+1], v[\\s+2]\n\t.set i_, \\s + 3\n\t.rept 6\n\tv_max3_f32 v[\\d], v[\\d], v[i_], v[i_+1]\n\t" \
    ".set i_, i_ + 2\n\t.endr\n\tv_max_f32 v[\\d], v[\\d], v[\\s+15]\n\t.endm\n\t" \
    ".macro xp d, s\n\tv_exp_f32 v[\\d], v[\\s]\n\tv_exp_f32 v[\\d+1], v[\\s+1]\n\tv_exp_f32 v[\\d+2], v[\\s+2]\n\tv_exp_f32 v[\\d+3], v[\\s+3]\n\t.endm\n\t" \
    ".macro smf o, v, p\n\tv_smfmac_f32_32x32x32_bf16 v[\\o:\\o+15], v[\\v:\\v+3], v[\\p:\\p+7], v76\n\t.endm\n\t" \
    ".macro qk1 s, k, q\n\tv_mfma_f32_32x32x16_bf16 v[\\s:\\s+15], v[\\k:\\k+3], v[\\q:\\q+3], 0\n\t.endm\n\t" \
    ".macro qk1c s, k, q, c\n\tv_mfma_f32_32x32x16_bf16 v[\\s:\\s+15], v[\\k:\\k+3], v[\\q:\\q+3], v[\\c:\\c+15]\n\t.endm\n\t" \
    ".macro qk2 s, k, q\n\tv_mfma_f32_32x32x8_bf16 v[\\s:\\s+15], v[\\k:\\k+1], v[\\q:\\q+1], v[\\s:\\s+15]\n\t.endm\n\t"
#define AT_ASM_PURGE ".purgem mx\n\t.purgem xp\n\t.purgem smf\n\t.purgem qk1\n\t.purgem qk1c\n\t.purgem qk2\n\t"

#define AT_A_TEXT AT_ASM_MACROS \
      ".macro pos j\n\t.set sl_, (\\j) >> 2\n\t.if sl_ >= 3\n\t.set sl_, sl_ - 3\n\t.endif\n\t" \
      ".set ar_, 128 + 3 * (sl_ >> 1)\n\t.set of_, (sl_ & 1) * 28672 + ((\\j) & 3) * 7168\n\t.endm\n\t" \
      ".macro dma part\n\t" \
      ".if di_ >= 0\n\t" \
      ".if \\part == 0\n\t" \
      ".if di_ == 1\n\ts_mov_b32 m0, s73\n\t.else\n\ts_add_u32 m0, s72, ((di_ + 2) %% 5) * 28672\n\t.endif\n\t" \
      "s_add_u32 s40, s54, s[56+2*di_]\n\ts_addc_u32 s41, s55, 0\n\t" \
      "global_load_lds_dwordx4 v137, s[40:41] sc1\n\t" \
      ".elseif \\part == 1\n\t" \
      "s_mov_b32 exec_hi, 0\n\tglobal_load_lds_dwordx4 v138, s[40:41] offset:1024 sc1\n\ts_mov_b32 exec_hi, -1\n\t" \
      "s_add_u32 s40, s54, s[57+2*di_]\n\ts_addc_u32 s41, s55, 0\n\t" \
      ".else\n\t" \
      "s_mov_b32 exec_lo, 0xfffff\n\ts_mov_b32 exec_hi, 0xfffff\n\t" \
      "global_load_lds_dwordx4 v139, s[40:41] offset:1536 sc1\n\tglobal_load_lds_dwordx4 v140, s[40:41] offset:2560 sc1\n\t" \
      "s_mov_b64 exec, -1\n\t" \
      ".endif\n\t.endif\n\t" \
        \
      ".set n_, -1\n\t" \
      ".if j_ >= 22 && j_ < 26 && 3 * (j_ - 22) + \\part < 10\n\t.set n_, 3 * (j_ - 22) + \\part\n\t.set bk_, 2 * (n_ / 5)\n\t.endif\n\t" \
      ".if j_ >= 28 && 3 * (j_ - 28) + \\part < 5\n\t.set n_, 3 * (j_ - 28) + \\part\n\t.set bk_, 1\n\t.endif\n\t" \
      ".if n_ >= 0\n\t.set pc_, n_ %% 5\n\t" \
      ".if bk_ == 2\n\ts_cmp_lt_i32 s46, 0\n\ts_cbranch_scc1 9f\n\t.endif\n\t" \
      ".if pc_ == 0\n\ts_mov_b32 m0, s[44+bk_]\n\ts_add_u32 s40, s42, bk_ * 8 * 32 * 1280\n\ts_addc_u32 s41, s43, 0\n\t.endif\n\t" \
      ".if pc_ == 4\n\ts_add_u32 m0, m0, 4096\n\ts_nop 0\n\t.endif\n\t" \
      "global_load_lds_dwordx4 v[248+pc_], s[40:41] offset:1024*(pc_&3)\n\t" \
      ".if bk_ == 2\n\t9:\n\t.endif\n\t.endif\n\t" \
      ".endm\n\t" \
      ".macro flg msk\n\tv_mov_b32 v142, s50\n\tv_mov_b32 v143, s51\n\tv_mov_b32 v108, s52\n\ts_mov_b64 exec, \\msk\n\ts_cbranch_execz 8f\n\ts_waitcnt vmcnt(0)\n\t" \
      "global_store_dwordx2 v108, v[142:143], s[54:55] sc1\n\t8:\n\ts_mov_b64 exec, -1\n\t.endm\n\t" \
      ".macro tabl d, r\n\tglobal_load_dwordx4 v[\\d:\\d+3], v79, s[40:41] offset:1024*(\\r)\n\t.endm\n\t" \
      ".macro old q\n\t.if (\\q) >= 0 && (\\q) < 10\n\tglobal_load_dwordx4 v[184+4*(\\q):187+4*(\\q)], v108, s[54:55] sc1\n\tv_add_u32 v108, " STR(OCT) "*4, v108\n\t.endif\n\t.endm\n\t" \
      "s_mov_b32 %[keep], m0\n\t" \
      ".set j_, %c[j0]\n\t" \
      ".rept %c[j1] - %c[j0]\n\t" \
      ".set a_, 80 + 14 * (j_ & 1)\n\t.set b_, 94 - 14 * (j_ & 1)\n\t" \
      ".if j_ == 4\n\t" AWAIT(142, 143, 141) ".set di_, 0\n\tdma 0\n\tdma 1\n\tdma 2\n\t.endif\n\t" \
      ".if j_ == 6\n\ts_waitcnt vmcnt(10)\n\ts_barrier\n\t.endif\n\t" \
      ".if j_ == 10 || j_ == 14 || j_ == 22\n\ts_waitcnt vmcnt(0)\n\ts_barrier\n\t.endif\n\t" \
      ".if j_ == 18\n\ts_waitcnt vmcnt(1)\n\ts_barrier\n\t.endif\n\t" \
      ".if j_ == 28 || j_ == 2\n\ts_barrier\n\t.endif\n\t" \
      AT_STAMP \
      ".if j_ < 2\n\ts_setprio 1 - j_\n\t" \
      ".elseif j_ == 10 || j_ == 11\n\ts_setprio 3\n\t.elseif j_ == 12 || j_ == 13\n\ts_setprio 0\n\t" \
      ".elseif j_ < 22\n\ts_setprio 3 - ((j_ + 2) & 3)\n\t.elseif j_ >= 28\n\ts_setprio 31 - j_\n\t" \
      ".elseif j_ != 23 && j_ != 25\n\ts_setprio 3 - (j_ - 21) / 2\n\t.endif\n\t" \
      ".if j_ == 14\n\tflg 1\n\t" \
      "v_mov_b32 v108, 0x3f803f80\n\tv_mov_b32 v109, v108\n\tv_mov_b32 v110, v108\n\tv_mov_b32 v111, v108\n\ts_mov_b64 exec, 15\n\t" \
      "ds_write_b128 v77, v[108:111]\n\ts_mov_b64 exec, -1\n\t.endif\n\t" \
      ".if j_ == 28\n\t" AWAIT(142, 143, 253) "v_add_u32 v108, %c[obr], v254\n\t.endif\n\t" \
      ".set di_, -1\n\t" \
      ".if j_ == 4 || j_ == 5 || j_ == 6\n\t.set di_, j_ - 3\n\t.endif\n\t.if j_ == 10\n\t.set di_, 4\n\t.endif\n\t" \
      ".if j_ >= 14 && j_ < 17\n\t.set di_, j_ - 9\n\t.endif\n\t" \
        \
      "pos j_+2\n\t" \
      "qk1 48, a_, 68\n\t.if j_ < 30\n\tds_read_b128 v[a_:a_+3], v[ar_] offset:of_\n\t.endif\n\told 4*(j_-28)\n\txp 112, 32\n\t" \
      "qk2 48, a_+4, 74\n\t.if j_ < 30\n\tds_read_b64 v[a_+4:a_+5], v[ar_+1] offset:of_\n\t.endif\n\told 4*(j_-28)+1\n\txp 116, 36\n\t" \
      "pos j_+1\n\t" \
      "smf 16, b_+10, 120\n\t.if j_ < 31\n\tds_read_b128 v[b_+10:b_+13], v[ar_+2] offset:of_+1024\n\t.endif\n\told 4*(j_-28)+2\n\txp 120, 40\n\t" \
      "smf 0, a_+6, 112\n\t.if j_ < 30\n\ts_waitcnt lgkmcnt(3)\n\t.elseif j_ == 30\n\ts_waitcnt lgkmcnt(1)\n\t.else\n\ts_waitcnt lgkmcnt(0)\n\t.endif\n\t" \
      "old 4*(j_-28)+3\n\t" \
      ".if j_ == 5\n\tglobal_load_dwordx4 v[236:239], v137, s[42:43] sc1\n\tglobal_load_dwordx2 v[240:241], v78, s[42:43] sc1\n\t.endif\n\t" \
      "xp 124, 44\n\t" \
        \
      ".if j_ < 31\n\tqk1 32, b_, 64\n\t.endif\n\tdma 0\n\t" \
      ".if j_ == 0\n\tflg s[74:75]\n\t.endif\n\t" \
      ".if j_ == 3\n\tglobal_load_dwordx2 v[142:143], v141, s[54:55] sc1\n\t.endif\n\t" \
      ".if j_ == 7\n\ts_mov_b64 s[40:41], s[44:45]\n\ttabl 248, 0\n\ttabl 252, 1\n\t.endif\n\t" \
      ".if j_ == 8\n\ttabl 220, 2\n\ttabl 224, 3\n\t.endif\n\t" \
      ".if j_ == 17\n\ts_mov_b32 m0, 143360\n\ts_nop 0\n\tglobal_load_lds_dword v141, s[74:75]\n\t.endif\n\t" \
      ".if j_ == 26\n\tglobal_load_dwordx2 v[142:143], v253, s[54:55] sc1\n\t.endif\n\t" \
      "xp 112, 48\n\t" \
      ".if j_ < 31\n\tqk2 32, b_+4, 72\n\t.endif\n\tdma 1\n\t" \
      ".if j_ == 7\n\ttabl 160, 2\n\ttabl 164, 3\n\ts_add_u32 s40, s40, 4096\n\ts_addc_u32 s41, s41, 0\n\t.endif\n\t" \
      ".if j_ == 8\n\ts_add_u32 s40, s40, 4096\n\ts_addc_u32 s41, s41, 0\n\ttabl 228, 0\n\ttabl 232, 1\n\t.endif\n\t" \
      "xp 116, 52\n\t" \
      "smf 0, a_+10, 120\n\tdma 2\n\t" \
      ".if j_ == 7\n\ttabl 168, 0\n\ttabl 172, 1\n\t.endif\n\t" \
      "xp 120, 56\n\t" \
      "pos j_+2\n\t" \
      "smf 16, a_+6, 112\n\t.if j_ < 30\n\tds_read_b128 v[a_+6:a_+9], v[ar_+2] offset:of_\n\t.endif\n\txp 124, 60\n\t" \
      ".set j_, j_ + 1\n\t" \
      ".endr\n\t" \
      ".if %c[j1] == 32\n\ts_nop 1\n\tsmf 16, 104, 120\n\ts_nop 15\n\t.endif\n\t" \
      "s_mov_b32 m0, %[keep]\n\t" \
      ".purgem pos\n\t.purgem dma\n\t.purgem flg\n\t.purgem tabl\n\t.purgem old\n\t" AT_ASM_PURGE
#define AT_STAMP
#define AT_STV
#define AT_STV_ARG
#define AT_STV_PASS
#define AT_A_STATE "+{v[0:31]}"(o), "+{v[32:47]}"(s0), "+{v[112:127]}"(pp), "+{v[80:111]}"(fr), "+{v[128:143]}"(ad), [keep] "=&s"(keep)
#define AT_A_CLOB "memory", "scc", "vcc", "s40", "s41", "s48", "v48", "v49", "v50", "v51", "v52", "v53", "v54", "v55", "v56", "v57", "v58", "v59", "v60", "v61", "v62", "v63"
__device__ __forceinline__ void at_a1(f32x32& o, f32x16& s0, f32x16& pp, f32x32& fr, const u32x16& q, u32x16& ad, const char* ws, u32x16 so,
                                      unsigned m0v, unsigned m0b, unsigned long long tag, unsigned flag, unsigned long long fmask,
                                      const char* qB, const void* tab, f32x16& mF, f32x16& mD, u32x8& qv, u32x8& tw AT_STV_ARG) {
  unsigned keep;
  asm volatile(AT_A_TEXT
      : AT_A_STATE, "=&{v[160:175]}"(mF), "=&{v[220:235]}"(mD), "=&{v[236:243]}"(qv), "=&{v[248:255]}"(tw) AT_STV
      : "{v[64:79]}"(q), "{s[54:55]}"(ws), "{s[56:71]}"(so), "{s72}"(m0v), "{s73}"(m0b), "{s[50:51]}"(tag), "{s52}"(flag), "{s[74:75]}"(fmask),
        "{s[42:43]}"(qB), "{s[44:45]}"(tab), [j0] "n"(0), [j1] "n"(12), [obr] "n"(O_BR)
      : AT_A_CLOB);
}
__device__ __forceinline__ void at_a2(f32x32& o, f32x16& s0, f32x16& pp, f32x32& fr, const u32x16& q, u32x16& ad, const char* ws, u32x16 so,
                                      unsigned m0v, unsigned long long tag, unsigned flag, const char* woa, const u32x8& wo, const char* src,
                                      unsigned d0, unsigned d1, unsigned d2, f32x32& of, f32x8& of2) {
  unsigned keep;
  asm volatile(AT_A_TEXT
      : AT_A_STATE, "=&{v[184:215]}"(of), "=&{v[216:223]}"(of2)
      : "{v[64:79]}"(q), "{s[54:55]}"(ws), "{s[56:71]}"(so), "{s72}"(m0v), "{s[50:51]}"(tag), "{s52}"(flag), "{s[74:75]}"(woa),
        "{v[248:255]}"(wo), "{s[42:43]}"(src), "{s44}"(d0), "{s45}"(d1), "{s46}"(d2), [j0] "n"(12), [j1] "n"(32), [obr] "n"(O_BR)
      : AT_A_CLOB);
}

__device__ __forceinline__ void at_b(f32x16& o, f32x16& mF, const f32x16& mD, u32x8& q, unsigned tb, unsigned qt, char* oA, const u32x16& qa,
                                     u32x16& ad, f32x32& k0, f32x16& k1, f32x32& k2, f32x16& k3) {
  asm volatile(AT_ASM_MACROS
      "v_add_u32 v242, %[tb], v128\n\tv_add_u32 v243, %[tb], v129\n\tv_add_u32 v245, %[tb], v130\n\t"
      "ds_read_b128 v[176:179], v242 offset:7*3584\n\tds_read_b64 v[180:181], v243 offset:7*3584\n\t"
      "ds_read_b128 v[184:187], v242 offset:8*3584\n\tds_read_b64 v[182:183], v243 offset:8*3584\n\t"
      "ds_read_b128 v[192:195], v242\n\tds_read_b64 v[196:197], v243\n\tds_read_b128 v[206:209], v242 offset:3584\n\tds_read_b64 v[210:211], v243 offset:3584\n\t"
      "ds_read_b128 v[198:201], v245\n\tds_read_b128 v[212:215], v245 offset:3584\n\tds_read_b128 v[202:205], v245 offset:1024\n\t"
      "s_mov_b32 exec_lo, 0\n\tv_mov_b64 v[240:241], 0\n\ts_mov_b32 exec_lo, -1\n\tv_mov_b64 v[188:189], -1\n\tv_mov_b64 v[190:191], -1\n\t"
      "s_waitcnt lgkmcnt(7)\n\t"
      "qk1 144, 176, 236\n\tqk2 144, 180, 240\n\tqk1c 48, 184, 236, 220\n\tqk2 48, 182, 240\n\t"
      "global_store_dwordx4 v137, v[188:191], s[42:43] sc1\n\ts_mov_b32 exec_hi, 0\n\tglobal_store_dwordx2 v78, v[188:189], s[42:43] sc1\n\ts_mov_b32 exec_hi, -1\n\t"
      "s_waitcnt lgkmcnt(5)\n\tqk1c 160, 192, 236, 160\n\tds_read_b128 v[192:195], v242 offset:7168\n\ts_cmp_eq_u32 %[qt], 0\n\ts_nop 1\n\t"
      "mx 216, 144\n\tmx 218, 48\n\ts_cbranch_scc1 1f\n\tv_max_f32 v218, v218, v216\n\t1:\n\tv_mov_b32 v219, v218\n\t"
      "v_mov_b64 v[144:145], 0\n\tv_mov_b64 v[146:147], 0\n\tv_permlane32_swap_b32 v218, v219\n\tv_max_f32 v218, v218, v219\n\tv_add_f32 v218, 0x42600000, v218\n\t"
      "s_mov_b32 exec_lo, 0\n\tv_cvt_pk_bf16_f32 v240, -v218, 0\n\ts_mov_b32 exec_lo, -1\n\tv_mov_b64 v[148:149], 0\n\t"
      "qk2 160, 196, 240\n\tds_read_b64 v[196:197], v243 offset:7168\n\t"
      ".set i_, 150\n\t.rept 5\n\tv_mov_b64 v[i_:i_+1], 0\n\t.set i_, i_ + 2\n\t.endr\n\t"
      "s_waitcnt lgkmcnt(2)\n\ts_nop 1\n\t"
      ".set t_, 0\n\t"
      ".rept 9\n\t"
      ".set a_, 192 + 14 * (t_ & 1)\n\t.set b_, 206 - 14 * (t_ & 1)\n\t.set c_, 160 - 112 * (t_ & 1)\n\t.set d_, 48 + 112 * (t_ & 1)\n\t"
      ".if t_ == 0 || t_ == 5\n\ts_setprio 2 - t_ / 5\n\t.endif\n\t"
      ".if t_ < 7\n\tqk1 d_, b_, 236\n\t.elseif t_ == 7\n\tqk1c d_, b_, 236, 220\n\t.endif\n\t"
      ".if t_ < 6\n\tds_read_b128 v[b_:b_+3], v242 offset:(t_+3)*3584\n\t.endif\n\t"
      "xp 176, c_\n\t"
      ".if t_ < 8\n\tqk2 d_, b_+4, 240\n\t.endif\n\t"
      ".if t_ < 6\n\tds_read_b64 v[b_+4:b_+5], v243 offset:(t_+3)*3584\n\t.endif\n\t"
      "xp 180, c_+4\n\t"
      ".if t_ > 0\n\tsmf 144, b_+10, 184\n\t.endif\n\t"
      ".if t_ < 8\n\tds_read_b128 v[b_+10:b_+13], v245 offset:(t_+1)*3584+1024\n\t.endif\n\t"
      "xp 184, c_+8\n\t"
      "smf 144, a_+6, 176\n\t"
      ".if t_ < 6\n\ts_waitcnt lgkmcnt(3)\n\t.elseif t_ < 8\n\ts_waitcnt lgkmcnt(1)\n\t.else\n\ts_waitcnt lgkmcnt(0)\n\t.endif\n\t"
      ".if t_ < 7\n\tds_read_b128 v[a_+6:a_+9], v245 offset:(t_+2)*3584\n\t.endif\n\t"
      "xp 188, c_+12\n\t"
      ".set t_, t_ + 1\n\t"
      ".endr\n\t"
      "s_nop 1\n\tsmf 144, 202, 184\n\ts_nop 11\n\t"
      AT_ASM_PURGE
      : "=&{v[144:159]}"(o), "+{v[160:175]}"(mF), "+{v[0:31]}"(k0), "+{v[32:47]}"(k1), "+{v[80:111]}"(k2), "+{v[112:127]}"(k3), "+{v[128:143]}"(ad),
        "+{v[236:243]}"(q)
      : "{v[220:235]}"(mD), "{v[64:79]}"(qa), [tb] "s"(tb), [qt] "s"(qt), "{s[42:43]}"(oA)
      : "memory", "scc", "v245", "v48", "v49", "v50", "v51", "v52", "v53", "v54", "v55", "v56", "v57", "v58", "v59", "v60", "v61", "v62", "v63",
        "v176", "v177", "v178", "v179", "v180", "v181", "v182", "v183", "v184", "v185", "v186", "v187", "v188", "v189", "v190", "v191",
        "v192", "v193", "v194", "v195", "v196", "v197", "v198", "v199", "v200", "v201", "v202", "v203", "v204", "v205", "v206", "v207",
        "v208", "v209", "v210", "v211", "v212", "v213", "v214", "v215", "v216", "v217", "v218", "v219");
}

#define PJ_MACROS \
      ".macro rdw k, sp\n\t.set d_, 80 + 20 * ((\\k) & 1)\n\t" \
      ".if (\\k) < \\sp\n\t.set a_, 120\n\t.set o_, (\\k) * 5120\n\t.else\n\t.set a_, 122\n\t.set o_, ((\\k) - \\sp) * 5120\n\t.endif\n\t.endm\n\t" \
      ".macro rd1 i\n\tds_read_b64_tr_b16 v[d_+4*\\i:d_+4*\\i+1], v[a_] offset:o_+32*\\i\n\tds_read_b64_tr_b16 v[d_+4*\\i+2:d_+4*\\i+3], v[a_+1] offset:o_+32*\\i\n\t.endm\n\t" \
      ".macro pm i, ac, of, last\n\t" \
      ".if k_ == 0\n\tv_mfma_f32_16x16x32_bf16 v[\\ac+4*\\i:\\ac+4*\\i+3], v[c_+4*\\i:c_+4*\\i+3], v[\\of:\\of+3], 0\n\t" \
      ".else\n\tv_mfma_f32_16x16x32_bf16 v[\\ac+4*\\i:\\ac+4*\\i+3], v[c_+4*\\i:c_+4*\\i+3], v[\\of+4*k_:\\of+3+4*k_], v[\\ac+4*\\i:\\ac+4*\\i+3]\n\t.endif\n\t" \
      ".if k_ < \\last\n\trd1 \\i\n\ts_waitcnt lgkmcnt(8)\n\t.elseif \\i < 4\n\ts_waitcnt lgkmcnt(6 - 2 * \\i)\n\t.endif\n\t.endm\n\t" \
      ".macro ldo\n\tv_mov_b32 v72, v254\n\t.set q_, 0\n\t.rept 10\n\t" \
      "global_load_dwordx4 v[32+4*q_:35+4*q_], v72, s[54:55] sc1\n\t.if q_ < 9\n\tv_add_u32 v72, " STR(OCT) "*4, v72\n\t.endif\n\t.set q_, q_ + 1\n\t.endr\n\t.endm\n\t"
#define PJ_PURGE ".purgem rdw\n\t.purgem rd1\n\t.purgem pm\n\t.purgem ldo\n\t"
#define PJ_CLOB "v72", "v80", "v81", "v82", "v83", "v84", "v85", "v86", "v87", "v88", "v89", "v90", "v91", "v92", "v93", "v94", "v95", \
        "v96", "v97", "v98", "v99", "v100", "v101", "v102", "v103", "v104", "v105", "v106", "v107", "v108", "v109", "v110", "v111", \
        "v112", "v113", "v114", "v115", "v116", "v117", "v118", "v119"
__device__ __forceinline__ void proj_1(f32x32& acc, const f32x32& of, const f32x8& of2, f32x32& oa, f32x8& oa2, u32x4 wa, const u32x8& wo,
                                       const char* src0, const char* src1, unsigned d0, unsigned d1, const char* ws) {
  unsigned keep;
  asm volatile(PJ_MACROS
      ".macro wp n\n\t.set n_, \\n\n\t.set pc_, n_ %% 5\n\t"
      ".if n_ < 10\n\t"
      ".if n_ >= 5\n\ts_cmp_lt_i32 s45, 0\n\ts_cbranch_scc1 9f\n\t.endif\n\t"
      ".if pc_ == 0\n\ts_mov_b32 m0, s[44+n_/5]\n\ts_nop 0\n\t.endif\n\t"
      ".if pc_ == 4\n\ts_add_u32 m0, m0, 4096\n\ts_nop 0\n\t.endif\n\t"
      "global_load_lds_dwordx4 v[248+pc_], s[42+4*(n_/5):43+4*(n_/5)] offset:1024*(pc_&3)\n\t"
      ".if n_ >= 5\n\t9:\n\t.endif\n\t.endif\n\t"
      ".if n_ == 12\n\tldo\n\t.endif\n\t"
      ".endm\n\t"
      "rdw 0, 6\n\trd1 0\n\trd1 1\n\trd1 2\n\trd1 3\n\trd1 4\n\t"
      "s_mov_b32 %[keep], m0\n\ts_barrier\n\ts_waitcnt lgkmcnt(8)\n\t"
      ".set k_, 0\n\t.rept 10\n\t.set c_, 80 + 20 * (k_ & 1)\n\trdw k_+1, 6\n\t"
      ".if k_ == 0 || k_ == 3 || k_ == 6\n\ts_setprio 2 - k_ / 3\n\t.endif\n\t"
      "pm 0, 0, 184, 9\n\twp 2*k_\n\tpm 1, 0, 184, 9\n\tpm 2, 0, 184, 9\n\twp 2*k_+1\n\tpm 3, 0, 184, 9\n\tpm 4, 0, 184, 9\n\t"
      ".set k_, k_ + 1\n\t.endr\n\t"
      "s_setprio 3\n\ts_mov_b32 m0, %[keep]\n\ts_waitcnt vmcnt(10)\n\ts_barrier\n\ts_nop 4\n\t"
      ".purgem wp\n\t" PJ_PURGE
      : "=&{v[0:31]}"(acc), "=&{v[32:63]}"(oa), "=&{v[64:71]}"(oa2), [keep] "=&s"(keep)
      : "{v[184:215]}"(of), "{v[216:223]}"(of2), "{v[120:123]}"(wa), "{v[248:255]}"(wo), "{s[42:43]}"(src0), "{s[46:47]}"(src1), "{s44}"(d0),
        "{s45}"(d1), "{s[54:55]}"(ws)
      : "memory", "scc", PJ_CLOB);
}
template <int K0>
__device__ __forceinline__ void proj_2(f32x32& acc, f32x32& oa, f32x8& oa2, u32x4 wa, const u32x8& wo, const char* ws) {
  asm volatile(PJ_MACROS
      "rdw %c[k0], 5\n\trd1 0\n\trd1 1\n\trd1 2\n\trd1 3\n\trd1 4\n\t"
      ".if %c[k0] == 0\n\t"
      "s_mov_b32 s48, 0x40000\n\t5:\n\t"
      "v_max3_u32 v72, v32, v33, v34\n\t.set q_, 35\n\t.rept 18\n\tv_max3_u32 v72, v72, v[q_], v[q_+1]\n\t.set q_, q_ + 2\n\t.endr\n\t"
      "v_max_u32 v72, v72, v71\n\tv_cmp_eq_u32 vcc, -1, v72\n\ts_sub_u32 s48, s48, 1\n\t"
      "s_cbranch_vccz 4f\n\ts_cbranch_scc1 4f\n\tldo\n\ts_waitcnt vmcnt(0)\n\ts_branch 5b\n\t4:\n\t"
      ".endif\n\t"
      "s_waitcnt lgkmcnt(8)\n\t"
      ".set k_, %c[k0]\n\t.rept 5\n\t.set c_, 80 + 20 * (k_ & 1)\n\trdw k_+1, 5\n\t"
      "pm 0, 160, 32, %c[k0]+4\n\tpm 1, 160, 32, %c[k0]+4\n\tpm 2, 160, 32, %c[k0]+4\n\tpm 3, 160, 32, %c[k0]+4\n\tpm 4, 160, 32, %c[k0]+4\n\t"
      ".set k_, k_ + 1\n\t.endr\n\t"
      ".if %c[k0] == 0\n\ts_waitcnt vmcnt(0)\n\ts_barrier\n\t.else\n\ts_nop 7\n\t.endif\n\t"
      PJ_PURGE
      : "+{v[160:191]}"(acc), "+{v[32:63]}"(oa), "+{v[64:71]}"(oa2)
      : "{v[120:123]}"(wa), "{v[248:255]}"(wo), "{s[54:55]}"(ws), [k0] "n"(K0)
      : "memory", "scc", "vcc", "s48", PJ_CLOB);
}
typedef __attribute__((address_space(3))) f32x4* lds_f4;
__device__ __forceinline__ void proj_send(const f32x32& acc, char* lds, int lw, int grp, int lane) {
  lds_ptr p = (lds_ptr)lds + lw * 5120 + lane * 16;
  if (grp) {
    *(lds_f4)(p) = f32x4{acc[0], acc[1], acc[2], acc[3]}; *(lds_f4)(p + 1024) = f32x4{acc[4], acc[5], acc[6], acc[7]};
    *(lds_f4)(p + 4096) = f32x4{acc[16], acc[17], acc[18], acc[19]};
  } else {
    *(lds_f4)(p + 2048) = f32x4{acc[8], acc[9], acc[10], acc[11]}; *(lds_f4)(p + 3072) = f32x4{acc[12], acc[13], acc[14], acc[15]};
  }
}
#define OUT_WT 0
template <bool WT>
__device__ __forceinline__ void proj_out(const f32x4& a, const f32x4& b, char* p, unsigned off) {
  const auto s0 = __builtin_amdgcn_permlane16_swap(pk_bf16(a[0], a[1]), pk_bf16(b[0], b[1]), false, false);
  const auto s1 = __builtin_amdgcn_permlane16_swap(pk_bf16(a[2], a[3]), pk_bf16(b[2], b[3]), false, false);
  const u32x4 v = {s0[0], s1[0], s0[1], s1[1]};
  if (WT) asm volatile("global_store_dwordx4 %0, %1, %2 sc1\n\ts_nop 1" :: "v"(off), "v"(v), "s"(p) : "memory");
  else asm volatile("global_store_dwordx4 %0, %1, %2\n\ts_nop 1" :: "v"(off), "v"(v), "s"(p) : "memory");
}
template <bool WT>
__device__ __forceinline__ void proj_finish(const f32x32& acc, char* lds, int lw, int grp, int lane, char* dst) {
  lds_ptr p = (lds_ptr)lds + lw * 5120 + lane * 16;
  const unsigned row = (lane & 15) * 2560, o16 = row + ((lane >> 4) & 1) * 32 + (lane >> 5) * 16;
  if (grp) {
    proj_out<WT>(f32x4{acc[8], acc[9], acc[10], acc[11]} + *(lds_f4)(p + 2048), f32x4{acc[12], acc[13], acc[14], acc[15]} + *(lds_f4)(p + 3072), dst + 64, o16);
  } else {
    const f32x4 r2 = f32x4{acc[16], acc[17], acc[18], acc[19]} + *(lds_f4)(p + 4096);
    proj_out<WT>(f32x4{acc[0], acc[1], acc[2], acc[3]} + *(lds_f4)(p), f32x4{acc[4], acc[5], acc[6], acc[7]} + *(lds_f4)(p + 1024), dst, o16);
    const u32x2 v = {pk_bf16(r2[0], r2[1]), pk_bf16(r2[2], r2[3])};
    if (WT) asm volatile("global_store_dwordx2 %0, %1, %2 offset:128 sc1" :: "v"(row + (lane >> 4) * 8), "v"(v), "s"(dst) : "memory");
    else asm volatile("global_store_dwordx2 %0, %1, %2 offset:128" :: "v"(row + (lane >> 4) * 8), "v"(v), "s"(dst) : "memory");
  }
}

struct LaneTab { unsigned v[10][64][4]; };
constexpr LaneTab make_tab() {
  LaneTab t = {};
  for (unsigned l = 0; l < 64; ++l) {
    for (unsigned i = 0; i < 5; ++i) {
      const unsigned v = 64 * i + l, rw = v / 10;
      t.v[i >> 2][l][i & 3] = (rw ^ ((rw >> 1) & 4)) * 1280 + (v - rw * 10) * 16 + 4096 - 1024 * (i & 3);
    }
    const unsigned g = l >> 4, q = (l & 15) >> 2, p = l & 3;
    t.v[1][l][1] = (l & 15) * 64 * 128;
    t.v[1][l][2] = g * OCT + (l & 15) * 16;
    t.v[1][l][3] = (g * 1280 + (q ^ (4 * (g & 1))) * 160 + p * 8) | ((g * 1280 + ((4 + q) ^ (4 * (g & 1))) * 160 + p * 8) << 16);
    for (unsigned r = 0; r < 16; ++r) {
      const unsigned key = (r & 3) + 8 * (r >> 2) + 4 * (l >> 5);
      t.v[2 + (r >> 2)][l][r & 3] = key > (l & 31) ? 0u : 0xFF800000u;
      t.v[6 + (r >> 2)][l][r & 3] = key <= (l & 31) ? 0u : 0xFF800000u;
    }
  }
  return t;
}
__device__ const LaneTab lane_tab = make_tab();
constexpr unsigned CODE_B = 34816;

__global__ void __launch_bounds__(512, 2)
k_fused(const char* __restrict__ xa_, const char* __restrict__ wa_, const char* __restrict__ woa_, const char* __restrict__ xb_,
        const char* __restrict__ wb_, const char* __restrict__ wob_, char* __restrict__ ws_, char* __restrict__ out_) {
  __shared__ __attribute__((aligned(16))) char lds[163840];
  const unsigned bid = blockIdx.x, xcd = bid & 7, slot = bid >> 3, br = xcd >> 2;
  u32x4 ka; u32x2 kb; u32x8 kc;
  {
    const void* kp = (const void*)__builtin_amdgcn_kernarg_segment_ptr();
    asm volatile("s_load_dwordx4 %0, %3, %4\n\ts_load_dwordx8 %2, %3, 0x20\n\ts_load_dwordx2 %1, %3, 0x10"
                 : "=&s"(ka), "=&s"(kb), "=&s"(kc) : "s"(kp), "s"(br * 24));
  }
  STAMP_DECL STAMP(0);
  unsigned lane = threadIdx.x & 63;
  const unsigned w = __builtin_amdgcn_readfirstlane(threadIdx.x >> 6);
  const unsigned grp = w >> 2, lw = w & 3;
  const unsigned mt = (xcd & 3) * 4 + (slot & 3), u = slot >> 2;
  const unsigned win = (5 * ((mt >> 1) & 3)) >> 1;
  const unsigned kv1 = (u + 3) >> 3;
  const unsigned j1 = win + u - 5;
#define ROT (5 * (u & 1) + (5 * (xcd & 3)) / 2)
  auto mod10 = [](unsigned x) { return __builtin_elementwise_min(x, x - 10); };
  const unsigned rot = mod10(ROT);
  const unsigned m0v = grp * 16384 + lw * 4096;
  const unsigned str1 = grp ? 64 * 3840 : 128;
  unsigned o1[4], tw;
  {
    const unsigned row = lane >> 3, pos = lane & 7;
    tw = row * 3840 + ((pos ^ (row & 2)) * 16);
    const unsigned t1 = grp ? tw : row * 1280 + ((pos ^ (row >> 1)) * 16);
    const unsigned gap = kv1 ? 1280 : 128, m1 = grp ? 0 : 64;
    o1[0] = t1 + 1024;
    o1[1] = (t1 ^ m1) + (grp ? gap : 8 * 1280);
    o1[2] = (t1 ^ m1 ^ 64) + (grp ? 8 * 3840 - 1024 : 16 * 1280 - 1024);
    o1[3] = (t1 ^ 64) + (grp ? 8 * 3840 - 2048 + gap : 24 * 1280 - 2048);
  }
  const unsigned offA = grp ? 16 * lw * 3840 + (kv1 ? 1280 + j1 * 128 : u * 256) : (mt * 128 + 32 * lw) * 1280;
  asm volatile("s_waitcnt lgkmcnt(0)" : "+s"(ka), "+s"(kb), "+s"(kc));
  auto ptr = [](unsigned lo, unsigned hi) { return (char*)(((unsigned long long)hi << 32) | lo); };
  const char* sA = ptr(grp ? ka[2] : ka[0], grp ? ka[3] : ka[1]) + (offA - 1024ll);
  char* const ws = ptr(kc[4], kc[5]);
  glds16x4(sA + rot * str1, o1, m0v);
  glds16x4(sA + mod10(rot + 1) * str1, o1, m0v + 32768);
  u32x2 ep; unsigned tch[4];
  asm volatile("s_load_dwordx2 %0, %1, %2" : "=&s"(ep) : "s"(ws), "s"((unsigned)CTL_WS));
  {
    const unsigned i64 = (slot * 8 + w) * 64;
    unsigned long long pc;
    asm volatile("s_getpc_b64 %0\n\ts_load_dword %1, %0, %5\n\ts_load_dword %2, %0, %6\n\ts_load_dword %3, %0, %7\n\ts_load_dword %4, %8, %9"
                 : "=&s"(pc), "=&s"(tch[0]), "=&s"(tch[1]), "=&s"(tch[2]), "=&s"(tch[3])
                 : "s"(i64), "s"(i64 + 16384), "s"(__builtin_elementwise_min(i64 + 32768, CODE_B)), "s"(&lane_tab),
                   "s"(__builtin_elementwise_min(i64, 10176u)));
  }
  const unsigned j2 = mod10(win + 3 + (u < 7 ? u : 6));
  const unsigned Tg = mt * 4 + lw;
  const unsigned vt1 = grp & kv1, vt2 = u < 7 ? grp : 2;
  const unsigned cg1 = kv1 ? 10 + 10 * grp + j1 : 2 * u + grp, cg2 = 10 + 10 * grp + j2;
  const unsigned head = xcd * 4 + (slot & 3), qb = u;
  const unsigned odd = head & 1, o2 = 5 * (head >> 1) + 2, o1h = odd ? o2 + 1 : o2 - 2;
  const unsigned qt = qb * 8 + 2 * lw + grp;
  u32x4 soA; u32x16 so;
  {
    const unsigned kb0 = 80 * OCT + w * 512, vb0 = (unsigned)V_WS - V_OFF + xcd * (256 * 1280) + w * 5120;
    unsigned ko[10], vo_[10];
#pragma unroll
    for (int i = 0; i < 8; ++i) { const unsigned c = (((head >> 3) + (i >> 1)) & 3) + 4 * (i & 1); ko[i] = kb0 + c * 4096; vo_[i] = vb0 + c * 40960; }
    const unsigned c8 = qb ? qb - 1 : 0;
    ko[8] = kb0 + (unsigned)QK_BR + c8 * 4096; vo_[8] = vb0 + (unsigned)V_BR + c8 * 40960;
    ko[9] = kb0 + (unsigned)QK_BR + qb * 4096; vo_[9] = vb0 + (unsigned)V_BR + qb * 40960;
    soA = u32x4{ko[0], vo_[0], ko[1], vo_[1]};
    so = u32x16{ko[2], vo_[2], ko[8], vo_[8], ko[9], vo_[9], ko[3], vo_[3], ko[4], vo_[4], ko[5], vo_[5], ko[6], vo_[6], ko[7], vo_[7]};
    asm volatile("" : "+{s[76:79]}"(soA), "+{s[56:71]}"(so));
  }
  unsigned flag1 = FQ_OFF + ((br * 30 + cg1) * 64 + Tg) * 128, flag2 = FQ_OFF + ((br * 30 + cg2) * 64 + Tg) * 128;
  unsigned flagO = FO_OFF + (head * 64 + qt) * 128;
  PINS(flag1); PINS(flag2); PINS(flagO);
  const char* sB = ptr(ka[2], ka[3]) + (16 * lw * 3840 + 256 + j2 * 128);
  const unsigned mytile = w * TILE_B;
  u32x16 qq, pk, at;
  {
    const unsigned r16 = lane & 15, g = lane >> 4, q = r16 >> 2, p = lane & 3, r32 = lane & 31, h = lane >> 5;
    const unsigned xa0 = (lw * 32 + r16) * 128 + ((g ^ (r16 >> 1)) * 16);
    const unsigned wa0 = 2048 * g + 1024 * grp + 128 * q + 16 * ((p >> 1) ^ (4 * (g & 1) + (q & 2))) + 8 * (p & 1);
    const unsigned jj = (20 * head + 19 * (lane & 1)) >> 6, b1 = (lane >> 1) & 1, c = (lane >> 2) & 7;
    const unsigned pol1 = FQ_OFF + (jj * 64 + ((lane & 8) ? (qt ^ b1) : b1 * 640 + (lane & 4) * 8 + (640 + (head >> 3) * 8 + w))) * 128;
    qq = u32x16{o1[0], o1[1], o1[2], o1[3], tw + 1024, tw + 1280, (tw ^ 64) + (8 * 3840 - 1024), (tw ^ 64) + (8 * 3840 - 2048 + 1280),
                pol1, xa0, xa0 ^ 64, xa0, wa0, wa0 ^ 32, wa0 ^ 64, wa0 ^ 96};
    const unsigned cB = __builtin_elementwise_max((c & 1) + qb, 1u) + (30 * 8 - 1);
    const unsigned pol2 = (jj * 64 + (lane >= 48 ? 30 * 64 + qt : b1 * 640 + (h ? cB : c) * 8 + (640 + w))) * 128 + FQ_OFF;
    const u32x2 s1 = g1_offs(br, cg1, Tg, lane), s2 = g1_offs(br, cg2, Tg, lane);
    const unsigned ab = grp * TILE_B;
    pk = u32x16{s1[0], s1[1], s2[0], s2[1],
                lane * 16 + ab,
                h ? (V_OFF + 320 + ab) : r32 * 16 + (K2_OFF + odd * 8 + ab),
                h * 512 + (r32 < 28 ? __builtin_elementwise_min(r32, 21u) : 28) * 16 + (V_OFF + ab),
                h * OCT + r32 * 16 + o1h * OCT,
                r32 * 16 + (o2 * OCT - 1024),
                h * 1280 + (__builtin_elementwise_min(r32 + 4 * odd, r32 + 4 * odd - 20) + (head & 3) * 20) * 16,
                pol2,
                g * 512 + __builtin_elementwise_min(r16, 11u) * 16 + (mytile + V_OFF + 320),
                r16 == 0 ? 0x3F803F80u : 0u,
                r32 * 16 + (o2 * OCT + odd * 8),
                lane * 512 + (3 * CHUNK_B + V_OFF + 320 + mytile),
                lane};
  }
  asm volatile("" : "+{v[64:79]}"(qq), "+{v[80:95]}"(pk) :: "memory");
  {
    const unsigned oo[4] = {qq[0], qq[1], qq[2], qq[3]};
    glds16x4(sA + mod10(rot + 2) * str1, oo, m0v + 65536);
  }
  asm volatile("s_waitcnt vmcnt(8) lgkmcnt(0)\n\ts_barrier" ::: "memory");
  PINS(ep); PINS(tch[0]); PINS(tch[1]); PINS(tch[2]); PINS(tch[3]);
  const unsigned long long tag = (((unsigned long long)ep[1] << 32) | ep[0]) + 1;
  STAMP(1);
  u32x4 cv4;
  STV_PUT(pk[15]);
  g1_all(vt1, kv1, vt2, qq, pk, at, cv4, sA, mod10(rot + 3) * str1, str1, sB, mod10(rot + grp) * (64 * 3840), m0v, tag, flag1, ws, mytile, soA,
         qt * 512);
  STV_GET(pk[15]);
  STAMP(8);
  if (bid == 255 && grp == 0) {
    const unsigned e = w * 64 + lane_id(), eu = e & 7, em = (e >> 3) & 15;
    poll_flags(ws, FQ_OFF + (((e >> 7) * 30 + (eu < 5 ? 2 * eu : 5 + eu + ((5 * ((em >> 1) & 3)) >> 1))) * 64 + 4 * em) * 128, tag);
  }
  BARRIER();
  STAMP(9);
  if (bid == 255 && w == 0) put_flag(ws, CTL_WS, tag);
  typedef __attribute__((address_space(3))) u32x4* lds_u4;
  *(lds_u4)((lds_ptr)lds + pk[11] + 2 * CHUNK_B) = cv4; *(lds_u4)((lds_ptr)lds + pk[11] + 3 * CHUNK_B) = cv4; *(lds_u4)((lds_ptr)lds + pk[11] + 4 * CHUNK_B) = cv4;
  if (qb == 0) {
    lds_ptr z = (lds_ptr)lds + 3 * CHUNK_B + mytile + qq[15];
    *(lds_f4)(z) = f32x4{}; *(lds_f4)(z + 1024) = f32x4{}; *(lds_f4)(z + 2048) = f32x4{}; *(lds_f4)(z + 2560) = f32x4{};
  }
  const f32x16 zero16 = {};
  auto exact = [&](int b, bf16x8 q1, s16x4 q2, int t0, int t1) {
    __builtin_amdgcn_fence(__ATOMIC_ACQUIRE, "agent");
    const int ln = lane_id(), r32 = ln & 31, h = ln >> 5;
    unsigned head = xcd * 4 + (slot & 3);
    PINS(head);
    const unsigned odd = head & 1, o2 = 5 * (head >> 1) + 2, o1h = odd ? o2 + 1 : o2 - 2;
    if (h) q2 = s16x4{0, 0, 0, 0};
    typedef __attribute__((address_space(1))) const char* gptr;
    gptr kg1 = (gptr)ws + QK_WS + b * QK_BR + (size_t)(80 + o1h + h) * OCT + r32 * 16;
    gptr kg2 = (gptr)ws + QK_WS + b * QK_BR + (size_t)(80 + o2) * OCT + r32 * 16 + odd * 8;
    gptr vg = (gptr)ws + V_WS + b * V_BR + (head >> 2) * (256 * 1280) + h * 1280 +
                     ((head & 3) * 20 + (odd ? (r32 < 16 ? r32 + 4 : r32 - 16) : r32)) * 16;
    const u32x4 vc = r32 == 20 ? u32x4{0x3F803F80u, 0x3F803F80u, 0x3F803F80u, 0x3F803F80u} : u32x4{0u, 0u, 0u, 0u};
    float m = -INFINITY;
    f32x16 r = zero16;
#pragma unroll 1
    for (int ps = 0; ps < 2; ++ps) {
#pragma unroll 1
      for (int t = t0; t <= t1; ++t) {
        f32x16 s = MFMA32(*(__attribute__((address_space(1))) const bf16x8*)(kg1 + t * 512), q1, zero16);
        s = MFMA32K8(*(__attribute__((address_space(1))) const s16x4*)(kg2 + t * 512), q2, s);
        if (b) {
          const int d = (t1 - t) * 32 + r32 - 4 * h;
#pragma unroll
          for (int e = 0; e < 16; ++e) s[e] = (unsigned)(d - ((e & 3) + 8 * (e >> 2))) < 256u ? s[e] : -INFINITY;
        }
        if (ps == 0) { m = max16(s, m); continue; }
#pragma unroll
        for (int e = 0; e < 16; ++e) s[e] = __builtin_amdgcn_exp2f(s[e] - m);
        u32x4 va = vc, vb = vc;
        if (r32 < 20) { va = *(__attribute__((address_space(1))) const u32x4*)(vg + t * 5120); vb = *(__attribute__((address_space(1))) const u32x4*)(vg + t * 5120 + 2560); }
        r = smfmac(__builtin_bit_cast(bf16x8, va), lo8(s), r);
        r = smfmac(__builtin_bit_cast(bf16x8, vb), hi8(s), r);
      }
      m = xhalf_max(m);
    }
    return r;
  };
  auto unsafe = [&](const f32x16& r, float l) {
    float amax = 0.f;
#pragma unroll
    for (int e = 0; e < 12; ++e) amax = __builtin_fmaxf(amax, __builtin_fabsf(r[e]));
    return __any(!(l <= 1.2980742e33f) || !(l >= 8.4703295e-22f) || !(amax <= 3.0e38f));
  };
  const char* const woa = ptr(kb[0], kb[1]);
  const char* const wob = ptr(kc[2], kc[3]);
  char* const oA = ws + (O_WS + qt * 512);
  f32x32 oab, of; f32x8 of2;
  u32x8 wo;
  unsigned wl;
  {
    f32x16 s0, pp;
    f32x32 fr;
    asm volatile(AT_ASM_MACROS
        "v_subrev_u32 v108, %[ab], v129\n\tds_read_b128 v[112:115], v79\n\tds_read_b64 v[116:117], v108\n\t"
        "ds_read_b128 v[80:83], v128\n\tds_read_b64 v[84:85], v129\n\tds_read_b128 v[94:97], v128 offset:7168\n\tds_read_b64 v[98:99], v129 offset:7168\n\t"
        "ds_read_b128 v[86:89], v130\n\tds_read_b128 v[100:103], v130 offset:7168\n\tds_read_b128 v[90:93], v130 offset:1024\n\t"
        "s_mov_b32 exec_lo, 0\n\tv_mov_b64 v[72:73], 0\n\tv_mov_b64 v[74:75], 0\n\ts_mov_b32 exec_lo, -1\n\t"
        "s_waitcnt lgkmcnt(7)\n\t"
        "qk1 32, 112, 64\n\tqk2 32, 116, 72\n\tqk1 48, 112, 68\n\tqk2 48, 116, 74\n\t"
        ".set i_, 0\n\t.rept 10\n\tv_mov_b64 v[i_:i_+1], 0\n\t.set i_, i_ + 2\n\t.endr\n\t"
        "mx 108, 32\n\tv_mov_b32 v109, v108\n\tmx 110, 48\n\tv_mov_b32 v111, v110\n\t"
        "v_permlane32_swap_b32 v108, v109\n\ts_waitcnt lgkmcnt(0)\n\tqk1 32, 80, 64\n\tv_permlane32_swap_b32 v110, v111\n\t"
        "v_max_f32 v108, v108, v109\n\tv_max_f32 v110, v110, v111\n\tv_add_f32 v108, 0x42600000, v108\n\tv_add_f32 v110, 0x42600000, v110\n\t"
        "s_mov_b32 exec_lo, 0\n\tv_cvt_pk_bf16_f32 v72, -v108, 0\n\tv_cvt_pk_bf16_f32 v74, -v110, 0\n\ts_mov_b32 exec_lo, -1\n\ts_nop 0\n\t"
        "qk2 32, 84, 72\n\t"
        ".set i_, 20\n\t.rept 6\n\tv_mov_b64 v[i_:i_+1], 0\n\t.set i_, i_ + 2\n\t.endr\n\t"
        "v_mov_b64 v[104:105], 0\n\tv_mov_b64 v[106:107], 0\n\t"
        "v_mov_b64 v[120:121], 0\n\tv_mov_b64 v[122:123], 0\n\tv_mov_b64 v[124:125], 0\n\tv_mov_b64 v[126:127], 0\n\t"
        AT_ASM_PURGE
        : "=&{v[0:31]}"(oab), "=&{v[112:127]}"(pp), "=&{v[32:47]}"(s0), "=&{v[80:111]}"(fr), "+{v[64:79]}"(qq), "+{v[128:143]}"(at)
        : [ab] "s"(grp * TILE_B)
        : "memory", "v48", "v49", "v50", "v51", "v52", "v53", "v54", "v55", "v56", "v57", "v58", "v59", "v60", "v61", "v62", "v63");
    STAMP(10);
    f32x16 maskF, maskD; u32x8 qv;
    at_a1(oab, s0, pp, fr, qq, at, ws, so, mytile, mytile + (qb ? 3 : 4) * CHUNK_B, tag, flag2, (0x7fu >> u) & 1u, ws + (QK_BR + qt * 512),
          &lane_tab, maskF, maskD, qv, wo AT_STV_PASS);
    STAMP(11);
    {
      f32x16 ob;
      at_b(ob, maskF, maskD, qv, 3 * CHUNK_B + 2 * lw * TILE_B, qt, oA, qq, at, oab, s0, fr, pp);
      wl = wo[7];
      wo[5] += FO_OFF + (grp * 16 * 64 + head * 2 + (lw >> 1)) * 128;
      wo[6] += (unsigned)O_WS + grp * 40 * OCT + (head * 64 + lw * 16) * 16;
      float l = row_sum(ob);
      if (__builtin_expect(unsafe(ob, l), 0)) {
        ob = exact(1, __builtin_bit_cast(bf16x8, u32x4{qv[0], qv[1], qv[2], qv[3]}), __builtin_bit_cast(s16x4, u32x2{qv[4], qv[5]}),
                   qb ? qt - 8 : 0, qt);
        l = row_sum(ob);
      }
      store_o<false>(ob, l, oA + O_BR, at[9], qq[14]);
    }
    STAMP(13);
    at[13] = (__builtin_elementwise_min(qq[15], 24u * 16) + (slot * 8 + w) * (25 * 16)) * 8;
    at_a2(oab, s0, pp, fr, qq, at, ws, so, mytile, tag, flagO, woa, wo, wob + (qb * 160 + w * (32 * 1280) - 4096ll), w * 5120, (w + 8) * 5120,
          grp ? 0x80000000u : w * 5120 + 143360, of, of2);
  }
  STAMP(12);
  {
    lds_ptr xb = (lds_ptr)lds + 3 * CHUNK_B + qq[15];
#pragma unroll
    for (int i = 0; i < 3; ++i) *(lds_f4)(xb + w * 3072 + i * 1024) = f32x4{oab[16 + 4 * i], oab[17 + 4 * i], oab[18 + 4 * i], oab[19 + 4 * i]};
    asm volatile("s_setprio 3\n\ts_waitcnt vmcnt(0) lgkmcnt(0)\n\ts_barrier" : "+{v[184:215]}"(of), "+{v[216:223]}"(of2) :: "memory");
    f32x16 o = {};
#pragma unroll
    for (int i = 0; i < 3; ++i) {
      const f32x4 r = *(lds_f4)(xb + (w ^ 4) * 3072 + i * 1024);
#pragma unroll
      for (int e = 0; e < 4; ++e) o[4 * i + e] = oab[4 * i + e] + r[e];
    }
    float l = row_sum(o);
    if (__builtin_expect(unsafe(o, l), 0)) {
      o = exact(0, __builtin_bit_cast(bf16x8, u32x4{qq[0], qq[1], qq[2], qq[3]}), __builtin_bit_cast(s16x4, u32x2{qq[8], qq[9]}), 0, 63);
      l = row_sum(o);
    }
    store_o<true>(o, l, oA, at[9], qq[14]);
  }
  STAMP(14);
  {
    f32x32 p1, p2, oa; f32x8 oa2;
    const unsigned wl0 = wl & 0xffff, wl1 = wl >> 16;
    const char* ws0 = woa + (qb * 160 + (w < 5 ? w : w + 5) * (32 * 1280) - 4096ll);
    const char* ws1 = woa + (qb * 160 + (13 + (w & 1)) * (32 * 1280) - 4096ll);
    {
      const unsigned b1 = grp * 51200, b2 = grp ? 143360 : 30720;
      proj_1(p1, of, of2, oa, oa2, u32x4{wl0 + b1, wl1 + b1, wl0 + b2, wl1 + b2}, wo, ws0, ws1, 86016 + w * 5120,
             w < 2 ? 86016 + (8 + w) * 5120 : 0x80000000u, ws);
    }
    STAMP(16);
    lane = lane_id();
    proj_send(p1, lds + 143360, lw, grp, lane);
    wo_dma(ws0 + 5 * 32 * 1280, w * 5120, wo);
    if (w < 2) { wo_dma(ws1 + 5 * 32 * 1280, (8 + w) * 5120, wo); WAITV(10); } else WAITV(5);
    char* dst = ptr(kc[6], kc[7]) + ((head * 64 + lw * 16) * 2560 + qb * 160);
    STAMP(17);
    const unsigned b1 = 86016 + grp * 25600, b2 = grp * 25600;
    const u32x4 wab = {wl0 + b1, wl1 + b1, wl0 + b2, wl1 + b2};
    asm volatile("" : "={v[160:191]}"(p2));
    proj_2<0>(p2, oa, oa2, wab, wo, ws);
    STAMP(18);
    proj_finish<(OUT_WT & 1) != 0>(p1, lds + 143360, lw, grp, lane, dst + 1280);
    proj_2<5>(p2, oa, oa2, wab, wo, ws);
    STAMP(20);
    proj_send(p2, lds + 86016, lw, grp, lane);
    asm volatile("s_waitcnt lgkmcnt(0)\n\ts_barrier" ::: "memory");
    proj_finish<(OUT_WT & 2) != 0>(p2, lds + 86016, lw, grp, lane, dst);
  }
  STAMP_OUT(ws, 0);
}

extern "C" void kernel_launch(void* const* d_in, const int* in_sizes, int n_in, void* d_out, int out_size, void* d_ws,
                              size_t ws_size, hipStream_t stream) {
  k_fused<<<256, 512, 0, stream>>>((const char*)d_in[0], (const char*)d_in[1], (const char*)d_in[2], (const char*)d_in[3],
                                   (const char*)d_in[4], (const char*)d_in[5], (char*)d_ws, (char*)d_out);
}
```

```cpp
#include <hip/hip_runtime.h>
#include <hip/hip_bf16.h>
#include <stdint.h>
#include <type_traits>

typedef __attribute__((ext_vector_type(8))) __bf16 bf16x8;
typedef __attribute__((ext_vector_type(16))) __bf16 bf16x16;
typedef __attribute__((ext_vector_type(4))) short s16x4;
typedef __attribute__((ext_vector_type(8))) short s16x8;
typedef __attribute__((ext_vector_type(2))) unsigned u32x2;
typedef __attribute__((ext_vector_type(4))) unsigned u32x4;
typedef __attribute__((ext_vector_type(4))) float f32x4;
typedef __attribute__((ext_vector_type(8))) float f32x8;
typedef __attribute__((ext_vector_type(16))) float f32x16;
typedef __attribute__((ext_vector_type(32))) float f32x32;
typedef __attribute__((address_space(3))) const char* lds_cptr;
typedef __attribute__((address_space(3))) char* lds_ptr;

#define OCT 33280
#define STR_(x) #x
#define STR(x) STR_(x)
constexpr size_t QK_WS = 0, QK_BR = (size_t)160 * OCT;
constexpr size_t V_WS = 2 * QK_BR, V_BR = (size_t)8 * 256 * 1280;
constexpr size_t O_WS = V_WS + 2 * V_BR, O_BR = (size_t)80 * OCT;
constexpr size_t WP_WS = O_WS + 2 * O_BR;
constexpr size_t CTL_WS = WP_WS + 2048 * 1024;
constexpr unsigned FQ_OFF = CTL_WS + 4096, FO_OFF = FQ_OFF + 2 * 30 * 64 * 128;
constexpr float QSCALE = 0.22360679774997896f * 1.4426950408889634f;

#define STAMP_DECL
#define STV_PUT(x)
#define STV_GET(x)
#define STAMP(i) do {} while (0)
#define STAMP_OUT(ws_, kid) do {} while (0)
__device__ __forceinline__ int lane_id() {
  int l; asm volatile("v_mbcnt_lo_u32_b32 %0, -1, 0\n\tv_mbcnt_hi_u32_b32 %0, -1, %0" : "=v"(l)); return l;
}
#define SBAR() __builtin_amdgcn_sched_barrier(0)
#define PIN(x) asm volatile("" : "+v"(x))
#define PINS(x) asm volatile("" : "+s"(x))
#define WAITV(N) asm volatile("s_waitcnt vmcnt(" #N ")" ::: "memory")
#define BARRIER() asm volatile("s_barrier" ::: "memory")
#define WAITV_BAR(N) asm volatile("s_waitcnt vmcnt(" #N ") lgkmcnt(0)\n\ts_barrier" ::: "memory")

__device__ __forceinline__ void glds16(const void* sbase, unsigned voff, unsigned lds) {
  unsigned keep;
  asm volatile("s_mov_b32 %0, m0\n\ts_mov_b32 m0, %3\n\ts_nop 0\n\tglobal_load_lds_dwordx4 %1, %2\n\ts_mov_b32 m0, %0"
               : "=&s"(keep) : "v"(voff), "s"(sbase), "s"(lds) : "memory");
}
__device__ __forceinline__ void glds16x4(const void* sbase, const unsigned (&voff)[4], unsigned lds) {
  unsigned keep;
  asm volatile("s_mov_b32 %0, m0\n\ts_mov_b32 m0, %6\n\ts_nop 0\n\tglobal_load_lds_dwordx4 %1, %5\n\t"
               "global_load_lds_dwordx4 %2, %5 offset:1024\n\tglobal_load_lds_dwordx4 %3, %5 offset:2048\n\t"
               "global_load_lds_dwordx4 %4, %5 offset:3072\n\ts_mov_b32 m0, %0"
               : "=&s"(keep) : "v"(voff[0]), "v"(voff[1]), "v"(voff[2]), "v"(voff[3]), "s"(sbase), "s"(lds) : "memory");
}
__device__ __forceinline__ unsigned pk_bf16(float lo, float hi) {
  typedef __attribute__((ext_vector_type(2))) float f32x2_;
  typedef __attribute__((ext_vector_type(2))) __bf16 bf16x2_;
  const f32x2_ v = {lo, hi};
  return __builtin_bit_cast(unsigned, __builtin_convertvector(v, bf16x2_));
}
__device__ __forceinline__ void st16(char* sbase, unsigned voff, u32x4 v) {
  asm volatile("global_store_dwordx4 %0, %1, %2 sc1\n\ts_nop 1" :: "v"(voff), "v"(v), "s"(sbase) : "memory");
}
__device__ __forceinline__ void st8lo(char* sbase, unsigned voff, u32x2 v) {
  asm volatile("s_mov_b32 exec_hi, 0\n\tglobal_store_dwordx2 %0, %1, %2 sc1\n\ts_mov_b32 exec_hi, -1" :: "v"(voff), "v"(v), "s"(sbase) : "memory");
}
__device__ __forceinline__ bf16x8 lds_b128(lds_cptr p) { return *(const __attribute__((address_space(3))) bf16x8*)p; }
__device__ __forceinline__ s16x4 lds_b64(lds_cptr p) { return *(const __attribute__((address_space(3))) s16x4*)p; }
__device__ __forceinline__ s16x4 lds_tr(lds_cptr p) {
  return __builtin_amdgcn_ds_read_tr16_b64_v4i16((__attribute__((address_space(3))) s16x4*)p);
}
__device__ __forceinline__ bf16x8 cat8(s16x4 a, s16x4 b) {
  s16x8 v = {a[0], a[1], a[2], a[3], b[0], b[1], b[2], b[3]};
  return __builtin_bit_cast(bf16x8, v);
}
#define MFMA32(a, b, c) __builtin_amdgcn_mfma_f32_32x32x16_bf16(a, b, c, 0, 0, 0)
#define MFMA32K8(a, b, c) __builtin_amdgcn_mfma_f32_32x32x8bf16_1k(a, b, c, 0, 0, 0)
#define MFMA16(a, b, c) __builtin_amdgcn_mfma_f32_16x16x32_bf16(a, b, c, 0, 0, 0)

typedef __attribute__((ext_vector_type(8))) unsigned u32x8;
typedef __attribute__((ext_vector_type(16))) unsigned u32x16;
#define AWAIT(d, d1, off) \
    "s_mov_b32 s48, 0x200000\n\t5:\n\ts_waitcnt vmcnt(0)\n\tv_cmp_ne_u64 vcc, s[50:51], v[" #d ":" #d1 "]\n\ts_sub_u32 s48, s48, 1\n\t" \
    "s_cbranch_vccz 4f\n\ts_cbranch_scc1 4f\n\ts_sleep 2\n\tglobal_load_dwordx2 v[" #d ":" #d1 "], v" #off ", s[54:55] sc1\n\ts_branch 5b\n\t4:\n\t"
#define G1_STAMP(i)
#define L1POS 1
#define NPOLL 22
#define NFLAG 6
#define NAW 28
#define KBAR 11
__device__ __forceinline__ void g1_all(unsigned vt1, unsigned kv1, unsigned vt2, u32x16& ad, u32x16& pk, u32x16& at, u32x4& cv4,
                                       const char* sA, unsigned off1, unsigned str1, const char* sB, unsigned off2, unsigned m0v,
                                       unsigned long long tag, unsigned flag, char* ws, unsigned tile, u32x4 so, unsigned qoff) {
  unsigned keep;
  f32x32 d0, d1, d2, d3, d4; f32x16 d5, d6; f32x8 d7; f32x4 d8;
  asm volatile(
      ".macro pack vt\n\t.set i_, 0\n\t.rept 4\n\t"
      ".if \\vt\n\t.set e_, 8 * i_\n\t.set f_, 8 * i_ + 4\n\t.else\n\t.set e_, 4 * i_\n\t.set f_, 4 * i_ + 16\n\t.endif\n\t"
      "v_cvt_pk_bf16_f32 v[48+4*i_], v[e_], v[e_+1]\n\tv_cvt_pk_bf16_f32 v[49+4*i_], v[e_+2], v[e_+3]\n\t"
      "v_cvt_pk_bf16_f32 v[50+4*i_], v[f_], v[f_+1]\n\tv_cvt_pk_bf16_f32 v[51+4*i_], v[f_+2], v[f_+3]\n\t"
      ".set i_, i_ + 1\n\t.endr\n\t.set i_, 0\n\t.rept 4\n\t"
      "v_permlane16_swap_b32 v[48+4*i_], v[50+4*i_]\n\tv_permlane16_swap_b32 v[49+4*i_], v[51+4*i_]\n\t"
      ".set i_, i_ + 1\n\t.endr\n\t.endm\n\t"
      ".macro mm vt, c, x, w\n\t"
      ".if (\\x) < 104\n\t"
      ".if \\vt\n\tv_mfma_f32_16x16x32_bf16 v[\\c:\\c+3], v[\\x:\\x+3], v[\\w:\\w+3], 0\n\t"
      ".else\n\tv_mfma_f32_16x16x32_bf16 v[\\c:\\c+3], v[\\w:\\w+3], v[\\x:\\x+3], 0\n\t.endif\n\t"
      ".elseif \\vt\n\tv_mfma_f32_16x16x32_bf16 v[\\c:\\c+3], v[\\x:\\x+3], v[\\w:\\w+3], v[\\c:\\c+3]\n\t"
      ".else\n\tv_mfma_f32_16x16x32_bf16 v[\\c:\\c+3], v[\\w:\\w+3], v[\\x:\\x+3], v[\\c:\\c+3]\n\t.endif\n\t"
      ".endm\n\t"
      ".macro rx d, a, off\n\tds_read_b128 v[\\d:\\d+3], v\\a offset:\\off\n\tds_read_b128 v[\\d+4:\\d+7], v\\a offset:\\off+2048\n\t.endm\n\t"
      ".macro rw d, a, off\n\tds_read_b64_tr_b16 v[\\d:\\d+1], v\\a offset:\\off\n\tds_read_b64_tr_b16 v[\\d+2:\\d+3], v\\a offset:\\off+512\n\t.endm\n\t"
      ".macro bump lo, n, val\n\t.set bi_, \\lo\n\t.rept \\n\n\tv_add_u32 v[bi_], \\val, v[bi_]\n\t.set bi_, bi_+1\n\t.endr\n\t.endm\n\t"
      ".macro src2\n\ts_add_u32 s40, s44, s47\n\ts_addc_u32 s41, s45, 0\n\t"
      "s_add_u32 s47, s47, 2*64*3840\n\ts_cmp_ge_u32 s47, 640*3840\n\ts_cselect_b32 s48, 640*3840, 0\n\ts_sub_u32 s47, s47, s48\n\t.endm\n\t"
      ".macro p1body vt\n\t"
      ".set st_, 0\n\t"
      ".rept 10\n\t"
      ".set xr_, 96 + 16 * st_\n\t"
      ".set o_, ((st_ %% 5) & 1) * 32768\n\t"
      ".set n_, (((st_ + 1) %% 5) & 1) * 32768 + 16384\n\t.if st_ == 9\n\t.set n_, 0\n\t.endif\n\t"
      "s_waitcnt lgkmcnt(6)\n\t"
      "mm \\vt, 0, xr_, 32\n\trw 48, 76, o_+16384+8192\n\t"
      "mm \\vt, 16, xr_+4, 32\n\trx xr_+8, 74, o_\n\ts_waitcnt lgkmcnt(8)\n\t"
      "mm \\vt, 4, xr_, 36\n\trw 52, 77, o_+16384+8192\n\t"
      "mm \\vt, 20, xr_+4, 36\n\trw 56, 78, o_+16384+8192\n\ts_waitcnt lgkmcnt(10)\n\t"
      "mm \\vt, 8, xr_, 40\n\trw 60, 79, o_+16384+8192\n\t"
      "mm \\vt, 24, xr_+4, 40\n\ts_waitcnt lgkmcnt(10)\n\t"
      "mm \\vt, 12, xr_, 44\n\t"
      ".if st_ < 7\n\t"
      "s_add_u32 s40, s42, s36\n\ts_addc_u32 s41, s43, 0\n\ts_add_u32 s36, s36, s37\n\ts_cmp_eq_u32 s36, s38\n\ts_cselect_b32 s36, 0, s36\n\t"
      ".else\n\tsrc2\n\t.endif\n\t"
      "mm \\vt, 28, xr_+4, 44\n\t"
      "s_add_u32 m0, s46, ((st_ + 3) %% 5) * 32768\n\t"
      ".if (st_ %% 5) == 1 || (st_ %% 5) == 3\n\tbump 76, 4, 0x10000\n\tbump 73, 2, 0x10000\n\t.endif\n\t"
      ".if (st_ %% 5) == 4\n\tbump 76, 4, 0xfffe0000\n\t.if st_ < 9\n\tbump 73, 2, 0xfffe0000\n\t.endif\n\t.endif\n\t"
      "s_waitcnt lgkmcnt(6)\n\t"
      "mm \\vt, 0, xr_+8, 48\n\t"
      "mm \\vt, 16, xr_+12, 48\n\t"
      ".if st_ < 9\n\ts_waitcnt vmcnt(4) lgkmcnt(0)\n\t.else\n\ts_cmp_eq_u32 %[vt2], 2\n\ts_cbranch_scc1 8f\n\ts_waitcnt vmcnt(4)\n\t8:\n\ts_waitcnt lgkmcnt(0)\n\t.endif\n\ts_barrier\n\t"
      ".set vo_, 64 + 4 * ((st_ + 3) / 10)\n\t"
      "mm \\vt, 4, xr_+8, 52\n\trw 32, 76, n_\n\t"
      ".if st_ == 9\n\ts_cbranch_scc1 8f\n\t.endif\n\tglobal_load_lds_dwordx4 v[vo_], s[40:41]\n\t8:\n\t"
      "mm \\vt, 20, xr_+12, 52\n\t"
      ".if st_ < 9\n\trx xr_+16, 73, n_-16384\n\t.endif\n\t"
      ".if st_ == 9\n\ts_cbranch_scc1 8f\n\t.endif\n\tglobal_load_lds_dwordx4 v[vo_+1], s[40:41] offset:1024\n\t8:\n\t"
      "mm \\vt, 8, xr_+8, 56\n\trw 36, 77, n_\n\t"
      ".if st_ == 9\n\ts_cbranch_scc1 8f\n\t.endif\n\tglobal_load_lds_dwordx4 v[vo_+2], s[40:41] offset:2048\n\t8:\n\t"
      "mm \\vt, 24, xr_+12, 56\n\trw 40, 78, n_\n\t"
      ".if st_ == 9\n\ts_cbranch_scc1 8f\n\t.endif\n\tglobal_load_lds_dwordx4 v[vo_+3], s[40:41] offset:3072\n\t8:\n\t"
      "mm \\vt, 12, xr_+8, 60\n\trw 44, 79, n_\n\t"
      "mm \\vt, 28, xr_+12, 60\n\t"
      ".set st_, st_ + 1\n\t"
      ".endr\n\t"
      "v_mov_b64 v[64:65], v[80:81]\n\tv_add_u32 v66, " STR(OCT) "*4 - (" STR(OCT) "*4 - 2560) * \\vt, v80\n\tv_add_u32 v67, " STR(OCT) "*4 - (" STR(OCT) "*4 - 2560) * \\vt, v81\n\t"
      ".endm\n\t"
      ".macro tdma i\n\t"
      "s_add_u32 m0, s72, \\i * 28672\n\ts_add_u32 s40, s54, s[76+2*\\i]\n\ts_addc_u32 s41, s55, 0\n\t"
      "global_load_lds_dwordx4 v137, s[40:41] sc1\n\ts_mov_b32 exec_hi, 0\n\tglobal_load_lds_dwordx4 v138, s[40:41] offset:1024 sc1\n\t"
      "s_add_u32 s40, s54, s[77+2*\\i]\n\ts_addc_u32 s41, s55, 0\n\ts_mov_b32 exec_lo, 0xfffff\n\ts_mov_b32 exec_hi, 0xfffff\n\t"
      "global_load_lds_dwordx4 v139, s[40:41] offset:1536 sc1\n\tglobal_load_lds_dwordx4 v140, s[40:41] offset:2560 sc1\n\ts_mov_b64 exec, -1\n\t"
      ".endm\n\t"
      ".macro slot n\n\t.set n_, \\n\n\t"
      ".if n_ >= 2 && n_ < 10\n\t"
      ".if ((n_ - 2) & 3) == 0\n\ts_add_u32 m0, s46, (3 + (n_ - 2) / 4) * 32768\n\tsrc2\n\t.endif\n\t"
      "global_load_lds_dwordx4 v[68+((n_-2)&3)], s[40:41] offset:1024*((n_-2)&3)\n\t"
      ".endif\n\t"
      ".if n_ == %c[nflag]\n\tv_mov_b32 v96, s50\n\tv_mov_b32 v97, s51\n\tv_mov_b32 v98, s52\n\ts_mov_b64 exec, 1\n\ts_waitcnt vmcnt(%c[nflag] - 1)\n\t"
      "global_store_dwordx2 v98, v[96:97], s[54:55] sc1\n\ts_mov_b64 exec, -1\n\t.endif\n\t"
      ".if n_ == 12\n\tv_mov_b64 v[128:129], v[84:85]\n\tv_mov_b32 v130, v86\n\tv_add_u32 v131, 57344, v84\n\tv_add_u32 v132, 57344, v85\n\t.endif\n\t"
      ".if n_ == 13\n\tv_add_u32 v133, 57344, v86\n\tv_add_u32 v134, 114688, v84\n\tv_add_u32 v135, 114688, v85\n\tv_add_u32 v136, 114688, v86\n\t.endif\n\t"
      ".if n_ == 14\n\tv_mov_b32 v137, v87\n\tv_mov_b64 v[138:139], v[88:89]\n\tv_add_u32 v140, 1536, v89\n\tv_mov_b32 v141, v90\n\t.endif\n\t"
      ".if n_ == 15\n\tv_mov_b32 v104, v92\n\tv_mov_b32 v105, v92\n\tv_mov_b32 v106, v92\n\tv_mov_b32 v107, v92\n\t.endif\n\t"
      ".if n_ == %c[naw] + 3\n\tds_write_b128 v91, v[104:107]\n\tds_write_b128 v91, v[104:107] offset:28672\n\t.endif\n\t"
      ".if n_ == %c[npoll]\n\tglobal_load_dwordx2 v[100:101], v72, s[54:55] sc1\n\t.endif\n\t"
      ".if n_ == %c[naw]\n\t" AWAIT(100, 101, 72) G1_STAMP(6) "tdma 0\n\t.endif\n\t"
      ".if n_ == %c[naw] + 1\n\ts_add_u32 s40, s54, s80\n\ts_addc_u32 s41, s55, 0\n\ts_xor_b32 s48, s80, 512\n\t"
      "global_load_dwordx4 v[64:67], v137, s[40:41] sc1\n\tglobal_load_dwordx2 v[72:73], v93, s[40:41] sc1\n\t"
      "s_add_u32 s40, s54, s48\n\ts_addc_u32 s41, s55, 0\n\t"
      "global_load_dwordx4 v[68:71], v137, s[40:41] sc1\n\tglobal_load_dwordx2 v[74:75], v93, s[40:41] sc1\n\t.endif\n\t"
      ".if n_ == %c[naw] + 2\n\ttdma 1\n\t.endif\n\t"
      ".endm\n\t"
      ".macro st1 i\n\t.if k_ == 0\n\tglobal_store_dwordx4 v[64+\\i], v[48+4*\\i:51+4*\\i], s[54:55] sc1\n\t.endif\n\t.endm\n\t"
      ".macro p2body vt\n\t"
      ".set k_, 0\n\t"
      ".rept 20\n\t"
      ".set xr_, 96 + 8 * k_\n\t"
      ".set c_, 32 + 16 * (k_ & 1)\n\t"
      ".set d_, 48 - 16 * (k_ & 1)\n\t"
      ".set o_, ((k_ + 1) & 7) * 8192\n\t"
      ".if k_ == 3\n\ts_waitcnt vmcnt(8)\n\ts_barrier\n\t.endif\n\t"
      ".if k_ == %c[kbar]\n\ts_waitcnt vmcnt(0)\n\ts_barrier\n\t.endif\n\t"
      ".if k_ < 19\n\t"
      ".if ((k_ + 1) & 7) == 0\n\tbump 76, 4, 0x10000\n\t.endif\n\t"
      "s_waitcnt lgkmcnt(6)\n\t"
      "mm \\vt, 0, xr_, c_\n\tst1 0\n\trw d_, 76, o_\n\t"
      "mm \\vt, 16, xr_+4, c_\n\tslot 2*k_\n\ts_waitcnt lgkmcnt(6)\n\t"
      "mm \\vt, 4, xr_, c_+4\n\tst1 1\n\trw d_+4, 77, o_\n\t"
      "mm \\vt, 20, xr_+4, c_+4\n\ts_waitcnt lgkmcnt(6)\n\t"
      "mm \\vt, 8, xr_, c_+8\n\tst1 2\n\trw d_+8, 78, o_\n\t"
      "mm \\vt, 24, xr_+4, c_+8\n\tslot 2*k_+1\n\ts_waitcnt lgkmcnt(6)\n\t"
      "mm \\vt, 12, xr_, c_+12\n\tst1 3\n\trw d_+12, 79, o_\n\t"
      "mm \\vt, 28, xr_+4, c_+12\n\t"
      ".else\n\t"
      "s_waitcnt lgkmcnt(0)\n\t"
      "mm \\vt, 0, xr_, c_\n\tmm \\vt, 16, xr_+4, c_\n\tmm \\vt, 4, xr_, c_+4\n\tmm \\vt, 20, xr_+4, c_+4\n\t"
      "mm \\vt, 8, xr_, c_+8\n\tmm \\vt, 24, xr_+4, c_+8\n\tmm \\vt, 12, xr_, c_+12\n\tmm \\vt, 28, xr_+4, c_+12\n\t"
      "s_setprio 3\n\tv_add_u32 v80, " STR(OCT) "*4 - (" STR(OCT) "*4 - 2560) * \\vt, v82\n\tv_add_u32 v81, " STR(OCT) "*4 - (" STR(OCT) "*4 - 2560) * \\vt, v83\n\ts_nop 4\n\tpack \\vt\n\t"
      ".endif\n\t"
      ".set k_, k_ + 1\n\t"
      ".endr\n\t"
      ".endm\n\t"
      "rw 32, 76, 16384\n\trx 96, 73, 0\n\trw 36, 77, 16384\n\trw 40, 78, 16384\n\trw 44, 79, 16384\n\t"
      "s_mov_b32 %[keep], m0\n\t"
      "s_cmp_lg_u32 %[vt1], 0\n\ts_cbranch_scc1 1f\n\t"
      "p1body 0\n\t"
      "s_mov_b32 s40, %[sc]\n\ts_cmp_lg_u32 %[kv1], 0\n\ts_nop 2\n\ts_cbranch_scc1 3f\n\t"
      ".set i_, 0\n\t.rept 16\n\tv_pk_mul_f32 v[i_:i_+1], v[i_:i_+1], s[40:41] op_sel_hi:[1,0]\n\t.set i_, i_ + 2\n\t.endr\n\t"
      "3:\n\tpack 0\n\ts_branch 2f\n\t"
      "1:\n\tp1body 1\n\ts_nop 4\n\tpack 1\n\t"
      "2:\n\t" G1_STAMP(3)
      "s_cmp_eq_u32 %[vt2], 1\n\ts_cbranch_scc1 1f\n\ts_cmp_eq_u32 %[vt2], 2\n\ts_cbranch_scc0 6f\n\t"
      ".set k_, 0\n\tst1 0\n\tst1 1\n\tst1 2\n\tst1 3\n\tslot 12\n\tslot 13\n\tslot 14\n\tslot 15\n\t"
      "v_mov_b32 v96, s50\n\tv_mov_b32 v97, s51\n\tv_mov_b32 v98, s52\n\t"
      "s_waitcnt vmcnt(0) lgkmcnt(0)\n\ts_mov_b64 exec, 1\n\tglobal_store_dwordx2 v98, v[96:97], s[54:55] sc1\n\ts_mov_b64 exec, -1\n\ts_barrier\n\t"
      "slot %c[npoll]\n\t"
      "slot %c[naw]\n\tslot %c[naw]+1\n\tslot %c[naw]+2\n\tslot %c[naw]+3\n\t"
      "s_waitcnt vmcnt(0)\n\ts_setprio 3\n\ts_branch 3f\n\t"
      "6:\n\tp2body 0\n\ts_branch 2f\n\t"
      "1:\n\tp2body 1\n\t"
      "2:\n\tglobal_store_dwordx4 v82, v[48:51], s[54:55] sc1\n\tglobal_store_dwordx4 v83, v[52:55], s[54:55] sc1\n\t"
      "global_store_dwordx4 v80, v[56:59], s[54:55] sc1\n\tglobal_store_dwordx4 v81, v[60:63], s[54:55] sc1\n\t"
      "3:\n\ts_mov_b32 m0, %[keep]\n\tv_mov_b32 v76, 0xdddd\n\tv_mov_b32 v77, v94\n\tv_mov_b32 v78, v93\n\t"
      "v_mbcnt_lo_u32_b32 v79, -1, 0\n\tv_mbcnt_hi_u32_b32 v79, -1, v79\n\tv_lshlrev_b32 v79, 4, v79\n\t"
      "s_waitcnt vmcnt(8) lgkmcnt(0)\n\t"
      ".purgem pack\n\t.purgem mm\n\t.purgem rx\n\t.purgem rw\n\t.purgem bump\n\t.purgem src2\n\t.purgem p1body\n\t.purgem tdma\n\t"
      ".purgem slot\n\t.purgem st1\n\t.purgem p2body\n\t"
      : "+{v[64:79]}"(ad), "+{v[80:95]}"(pk), "=&{v[128:143]}"(at), "=&{v[104:107]}"(cv4), [keep] "=&s"(keep), "+{s36}"(off1), "+{s47}"(off2),
        "=&{v[0:31]}"(d0), "=&{v[32:63]}"(d1), "=&{v[160:191]}"(d2), "=&{v[192:223]}"(d3), "=&{v[224:255]}"(d4), "=&{v[112:127]}"(d5),
        "=&{v[144:159]}"(d6), "=&{v[96:103]}"(d7), "=&{v[108:111]}"(d8)
      : [vt1] "s"(vt1), [kv1] "s"(kv1), [vt2] "s"(vt2), "{s[42:43]}"(sA), "{s37}"(str1), "{s38}"(10 * str1), "{s[44:45]}"(sB), "{s46}"(m0v),
        "{s[50:51]}"(tag), "{s52}"(flag), "{s[54:55]}"(ws), "{s72}"(tile), "{s[76:79]}"(so), "{s80}"(qoff),
        [sc] "n"(__builtin_bit_cast(unsigned, QSCALE)), [npoll] "n"(NPOLL), [nflag] "n"(NFLAG), [naw] "n"(NAW), [kbar] "n"(KBAR)
      : "memory", "scc", "vcc", "s40", "s41", "s48");
}

__device__ __forceinline__ u32x2 g1_offs(unsigned br, unsigned cg, unsigned T, unsigned lane) {
  const unsigned l16 = (lane & 31) * 16, h = lane >> 5;
  const unsigned a = h * OCT + l16 + (br * (unsigned)QK_BR + cg * (8 * OCT) + T * 512);
  const unsigned G = (cg - 20) * 4 + ((lane >> 4) & 1);
  const unsigned b = h * 1280 + l16 + ((unsigned)V_WS + br * (unsigned)V_BR + (cg - 20) * 1024 + T * 5120);
  return cg < 20 ? u32x2{a, a + 2 * OCT}
                 : u32x2{((G * 52) >> 8) * (256 * 1280 - 1280) + b, (((G + 2) * 52) >> 8) * (256 * 1280 - 1280) + b + 512};
}

constexpr int TILE_B = 3584, K2_OFF = 1024, V_OFF = 1536;
constexpr int CHUNK_B = 8 * TILE_B;

__device__ __forceinline__ f32x16 smfmac(bf16x8 a, f32x8 p, f32x16 c) {
  return __builtin_amdgcn_smfmac_f32_32x32x32_bf16(a, __builtin_bit_cast(bf16x16, p), c, 0xDDDD, 0, 0);
}
__device__ __forceinline__ f32x8 lo8(const f32x16& s) { return __builtin_shufflevector(s, s, 0, 1, 2, 3, 4, 5, 6, 7); }
__device__ __forceinline__ f32x8 hi8(const f32x16& s) { return __builtin_shufflevector(s, s, 8, 9, 10, 11, 12, 13, 14, 15); }
#define MX3(a, b, c) __builtin_fmaxf(__builtin_fmaxf((a), (b)), (c))
__device__ __forceinline__ float max16(const f32x16& s, float m) {
#pragma unroll
  for (int r = 0; r < 16; r += 2) m = MX3(m, s[r], s[r + 1]);
  return m;
}
__device__ __forceinline__ float xhalf_max(float m) {
  auto rr = __builtin_amdgcn_permlane32_swap(__float_as_uint(m), __float_as_uint(m), false, false);
  return __builtin_fmaxf(__uint_as_float(rr[0]), __uint_as_float(rr[1]));
}
__device__ __forceinline__ float row_sum(const f32x16& o) {
  auto rr = __builtin_amdgcn_permlane32_swap(__float_as_uint(o[8]), __float_as_uint(o[8]), false, false);
  return __uint_as_float(rr[1]);
}
template <bool MARK>
__device__ __forceinline__ void store_o(const f32x16& o, float l, char* base, unsigned lo1, unsigned lo2) {
  const float rl = __builtin_amdgcn_rcpf(l);
  const unsigned a0 = pk_bf16(o[0] * rl, o[1] * rl), a1 = pk_bf16(o[2] * rl, o[3] * rl);
  const unsigned b0 = pk_bf16(o[4] * rl, o[5] * rl), b1 = pk_bf16(o[6] * rl, o[7] * rl);
  const auto r0 = __builtin_amdgcn_permlane32_swap(a0, b0, false, false);
  const auto r1 = __builtin_amdgcn_permlane32_swap(a1, b1, false, false);
  u32x4 v = {r0[0], r1[0], r0[1], r1[1]};
  u32x2 c = {pk_bf16(o[8] * rl, o[9] * rl), pk_bf16(o[10] * rl, o[11] * rl)};
  if (MARK) {
    v = __builtin_elementwise_min(v, u32x4{~1u, ~1u, ~1u, ~1u}); c = __builtin_elementwise_min(c, u32x2{~1u, ~1u});
  }
  st16(base, lo1, v);
  st8lo(base, lo2, c);
}

__device__ __forceinline__ void poll_flags(const char* ws, unsigned off, unsigned long long tag) {
  asm volatile("global_load_dwordx2 v[124:125], v126, s[54:55] sc1\n\t" AWAIT(124, 125, 126)
               :: "{v126}"(off), "{s[54:55]}"(ws), "{s[50:51]}"(tag) : "memory", "scc", "vcc", "s48", "v124", "v125");
}
__device__ __forceinline__ void put_flag(char* ws, unsigned off, unsigned long long tag) {
  asm volatile("s_mov_b64 exec, 1\n\tglobal_store_dwordx2 %0, %1, %2 sc1\n\ts_mov_b64 exec, -1" :: "v"(off), "v"(tag), "s"(ws) : "memory");
}
#define AT_ASM_MACROS \
    ".macro mx d, s\n\tv_max3_f32 v[\\d], v[\\s], v[\\s+1], v[\\s+2]\n\t.set i_, \\s + 3\n\t.rept 6\n\tv_max3_f32 v[\\d], v[\\d], v[i_], v[i_+1]\n\t" \
    ".set i_, i_ + 2\n\t.endr\n\tv_max_f32 v[\\d], v[\\d], v[\\s+15]\n\t.endm\n\t" \
    ".macro xp d, s\n\tv_exp_f32 v[\\d], v[\\s]\n\tv_exp_f32 v[\\d+1], v[\\s+1]\n\tv_exp_f32 v[\\d+2], v[\\s+2]\n\tv_exp_f32 v[\\d+3], v[\\s+3]\n\t.endm\n\t" \
    ".macro smf o, v, p\n\tv_smfmac_f32_32x32x32_bf16 v[\\o:\\o+15], v[\\v:\\v+3], v[\\p:\\p+7], v76\n\t.endm\n\t" \
    ".macro qk1 s, k, q\n\tv_mfma_f32_32x32x16_bf16 v[\\s:\\s+15], v[\\k:\\k+3], v[\\q:\\q+3], 0\n\t.endm\n\t" \
    ".macro qk1c s, k, q, c\n\tv_mfma_f32_32x32x16_bf16 v[\\s:\\s+15], v[\\k:\\k+3], v[\\q:\\q+3], v[\\c:\\c+15]\n\t.endm\n\t" \
    ".macro qk2 s, k, q\n\tv_mfma_f32_32x32x8_bf16 v[\\s:\\s+15], v[\\k:\\k+1], v[\\q:\\q+1], v[\\s:\\s+15]\n\t.endm\n\t"
#define AT_ASM_PURGE ".purgem mx\n\t.purgem xp\n\t.purgem smf\n\t.purgem qk1\n\t.purgem qk1c\n\t.purgem qk2\n\t"

#define AT_A_TEXT AT_ASM_MACROS \
      ".macro pos j\n\t.set sl_, (\\j) >> 2\n\t.if sl_ >= 3\n\t.set sl_, sl_ - 3\n\t.endif\n\t" \
      ".set ar_, 128 + 3 * (sl_ >> 1)\n\t.set of_, (sl_ & 1) * 28672 + ((\\j) & 3) * 7168\n\t.endm\n\t" \
      ".macro dma part\n\t" \
      ".if di_ >= 0\n\t" \
      ".if \\part == 0\n\t" \
      ".if di_ == 1\n\ts_mov_b32 m0, s73\n\t.else\n\ts_add_u32 m0, s72, ((di_ + 2) %% 5) * 28672\n\t.endif\n\t" \
      "s_add_u32 s40, s54, s[56+2*di_]\n\ts_addc_u32 s41, s55, 0\n\t" \
      "global_load_lds_dwordx4 v137, s[40:41] sc1\n\t" \
      ".elseif \\part == 1\n\t" \
      "s_mov_b32 exec_hi, 0\n\tglobal_load_lds_dwordx4 v138, s[40:41] offset:1024 sc1\n\ts_mov_b32 exec_hi, -1\n\t" \
      "s_add_u32 s40, s54, s[57+2*di_]\n\ts_addc_u32 s41, s55, 0\n\t" \
      ".else\n\t" \
      "s_mov_b32 exec_lo, 0xfffff\n\ts_mov_b32 exec_hi, 0xfffff\n\t" \
      "global_load_lds_dwordx4 v139, s[40:41] offset:1536 sc1\n\tglobal_load_lds_dwordx4 v140, s[40:41] offset:2560 sc1\n\t" \
      "s_mov_b64 exec, -1\n\t" \
      ".endif\n\t.endif\n\t" \
        \
      ".set n_, -1\n\t" \
      ".if j_ >= 22 && j_ < 25 && 3 * (j_ - 22) + \\part < 8\n\t.set n_, 3 * (j_ - 22) + \\part\n\t.endif\n\t" \
      ".if j_ >= 28 && 3 * (j_ - 28) + \\part < 5\n\t.set n_, 10 + 3 * (j_ - 28) + \\part\n\t.endif\n\t" \
      ".if n_ >= 0\n\t.set pc_, n_ %% 5\n\t" \
      ".if n_ == 0 || n_ == 10\n\ts_add_u32 m0, s44, n_ * 4096\n\ts_add_u32 s40, s42, m0\n\ts_addc_u32 s41, s43, 0\n\t.endif\n\t" \
      ".if n_ == 5\n\ts_add_u32 m0, s45, 143360 - 81920\n\ts_add_u32 s40, s42, s45\n\ts_addc_u32 s41, s43, 0\n\t.endif\n\t" \
      ".if pc_ == 4\n\ts_add_u32 m0, m0, 4096\n\ts_add_u32 s40, s40, 4096\n\ts_addc_u32 s41, s41, 0\n\t.endif\n\t" \
      "global_load_lds_dwordx4 v79, s[40:41] offset:1024*(pc_&3) sc1\n\t.endif\n\t" \
      ".endm\n\t" \
      ".macro flg msk\n\tv_mov_b32 v142, s50\n\tv_mov_b32 v143, s51\n\tv_mov_b32 v108, s52\n\ts_mov_b64 exec, \\msk\n\ts_cbranch_execz 8f\n\ts_waitcnt vmcnt(0)\n\t" \
      "global_store_dwordx2 v108, v[142:143], s[54:55] sc1\n\t8:\n\ts_mov_b64 exec, -1\n\t.endm\n\t" \
      ".macro tabl d, r\n\tglobal_load_dwordx4 v[\\d:\\d+3], v79, s[40:41] offset:1024*(\\r)\n\t.endm\n\t" \
      ".macro old q\n\t.if (\\q) >= 0 && (\\q) < 10\n\tglobal_load_dwordx4 v[184+4*(\\q):187+4*(\\q)], v108, s[54:55] sc1\n\tv_add_u32 v108, " STR(OCT) "*4, v108\n\t.endif\n\t.endm\n\t" \
      "s_mov_b32 %[keep], m0\n\t" \
      ".set j_, %c[j0]\n\t" \
      ".rept %c[j1] - %c[j0]\n\t" \
      ".set a_, 80 + 14 * (j_ & 1)\n\t.set b_, 94 - 14 * (j_ & 1)\n\t" \
      ".if j_ == 4\n\t" AWAIT(142, 143, 141) ".set di_, 0\n\tdma 0\n\tdma 1\n\tdma 2\n\t.endif\n\t" \
      ".if j_ == 6\n\ts_waitcnt vmcnt(10)\n\ts_barrier\n\t.endif\n\t" \
      ".if j_ == 10\n\ts_waitcnt vmcnt(1)\n\ts_barrier\n\t.endif\n\t" \
      ".if j_ == 14 || j_ == 18\n\ts_waitcnt vmcnt(0)\n\ts_barrier\n\t.endif\n\t" \
      ".if j_ == 22\n\t" AWAIT(142, 143, 141) ".endif\n\t" \
      ".if j_ == 28\n\t" AWAIT(142, 143, 252) "v_add_u32 v108, %c[obr], v253\n\t.endif\n\t" \
      ".if j_ == 28 || j_ == 22 || j_ == 2\n\ts_barrier\n\t.endif\n\t" \
      AT_STAMP \
      ".if j_ < 2\n\ts_setprio 1 - j_\n\t" \
      ".elseif j_ == 10 || j_ == 11\n\ts_setprio 3\n\t.elseif j_ == 12 || j_ == 13\n\ts_setprio 0\n\t" \
      ".elseif j_ < 22\n\ts_setprio 3 - ((j_ + 2) & 3)\n\t.elseif j_ >= 28\n\ts_setprio 31 - j_\n\t" \
      ".elseif j_ != 23 && j_ != 25\n\ts_setprio 3 - (j_ - 21) / 2\n\t.endif\n\t" \
      ".if j_ == 14\n\tflg 1\n\t" \
      "v_mov_b32 v108, 0x3f803f80\n\tv_mov_b32 v109, v108\n\tv_mov_b32 v110, v108\n\tv_mov_b32 v111, v108\n\ts_mov_b64 exec, 15\n\t" \
      "ds_write_b128 v77, v[108:111]\n\ts_mov_b64 exec, -1\n\t.endif\n\t" \
      ".set di_, -1\n\t" \
      ".if j_ == 4 || j_ == 5 || j_ == 6\n\t.set di_, j_ - 3\n\t.endif\n\t.if j_ == 10\n\t.set di_, 4\n\t.endif\n\t" \
      ".if j_ >= 14 && j_ < 17\n\t.set di_, j_ - 9\n\t.endif\n\t" \
        \
      "pos j_+2\n\t" \
      "qk1 48, a_, 68\n\t.if j_ < 30\n\tds_read_b128 v[a_:a_+3], v[ar_] offset:of_\n\t.endif\n\told 4*(j_-28)\n\txp 112, 32\n\t" \
      "qk2 48, a_+4, 74\n\t.if j_ < 30\n\tds_read_b64 v[a_+4:a_+5], v[ar_+1] offset:of_\n\t.endif\n\told 4*(j_-28)+1\n\txp 116, 36\n\t" \
      "pos j_+1\n\t" \
      "smf 16, b_+10, 120\n\t.if j_ < 31\n\tds_read_b128 v[b_+10:b_+13], v[ar_+2] offset:of_+1024\n\t.endif\n\told 4*(j_-28)+2\n\txp 120, 40\n\t" \
      "smf 0, a_+6, 112\n\t.if j_ < 30\n\ts_waitcnt lgkmcnt(3)\n\t.elseif j_ == 30\n\ts_waitcnt lgkmcnt(1)\n\t.else\n\ts_waitcnt lgkmcnt(0)\n\t.endif\n\t" \
      "old 4*(j_-28)+3\n\t" \
      ".if j_ == 5\n\tglobal_load_dwordx4 v[236:239], v137, s[42:43] sc1\n\tglobal_load_dwordx2 v[240:241], v78, s[42:43] sc1\n\t.endif\n\t" \
      "xp 124, 44\n\t" \
        \
      ".if j_ < 31\n\tqk1 32, b_, 64\n\t.endif\n\tdma 0\n\t" \
      ".if j_ == 0\n\tflg s[74:75]\n\t.endif\n\t" \
      ".if j_ == 3\n\tglobal_load_dwordx2 v[142:143], v141, s[54:55] sc1\n\t.endif\n\t" \
      ".if j_ == 7\n\ts_mov_b64 s[40:41], s[44:45]\n\ttabl 252, 0\n\ttabl 160, 1\n\t.endif\n\t" \
      ".if j_ == 8\n\ttabl 224, 2\n\ttabl 228, 3\n\t.endif\n\t" \
      ".if j_ == 20\n\tglobal_load_dwordx2 v[142:143], v141, s[54:55] sc1\n\t.endif\n\t" \
      ".if j_ == 25\n\ts_mov_b32 m0, 40960\n\tv_min_u32 v140, 384, v79\n\tv_lshl_add_u32 v140, v140, 3, s46\n\tglobal_load_lds_dword v140, s[54:55] sc1\n\t.endif\n\t" \
      ".if j_ == 26\n\tglobal_load_dwordx2 v[142:143], v252, s[54:55] sc1\n\t.endif\n\t" \
      "xp 112, 48\n\t" \
      ".if j_ < 31\n\tqk2 32, b_+4, 72\n\t.endif\n\tdma 1\n\t" \
      ".if j_ == 7\n\ttabl 164, 2\n\ttabl 168, 3\n\ts_add_u32 s40, s40, 4096\n\ts_addc_u32 s41, s41, 0\n\t.endif\n\t" \
      ".if j_ == 8\n\ts_add_u32 s40, s40, 4096\n\ts_addc_u32 s41, s41, 0\n\ttabl 232, 0\n\t.endif\n\t" \
      "xp 116, 52\n\t" \
      "smf 0, a_+10, 120\n\tdma 2\n\t" \
      ".if j_ == 7\n\ttabl 172, 0\n\ttabl 220, 1\n\t.endif\n\t" \
      ".if j_ == 9\n\tv_lshrrev_b32 v108, 5, v79\n\tv_mul_u32_u24 v108, 1248, v108\n\ts_mov_b32 m0, s47\n\tv_add_u32 v108, v108, v79\n\t" \
      "global_load_lds_dwordx4 v108, s[76:77]\n\t.endif\n\t" \
      "xp 120, 56\n\t" \
      "pos j_+2\n\t" \
      "smf 16, a_+6, 112\n\t.if j_ < 30\n\tds_read_b128 v[a_+6:a_+9], v[ar_+2] offset:of_\n\t.endif\n\txp 124, 60\n\t" \
      ".set j_, j_ + 1\n\t" \
      ".endr\n\t" \
      ".if %c[j1] == 32\n\ts_nop 1\n\tsmf 16, 104, 120\n\ts_nop 15\n\t.endif\n\t" \
      "s_mov_b32 m0, %[keep]\n\t" \
      ".purgem pos\n\t.purgem dma\n\t.purgem flg\n\t.purgem tabl\n\t.purgem old\n\t" AT_ASM_PURGE
#define AT_STAMP
#define AT_STV
#define AT_STV_ARG
#define AT_STV_PASS
#define AT_A_STATE "+{v[0:31]}"(o), "+{v[32:47]}"(s0), "+{v[112:127]}"(pp), "+{v[80:111]}"(fr), "+{v[128:143]}"(ad), [keep] "=&s"(keep)
#define AT_A_CLOB "memory", "scc", "vcc", "s40", "s41", "s48", "v48", "v49", "v50", "v51", "v52", "v53", "v54", "v55", "v56", "v57", "v58", "v59", "v60", "v61", "v62", "v63"
__device__ __forceinline__ void at_a1(f32x32& o, f32x16& s0, f32x16& pp, f32x32& fr, const u32x16& q, u32x16& ad, const char* ws, u32x16 so,
                                      unsigned m0v, unsigned m0b, unsigned long long tag, unsigned flag, unsigned long long fmask,
                                      const char* qB, const void* tab, f32x16& mF, f32x16& mD, u32x8& qv, u32x4& tw,
                                      const char* fsrc, unsigned fl AT_STV_ARG) {
  unsigned keep;
  asm volatile(AT_A_TEXT
      : AT_A_STATE, "=&{v[160:175]}"(mF), "=&{v[220:235]}"(mD), "=&{v[236:243]}"(qv), "=&{v[252:255]}"(tw) AT_STV
      : "{v[64:79]}"(q), "{s[54:55]}"(ws), "{s[56:71]}"(so), "{s72}"(m0v), "{s73}"(m0b), "{s[50:51]}"(tag), "{s52}"(flag), "{s[74:75]}"(fmask),
        "{s[42:43]}"(qB), "{s[44:45]}"(tab), "{s[76:77]}"(fsrc), "{s47}"(fl), [j0] "n"(0), [j1] "n"(12), [obr] "n"(O_BR)
      : AT_A_CLOB);
}
__device__ __forceinline__ void at_a2(f32x32& o, f32x16& s0, f32x16& pp, f32x32& fr, const u32x16& q, u32x16& ad, const char* ws, u32x16 so,
                                      unsigned m0v, unsigned long long tag, unsigned flag, const u32x4& tw, const char* src, unsigned d0,
                                      unsigned d1, unsigned tch, f32x32& of, f32x8& of2) {
  unsigned keep;
  asm volatile(AT_A_TEXT
      : AT_A_STATE, "=&{v[184:215]}"(of), "=&{v[216:223]}"(of2)
      : "{v[64:79]}"(q), "{s[54:55]}"(ws), "{s[56:71]}"(so), "{s72}"(m0v), "{s[50:51]}"(tag), "{s52}"(flag),
        "{v[252:255]}"(tw), "{s[42:43]}"(src), "{s44}"(d0), "{s45}"(d1), "{s46}"(tch), [j0] "n"(12), [j1] "n"(32), [obr] "n"(O_BR)
      : AT_A_CLOB);
}

__device__ __forceinline__ void at_b(f32x16& o, f32x16& mF, const f32x16& mD, u32x8& q, unsigned tb, unsigned qt, char* oA, const u32x16& qa,
                                     u32x16& ad, f32x32& k0, f32x16& k1, f32x32& k2, f32x16& k3, unsigned fl, unsigned fd, char* ws) {
  asm volatile(AT_ASM_MACROS
      "v_add_u32 v242, %[tb], v128\n\tv_add_u32 v243, %[tb], v129\n\tv_add_u32 v245, %[tb], v130\n\t"
      "ds_read_b128 v[176:179], v242 offset:7*3584\n\tds_read_b64 v[180:181], v243 offset:7*3584\n\t"
      "ds_read_b128 v[184:187], v242 offset:8*3584\n\tds_read_b64 v[182:183], v243 offset:8*3584\n\t"
      "ds_read_b128 v[192:195], v242\n\tds_read_b64 v[196:197], v243\n\tds_read_b128 v[206:209], v242 offset:3584\n\tds_read_b64 v[210:211], v243 offset:3584\n\t"
      "ds_read_b128 v[198:201], v245\n\tds_read_b128 v[212:215], v245 offset:3584\n\tds_read_b128 v[202:205], v245 offset:1024\n\t"
      "s_mov_b32 exec_lo, 0\n\tv_mov_b64 v[240:241], 0\n\ts_mov_b32 exec_lo, -1\n\tv_mov_b64 v[188:189], -1\n\tv_mov_b64 v[190:191], -1\n\t"
      "s_waitcnt lgkmcnt(7)\n\t"
      "qk1 144, 176, 236\n\tqk2 144, 180, 240\n\tqk1c 48, 184, 236, 220\n\tqk2 48, 182, 240\n\t"
      "global_store_dwordx4 v137, v[188:191], s[42:43] sc1\n\ts_mov_b32 exec_hi, 0\n\tglobal_store_dwordx2 v78, v[188:189], s[42:43] sc1\n\ts_mov_b32 exec_hi, -1\n\t"
      "s_waitcnt lgkmcnt(5)\n\tqk1c 160, 192, 236, 160\n\tds_read_b128 v[192:195], v242 offset:7168\n\ts_cmp_eq_u32 %[qt], 0\n\ts_nop 1\n\t"
      "mx 216, 144\n\tmx 218, 48\n\ts_cbranch_scc1 1f\n\tv_max_f32 v218, v218, v216\n\t1:\n\tv_mov_b32 v219, v218\n\t"
      "v_mov_b64 v[144:145], 0\n\tv_mov_b64 v[146:147], 0\n\tv_permlane32_swap_b32 v218, v219\n\tv_max_f32 v218, v218, v219\n\tv_add_f32 v218, 0x42600000, v218\n\t"
      "s_mov_b32 exec_lo, 0\n\tv_cvt_pk_bf16_f32 v240, -v218, 0\n\ts_mov_b32 exec_lo, -1\n\tv_mov_b64 v[148:149], 0\n\t"
      "qk2 160, 196, 240\n\tds_read_b64 v[196:197], v243 offset:7168\n\t"
      ".set i_, 150\n\t.rept 5\n\tv_mov_b64 v[i_:i_+1], 0\n\t.set i_, i_ + 2\n\t.endr\n\t"
      "s_waitcnt lgkmcnt(2)\n\ts_nop 1\n\t"
      ".set t_, 0\n\t"
      ".rept 9\n\t"
      ".set a_, 192 + 14 * (t_ & 1)\n\t.set b_, 206 - 14 * (t_ & 1)\n\t.set c_, 160 - 112 * (t_ & 1)\n\t.set d_, 48 + 112 * (t_ & 1)\n\t"
      ".if t_ == 0 || t_ == 5\n\ts_setprio 2 - t_ / 5\n\t.endif\n\t"
      ".if t_ < 7\n\tqk1 d_, b_, 236\n\t.elseif t_ == 7\n\tqk1c d_, b_, 236, 220\n\t.endif\n\t"
      ".if t_ < 6\n\tds_read_b128 v[b_:b_+3], v242 offset:(t_+3)*3584\n\t.endif\n\t"
      "xp 176, c_\n\t"
      ".if t_ < 8\n\tqk2 d_, b_+4, 240\n\t.endif\n\t"
      ".if t_ < 6\n\tds_read_b64 v[b_+4:b_+5], v243 offset:(t_+3)*3584\n\t.endif\n\t"
      ".if t_ == 4\n\tv_and_b32 v142, 0xf0, v79\n\tv_lshrrev_b32 v142, 1, v142\n\tv_sub_u32 v142, v79, v142\n\tv_add_u32 v142, %[fl], v142\n\ts_waitcnt vmcnt(6)\n\t"
      "ds_read_b64_tr_b16 v[108:109], v142\n\tds_read_b64_tr_b16 v[110:111], v142 offset:128\n\tv_add_u32 v142, %[fd], v79\n\t.endif\n\t"
      ".if t_ == 6\n\tglobal_store_dwordx4 v142, v[108:111], %[ws] sc1\n\t.endif\n\t"
      "xp 180, c_+4\n\t"
      ".if t_ > 0\n\tsmf 144, b_+10, 184\n\t.endif\n\t"
      ".if t_ < 8\n\tds_read_b128 v[b_+10:b_+13], v245 offset:(t_+1)*3584+1024\n\t.endif\n\t"
      "xp 184, c_+8\n\t"
      "smf 144, a_+6, 176\n\t"
      ".if t_ < 6\n\ts_waitcnt lgkmcnt(3)\n\t.elseif t_ < 8\n\ts_waitcnt lgkmcnt(1)\n\t.else\n\ts_waitcnt lgkmcnt(0)\n\t.endif\n\t"
      ".if t_ < 7\n\tds_read_b128 v[a_+6:a_+9], v245 offset:(t_+2)*3584\n\t.endif\n\t"
      "xp 188, c_+12\n\t"
      ".set t_, t_ + 1\n\t"
      ".endr\n\t"
      "s_nop 1\n\tsmf 144, 202, 184\n\ts_nop 11\n\t"
      AT_ASM_PURGE
      : "=&{v[144:159]}"(o), "+{v[160:175]}"(mF), "+{v[0:31]}"(k0), "+{v[32:47]}"(k1), "+{v[80:111]}"(k2), "+{v[112:127]}"(k3), "+{v[128:143]}"(ad),
        "+{v[236:243]}"(q)
      : "{v[220:235]}"(mD), "{v[64:79]}"(qa), [tb] "s"(tb), [qt] "s"(qt), "{s[42:43]}"(oA), [fl] "s"(fl), [fd] "s"(fd), [ws] "s"(ws)
      : "memory", "scc", "v245", "v48", "v49", "v50", "v51", "v52", "v53", "v54", "v55", "v56", "v57", "v58", "v59", "v60", "v61", "v62", "v63",
        "v176", "v177", "v178", "v179", "v180", "v181", "v182", "v183", "v184", "v185", "v186", "v187", "v188", "v189", "v190", "v191",
        "v192", "v193", "v194", "v195", "v196", "v197", "v198", "v199", "v200", "v201", "v202", "v203", "v204", "v205", "v206", "v207",
        "v208", "v209", "v210", "v211", "v212", "v213", "v214", "v215", "v216", "v217", "v218", "v219");
}

#define PJ_STAMP(i)
#define KLD 9
__device__ __forceinline__ void proj_all(const f32x32& of, const f32x8& of2, unsigned l16, const u32x4& tw, const char* src, unsigned pos,
                                         unsigned x, unsigned grp, const char* ws, char* out, unsigned long long m5 AT_STV_ARG) {
  f32x32 t0, t1, t2; f32x16 t3, t4, t5; f32x8 t6, t7; f32x4 t8; u32x2 t9;
  asm volatile(
      ".macro rdw k, sp, ab\n\t.set d_, 80 + 20 * ((\\k) & 1)\n\t"
      ".if (\\k) < \\sp\n\t.set a_, \\ab\n\t.set o_, (\\k) * 5120\n\t.else\n\t.set a_, \\ab+1\n\t.set o_, ((\\k) - \\sp) * 5120\n\t.endif\n\t.endm\n\t"
      ".macro rd1 i\n\tds_read_b128 v[d_+4*\\i:d_+4*\\i+3], v[a_] offset:o_+1024*\\i\n\t.endm\n\t"
      ".macro rd5\n\trd1 0\n\trd1 1\n\trd1 2\n\trd1 3\n\trd1 4\n\t.endm\n\t"
      ".macro pm i, ac, of, last\n\t"
      ".if k_ == 0\n\tv_mfma_f32_16x16x32_bf16 v[\\ac+4*\\i:\\ac+4*\\i+3], v[c_+4*\\i:c_+4*\\i+3], v[\\of:\\of+3], 0\n\t"
      ".else\n\tv_mfma_f32_16x16x32_bf16 v[\\ac+4*\\i:\\ac+4*\\i+3], v[c_+4*\\i:c_+4*\\i+3], v[\\of+4*k_:\\of+3+4*k_], v[\\ac+4*\\i:\\ac+4*\\i+3]\n\t.endif\n\t"
      ".if k_ < \\last\n\trd1 \\i\n\ts_waitcnt lgkmcnt(4)\n\t.elseif \\i < 4\n\ts_waitcnt lgkmcnt(3 - \\i)\n\t.endif\n\t.endm\n\t"
      ".macro ldo\n\tv_mov_b32 v72, v253\n\t.set q_, 0\n\t.rept 10\n\t"
      "global_load_dwordx4 v[32+4*q_:35+4*q_], v72, s[54:55] sc1\n\t.if q_ < 9\n\tv_add_u32 v72, " STR(OCT) "*4, v72\n\t.endif\n\t.set q_, q_ + 1\n\t.endr\n\t.endm\n\t"
      ".macro wp n, hf\n\t.set n_, \\n\n\t.if n_ < 7\n\t"
      ".if (n_ & 3) == 0\n\ts_add_u32 m0, s44, 86016 * (1 - \\hf) + 1024 * n_\n\ts_add_u32 s40, s42, 51200 * \\hf + 1024 * n_\n\ts_addc_u32 s41, s43, 0\n\t.endif\n\t"
      "global_load_lds_dwordx4 v79, s[40:41] offset:1024*(n_&3) sc1\n\t.endif\n\t.endm\n\t"
      ".macro snd ac, off\n\tds_write_b128 v124, v[\\ac+8:\\ac+11] offset:\\off\n\tds_write_b128 v124, v[\\ac+12:\\ac+15] offset:\\off+1024\n\t"
      "ds_write_b128 v124, v[\\ac+16:\\ac+19] offset:\\off+2048\n\t.endm\n\t"
      ".macro rcv off\n\tds_read_b128 v[128:131], v125 offset:\\off\n\tds_read_b128 v[132:135], v125 offset:\\off+1024\n\tds_read_b128 v[136:139], v125 offset:\\off+2048\n\t.endm\n\t"
      ".macro fin ac, off\n\t"
      "v_pk_add_f32 v[128:129], v[128:129], v[\\ac:\\ac+1]\n\tv_pk_add_f32 v[130:131], v[130:131], v[\\ac+2:\\ac+3]\n\t"
      "v_pk_add_f32 v[132:133], v[132:133], v[\\ac+4:\\ac+5]\n\tv_pk_add_f32 v[134:135], v[134:135], v[\\ac+6:\\ac+7]\n\t"
      "v_cvt_pk_bf16_f32 v140, v128, v129\n\tv_cvt_pk_bf16_f32 v141, v130, v131\n\tv_cvt_pk_bf16_f32 v142, v132, v133\n\tv_cvt_pk_bf16_f32 v143, v134, v135\n\t"
      "v_pk_add_f32 v[136:137], v[136:137], v[\\ac+16:\\ac+17]\n\tv_pk_add_f32 v[138:139], v[138:139], v[\\ac+18:\\ac+19]\n\t"
      "v_permlane16_swap_b32 v140, v142\n\tv_permlane16_swap_b32 v141, v143\n\t"
      "v_cvt_pk_bf16_f32 v136, v136, v137\n\tv_cvt_pk_bf16_f32 v137, v138, v139\n\t"
      "global_store_dwordx4 v254, v[140:143], s[56:57] offset:\\off\n\t"
      "s_mov_b64 exec, s[58:59]\n\tglobal_store_dwordx2 v255, v[136:137], s[56:57] offset:\\off\n\ts_mov_b64 exec, -1\n\t.endm\n\t"
      "s_mul_i32 s48, s46, 51200\n\tv_add_u32 v120, s48, v79\n\trdw 0, 6, 120\n\trd5\n\t"
      "s_mul_i32 s49, s46, 143360 - 30720\n\ts_add_u32 s49, s49, 30720\n\tv_add_u32 v121, s49, v79\n\t"
      "s_lshr_b32 s48, s48, 1\n\tv_add_u32 v123, s48, v79\n\tv_add_u32 v122, 86016, v123\n\t"
      "s_add_u32 s48, s45, 51200\n\tv_add_u32 v124, s48, v79\n\ts_mul_i32 s49, s46, 24576\n\ts_sub_u32 s49, 12288, s49\n\tv_add_u32 v125, s49, v124\n\t"
      "s_barrier\n\ts_waitcnt lgkmcnt(4)\n\t"
      ".set k_, 0\n\t.rept 10\n\t.set c_, 80 + 20 * (k_ & 1)\n\trdw k_+1, 6, 120\n\t"
      ".if k_ == 0 || k_ == 3 || k_ == 6\n\ts_setprio 2 - k_ / 3\n\t.endif\n\t"
      ".if k_ == %c[kld]\n\tldo\n\t.endif\n\t"
      "pm 0, 0, 184, 9\n\twp 2*k_, 0\n\tpm 1, 0, 184, 9\n\tpm 2, 0, 184, 9\n\twp 2*k_+1, 0\n\tpm 3, 0, 184, 9\n\tpm 4, 0, 184, 9\n\t"
      ".set k_, k_ + 1\n\t.endr\n\t"
      "s_setprio 3\n\ts_mov_b32 s48, 0x40000\n\ts_waitcnt vmcnt(10)\n\ts_barrier\n\t"
      PJ_STAMP(16)
      "rdw 0, 5, 122\n\trd5\n\tsnd 0, 0\n\twp 0, 1\n\twp 1, 1\n\twp 2, 1\n\twp 3, 1\n\twp 4, 1\n\twp 5, 1\n\twp 6, 1\n\ts_waitcnt vmcnt(7)\n\t"
      "5:\n\t"
      "v_max3_u32 v72, v32, v33, v34\n\t.set q_, 35\n\t.rept 18\n\tv_max3_u32 v72, v72, v[q_], v[q_+1]\n\t.set q_, q_ + 2\n\t.endr\n\t"
      "v_max_u32 v72, v72, v71\n\tv_cmp_eq_u32 vcc, -1, v72\n\ts_sub_u32 s48, s48, 1\n\t"
      "s_cbranch_vccz 4f\n\ts_cbranch_scc1 4f\n\tldo\n\ts_waitcnt vmcnt(0)\n\ts_branch 5b\n\t4:\n\t"
      PJ_STAMP(17)
      "s_waitcnt lgkmcnt(0)\n\t"
      ".set k_, 0\n\t.rept 5\n\t.set c_, 80 + 20 * (k_ & 1)\n\trdw k_+1, 5, 122\n\t"
      "pm 0, 160, 32, 4\n\tpm 1, 160, 32, 4\n\tpm 2, 160, 32, 4\n\tpm 3, 160, 32, 4\n\tpm 4, 160, 32, 4\n\t"
      ".set k_, k_ + 1\n\t.endr\n\t"
      "s_waitcnt vmcnt(0)\n\ts_barrier\n\t"
      PJ_STAMP(18)
      "rcv 0\n\trd5\n\ts_waitcnt lgkmcnt(4)\n\t"
      ".rept 5\n\t.set c_, 80 + 20 * (k_ & 1)\n\trdw k_+1, 5, 122\n\t"
      "pm 0, 160, 32, 9\n\tpm 1, 160, 32, 9\n\tpm 2, 160, 32, 9\n\tpm 3, 160, 32, 9\n\tpm 4, 160, 32, 9\n\t"
      ".if k_ == 5\n\tfin 0, 1280\n\t.endif\n\t"
      ".set k_, k_ + 1\n\t.endr\n\t"
      "s_nop 6\n\tsnd 160, 86016-51200\n\ts_waitcnt lgkmcnt(0)\n\ts_barrier\n\t"
      PJ_STAMP(20)
      "rcv 86016-51200\n\ts_waitcnt lgkmcnt(0)\n\tfin 160, 0\n\t"
      ".purgem rdw\n\t.purgem rd1\n\t.purgem rd5\n\t.purgem pm\n\t.purgem ldo\n\t.purgem wp\n\t.purgem snd\n\t.purgem rcv\n\t.purgem fin\n\t"
      : "=&{v[0:31]}"(t0), "=&{v[32:63]}"(t1), "=&{v[80:111]}"(t2), "=&{v[112:127]}"(t3), "=&{v[128:143]}"(t4), "=&{v[160:175]}"(t5),
        "=&{v[64:71]}"(t6), "=&{v[176:183]}"(t7), "=&{v[72:73]}"(t9) AT_STV
      : "{v[184:215]}"(of), "{v[216:223]}"(of2), "{v79}"(l16), "{v[252:255]}"(tw), "{s[42:43]}"(src), "{s44}"(pos), "{s45}"(x), "{s46}"(grp),
        "{s[54:55]}"(ws), "{s[56:57]}"(out), "{s[58:59]}"(m5), [kld] "n"(KLD)
      : "memory", "scc", "vcc", "s40", "s41", "s48", "s49");
}
typedef __attribute__((address_space(3))) f32x4* lds_f4;

struct LaneTab { unsigned v[9][64][4]; };
constexpr LaneTab make_tab() {
  LaneTab t = {};
  for (unsigned l = 0; l < 64; ++l) {
    const unsigned g = l >> 4;
    t.v[0][l][0] = (l & 15) * 64 * 128;
    t.v[0][l][1] = g * OCT + (l & 15) * 16;
    t.v[0][l][2] = (l & 15) * 2560 + (g & 1) * 32 + (g >> 1) * 16;
    t.v[0][l][3] = (l & 15) * 2560 + 128 + g * 8;
    for (unsigned r = 0; r < 16; ++r) {
      const unsigned key = (r & 3) + 8 * (r >> 2) + 4 * (l >> 5);
      t.v[1 + (r >> 2)][l][r & 3] = key > (l & 31) ? 0u : 0xFF800000u;
      t.v[5 + (r >> 2)][l][r & 3] = key <= (l & 31) ? 0u : 0xFF800000u;
    }
  }
  return t;
}
__device__ const LaneTab lane_tab = make_tab();
constexpr unsigned CODE_B = 34816;

__global__ void __launch_bounds__(512, 2)
k_fused(const char* __restrict__ xa_, const char* __restrict__ wa_, const char* __restrict__ woa_, const char* __restrict__ xb_,
        const char* __restrict__ wb_, const char* __restrict__ wob_, char* __restrict__ ws_, char* __restrict__ out_) {
  __shared__ __attribute__((aligned(16))) char lds[163840];
  const unsigned bid = blockIdx.x, xcd = bid & 7, slot = bid >> 3, br = xcd >> 2;
  u32x4 ka; u32x2 kb; u32x8 kc;
  {
    const void* kp = (const void*)__builtin_amdgcn_kernarg_segment_ptr();
    asm volatile("s_load_dwordx4 %0, %3, %4\n\ts_load_dwordx8 %2, %3, 0x20\n\ts_load_dwordx2 %1, %3, 0x10"
                 : "=&s"(ka), "=&s"(kb), "=&s"(kc) : "s"(kp), "s"(br * 24));
  }
  STAMP_DECL STAMP(0);
  unsigned lane = threadIdx.x & 63;
  const unsigned w = __builtin_amdgcn_readfirstlane(threadIdx.x >> 6);
  const unsigned grp = w >> 2, lw = w & 3;
  const unsigned mt = (xcd & 3) * 4 + (slot & 3), u = slot >> 2;
  const unsigned win = (5 * ((mt >> 1) & 3)) >> 1;
  const unsigned kv1 = (u + 3) >> 3;
  const unsigned j1 = win + u - 5;
#define ROT (5 * ((u + slot) & 1) + 3 * ((xcd >> 1) & 1) + (xcd & 1))
  auto mod10 = [](unsigned x) { return __builtin_elementwise_min(x, x - 10); };
  const unsigned rot = mod10(ROT);
  const unsigned m0v = grp * 16384 + lw * 4096;
  const unsigned str1 = grp ? 64 * 3840 : 128;
  unsigned o1[4], tw;
  {
    const unsigned row = lane >> 3, pos = lane & 7;
    tw = row * 3840 + ((pos ^ (row & 2)) * 16);
    const unsigned t1 = grp ? tw : row * 1280 + ((pos ^ (row >> 1)) * 16);
    const unsigned gap = kv1 ? 1280 : 128, m1 = grp ? 0 : 64;
    o1[0] = t1 + 1024;
    o1[1] = (t1 ^ m1) + (grp ? gap : 8 * 1280);
    o1[2] = (t1 ^ m1 ^ 64) + (grp ? 8 * 3840 - 1024 : 16 * 1280 - 1024);
    o1[3] = (t1 ^ 64) + (grp ? 8 * 3840 - 2048 + gap : 24 * 1280 - 2048);
  }
  const unsigned offA = grp ? 16 * lw * 3840 + (kv1 ? 1280 + j1 * 128 : u * 256) : (mt * 128 + 32 * lw) * 1280;
  asm volatile("s_waitcnt lgkmcnt(0)" : "+s"(ka), "+s"(kb), "+s"(kc));
  auto ptr = [](unsigned lo, unsigned hi) { return (char*)(((unsigned long long)hi << 32) | lo); };
  const char* sA = ptr(grp ? ka[2] : ka[0], grp ? ka[3] : ka[1]) + (offA - 1024ll);
  char* const ws = ptr(kc[4], kc[5]);
  glds16x4(sA + rot * str1, o1, m0v);
  u32x2 ep; unsigned tch[4];
  asm volatile("s_load_dwordx2 %0, %1, %2" : "=&s"(ep) : "s"(ws), "s"((unsigned)CTL_WS));
  {
    const unsigned i64 = (slot * 8 + w) * 64;
    unsigned long long pc;
    asm volatile("s_getpc_b64 %0\n\ts_load_dword %1, %0, %5\n\ts_load_dword %2, %0, %6\n\ts_load_dword %3, %0, %7\n\ts_load_dword %4, %8, %9"
                 : "=&s"(pc), "=&s"(tch[0]), "=&s"(tch[1]), "=&s"(tch[2]), "=&s"(tch[3])
                 : "s"(i64), "s"(i64 + 16384), "s"(__builtin_elementwise_min(i64 + 32768, CODE_B)), "s"(&lane_tab),
                   "s"(__builtin_elementwise_min(i64, 9152u)));
  }
  const unsigned j2 = mod10(win + 3 + u);
  const unsigned Tg = mt * 4 + lw;
  const unsigned vt1 = grp & kv1, vt2 = u < 7 ? grp : 2;
  const unsigned cg1 = kv1 ? 10 + 10 * grp + j1 : 2 * u + grp, cg2 = 10 + 10 * grp + j2;
  const unsigned head = xcd * 4 + (slot & 3), qb = u;
  const unsigned odd = head & 1, o2 = 5 * (head >> 1) + 2, o1h = odd ? o2 + 1 : o2 - 2;
  const unsigned qt = qb * 8 + 2 * lw + grp;
  u32x4 soA; u32x16 so;
  {
    const unsigned kb0 = 80 * OCT + w * 512, vb0 = (unsigned)V_WS - V_OFF + xcd * (256 * 1280) + w * 5120;
    unsigned ko[10], vo_[10];
#pragma unroll
    for (int i = 0; i < 8; ++i) { const unsigned c = (((head >> 3) + (i >> 1)) & 3) + 4 * (i & 1); ko[i] = kb0 + c * 4096; vo_[i] = vb0 + c * 40960; }
    const unsigned c8 = qb ? qb - 1 : 0;
    ko[8] = kb0 + (unsigned)QK_BR + c8 * 4096; vo_[8] = vb0 + (unsigned)V_BR + c8 * 40960;
    ko[9] = kb0 + (unsigned)QK_BR + qb * 4096; vo_[9] = vb0 + (unsigned)V_BR + qb * 40960;
    soA = u32x4{ko[0], vo_[0], ko[1], vo_[1]};
    so = u32x16{ko[2], vo_[2], ko[8], vo_[8], ko[9], vo_[9], ko[3], vo_[3], ko[4], vo_[4], ko[5], vo_[5], ko[6], vo_[6], ko[7], vo_[7]};
    asm volatile("" : "+{s[76:79]}"(soA), "+{s[56:71]}"(so));
  }
  unsigned flag1 = FQ_OFF + ((br * 30 + cg1) * 64 + Tg) * 128, flag2 = FQ_OFF + ((br * 30 + cg2) * 64 + Tg) * 128;
  unsigned flagO = FO_OFF + (head * 64 + qt) * 128;
  PINS(flag1); PINS(flag2); PINS(flagO);
  glds16x4(sA + mod10(rot + 1) * str1, o1, m0v + 32768);
  const char* sB = ptr(ka[2], ka[3]) + (16 * lw * 3840 + 256 + j2 * 128);
  const unsigned mytile = w * TILE_B;
  const char* fsrc; unsigned fdst;
  {
    const unsigned F = __builtin_elementwise_min((head * 8 + qb) * 8 + w, 1599u);
    const unsigned bl = (F * 13108) >> 16, np = F - 5 * bl, t = (bl * 3277) >> 16, ps = bl - 20 * t;
    const unsigned ks = ((t & 1) || ps < 5 || ps >= 15) ? ps : (ps < 10 ? ps + 5 : ps - 5);
    fsrc = ptr((t & 1) ? kc[2] : kb[0], (t & 1) ? kc[3] : kb[1]) + (ks * (32 * 1280) + (t >> 1) * 160 + ((ks >= 10 && np < 4) ? np ^ 2 : np) * 32);
    fdst = (unsigned)WP_WS + ((head * 8 + qb) * 8 + w) * 1024;
  }
  u32x4 late = {FO_OFF + (grp * 16 * 64 + head * 2 + (lw >> 1)) * 128,
                (unsigned)O_WS + grp * 40 * OCT + (head * 64 + lw * 16) * 16,
                (unsigned)WP_WS + qb * 204800,
                (head * 64 + lw * 16) * 2560 + qb * 160};
  asm volatile("" : "+s"(fsrc), "+s"(fdst), "+s"(late));
  u32x16 qq, pk, at;
  {
    const unsigned r16 = lane & 15, g = lane >> 4, q = r16 >> 2, p = lane & 3, r32 = lane & 31, h = lane >> 5;
    const unsigned xa0 = (lw * 32 + r16) * 128 + ((g ^ (r16 >> 1)) * 16);
    const unsigned wa0 = 2048 * g + 1024 * grp + 128 * q + 16 * ((p >> 1) ^ (4 * (g & 1) + (q & 2))) + 8 * (p & 1);
    const unsigned jj = (20 * head + 19 * (lane & 1)) >> 6, b1 = (lane >> 1) & 1, c = (lane >> 2) & 7;
    const unsigned pol1 = FQ_OFF + (jj * 64 + ((lane & 8) ? (qt ^ b1) : b1 * 640 + (lane & 4) * 8 + (640 + (head >> 3) * 8 + w))) * 128;
    qq = u32x16{o1[0], o1[1], o1[2], o1[3], tw + 1024, tw + 1280, (tw ^ 64) + (8 * 3840 - 1024), (tw ^ 64) + (8 * 3840 - 2048 + 1280),
                pol1, xa0, xa0 ^ 64, xa0, wa0, wa0 ^ 32, wa0 ^ 64, wa0 ^ 96};
    const unsigned cB = __builtin_elementwise_max((c & 1) + qb, 1u) + (30 * 8 - 1);
    const unsigned pol2 = (jj * 64 + (lane >= 48 ? 30 * 64 + qt : b1 * 640 + (h ? cB : c) * 8 + (640 + w))) * 128 + FQ_OFF;
    const u32x2 s1 = g1_offs(br, cg1, Tg, lane), s2 = g1_offs(br, cg2, Tg, lane);
    const unsigned ab = grp * TILE_B;
    pk = u32x16{s1[0], s1[1], s2[0], s2[1],
                lane * 16 + ab,
                h ? (V_OFF + 320 + ab) : r32 * 16 + (K2_OFF + odd * 8 + ab),
                h * 512 + (r32 < 28 ? __builtin_elementwise_min(r32, 21u) : 28) * 16 + (V_OFF + ab),
                h * OCT + r32 * 16 + o1h * OCT,
                r32 * 16 + (o2 * OCT - 1024),
                h * 1280 + (__builtin_elementwise_min(r32 + 4 * odd, r32 + 4 * odd - 20) + (head & 3) * 20) * 16,
                pol2,
                g * 512 + __builtin_elementwise_min(r16, 11u) * 16 + (mytile + V_OFF + 320),
                r16 == 0 ? 0x3F803F80u : 0u,
                r32 * 16 + (o2 * OCT + odd * 8),
                lane * 512 + (3 * CHUNK_B + V_OFF + 320 + mytile),
                lane};
  }
  asm volatile("" : "+{v[64:79]}"(qq), "+{v[80:95]}"(pk) :: "memory");
  {
    const unsigned oo[4] = {qq[0], qq[1], qq[2], qq[3]};
    glds16x4(sA + mod10(rot + 2) * str1, oo, m0v + 65536);
  }
  asm volatile("s_waitcnt vmcnt(8) lgkmcnt(0)\n\ts_barrier" ::: "memory");
  PINS(ep); PINS(tch[0]); PINS(tch[1]); PINS(tch[2]); PINS(tch[3]);
  const unsigned long long tag = (((unsigned long long)ep[1] << 32) | ep[0]) + 1;
  STAMP(1);
  u32x4 cv4;
  STV_PUT(pk[15]);
  g1_all(vt1, kv1, vt2, qq, pk, at, cv4, sA, mod10(rot + 3) * str1, str1, sB, mod10(rot + grp) * (64 * 3840), m0v, tag, flag1, ws, mytile, soA,
         qt * 512);
  STV_GET(pk[15]);
  STAMP(8);
  if (bid == 255 && grp == 0) {
    const unsigned e = w * 64 + lane_id(), eu = e & 7, em = (e >> 3) & 15;
    poll_flags(ws, FQ_OFF + (((e >> 7) * 30 + (eu < 5 ? 2 * eu : 5 + eu + ((5 * ((em >> 1) & 3)) >> 1))) * 64 + 4 * em) * 128, tag);
  }
  BARRIER();
  STAMP(9);
  if (bid == 255 && w == 0) put_flag(ws, CTL_WS, tag);
  typedef __attribute__((address_space(3))) u32x4* lds_u4;
  *(lds_u4)((lds_ptr)lds + pk[11] + 2 * CHUNK_B) = cv4; *(lds_u4)((lds_ptr)lds + pk[11] + 3 * CHUNK_B) = cv4; *(lds_u4)((lds_ptr)lds + pk[11] + 4 * CHUNK_B) = cv4;
  if (qb == 0) {
    lds_ptr z = (lds_ptr)lds + 3 * CHUNK_B + mytile + qq[15];
    *(lds_f4)(z) = f32x4{}; *(lds_f4)(z + 1024) = f32x4{}; *(lds_f4)(z + 2048) = f32x4{}; *(lds_f4)(z + 2560) = f32x4{};
  }
  const f32x16 zero16 = {};
  auto exact = [&](int b, bf16x8 q1, s16x4 q2, int t0, int t1) {
    __builtin_amdgcn_fence(__ATOMIC_ACQUIRE, "agent");
    const int ln = lane_id(), r32 = ln & 31, h = ln >> 5;
    unsigned head = xcd * 4 + (slot & 3);
    PINS(head);
    const unsigned odd = head & 1, o2 = 5 * (head >> 1) + 2, o1h = odd ? o2 + 1 : o2 - 2;
    if (h) q2 = s16x4{0, 0, 0, 0};
    typedef __attribute__((address_space(1))) const char* gptr;
    gptr kg1 = (gptr)ws + QK_WS + b * QK_BR + (size_t)(80 + o1h + h) * OCT + r32 * 16;
    gptr kg2 = (gptr)ws + QK_WS + b * QK_BR + (size_t)(80 + o2) * OCT + r32 * 16 + odd * 8;
    gptr vg = (gptr)ws + V_WS + b * V_BR + (head >> 2) * (256 * 1280) + h * 1280 +
                     ((head & 3) * 20 + (odd ? (r32 < 16 ? r32 + 4 : r32 - 16) : r32)) * 16;
    const u32x4 vc = r32 == 20 ? u32x4{0x3F803F80u, 0x3F803F80u, 0x3F803F80u, 0x3F803F80u} : u32x4{0u, 0u, 0u, 0u};
    float m = -INFINITY;
    f32x16 r = zero16;
#pragma unroll 1
    for (int ps = 0; ps < 2; ++ps) {
#pragma unroll 1
      for (int t = t0; t <= t1; ++t) {
        f32x16 s = MFMA32(*(__attribute__((address_space(1))) const bf16x8*)(kg1 + t * 512), q1, zero16);
        s = MFMA32K8(*(__attribute__((address_space(1))) const s16x4*)(kg2 + t * 512), q2, s);
        if (b) {
          const int d = (t1 - t) * 32 + r32 - 4 * h;
#pragma unroll
          for (int e = 0; e < 16; ++e) s[e] = (unsigned)(d - ((e & 3) + 8 * (e >> 2))) < 256u ? s[e] : -INFINITY;
        }
        if (ps == 0) { m = max16(s, m); continue; }
#pragma unroll
        for (int e = 0; e < 16; ++e) s[e] = __builtin_amdgcn_exp2f(s[e] - m);
        u32x4 va = vc, vb = vc;
        if (r32 < 20) { va = *(__attribute__((address_space(1))) const u32x4*)(vg + t * 5120); vb = *(__attribute__((address_space(1))) const u32x4*)(vg + t * 5120 + 2560); }
        r = smfmac(__builtin_bit_cast(bf16x8, va), lo8(s), r);
        r = smfmac(__builtin_bit_cast(bf16x8, vb), hi8(s), r);
      }
      m = xhalf_max(m);
    }
    return r;
  };
  auto unsafe = [&](const f32x16& r, float l) {
    float amax = 0.f;
#pragma unroll
    for (int e = 0; e < 12; ++e) amax = __builtin_fmaxf(amax, __builtin_fabsf(r[e]));
    return __any(!(l <= 1.2980742e33f) || !(l >= 8.4703295e-22f) || !(amax <= 3.0e38f));
  };
  char* const oA = ws + (O_WS + qt * 512);
  f32x32 oab, of; f32x8 of2;
  u32x4 lt;
  {
    f32x16 s0, pp;
    f32x32 fr;
    asm volatile(AT_ASM_MACROS
        "v_subrev_u32 v108, %[ab], v129\n\tds_read_b128 v[112:115], v79\n\tds_read_b64 v[116:117], v108\n\t"
        "ds_read_b128 v[80:83], v128\n\tds_read_b64 v[84:85], v129\n\tds_read_b128 v[94:97], v128 offset:7168\n\tds_read_b64 v[98:99], v129 offset:7168\n\t"
        "ds_read_b128 v[86:89], v130\n\tds_read_b128 v[100:103], v130 offset:7168\n\tds_read_b128 v[90:93], v130 offset:1024\n\t"
        "s_mov_b32 exec_lo, 0\n\tv_mov_b64 v[72:73], 0\n\tv_mov_b64 v[74:75], 0\n\ts_mov_b32 exec_lo, -1\n\t"
        "s_waitcnt lgkmcnt(7)\n\t"
        "qk1 32, 112, 64\n\tqk2 32, 116, 72\n\tqk1 48, 112, 68\n\tqk2 48, 116, 74\n\t"
        ".set i_, 0\n\t.rept 10\n\tv_mov_b64 v[i_:i_+1], 0\n\t.set i_, i_ + 2\n\t.endr\n\t"
        "mx 108, 32\n\tv_mov_b32 v109, v108\n\tmx 110, 48\n\tv_mov_b32 v111, v110\n\t"
        "v_permlane32_swap_b32 v108, v109\n\ts_waitcnt lgkmcnt(0)\n\tqk1 32, 80, 64\n\tv_permlane32_swap_b32 v110, v111\n\t"
        "v_max_f32 v108, v108, v109\n\tv_max_f32 v110, v110, v111\n\tv_add_f32 v108, 0x42600000, v108\n\tv_add_f32 v110, 0x42600000, v110\n\t"
        "s_mov_b32 exec_lo, 0\n\tv_cvt_pk_bf16_f32 v72, -v108, 0\n\tv_cvt_pk_bf16_f32 v74, -v110, 0\n\ts_mov_b32 exec_lo, -1\n\ts_nop 0\n\t"
        "qk2 32, 84, 72\n\t"
        ".set i_, 20\n\t.rept 6\n\tv_mov_b64 v[i_:i_+1], 0\n\t.set i_, i_ + 2\n\t.endr\n\t"
        "v_mov_b64 v[104:105], 0\n\tv_mov_b64 v[106:107], 0\n\t"
        "v_mov_b64 v[120:121], 0\n\tv_mov_b64 v[122:123], 0\n\tv_mov_b64 v[124:125], 0\n\tv_mov_b64 v[126:127], 0\n\t"
        AT_ASM_PURGE
        : "=&{v[0:31]}"(oab), "=&{v[112:127]}"(pp), "=&{v[32:47]}"(s0), "=&{v[80:111]}"(fr), "+{v[64:79]}"(qq), "+{v[128:143]}"(at)
        : [ab] "s"(grp * TILE_B)
        : "memory", "v48", "v49", "v50", "v51", "v52", "v53", "v54", "v55", "v56", "v57", "v58", "v59", "v60", "v61", "v62", "v63");
    STAMP(10);
    f32x16 maskF, maskD; u32x8 qv;
    at_a1(oab, s0, pp, fr, qq, at, ws, so, mytile, mytile + (qb ? 3 : 4) * CHUNK_B, tag, flag2, (0x7fu >> u) & 1u, ws + (QK_BR + qt * 512),
          &lane_tab, maskF, maskD, qv, lt, fsrc, 143360 + w * 1024 AT_STV_PASS);
    STAMP(11);
    {
      f32x16 ob;
      at_b(ob, maskF, maskD, qv, 3 * CHUNK_B + 2 * lw * TILE_B, qt, oA, qq, at, oab, s0, fr, pp, 143360 + w * 1024, fdst, ws);
      lt[0] += late[0]; lt[1] += late[1]; lt[2] += grp * 64;
      float l = row_sum(ob);
      if (__builtin_expect(unsafe(ob, l), 0)) {
        ob = exact(1, __builtin_bit_cast(bf16x8, u32x4{qv[0], qv[1], qv[2], qv[3]}), __builtin_bit_cast(s16x4, u32x2{qv[4], qv[5]}),
                   qb ? qt - 8 : 0, qt);
        l = row_sum(ob);
      }
      store_o<false>(ob, l, oA + O_BR, at[9], qq[14]);
    }
    STAMP(13);
    at[13] = __builtin_elementwise_min(qq[15], 24u * 16) * 64 + (FO_OFF + qb * 25 * 1024);
    at_a2(oab, s0, pp, fr, qq, at, ws, so, mytile, tag, flagO, lt, ws + (late[2] + 102400), w * 5120,
          81920 + __builtin_elementwise_min(3 * w, 17u) * 1024, late[2] + ((slot & 3) * 8 + w) * 3200, of, of2);
  }
  STAMP(12);
  {
    lds_ptr xb = (lds_ptr)lds + 3 * CHUNK_B + qq[15];
#pragma unroll
    for (int i = 0; i < 3; ++i) *(lds_f4)(xb + w * 3072 + i * 1024) = f32x4{oab[16 + 4 * i], oab[17 + 4 * i], oab[18 + 4 * i], oab[19 + 4 * i]};
    asm volatile("s_setprio 3\n\ts_waitcnt vmcnt(0) lgkmcnt(0)\n\ts_barrier" : "+{v[184:215]}"(of), "+{v[216:223]}"(of2) :: "memory");
    f32x16 o = {};
#pragma unroll
    for (int i = 0; i < 3; ++i) {
      const f32x4 r = *(lds_f4)(xb + (w ^ 4) * 3072 + i * 1024);
#pragma unroll
      for (int e = 0; e < 4; ++e) o[4 * i + e] = oab[4 * i + e] + r[e];
    }
    float l = row_sum(o);
    if (__builtin_expect(unsafe(o, l), 0)) {
      o = exact(0, __builtin_bit_cast(bf16x8, u32x4{qq[0], qq[1], qq[2], qq[3]}), __builtin_bit_cast(s16x4, u32x2{qq[8], qq[9]}), 0, 63);
      l = row_sum(o);
    }
    store_o<true>(o, l, oA, at[9], qq[14]);
  }
  STAMP(14);
  {
    const unsigned pos = __builtin_elementwise_min(7 * w, 43u) * 1024;
    proj_all(of, of2, qq[15], lt, ws + (late[2] + pos), pos, w * 3072, grp, ws, ptr(kc[6], kc[7]) + late[3],
             0xffffffffull << (32 * grp) AT_STV_PASS);
  }
  STAMP_OUT(ws, 0);
}

extern "C" void kernel_launch(void* const* d_in, const int* in_sizes, int n_in, void* d_out, int out_size, void* d_ws,
                              size_t ws_size, hipStream_t stream) {
  k_fused<<<256, 512, 0, stream>>>((const char*)d_in[0], (const char*)d_in[1], (const char*)d_in[2], (const char*)d_in[3],
                                   (const char*)d_in[4], (const char*)d_in[5], (char*)d_ws, (char*)d_out);
}
```

```cpp
#include <hip/hip_runtime.h>
#include <hip/hip_bf16.h>
#include <stdint.h>
#include <type_traits>

typedef __attribute__((ext_vector_type(8))) __bf16 bf16x8;
typedef __attribute__((ext_vector_type(16))) __bf16 bf16x16;
typedef __attribute__((ext_vector_type(4))) short s16x4;
typedef __attribute__((ext_vector_type(8))) short s16x8;
typedef __attribute__((ext_vector_type(2))) unsigned u32x2;
typedef __attribute__((ext_vector_type(4))) unsigned u32x4;
typedef __attribute__((ext_vector_type(4))) float f32x4;
typedef __attribute__((ext_vector_type(8))) float f32x8;
typedef __attribute__((ext_vector_type(16))) float f32x16;
typedef __attribute__((ext_vector_type(32))) float f32x32;
typedef __attribute__((address_space(3))) const char* lds_cptr;
typedef __attribute__((address_space(3))) char* lds_ptr;

#define OCT 33280
#define STR_(x) #x
#define STR(x) STR_(x)
constexpr size_t QK_WS = 0, QK_BR = (size_t)160 * OCT;
constexpr size_t V_WS = 2 * QK_BR, V_BR = (size_t)8 * 256 * 1280;
constexpr size_t O_WS = V_WS + 2 * V_BR, O_BR = (size_t)80 * OCT;
constexpr size_t WP_WS = O_WS + 2 * O_BR;
constexpr size_t CTL_WS = WP_WS + 2048 * 1024;
constexpr unsigned FQ_OFF = CTL_WS + 4096, FO_OFF = FQ_OFF + 2 * 30 * 64 * 128;
constexpr float QSCALE = 0.22360679774997896f * 1.4426950408889634f;

#define STAMP_DECL
#define STV_PUT(x)
#define STV_GET(x)
#define STAMP(i) do {} while (0)
#define STAMP_OUT(ws_, kid) do {} while (0)
__device__ __forceinline__ int lane_id() {
  int l; asm volatile("v_mbcnt_lo_u32_b32 %0, -1, 0\n\tv_mbcnt_hi_u32_b32 %0, -1, %0" : "=v"(l)); return l;
}
#define SBAR() __builtin_amdgcn_sched_barrier(0)
#define PIN(x) asm volatile("" : "+v"(x))
#define PINS(x) asm volatile("" : "+s"(x))
#define WAITV(N) asm volatile("s_waitcnt vmcnt(" #N ")" ::: "memory")
#define BARRIER() asm volatile("s_barrier" ::: "memory")
#define WAITV_BAR(N) asm volatile("s_waitcnt vmcnt(" #N ") lgkmcnt(0)\n\ts_barrier" ::: "memory")

__device__ __forceinline__ void glds16(const void* sbase, unsigned voff, unsigned lds) {
  unsigned keep;
  asm volatile("s_mov_b32 %0, m0\n\ts_mov_b32 m0, %3\n\ts_nop 0\n\tglobal_load_lds_dwordx4 %1, %2\n\ts_mov_b32 m0, %0"
               : "=&s"(keep) : "v"(voff), "s"(sbase), "s"(lds) : "memory");
}
__device__ __forceinline__ void glds16x4(const void* sbase, const unsigned (&voff)[4], unsigned lds) {
  unsigned keep;
  asm volatile("s_mov_b32 %0, m0\n\ts_mov_b32 m0, %6\n\ts_nop 0\n\tglobal_load_lds_dwordx4 %1, %5\n\t"
               "global_load_lds_dwordx4 %2, %5 offset:1024\n\tglobal_load_lds_dwordx4 %3, %5 offset:2048\n\t"
               "global_load_lds_dwordx4 %4, %5 offset:3072\n\ts_mov_b32 m0, %0"
               : "=&s"(keep) : "v"(voff[0]), "v"(voff[1]), "v"(voff[2]), "v"(voff[3]), "s"(sbase), "s"(lds) : "memory");
}
__device__ __forceinline__ unsigned pk_bf16(float lo, float hi) {
  typedef __attribute__((ext_vector_type(2))) float f32x2_;
  typedef __attribute__((ext_vector_type(2))) __bf16 bf16x2_;
  const f32x2_ v = {lo, hi};
  return __builtin_bit_cast(unsigned, __builtin_convertvector(v, bf16x2_));
}
__device__ __forceinline__ void st16(char* sbase, unsigned voff, u32x4 v) {
  asm volatile("global_store_dwordx4 %0, %1, %2 sc1\n\ts_nop 1" :: "v"(voff), "v"(v), "s"(sbase) : "memory");
}
__device__ __forceinline__ void st8lo(char* sbase, unsigned voff, u32x2 v) {
  asm volatile("s_mov_b32 exec_hi, 0\n\tglobal_store_dwordx2 %0, %1, %2 sc1\n\ts_mov_b32 exec_hi, -1" :: "v"(voff), "v"(v), "s"(sbase) : "memory");
}
__device__ __forceinline__ bf16x8 lds_b128(lds_cptr p) { return *(const __attribute__((address_space(3))) bf16x8*)p; }
__device__ __forceinline__ s16x4 lds_b64(lds_cptr p) { return *(const __attribute__((address_space(3))) s16x4*)p; }
__device__ __forceinline__ s16x4 lds_tr(lds_cptr p) {
  return __builtin_amdgcn_ds_read_tr16_b64_v4i16((__attribute__((address_space(3))) s16x4*)p);
}
__device__ __forceinline__ bf16x8 cat8(s16x4 a, s16x4 b) {
  s16x8 v = {a[0], a[1], a[2], a[3], b[0], b[1], b[2], b[3]};
  return __builtin_bit_cast(bf16x8, v);
}
#define MFMA32(a, b, c) __builtin_amdgcn_mfma_f32_32x32x16_bf16(a, b, c, 0, 0, 0)
#define MFMA32K8(a, b, c) __builtin_amdgcn_mfma_f32_32x32x8bf16_1k(a, b, c, 0, 0, 0)
#define MFMA16(a, b, c) __builtin_amdgcn_mfma_f32_16x16x32_bf16(a, b, c, 0, 0, 0)

typedef __attribute__((ext_vector_type(8))) unsigned u32x8;
typedef __attribute__((ext_vector_type(16))) unsigned u32x16;
#define AWAIT(d, d1, off) \
    "s_mov_b32 s48, 0x200000\n\t5:\n\ts_waitcnt vmcnt(0)\n\tv_cmp_ne_u64 vcc, s[50:51], v[" #d ":" #d1 "]\n\ts_sub_u32 s48, s48, 1\n\t" \
    "s_cbranch_vccz 4f\n\ts_cbranch_scc1 4f\n\ts_sleep 2\n\tglobal_load_dwordx2 v[" #d ":" #d1 "], v" #off ", s[54:55] sc1\n\ts_branch 5b\n\t4:\n\t"
#define G1_STAMP(i)
#define L1POS 1
#define NPOLL 22
#define NFLAG 6
#define NAW 28
#define KBAR 11
__device__ __forceinline__ void g1_all(unsigned vt1, unsigned kv1, unsigned vt2, u32x16& ad, u32x16& pk, u32x16& at, u32x4& cv4,
                                       const char* sA, unsigned off1, unsigned str1, const char* sB, unsigned off2, unsigned m0v,
                                       unsigned long long tag, unsigned flag, char* ws, unsigned tile, u32x4 so, unsigned qoff) {
  unsigned keep;
  f32x32 d0, d1, d2, d3, d4; f32x16 d5, d6; f32x8 d7; f32x4 d8;
  asm volatile(
      ".macro pack vt\n\t.set i_, 0\n\t.rept 4\n\t"
      ".if \\vt\n\t.set e_, 8 * i_\n\t.set f_, 8 * i_ + 4\n\t.else\n\t.set e_, 4 * i_\n\t.set f_, 4 * i_ + 16\n\t.endif\n\t"
      "v_cvt_pk_bf16_f32 v[48+4*i_], v[e_], v[e_+1]\n\tv_cvt_pk_bf16_f32 v[49+4*i_], v[e_+2], v[e_+3]\n\t"
      "v_cvt_pk_bf16_f32 v[50+4*i_], v[f_], v[f_+1]\n\tv_cvt_pk_bf16_f32 v[51+4*i_], v[f_+2], v[f_+3]\n\t"
      ".set i_, i_ + 1\n\t.endr\n\t.set i_, 0\n\t.rept 4\n\t"
      "v_permlane16_swap_b32 v[48+4*i_], v[50+4*i_]\n\tv_permlane16_swap_b32 v[49+4*i_], v[51+4*i_]\n\t"
      ".set i_, i_ + 1\n\t.endr\n\t.endm\n\t"
      ".macro mm vt, c, x, w\n\t"
      ".if (\\x) < 104\n\t"
      ".if \\vt\n\tv_mfma_f32_16x16x32_bf16 v[\\c:\\c+3], v[\\x:\\x+3], v[\\w:\\w+3], 0\n\t"
      ".else\n\tv_mfma_f32_16x16x32_bf16 v[\\c:\\c+3], v[\\w:\\w+3], v[\\x:\\x+3], 0\n\t.endif\n\t"
      ".elseif \\vt\n\tv_mfma_f32_16x16x32_bf16 v[\\c:\\c+3], v[\\x:\\x+3], v[\\w:\\w+3], v[\\c:\\c+3]\n\t"
      ".else\n\tv_mfma_f32_16x16x32_bf16 v[\\c:\\c+3], v[\\w:\\w+3], v[\\x:\\x+3], v[\\c:\\c+3]\n\t.endif\n\t"
      ".endm\n\t"
      ".macro rx d, a, off\n\tds_read_b128 v[\\d:\\d+3], v\\a offset:\\off\n\tds_read_b128 v[\\d+4:\\d+7], v\\a offset:\\off+2048\n\t.endm\n\t"
      ".macro rw d, a, off\n\tds_read_b64_tr_b16 v[\\d:\\d+1], v\\a offset:\\off\n\tds_read_b64_tr_b16 v[\\d+2:\\d+3], v\\a offset:\\off+512\n\t.endm\n\t"
      ".macro bump lo, n, val\n\t.set bi_, \\lo\n\t.rept \\n\n\tv_add_u32 v[bi_], \\val, v[bi_]\n\t.set bi_, bi_+1\n\t.endr\n\t.endm\n\t"
      ".macro src2\n\ts_add_u32 s40, s44, s47\n\ts_addc_u32 s41, s45, 0\n\t"
      "s_add_u32 s47, s47, 2*64*3840\n\ts_cmp_ge_u32 s47, 640*3840\n\ts_cselect_b32 s48, 640*3840, 0\n\ts_sub_u32 s47, s47, s48\n\t.endm\n\t"
      ".macro p1body vt\n\t"
      ".set st_, 0\n\t"
      ".rept 10\n\t"
      ".set xr_, 96 + 16 * st_\n\t"
      ".set o_, ((st_ %% 5) & 1) * 32768\n\t"
      ".set n_, (((st_ + 1) %% 5) & 1) * 32768 + 16384\n\t.if st_ == 9\n\t.set n_, 0\n\t.endif\n\t"
      "s_waitcnt lgkmcnt(6)\n\t"
      "mm \\vt, 0, xr_, 32\n\trw 48, 76, o_+16384+8192\n\t"
      "mm \\vt, 16, xr_+4, 32\n\trx xr_+8, 74, o_\n\ts_waitcnt lgkmcnt(8)\n\t"
      "mm \\vt, 4, xr_, 36\n\trw 52, 77, o_+16384+8192\n\t"
      "mm \\vt, 20, xr_+4, 36\n\trw 56, 78, o_+16384+8192\n\ts_waitcnt lgkmcnt(10)\n\t"
      "mm \\vt, 8, xr_, 40\n\trw 60, 79, o_+16384+8192\n\t"
      "mm \\vt, 24, xr_+4, 40\n\ts_waitcnt lgkmcnt(10)\n\t"
      "mm \\vt, 12, xr_, 44\n\t"
      ".if st_ < 7\n\t"
      "s_add_u32 s40, s42, s36\n\ts_addc_u32 s41, s43, 0\n\ts_add_u32 s36, s36, s37\n\ts_cmp_eq_u32 s36, s38\n\ts_cselect_b32 s36, 0, s36\n\t"
      ".else\n\tsrc2\n\t.endif\n\t"
      "mm \\vt, 28, xr_+4, 44\n\t"
      "s_add_u32 m0, s46, ((st_ + 3) %% 5) * 32768\n\t"
      ".if (st_ %% 5) == 1 || (st_ %% 5) == 3\n\tbump 76, 4, 0x10000\n\tbump 73, 2, 0x10000\n\t.endif\n\t"
      ".if (st_ %% 5) == 4\n\tbump 76, 4, 0xfffe0000\n\t.if st_ < 9\n\tbump 73, 2, 0xfffe0000\n\t.endif\n\t.endif\n\t"
      "s_waitcnt lgkmcnt(6)\n\t"
      "mm \\vt, 0, xr_+8, 48\n\t"
      "mm \\vt, 16, xr_+12, 48\n\t"
      ".if st_ < 9\n\ts_waitcnt vmcnt(4) lgkmcnt(0)\n\t.else\n\ts_cmp_eq_u32 %[vt2], 2\n\ts_cbranch_scc1 8f\n\ts_waitcnt vmcnt(4)\n\t8:\n\ts_waitcnt lgkmcnt(0)\n\t.endif\n\ts_barrier\n\t"
      ".set vo_, 64 + 4 * ((st_ + 3) / 10)\n\t"
      "mm \\vt, 4, xr_+8, 52\n\trw 32, 76, n_\n\t"
      ".if st_ == 9\n\ts_cbranch_scc1 8f\n\t.endif\n\tglobal_load_lds_dwordx4 v[vo_], s[40:41]\n\t8:\n\t"
      "mm \\vt, 20, xr_+12, 52\n\t"
      ".if st_ < 9\n\trx xr_+16, 73, n_-16384\n\t.endif\n\t"
      ".if st_ == 9\n\ts_cbranch_scc1 8f\n\t.endif\n\tglobal_load_lds_dwordx4 v[vo_+1], s[40:41] offset:1024\n\t8:\n\t"
      "mm \\vt, 8, xr_+8, 56\n\trw 36, 77, n_\n\t"
      ".if st_ == 9\n\ts_cbranch_scc1 8f\n\t.endif\n\tglobal_load_lds_dwordx4 v[vo_+2], s[40:41] offset:2048\n\t8:\n\t"
      "mm \\vt, 24, xr_+12, 56\n\trw 40, 78, n_\n\t"
      ".if st_ == 9\n\ts_cbranch_scc1 8f\n\t.endif\n\tglobal_load_lds_dwordx4 v[vo_+3], s[40:41] offset:3072\n\t8:\n\t"
      "mm \\vt, 12, xr_+8, 60\n\trw 44, 79, n_\n\t"
      "mm \\vt, 28, xr_+12, 60\n\t"
      ".set st_, st_ + 1\n\t"
      ".endr\n\t"
      "v_mov_b64 v[64:65], v[80:81]\n\tv_add_u32 v66, " STR(OCT) "*4 - (" STR(OCT) "*4 - 2560) * \\vt, v80\n\tv_add_u32 v67, " STR(OCT) "*4 - (" STR(OCT) "*4 - 2560) * \\vt, v81\n\t"
      ".endm\n\t"
      ".macro tdma i\n\t"
      "s_add_u32 m0, s72, \\i * 28672\n\ts_add_u32 s40, s54, s[76+2*\\i]\n\ts_addc_u32 s41, s55, 0\n\t"
      "global_load_lds_dwordx4 v137, s[40:41] sc1\n\ts_mov_b32 exec_hi, 0\n\tglobal_load_lds_dwordx4 v138, s[40:41] offset:1024 sc1\n\t"
      "s_add_u32 s40, s54, s[77+2*\\i]\n\ts_addc_u32 s41, s55, 0\n\ts_mov_b32 exec_lo, 0xfffff\n\ts_mov_b32 exec_hi, 0xfffff\n\t"
      "global_load_lds_dwordx4 v139, s[40:41] offset:1536 sc1\n\tglobal_load_lds_dwordx4 v140, s[40:41] offset:2560 sc1\n\ts_mov_b64 exec, -1\n\t"
      ".endm\n\t"
      ".macro slot n\n\t.set n_, \\n\n\t"
      ".if n_ >= 2 && n_ < 10\n\t"
      ".if ((n_ - 2) & 3) == 0\n\ts_add_u32 m0, s46, (3 + (n_ - 2) / 4) * 32768\n\tsrc2\n\t.endif\n\t"
      "global_load_lds_dwordx4 v[68+((n_-2)&3)], s[40:41] offset:1024*((n_-2)&3)\n\t"
      ".endif\n\t"
      ".if n_ == %c[nflag]\n\tv_mov_b32 v96, s50\n\tv_mov_b32 v97, s51\n\tv_mov_b32 v98, s52\n\ts_mov_b64 exec, 1\n\ts_waitcnt vmcnt(%c[nflag] - 1)\n\t"
      "global_store_dwordx2 v98, v[96:97], s[54:55] sc1\n\ts_mov_b64 exec, -1\n\t.endif\n\t"
      ".if n_ == 12\n\tv_mov_b64 v[128:129], v[84:85]\n\tv_mov_b32 v130, v86\n\tv_add_u32 v131, 57344, v84\n\tv_add_u32 v132, 57344, v85\n\t.endif\n\t"
      ".if n_ == 13\n\tv_add_u32 v133, 57344, v86\n\tv_add_u32 v134, 114688, v84\n\tv_add_u32 v135, 114688, v85\n\tv_add_u32 v136, 114688, v86\n\t.endif\n\t"
      ".if n_ == 14\n\tv_mov_b32 v137, v87\n\tv_mov_b64 v[138:139], v[88:89]\n\tv_add_u32 v140, 1536, v89\n\tv_mov_b32 v141, v90\n\t.endif\n\t"
      ".if n_ == 15\n\tv_mov_b32 v104, v92\n\tv_mov_b32 v105, v92\n\tv_mov_b32 v106, v92\n\tv_mov_b32 v107, v92\n\t.endif\n\t"
      ".if n_ == %c[naw] + 3\n\tds_write_b128 v91, v[104:107]\n\tds_write_b128 v91, v[104:107] offset:28672\n\t.endif\n\t"
      ".if n_ == %c[npoll]\n\tglobal_load_dwordx2 v[100:101], v72, s[54:55] sc1\n\t.endif\n\t"
      ".if n_ == %c[naw]\n\t" AWAIT(100, 101, 72) G1_STAMP(6) "tdma 0\n\t.endif\n\t"
      ".if n_ == %c[naw] + 1\n\ts_add_u32 s40, s54, s80\n\ts_addc_u32 s41, s55, 0\n\ts_xor_b32 s48, s80, 512\n\t"
      "global_load_dwordx4 v[64:67], v137, s[40:41] sc1\n\tglobal_load_dwordx2 v[72:73], v93, s[40:41] sc1\n\t"
      "s_add_u32 s40, s54, s48\n\ts_addc_u32 s41, s55, 0\n\t"
      "global_load_dwordx4 v[68:71], v137, s[40:41] sc1\n\tglobal_load_dwordx2 v[74:75], v93, s[40:41] sc1\n\t.endif\n\t"
      ".if n_ == %c[naw] + 2\n\ttdma 1\n\t.endif\n\t"
      ".endm\n\t"
      ".macro st1 i\n\t.if k_ == 0\n\tglobal_store_dwordx4 v[64+\\i], v[48+4*\\i:51+4*\\i], s[54:55] sc1\n\t.endif\n\t.endm\n\t"
      ".macro p2body vt\n\t"
      ".set k_, 0\n\t"
      ".rept 20\n\t"
      ".set xr_, 96 + 8 * k_\n\t"
      ".set c_, 32 + 16 * (k_ & 1)\n\t"
      ".set d_, 48 - 16 * (k_ & 1)\n\t"
      ".set o_, ((k_ + 1) & 7) * 8192\n\t"
      ".if k_ == 3\n\ts_waitcnt vmcnt(8)\n\ts_barrier\n\t.endif\n\t"
      ".if k_ == %c[kbar]\n\ts_waitcnt vmcnt(0)\n\ts_barrier\n\t.endif\n\t"
      ".if k_ < 19\n\t"
      ".if ((k_ + 1) & 7) == 0\n\tbump 76, 4, 0x10000\n\t.endif\n\t"
      "s_waitcnt lgkmcnt(6)\n\t"
      "mm \\vt, 0, xr_, c_\n\tst1 0\n\trw d_, 76, o_\n\t"
      "mm \\vt, 16, xr_+4, c_\n\tslot 2*k_\n\ts_waitcnt lgkmcnt(6)\n\t"
      "mm \\vt, 4, xr_, c_+4\n\tst1 1\n\trw d_+4, 77, o_\n\t"
      "mm \\vt, 20, xr_+4, c_+4\n\ts_waitcnt lgkmcnt(6)\n\t"
      "mm \\vt, 8, xr_, c_+8\n\tst1 2\n\trw d_+8, 78, o_\n\t"
      "mm \\vt, 24, xr_+4, c_+8\n\tslot 2*k_+1\n\ts_waitcnt lgkmcnt(6)\n\t"
      "mm \\vt, 12, xr_, c_+12\n\tst1 3\n\trw d_+12, 79, o_\n\t"
      "mm \\vt, 28, xr_+4, c_+12\n\t"
      ".else\n\t"
      "s_waitcnt lgkmcnt(0)\n\t"
      "mm \\vt, 0, xr_, c_\n\tmm \\vt, 16, xr_+4, c_\n\tmm \\vt, 4, xr_, c_+4\n\tmm \\vt, 20, xr_+4, c_+4\n\t"
      "mm \\vt, 8, xr_, c_+8\n\tmm \\vt, 24, xr_+4, c_+8\n\tmm \\vt, 12, xr_, c_+12\n\tmm \\vt, 28, xr_+4, c_+12\n\t"
      "s_setprio 3\n\tv_add_u32 v80, " STR(OCT) "*4 - (" STR(OCT) "*4 - 2560) * \\vt, v82\n\tv_add_u32 v81, " STR(OCT) "*4 - (" STR(OCT) "*4 - 2560) * \\vt, v83\n\ts_nop 4\n\tpack \\vt\n\t"
      ".endif\n\t"
      ".set k_, k_ + 1\n\t"
      ".endr\n\t"
      ".endm\n\t"
      "rw 32, 76, 16384\n\trx 96, 73, 0\n\trw 36, 77, 16384\n\trw 40, 78, 16384\n\trw 44, 79, 16384\n\t"
      "s_mov_b32 %[keep], m0\n\t"
      "s_cmp_lg_u32 %[vt1], 0\n\ts_cbranch_scc1 1f\n\t"
      "p1body 0\n\t"
      "s_mov_b32 s40, %[sc]\n\ts_cmp_lg_u32 %[kv1], 0\n\ts_nop 2\n\ts_cbranch_scc1 3f\n\t"
      ".set i_, 0\n\t.rept 16\n\tv_pk_mul_f32 v[i_:i_+1], v[i_:i_+1], s[40:41] op_sel_hi:[1,0]\n\t.set i_, i_ + 2\n\t.endr\n\t"
      "3:\n\tpack 0\n\ts_branch 2f\n\t"
      "1:\n\tp1body 1\n\ts_nop 4\n\tpack 1\n\t"
      "2:\n\t" G1_STAMP(3)
      "s_cmp_eq_u32 %[vt2], 1\n\ts_cbranch_scc1 1f\n\ts_cmp_eq_u32 %[vt2], 2\n\ts_cbranch_scc0 6f\n\t"
      ".set k_, 0\n\tst1 0\n\tst1 1\n\tst1 2\n\tst1 3\n\tslot 12\n\tslot 13\n\tslot 14\n\tslot 15\n\t"
      "v_mov_b32 v96, s50\n\tv_mov_b32 v97, s51\n\tv_mov_b32 v98, s52\n\t"
      "s_waitcnt vmcnt(0) lgkmcnt(0)\n\ts_mov_b64 exec, 1\n\tglobal_store_dwordx2 v98, v[96:97], s[54:55] sc1\n\ts_mov_b64 exec, -1\n\ts_barrier\n\t"
      "slot %c[npoll]\n\t"
      "slot %c[naw]\n\tslot %c[naw]+1\n\tslot %c[naw]+2\n\tslot %c[naw]+3\n\t"
      "s_waitcnt vmcnt(0)\n\ts_setprio 3\n\ts_branch 3f\n\t"
      "6:\n\tp2body 0\n\ts_branch 2f\n\t"
      "1:\n\tp2body 1\n\t"
      "2:\n\tglobal_store_dwordx4 v82, v[48:51], s[54:55] sc1\n\tglobal_store_dwordx4 v83, v[52:55], s[54:55] sc1\n\t"
      "global_store_dwordx4 v80, v[56:59], s[54:55] sc1\n\tglobal_store_dwordx4 v81, v[60:63], s[54:55] sc1\n\t"
      "3:\n\ts_mov_b32 m0, %[keep]\n\tv_mov_b32 v76, 0xdddd\n\tv_mov_b32 v77, v94\n\tv_mov_b32 v78, v93\n\t"
      "v_mbcnt_lo_u32_b32 v79, -1, 0\n\tv_mbcnt_hi_u32_b32 v79, -1, v79\n\tv_lshlrev_b32 v79, 4, v79\n\t"
      "s_waitcnt vmcnt(8) lgkmcnt(0)\n\t"
      ".purgem pack\n\t.purgem mm\n\t.purgem rx\n\t.purgem rw\n\t.purgem bump\n\t.purgem src2\n\t.purgem p1body\n\t.purgem tdma\n\t"
      ".purgem slot\n\t.purgem st1\n\t.purgem p2body\n\t"
      : "+{v[64:79]}"(ad), "+{v[80:95]}"(pk), "=&{v[128:143]}"(at), "=&{v[104:107]}"(cv4), [keep] "=&s"(keep), "+{s36}"(off1), "+{s47}"(off2),
        "=&{v[0:31]}"(d0), "=&{v[32:63]}"(d1), "=&{v[160:191]}"(d2), "=&{v[192:223]}"(d3), "=&{v[224:255]}"(d4), "=&{v[112:127]}"(d5),
        "=&{v[144:159]}"(d6), "=&{v[96:103]}"(d7), "=&{v[108:111]}"(d8)
      : [vt1] "s"(vt1), [kv1] "s"(kv1), [vt2] "s"(vt2), "{s[42:43]}"(sA), "{s37}"(str1), "{s38}"(10 * str1), "{s[44:45]}"(sB), "{s46}"(m0v),
        "{s[50:51]}"(tag), "{s52}"(flag), "{s[54:55]}"(ws), "{s72}"(tile), "{s[76:79]}"(so), "{s80}"(qoff),
        [sc] "n"(__builtin_bit_cast(unsigned, QSCALE)), [npoll] "n"(NPOLL), [nflag] "n"(NFLAG), [naw] "n"(NAW), [kbar] "n"(KBAR)
      : "memory", "scc", "vcc", "s40", "s41", "s48");
}

__device__ __forceinline__ u32x2 g1_offs(unsigned br, unsigned cg, unsigned T, unsigned lane) {
  const unsigned l16 = (lane & 31) * 16, h = lane >> 5;
  const unsigned a = h * OCT + l16 + (br * (unsigned)QK_BR + cg * (8 * OCT) + T * 512);
  const unsigned G = (cg - 20) * 4 + ((lane >> 4) & 1);
  const unsigned b = h * 1280 + l16 + ((unsigned)V_WS + br * (unsigned)V_BR + (cg - 20) * 1024 + T * 5120);
  return cg < 20 ? u32x2{a, a + 2 * OCT}
                 : u32x2{((G * 52) >> 8) * (256 * 1280 - 1280) + b, (((G + 2) * 52) >> 8) * (256 * 1280 - 1280) + b + 512};
}

constexpr int TILE_B = 3584, K2_OFF = 1024, V_OFF = 1536;
constexpr int CHUNK_B = 8 * TILE_B;

__device__ __forceinline__ f32x16 smfmac(bf16x8 a, f32x8 p, f32x16 c) {
  return __builtin_amdgcn_smfmac_f32_32x32x32_bf16(a, __builtin_bit_cast(bf16x16, p), c, 0xDDDD, 0, 0);
}
__device__ __forceinline__ f32x8 lo8(const f32x16& s) { return __builtin_shufflevector(s, s, 0, 1, 2, 3, 4, 5, 6, 7); }
__device__ __forceinline__ f32x8 hi8(const f32x16& s) { return __builtin_shufflevector(s, s, 8, 9, 10, 11, 12, 13, 14, 15); }
#define MX3(a, b, c) __builtin_fmaxf(__builtin_fmaxf((a), (b)), (c))
__device__ __forceinline__ float max16(const f32x16& s, float m) {
#pragma unroll
  for (int r = 0; r < 16; r += 2) m = MX3(m, s[r], s[r + 1]);
  return m;
}
__device__ __forceinline__ float xhalf_max(float m) {
  auto rr = __builtin_amdgcn_permlane32_swap(__float_as_uint(m), __float_as_uint(m), false, false);
  return __builtin_fmaxf(__uint_as_float(rr[0]), __uint_as_float(rr[1]));
}
__device__ __forceinline__ float row_sum(const f32x16& o) {
  auto rr = __builtin_amdgcn_permlane32_swap(__float_as_uint(o[8]), __float_as_uint(o[8]), false, false);
  return __uint_as_float(rr[1]);
}
template <bool MARK>
__device__ __forceinline__ void store_o(const f32x16& o, float l, char* base, unsigned lo1, unsigned lo2) {
  const float rl = __builtin_amdgcn_rcpf(l);
  const unsigned a0 = pk_bf16(o[0] * rl, o[1] * rl), a1 = pk_bf16(o[2] * rl, o[3] * rl);
  const unsigned b0 = pk_bf16(o[4] * rl, o[5] * rl), b1 = pk_bf16(o[6] * rl, o[7] * rl);
  const auto r0 = __builtin_amdgcn_permlane32_swap(a0, b0, false, false);
  const auto r1 = __builtin_amdgcn_permlane32_swap(a1, b1, false, false);
  u32x4 v = {r0[0], r1[0], r0[1], r1[1]};
  u32x2 c = {pk_bf16(o[8] * rl, o[9] * rl), pk_bf16(o[10] * rl, o[11] * rl)};
  if (MARK) {
    v = __builtin_elementwise_min(v, u32x4{~1u, ~1u, ~1u, ~1u}); c = __builtin_elementwise_min(c, u32x2{~1u, ~1u});
  }
  st16(base, lo1, v);
  st8lo(base, lo2, c);
}

__device__ __forceinline__ void poll_flags(const char* ws, unsigned off, unsigned long long tag) {
  asm volatile("global_load_dwordx2 v[124:125], v126, s[54:55] sc1\n\t" AWAIT(124, 125, 126)
               :: "{v126}"(off), "{s[54:55]}"(ws), "{s[50:51]}"(tag) : "memory", "scc", "vcc", "s48", "v124", "v125");
}
__device__ __forceinline__ void put_flag(char* ws, unsigned off, unsigned long long tag) {
  asm volatile("s_mov_b64 exec, 1\n\tglobal_store_dwordx2 %0, %1, %2 sc1\n\ts_mov_b64 exec, -1" :: "v"(off), "v"(tag), "s"(ws) : "memory");
}
#define AT_ASM_MACROS \
    ".macro mx d, s\n\tv_max3_f32 v[\\d], v[\\s], v[\\s+1], v[\\s+2]\n\t.set i_, \\s + 3\n\t.rept 6\n\tv_max3_f32 v[\\d], v[\\d], v[i_], v[i_+1]\n\t" \
    ".set i_, i_ + 2\n\t.endr\n\tv_max_f32 v[\\d], v[\\d], v[\\s+15]\n\t.endm\n\t" \
    ".macro xp d, s\n\tv_exp_f32 v[\\d], v[\\s]\n\tv_exp_f32 v[\\d+1], v[\\s+1]\n\tv_exp_f32 v[\\d+2], v[\\s+2]\n\tv_exp_f32 v[\\d+3], v[\\s+3]\n\t.endm\n\t" \
    ".macro smf o, v, p\n\tv_smfmac_f32_32x32x32_bf16 v[\\o:\\o+15], v[\\v:\\v+3], v[\\p:\\p+7], v76\n\t.endm\n\t" \
    ".macro qk1 s, k, q\n\tv_mfma_f32_32x32x16_bf16 v[\\s:\\s+15], v[\\k:\\k+3], v[\\q:\\q+3], 0\n\t.endm\n\t" \
    ".macro qk1c s, k, q, c\n\tv_mfma_f32_32x32x16_bf16 v[\\s:\\s+15], v[\\k:\\k+3], v[\\q:\\q+3], v[\\c:\\c+15]\n\t.endm\n\t" \
    ".macro qk2 s, k, q\n\tv_mfma_f32_32x32x8_bf16 v[\\s:\\s+15], v[\\k:\\k+1], v[\\q:\\q+1], v[\\s:\\s+15]\n\t.endm\n\t"
#define AT_ASM_PURGE ".purgem mx\n\t.purgem xp\n\t.purgem smf\n\t.purgem qk1\n\t.purgem qk1c\n\t.purgem qk2\n\t"

#define AT_A_TEXT AT_ASM_MACROS \
      ".macro pos j\n\t.set sl_, (\\j) >> 2\n\t.if sl_ >= 3\n\t.set sl_, sl_ - 3\n\t.endif\n\t" \
      ".set ar_, 128 + 3 * (sl_ >> 1)\n\t.set of_, (sl_ & 1) * 28672 + ((\\j) & 3) * 7168\n\t.endm\n\t" \
      ".macro dma part\n\t" \
      ".if di_ >= 0\n\t" \
      ".if \\part == 0\n\t" \
      ".if di_ == 1\n\ts_mov_b32 m0, s73\n\t.else\n\ts_add_u32 m0, s72, ((di_ + 2) %% 5) * 28672\n\t.endif\n\t" \
      "s_add_u32 s40, s54, s[56+2*di_]\n\ts_addc_u32 s41, s55, 0\n\t" \
      "global_load_lds_dwordx4 v137, s[40:41] sc1\n\t" \
      ".elseif \\part == 1\n\t" \
      "s_mov_b32 exec_hi, 0\n\tglobal_load_lds_dwordx4 v138, s[40:41] offset:1024 sc1\n\ts_mov_b32 exec_hi, -1\n\t" \
      "s_add_u32 s40, s54, s[57+2*di_]\n\ts_addc_u32 s41, s55, 0\n\t" \
      ".else\n\t" \
      "s_mov_b32 exec_lo, 0xfffff\n\ts_mov_b32 exec_hi, 0xfffff\n\t" \
      "global_load_lds_dwordx4 v139, s[40:41] offset:1536 sc1\n\tglobal_load_lds_dwordx4 v140, s[40:41] offset:2560 sc1\n\t" \
      "s_mov_b64 exec, -1\n\t" \
      ".endif\n\t.endif\n\t" \
        \
      ".set n_, -1\n\t" \
      ".if j_ >= 22 && j_ < 25 && 3 * (j_ - 22) + \\part < 8\n\t.set n_, 3 * (j_ - 22) + \\part\n\t.endif\n\t" \
      ".if j_ >= 28 && 3 * (j_ - 28) + \\part < 5\n\t.set n_, 10 + 3 * (j_ - 28) + \\part\n\t.endif\n\t" \
      ".if n_ >= 0\n\t.set pc_, n_ %% 5\n\t" \
      ".if n_ == 0 || n_ == 10\n\ts_add_u32 m0, s44, n_ * 4096\n\ts_add_u32 s40, s42, m0\n\ts_addc_u32 s41, s43, 0\n\t.endif\n\t" \
      ".if n_ == 5\n\ts_add_u32 m0, s45, 143360 - 81920\n\ts_add_u32 s40, s42, s45\n\ts_addc_u32 s41, s43, 0\n\t.endif\n\t" \
      ".if pc_ == 4\n\ts_add_u32 m0, m0, 4096\n\ts_add_u32 s40, s40, 4096\n\ts_addc_u32 s41, s41, 0\n\t.endif\n\t" \
      "global_load_lds_dwordx4 v79, s[40:41] offset:1024*(pc_&3) sc1\n\t.endif\n\t" \
      ".endm\n\t" \
      ".macro flg msk\n\tv_mov_b32 v142, s50\n\tv_mov_b32 v143, s51\n\tv_mov_b32 v108, s52\n\ts_mov_b64 exec, \\msk\n\ts_cbranch_execz 8f\n\ts_waitcnt vmcnt(0)\n\t" \
      "global_store_dwordx2 v108, v[142:143], s[54:55] sc1\n\t8:\n\ts_mov_b64 exec, -1\n\t.endm\n\t" \
      ".macro tabl d, r\n\tglobal_load_dwordx4 v[\\d:\\d+3], v79, s[40:41] offset:1024*(\\r)\n\t.endm\n\t" \
      ".macro old q\n\t.if (\\q) >= 0 && (\\q) < 10\n\tglobal_load_dwordx4 v[184+4*(\\q):187+4*(\\q)], v108, s[54:55] sc1\n\tv_add_u32 v108, " STR(OCT) "*4, v108\n\t.endif\n\t.endm\n\t" \
      "s_mov_b32 %[keep], m0\n\t" \
      ".set j_, %c[j0]\n\t" \
      ".rept %c[j1] - %c[j0]\n\t" \
      ".set a_, 80 + 14 * (j_ & 1)\n\t.set b_, 94 - 14 * (j_ & 1)\n\t" \
      ".if j_ == 4\n\t" AWAIT(142, 143, 141) ".set di_, 0\n\tdma 0\n\tdma 1\n\tdma 2\n\t.endif\n\t" \
      ".if j_ == 6\n\ts_waitcnt vmcnt(10)\n\ts_barrier\n\t.endif\n\t" \
      ".if j_ == 10\n\ts_waitcnt vmcnt(1)\n\ts_barrier\n\t.endif\n\t" \
      ".if j_ == 14 || j_ == 18\n\ts_waitcnt vmcnt(0)\n\ts_barrier\n\t.endif\n\t" \
      ".if j_ == 22\n\t" AWAIT(142, 143, 141) ".endif\n\t" \
      ".if j_ == 28\n\t" AWAIT(142, 143, 252) "v_add_u32 v108, %c[obr], v253\n\t.endif\n\t" \
      ".if j_ == 28 || j_ == 22 || j_ == 2\n\ts_barrier\n\t.endif\n\t" \
      AT_STAMP \
      ".if j_ < 2\n\ts_setprio 1 - j_\n\t" \
      ".elseif j_ == 10 || j_ == 11\n\ts_setprio 3\n\t.elseif j_ == 12 || j_ == 13\n\ts_setprio 0\n\t" \
      ".elseif j_ < 22\n\ts_setprio 3 - ((j_ + 2) & 3)\n\t.elseif j_ >= 28\n\ts_setprio 31 - j_\n\t" \
      ".elseif j_ != 23 && j_ != 25\n\ts_setprio 3 - (j_ - 21) / 2\n\t.endif\n\t" \
      ".if j_ == 14\n\tflg 1\n\t" \
      "v_mov_b32 v108, 0x3f803f80\n\tv_mov_b32 v109, v108\n\tv_mov_b32 v110, v108\n\tv_mov_b32 v111, v108\n\ts_mov_b64 exec, 15\n\t" \
      "ds_write_b128 v77, v[108:111]\n\ts_mov_b64 exec, -1\n\t.endif\n\t" \
      ".set di_, -1\n\t" \
      ".if j_ == 4 || j_ == 5 || j_ == 6\n\t.set di_, j_ - 3\n\t.endif\n\t.if j_ == 10\n\t.set di_, 4\n\t.endif\n\t" \
      ".if j_ >= 14 && j_ < 17\n\t.set di_, j_ - 9\n\t.endif\n\t" \
        \
      "pos j_+2\n\t" \
      "qk1 48, a_, 68\n\t.if j_ < 30\n\tds_read_b128 v[a_:a_+3], v[ar_] offset:of_\n\t.endif\n\told 4*(j_-28)\n\txp 112, 32\n\t" \
      "qk2 48, a_+4, 74\n\t.if j_ < 30\n\tds_read_b64 v[a_+4:a_+5], v[ar_+1] offset:of_\n\t.endif\n\told 4*(j_-28)+1\n\txp 116, 36\n\t" \
      "pos j_+1\n\t" \
      "smf 16, b_+10, 120\n\t.if j_ < 31\n\tds_read_b128 v[b_+10:b_+13], v[ar_+2] offset:of_+1024\n\t.endif\n\told 4*(j_-28)+2\n\txp 120, 40\n\t" \
      "smf 0, a_+6, 112\n\t.if j_ < 30\n\ts_waitcnt lgkmcnt(3)\n\t.elseif j_ == 30\n\ts_waitcnt lgkmcnt(1)\n\t.else\n\ts_waitcnt lgkmcnt(0)\n\t.endif\n\t" \
      "old 4*(j_-28)+3\n\t" \
      ".if j_ == 5\n\tglobal_load_dwordx4 v[236:239], v137, s[42:43] sc1\n\tglobal_load_dwordx2 v[240:241], v78, s[42:43] sc1\n\t.endif\n\t" \
      "xp 124, 44\n\t" \
        \
      ".if j_ < 31\n\tqk1 32, b_, 64\n\t.endif\n\tdma 0\n\t" \
      ".if j_ == 0\n\tflg s[74:75]\n\t.endif\n\t" \
      ".if j_ == 3\n\tglobal_load_dwordx2 v[142:143], v141, s[54:55] sc1\n\t.endif\n\t" \
      ".if j_ == 7\n\ts_mov_b64 s[40:41], s[44:45]\n\ttabl 252, 0\n\ttabl 160, 1\n\t.endif\n\t" \
      ".if j_ == 8\n\ttabl 224, 2\n\ttabl 228, 3\n\t.endif\n\t" \
      ".if j_ == 20\n\tglobal_load_dwordx2 v[142:143], v141, s[54:55] sc1\n\t.endif\n\t" \
      ".if j_ == 25\n\ts_mov_b32 m0, 40960\n\tv_min_u32 v140, 384, v79\n\tv_lshl_add_u32 v140, v140, 3, s46\n\tglobal_load_lds_dword v140, s[54:55] sc1\n\t.endif\n\t" \
      ".if j_ == 26\n\tglobal_load_dwordx2 v[142:143], v252, s[54:55] sc1\n\t.endif\n\t" \
      "xp 112, 48\n\t" \
      ".if j_ < 31\n\tqk2 32, b_+4, 72\n\t.endif\n\tdma 1\n\t" \
      ".if j_ == 7\n\ttabl 164, 2\n\ttabl 168, 3\n\ts_add_u32 s40, s40, 4096\n\ts_addc_u32 s41, s41, 0\n\t.endif\n\t" \
      ".if j_ == 8\n\ts_add_u32 s40, s40, 4096\n\ts_addc_u32 s41, s41, 0\n\ttabl 232, 0\n\t.endif\n\t" \
      "xp 116, 52\n\t" \
      "smf 0, a_+10, 120\n\tdma 2\n\t" \
      ".if j_ == 7\n\ttabl 172, 0\n\ttabl 220, 1\n\t.endif\n\t" \
      ".if j_ == 9\n\tv_lshrrev_b32 v108, 5, v79\n\tv_mul_u32_u24 v108, 1248, v108\n\ts_mov_b32 m0, s47\n\tv_add_u32 v108, v108, v79\n\t" \
      "global_load_lds_dwordx4 v108, s[76:77]\n\t.endif\n\t" \
      "xp 120, 56\n\t" \
      "pos j_+2\n\t" \
      "smf 16, a_+6, 112\n\t.if j_ < 30\n\tds_read_b128 v[a_+6:a_+9], v[ar_+2] offset:of_\n\t.endif\n\txp 124, 60\n\t" \
      ".set j_, j_ + 1\n\t" \
      ".endr\n\t" \
      ".if %c[j1] == 32\n\ts_nop 1\n\tsmf 16, 104, 120\n\ts_nop 15\n\t.endif\n\t" \
      "s_mov_b32 m0, %[keep]\n\t" \
      ".purgem pos\n\t.purgem dma\n\t.purgem flg\n\t.purgem tabl\n\t.purgem old\n\t" AT_ASM_PURGE
#define AT_STAMP
#define AT_STV
#define AT_STV_ARG
#define AT_STV_PASS
#define AT_A_STATE "+{v[0:31]}"(o), "+{v[32:47]}"(s0), "+{v[112:127]}"(pp), "+{v[80:111]}"(fr), "+{v[128:143]}"(ad), [keep] "=&s"(keep)
#define AT_A_CLOB "memory", "scc", "vcc", "s40", "s41", "s48", "v48", "v49", "v50", "v51", "v52", "v53", "v54", "v55", "v56", "v57", "v58", "v59", "v60", "v61", "v62", "v63"
__device__ __forceinline__ void at_a1(f32x32& o, f32x16& s0, f32x16& pp, f32x32& fr, const u32x16& q, u32x16& ad, const char* ws, u32x16 so,
                                      unsigned m0v, unsigned m0b, unsigned long long tag, unsigned flag, unsigned long long fmask,
                                      const char* qB, const void* tab, f32x16& mF, f32x16& mD, u32x8& qv, u32x4& tw,
                                      const char* fsrc, unsigned fl AT_STV_ARG) {
  unsigned keep;
  asm volatile(AT_A_TEXT
      : AT_A_STATE, "=&{v[160:175]}"(mF), "=&{v[220:235]}"(mD), "=&{v[236:243]}"(qv), "=&{v[252:255]}"(tw) AT_STV
      : "{v[64:79]}"(q), "{s[54:55]}"(ws), "{s[56:71]}"(so), "{s72}"(m0v), "{s73}"(m0b), "{s[50:51]}"(tag), "{s52}"(flag), "{s[74:75]}"(fmask),
        "{s[42:43]}"(qB), "{s[44:45]}"(tab), "{s[76:77]}"(fsrc), "{s47}"(fl), [j0] "n"(0), [j1] "n"(12), [obr] "n"(O_BR)
      : AT_A_CLOB);
}
__device__ __forceinline__ void at_a2(f32x32& o, f32x16& s0, f32x16& pp, f32x32& fr, const u32x16& q, u32x16& ad, const char* ws, u32x16 so,
                                      unsigned m0v, unsigned long long tag, unsigned flag, const u32x4& tw, const char* src, unsigned d0,
                                      unsigned d1, unsigned tch, f32x32& of, f32x8& of2) {
  unsigned keep;
  asm volatile(AT_A_TEXT
      : AT_A_STATE, "=&{v[184:215]}"(of), "=&{v[216:223]}"(of2)
      : "{v[64:79]}"(q), "{s[54:55]}"(ws), "{s[56:71]}"(so), "{s72}"(m0v), "{s[50:51]}"(tag), "{s52}"(flag),
        "{v[252:255]}"(tw), "{s[42:43]}"(src), "{s44}"(d0), "{s45}"(d1), "{s46}"(tch), [j0] "n"(12), [j1] "n"(32), [obr] "n"(O_BR)
      : AT_A_CLOB);
}

__device__ __forceinline__ void at_b(f32x16& o, f32x16& mF, const f32x16& mD, u32x8& q, unsigned tb, unsigned qt, char* oA, const u32x16& qa,
                                     u32x16& ad, f32x32& k0, f32x16& k1, f32x32& k2, f32x16& k3, unsigned fl, unsigned fd, char* ws) {
  asm volatile(AT_ASM_MACROS
      "v_add_u32 v242, %[tb], v128\n\tv_add_u32 v243, %[tb], v129\n\tv_add_u32 v245, %[tb], v130\n\t"
      "ds_read_b128 v[176:179], v242 offset:7*3584\n\tds_read_b64 v[180:181], v243 offset:7*3584\n\t"
      "ds_read_b128 v[184:187], v242 offset:8*3584\n\tds_read_b64 v[182:183], v243 offset:8*3584\n\t"
      "ds_read_b128 v[192:195], v242\n\tds_read_b64 v[196:197], v243\n\tds_read_b128 v[206:209], v242 offset:3584\n\tds_read_b64 v[210:211], v243 offset:3584\n\t"
      "ds_read_b128 v[198:201], v245\n\tds_read_b128 v[212:215], v245 offset:3584\n\tds_read_b128 v[202:205], v245 offset:1024\n\t"
      "s_mov_b32 exec_lo, 0\n\tv_mov_b64 v[240:241], 0\n\ts_mov_b32 exec_lo, -1\n\tv_mov_b64 v[188:189], -1\n\tv_mov_b64 v[190:191], -1\n\t"
      "s_waitcnt lgkmcnt(7)\n\t"
      "qk1 144, 176, 236\n\tqk2 144, 180, 240\n\tqk1c 48, 184, 236, 220\n\tqk2 48, 182, 240\n\t"
      "global_store_dwordx4 v137, v[188:191], s[42:43] sc1\n\ts_mov_b32 exec_hi, 0\n\tglobal_store_dwordx2 v78, v[188:189], s[42:43] sc1\n\ts_mov_b32 exec_hi, -1\n\t"
      "s_waitcnt lgkmcnt(5)\n\tqk1c 160, 192, 236, 160\n\tds_read_b128 v[192:195], v242 offset:7168\n\ts_cmp_eq_u32 %[qt], 0\n\ts_nop 1\n\t"
      "mx 216, 144\n\tmx 218, 48\n\ts_cbranch_scc1 1f\n\tv_max_f32 v218, v218, v216\n\t1:\n\tv_mov_b32 v219, v218\n\t"
      "v_mov_b64 v[144:145], 0\n\tv_mov_b64 v[146:147], 0\n\tv_permlane32_swap_b32 v218, v219\n\tv_max_f32 v218, v218, v219\n\tv_add_f32 v218, 0x42600000, v218\n\t"
      "s_mov_b32 exec_lo, 0\n\tv_cvt_pk_bf16_f32 v240, -v218, 0\n\ts_mov_b32 exec_lo, -1\n\tv_mov_b64 v[148:149], 0\n\t"
      "qk2 160, 196, 240\n\tds_read_b64 v[196:197], v243 offset:7168\n\t"
      ".set i_, 150\n\t.rept 5\n\tv_mov_b64 v[i_:i_+1], 0\n\t.set i_, i_ + 2\n\t.endr\n\t"
      "s_waitcnt lgkmcnt(2)\n\ts_nop 1\n\t"
      ".set t_, 0\n\t"
      ".rept 9\n\t"
      ".set a_, 192 + 14 * (t_ & 1)\n\t.set b_, 206 - 14 * (t_ & 1)\n\t.set c_, 160 - 112 * (t_ & 1)\n\t.set d_, 48 + 112 * (t_ & 1)\n\t"
      ".if t_ == 0 || t_ == 5\n\ts_setprio 2 - t_ / 5\n\t.endif\n\t"
      ".if t_ < 7\n\tqk1 d_, b_, 236\n\t.elseif t_ == 7\n\tqk1c d_, b_, 236, 220\n\t.endif\n\t"
      ".if t_ < 6\n\tds_read_b128 v[b_:b_+3], v242 offset:(t_+3)*3584\n\t.endif\n\t"
      "xp 176, c_\n\t"
      ".if t_ < 8\n\tqk2 d_, b_+4, 240\n\t.endif\n\t"
      ".if t_ < 6\n\tds_read_b64 v[b_+4:b_+5], v243 offset:(t_+3)*3584\n\t.endif\n\t"
      ".if t_ == 4\n\tv_and_b32 v142, 0xf0, v79\n\tv_lshrrev_b32 v142, 1, v142\n\tv_sub_u32 v142, v79, v142\n\tv_add_u32 v142, %[fl], v142\n\ts_waitcnt vmcnt(6)\n\t"
      "ds_read_b64_tr_b16 v[108:109], v142\n\tds_read_b64_tr_b16 v[110:111], v142 offset:128\n\tv_add_u32 v142, %[fd], v79\n\t.endif\n\t"
      ".if t_ == 6\n\tglobal_store_dwordx4 v142, v[108:111], %[ws] sc1\n\t.endif\n\t"
      "xp 180, c_+4\n\t"
      ".if t_ > 0\n\tsmf 144, b_+10, 184\n\t.endif\n\t"
      ".if t_ < 8\n\tds_read_b128 v[b_+10:b_+13], v245 offset:(t_+1)*3584+1024\n\t.endif\n\t"
      "xp 184, c_+8\n\t"
      "smf 144, a_+6, 176\n\t"
      ".if t_ < 6\n\ts_waitcnt lgkmcnt(3)\n\t.elseif t_ < 8\n\ts_waitcnt lgkmcnt(1)\n\t.else\n\ts_waitcnt lgkmcnt(0)\n\t.endif\n\t"
      ".if t_ < 7\n\tds_read_b128 v[a_+6:a_+9], v245 offset:(t_+2)*3584\n\t.endif\n\t"
      "xp 188, c_+12\n\t"
      ".set t_, t_ + 1\n\t"
      ".endr\n\t"
      "s_nop 1\n\tsmf 144, 202, 184\n\ts_nop 11\n\t"
      AT_ASM_PURGE
      : "=&{v[144:159]}"(o), "+{v[160:175]}"(mF), "+{v[0:31]}"(k0), "+{v[32:47]}"(k1), "+{v[80:111]}"(k2), "+{v[112:127]}"(k3), "+{v[128:143]}"(ad),
        "+{v[236:243]}"(q)
      : "{v[220:235]}"(mD), "{v[64:79]}"(qa), [tb] "s"(tb), [qt] "s"(qt), "{s[42:43]}"(oA), [fl] "s"(fl), [fd] "s"(fd), [ws] "s"(ws)
      : "memory", "scc", "v245", "v48", "v49", "v50", "v51", "v52", "v53", "v54", "v55", "v56", "v57", "v58", "v59", "v60", "v61", "v62", "v63",
        "v176", "v177", "v178", "v179", "v180", "v181", "v182", "v183", "v184", "v185", "v186", "v187", "v188", "v189", "v190", "v191",
        "v192", "v193", "v194", "v195", "v196", "v197", "v198", "v199", "v200", "v201", "v202", "v203", "v204", "v205", "v206", "v207",
        "v208", "v209", "v210", "v211", "v212", "v213", "v214", "v215", "v216", "v217", "v218", "v219");
}

#define PJ_STAMP(i)
#define KLD 9
__device__ __forceinline__ void proj_all(const f32x32& of, const f32x8& of2, unsigned l16, const u32x4& tw, const char* src, unsigned pos,
                                         unsigned x, unsigned grp, const char* ws, char* out, unsigned long long m5 AT_STV_ARG) {
  f32x32 t0, t1, t2; f32x16 t3, t4, t5; f32x8 t6, t7; f32x4 t8; u32x2 t9;
  asm volatile(
      ".macro rdw k, sp, ab\n\t.set d_, 80 + 20 * ((\\k) & 1)\n\t"
      ".if (\\k) < \\sp\n\t.set a_, \\ab\n\t.set o_, (\\k) * 5120\n\t.else\n\t.set a_, \\ab+1\n\t.set o_, ((\\k) - \\sp) * 5120\n\t.endif\n\t.endm\n\t"
      ".macro rd1 i\n\tds_read_b128 v[d_+4*\\i:d_+4*\\i+3], v[a_] offset:o_+1024*\\i\n\t.endm\n\t"
      ".macro rd5\n\trd1 0\n\trd1 1\n\trd1 2\n\trd1 3\n\trd1 4\n\t.endm\n\t"
      ".macro pm i, ac, of, last\n\t"
      ".if k_ == 0\n\tv_mfma_f32_16x16x32_bf16 v[\\ac+4*\\i:\\ac+4*\\i+3], v[c_+4*\\i:c_+4*\\i+3], v[\\of:\\of+3], 0\n\t"
      ".else\n\tv_mfma_f32_16x16x32_bf16 v[\\ac+4*\\i:\\ac+4*\\i+3], v[c_+4*\\i:c_+4*\\i+3], v[\\of+4*k_:\\of+3+4*k_], v[\\ac+4*\\i:\\ac+4*\\i+3]\n\t.endif\n\t"
      ".if k_ < \\last\n\trd1 \\i\n\ts_waitcnt lgkmcnt(4)\n\t.elseif \\i < 4\n\ts_waitcnt lgkmcnt(3 - \\i)\n\t.endif\n\t.endm\n\t"
      ".macro ldo\n\tv_mov_b32 v72, v253\n\t.set q_, 0\n\t.rept 10\n\t"
      "global_load_dwordx4 v[32+4*q_:35+4*q_], v72, s[54:55] sc1\n\t.if q_ < 9\n\tv_add_u32 v72, " STR(OCT) "*4, v72\n\t.endif\n\t.set q_, q_ + 1\n\t.endr\n\t.endm\n\t"
      ".macro wp n, hf\n\t.set n_, \\n\n\t.if n_ < 7\n\t"
      ".if (n_ & 3) == 0\n\ts_add_u32 m0, s44, 86016 * (1 - \\hf) + 1024 * n_\n\ts_add_u32 s40, s42, 51200 * \\hf + 1024 * n_\n\ts_addc_u32 s41, s43, 0\n\t.endif\n\t"
      "global_load_lds_dwordx4 v79, s[40:41] offset:1024*(n_&3) sc1\n\t.endif\n\t.endm\n\t"
      ".macro snd ac, off\n\tds_write_b128 v124, v[\\ac+8:\\ac+11] offset:\\off\n\tds_write_b128 v124, v[\\ac+12:\\ac+15] offset:\\off+1024\n\t"
      "ds_write_b128 v124, v[\\ac+16:\\ac+19] offset:\\off+2048\n\t.endm\n\t"
      ".macro rcv off\n\tds_read_b128 v[128:131], v125 offset:\\off\n\tds_read_b128 v[132:135], v125 offset:\\off+1024\n\tds_read_b128 v[136:139], v125 offset:\\off+2048\n\t.endm\n\t"
      ".macro fin ac, off\n\t"
      "v_pk_add_f32 v[128:129], v[128:129], v[\\ac:\\ac+1]\n\tv_pk_add_f32 v[130:131], v[130:131], v[\\ac+2:\\ac+3]\n\t"
      "v_pk_add_f32 v[132:133], v[132:133], v[\\ac+4:\\ac+5]\n\tv_pk_add_f32 v[134:135], v[134:135], v[\\ac+6:\\ac+7]\n\t"
      "v_cvt_pk_bf16_f32 v140, v128, v129\n\tv_cvt_pk_bf16_f32 v141, v130, v131\n\tv_cvt_pk_bf16_f32 v142, v132, v133\n\tv_cvt_pk_bf16_f32 v143, v134, v135\n\t"
      "v_pk_add_f32 v[136:137], v[136:137], v[\\ac+16:\\ac+17]\n\tv_pk_add_f32 v[138:139], v[138:139], v[\\ac+18:\\ac+19]\n\t"
      "v_permlane16_swap_b32 v140, v142\n\tv_permlane16_swap_b32 v141, v143\n\t"
      "v_cvt_pk_bf16_f32 v136, v136, v137\n\tv_cvt_pk_bf16_f32 v137, v138, v139\n\t"
      "global_store_dwordx4 v254, v[140:143], s[56:57] offset:\\off sc1\n\t"
      "s_mov_b64 exec, s[58:59]\n\tglobal_store_dwordx2 v255, v[136:137], s[56:57] offset:\\off sc1\n\ts_mov_b64 exec, -1\n\t.endm\n\t"
      "s_mul_i32 s48, s46, 51200\n\tv_add_u32 v120, s48, v79\n\trdw 0, 6, 120\n\trd5\n\t"
      "s_mul_i32 s49, s46, 143360 - 30720\n\ts_add_u32 s49, s49, 30720\n\tv_add_u32 v121, s49, v79\n\t"
      "s_lshr_b32 s48, s48, 1\n\tv_add_u32 v123, s48, v79\n\tv_add_u32 v122, 86016, v123\n\t"
      "s_add_u32 s48, s45, 51200\n\tv_add_u32 v124, s48, v79\n\ts_mul_i32 s49, s46, 24576\n\ts_sub_u32 s49, 12288, s49\n\tv_add_u32 v125, s49, v124\n\t"
      "s_barrier\n\ts_waitcnt lgkmcnt(4)\n\t"
      ".set k_, 0\n\t.rept 10\n\t.set c_, 80 + 20 * (k_ & 1)\n\trdw k_+1, 6, 120\n\t"
      ".if k_ == 0 || k_ == 3 || k_ == 6\n\ts_setprio 2 - k_ / 3\n\t.endif\n\t"
      ".if k_ == %c[kld]\n\tldo\n\t.endif\n\t"
      "pm 0, 0, 184, 9\n\twp 2*k_, 0\n\tpm 1, 0, 184, 9\n\tpm 2, 0, 184, 9\n\twp 2*k_+1, 0\n\tpm 3, 0, 184, 9\n\tpm 4, 0, 184, 9\n\t"
      ".set k_, k_ + 1\n\t.endr\n\t"
      "s_setprio 3\n\ts_mov_b32 s48, 0x40000\n\ts_waitcnt vmcnt(10)\n\ts_barrier\n\t"
      PJ_STAMP(16)
      "rdw 0, 5, 122\n\trd5\n\tsnd 0, 0\n\twp 0, 1\n\twp 1, 1\n\twp 2, 1\n\twp 3, 1\n\twp 4, 1\n\twp 5, 1\n\twp 6, 1\n\ts_waitcnt vmcnt(7)\n\t"
      "5:\n\t"
      "v_max3_u32 v72, v32, v33, v34\n\t.set q_, 35\n\t.rept 18\n\tv_max3_u32 v72, v72, v[q_], v[q_+1]\n\t.set q_, q_ + 2\n\t.endr\n\t"
      "v_max_u32 v72, v72, v71\n\tv_cmp_eq_u32 vcc, -1, v72\n\ts_sub_u32 s48, s48, 1\n\t"
      "s_cbranch_vccz 4f\n\ts_cbranch_scc1 4f\n\tldo\n\ts_waitcnt vmcnt(0)\n\ts_branch 5b\n\t4:\n\t"
      PJ_STAMP(17)
      "s_waitcnt lgkmcnt(0)\n\t"
      ".set k_, 0\n\t.rept 5\n\t.set c_, 80 + 20 * (k_ & 1)\n\trdw k_+1, 5, 122\n\t"
      "pm 0, 160, 32, 4\n\tpm 1, 160, 32, 4\n\tpm 2, 160, 32, 4\n\tpm 3, 160, 32, 4\n\tpm 4, 160, 32, 4\n\t"
      ".set k_, k_ + 1\n\t.endr\n\t"
      "s_waitcnt vmcnt(0)\n\ts_barrier\n\t"
      PJ_STAMP(18)
      "rcv 0\n\trd5\n\ts_waitcnt lgkmcnt(4)\n\t"
      ".rept 5\n\t.set c_, 80 + 20 * (k_ & 1)\n\trdw k_+1, 5, 122\n\t"
      "pm 0, 160, 32, 9\n\tpm 1, 160, 32, 9\n\tpm 2, 160, 32, 9\n\tpm 3, 160, 32, 9\n\tpm 4, 160, 32, 9\n\t"
      ".if k_ == 5\n\tfin 0, 1280\n\t.endif\n\t"
      ".set k_, k_ + 1\n\t.endr\n\t"
      "s_nop 6\n\tsnd 160, 86016-51200\n\ts_waitcnt lgkmcnt(0)\n\ts_barrier\n\t"
      PJ_STAMP(20)
      "rcv 86016-51200\n\ts_waitcnt lgkmcnt(0)\n\tfin 160, 0\n\t"
      ".purgem rdw\n\t.purgem rd1\n\t.purgem rd5\n\t.purgem pm\n\t.purgem ldo\n\t.purgem wp\n\t.purgem snd\n\t.purgem rcv\n\t.purgem fin\n\t"
      : "=&{v[0:31]}"(t0), "=&{v[32:63]}"(t1), "=&{v[80:111]}"(t2), "=&{v[112:127]}"(t3), "=&{v[128:143]}"(t4), "=&{v[160:175]}"(t5),
        "=&{v[64:71]}"(t6), "=&{v[176:183]}"(t7), "=&{v[72:73]}"(t9) AT_STV
      : "{v[184:215]}"(of), "{v[216:223]}"(of2), "{v79}"(l16), "{v[252:255]}"(tw), "{s[42:43]}"(src), "{s44}"(pos), "{s45}"(x), "{s46}"(grp),
        "{s[54:55]}"(ws), "{s[56:57]}"(out), "{s[58:59]}"(m5), [kld] "n"(KLD)
      : "memory", "scc", "vcc", "s40", "s41", "s48", "s49");
}
typedef __attribute__((address_space(3))) f32x4* lds_f4;

struct LaneTab { unsigned v[9][64][4]; };
constexpr LaneTab make_tab() {
  LaneTab t = {};
  for (unsigned l = 0; l < 64; ++l) {
    const unsigned g = l >> 4;
    t.v[0][l][0] = (l & 15) * 64 * 128;
    t.v[0][l][1] = g * OCT + (l & 15) * 16;
    t.v[0][l][2] = (l & 15) * 2560 + (g & 1) * 32 + (g >> 1) * 16;
    t.v[0][l][3] = (l & 15) * 2560 + 128 + g * 8;
    for (unsigned r = 0; r < 16; ++r) {
      const unsigned key = (r & 3) + 8 * (r >> 2) + 4 * (l >> 5);
      t.v[1 + (r >> 2)][l][r & 3] = key > (l & 31) ? 0u : 0xFF800000u;
      t.v[5 + (r >> 2)][l][r & 3] = key <= (l & 31) ? 0u : 0xFF800000u;
    }
  }
  return t;
}
__device__ const LaneTab lane_tab = make_tab();
constexpr unsigned CODE_B = 34816;

__global__ void __launch_bounds__(512, 2)
k_fused(const char* __restrict__ xa_, const char* __restrict__ wa_, const char* __restrict__ woa_, const char* __restrict__ xb_,
        const char* __restrict__ wb_, const char* __restrict__ wob_, char* __restrict__ ws_, char* __restrict__ out_) {
  __shared__ __attribute__((aligned(16))) char lds[163840];
  const unsigned bid = blockIdx.x, xcd = bid & 7, slot = bid >> 3, br = xcd >> 2;
  u32x4 ka; u32x2 kb; u32x8 kc;
  {
    const void* kp = (const void*)__builtin_amdgcn_kernarg_segment_ptr();
    asm volatile("s_load_dwordx4 %0, %3, %4\n\ts_load_dwordx8 %2, %3, 0x20\n\ts_load_dwordx2 %1, %3, 0x10"
                 : "=&s"(ka), "=&s"(kb), "=&s"(kc) : "s"(kp), "s"(br * 24));
  }
  STAMP_DECL STAMP(0);
  unsigned long long tstart; asm volatile("s_memrealtime %0\n\ts_waitcnt lgkmcnt(0)" : "=s"(tstart));
  unsigned lane = threadIdx.x & 63;
  const unsigned w = __builtin_amdgcn_readfirstlane(threadIdx.x >> 6);
  const unsigned grp = w >> 2, lw = w & 3;
  const unsigned mt = (xcd & 3) * 4 + (slot & 3), u = slot >> 2;
  const unsigned win = (5 * ((mt >> 1) & 3)) >> 1;
  const unsigned kv1 = (u + 3) >> 3;
  const unsigned j1 = win + u - 5;
#define ROT (5 * ((u + slot) & 1) + 3 * ((xcd >> 1) & 1) + (xcd & 1))
  auto mod10 = [](unsigned x) { return __builtin_elementwise_min(x, x - 10); };
  const unsigned rot = mod10(ROT);
  const unsigned m0v = grp * 16384 + lw * 4096;
  const unsigned str1 = grp ? 64 * 3840 : 128;
  unsigned o1[4], tw;
  {
    const unsigned row = lane >> 3, pos = lane & 7;
    tw = row * 3840 + ((pos ^ (row & 2)) * 16);
    const unsigned t1 = grp ? tw : row * 1280 + ((pos ^ (row >> 1)) * 16);
    const unsigned gap = kv1 ? 1280 : 128, m1 = grp ? 0 : 64;
    o1[0] = t1 + 1024;
    o1[1] = (t1 ^ m1) + (grp ? gap : 8 * 1280);
    o1[2] = (t1 ^ m1 ^ 64) + (grp ? 8 * 3840 - 1024 : 16 * 1280 - 1024);
    o1[3] = (t1 ^ 64) + (grp ? 8 * 3840 - 2048 + gap : 24 * 1280 - 2048);
  }
  const unsigned offA = grp ? 16 * lw * 3840 + (kv1 ? 1280 + j1 * 128 : u * 256) : (mt * 128 + 32 * lw) * 1280;
  asm volatile("s_waitcnt lgkmcnt(0)" : "+s"(ka), "+s"(kb), "+s"(kc));
  auto ptr = [](unsigned lo, unsigned hi) { return (char*)(((unsigned long long)hi << 32) | lo); };
  const char* sA = ptr(grp ? ka[2] : ka[0], grp ? ka[3] : ka[1]) + (offA - 1024ll);
  char* const ws = ptr(kc[4], kc[5]);
  glds16x4(sA + rot * str1, o1, m0v);
  u32x2 ep; unsigned tch[4];
  asm volatile("s_load_dwordx2 %0, %1, %2" : "=&s"(ep) : "s"(ws), "s"((unsigned)CTL_WS));
  {
    const unsigned i64 = (slot * 8 + w) * 64;
    unsigned long long pc;
    asm volatile("s_getpc_b64 %0\n\ts_load_dword %1, %0, %5\n\ts_load_dword %2, %0, %6\n\ts_load_dword %3, %0, %7\n\ts_load_dword %4, %8, %9"
                 : "=&s"(pc), "=&s"(tch[0]), "=&s"(tch[1]), "=&s"(tch[2]), "=&s"(tch[3])
                 : "s"(i64), "s"(i64 + 16384), "s"(__builtin_elementwise_min(i64 + 32768, CODE_B)), "s"(&lane_tab),
                   "s"(__builtin_elementwise_min(i64, 9152u)));
  }
  const unsigned j2 = mod10(win + 3 + u);
  const unsigned Tg = mt * 4 + lw;
  const unsigned vt1 = grp & kv1, vt2 = u < 7 ? grp : 2;
  const unsigned cg1 = kv1 ? 10 + 10 * grp + j1 : 2 * u + grp, cg2 = 10 + 10 * grp + j2;
  const unsigned head = xcd * 4 + (slot & 3), qb = u;
  const unsigned odd = head & 1, o2 = 5 * (head >> 1) + 2, o1h = odd ? o2 + 1 : o2 - 2;
  const unsigned qt = qb * 8 + 2 * lw + grp;
  u32x4 soA; u32x16 so;
  {
    const unsigned kb0 = 80 * OCT + w * 512, vb0 = (unsigned)V_WS - V_OFF + xcd * (256 * 1280) + w * 5120;
    unsigned ko[10], vo_[10];
#pragma unroll
    for (int i = 0; i < 8; ++i) { const unsigned c = (((head >> 3) + (i >> 1)) & 3) + 4 * (i & 1); ko[i] = kb0 + c * 4096; vo_[i] = vb0 + c * 40960; }
    const unsigned c8 = qb ? qb - 1 : 0;
    ko[8] = kb0 + (unsigned)QK_BR + c8 * 4096; vo_[8] = vb0 + (unsigned)V_BR + c8 * 40960;
    ko[9] = kb0 + (unsigned)QK_BR + qb * 4096; vo_[9] = vb0 + (unsigned)V_BR + qb * 40960;
    soA = u32x4{ko[0], vo_[0], ko[1], vo_[1]};
    so = u32x16{ko[2], vo_[2], ko[8], vo_[8], ko[9], vo_[9], ko[3], vo_[3], ko[4], vo_[4], ko[5], vo_[5], ko[6], vo_[6], ko[7], vo_[7]};
    asm volatile("" : "+{s[76:79]}"(soA), "+{s[56:71]}"(so));
  }
  unsigned flag1 = FQ_OFF + ((br * 30 + cg1) * 64 + Tg) * 128, flag2 = FQ_OFF + ((br * 30 + cg2) * 64 + Tg) * 128;
  unsigned flagO = FO_OFF + (head * 64 + qt) * 128;
  PINS(flag1); PINS(flag2); PINS(flagO);
  glds16x4(sA + mod10(rot + 1) * str1, o1, m0v + 32768);
  const char* sB = ptr(ka[2], ka[3]) + (16 * lw * 3840 + 256 + j2 * 128);
  const unsigned mytile = w * TILE_B;
  const char* fsrc; unsigned fdst;
  {
    const unsigned F = __builtin_elementwise_min((head * 8 + qb) * 8 + w, 1599u);
    const unsigned bl = (F * 13108) >> 16, np = F - 5 * bl, t = (bl * 3277) >> 16, ps = bl - 20 * t;
    const unsigned ks = ((t & 1) || ps < 5 || ps >= 15) ? ps : (ps < 10 ? ps + 5 : ps - 5);
    fsrc = ptr((t & 1) ? kc[2] : kb[0], (t & 1) ? kc[3] : kb[1]) + (ks * (32 * 1280) + (t >> 1) * 160 + ((ks >= 10 && np < 4) ? np ^ 2 : np) * 32);
    fdst = (unsigned)WP_WS + ((head * 8 + qb) * 8 + w) * 1024;
  }
  u32x4 late = {FO_OFF + (grp * 16 * 64 + head * 2 + (lw >> 1)) * 128,
                (unsigned)O_WS + grp * 40 * OCT + (head * 64 + lw * 16) * 16,
                (unsigned)WP_WS + qb * 204800,
                (head * 64 + lw * 16) * 2560 + qb * 160};
  asm volatile("" : "+s"(fsrc), "+s"(fdst), "+s"(late));
  u32x16 qq, pk, at;
  {
    const unsigned r16 = lane & 15, g = lane >> 4, q = r16 >> 2, p = lane & 3, r32 = lane & 31, h = lane >> 5;
    const unsigned xa0 = (lw * 32 + r16) * 128 + ((g ^ (r16 >> 1)) * 16);
    const unsigned wa0 = 2048 * g + 1024 * grp + 128 * q + 16 * ((p >> 1) ^ (4 * (g & 1) + (q & 2))) + 8 * (p & 1);
    const unsigned jj = (20 * head + 19 * (lane & 1)) >> 6, b1 = (lane >> 1) & 1, c = (lane >> 2) & 7;
    const unsigned pol1 = FQ_OFF + (jj * 64 + ((lane & 8) ? (qt ^ b1) : b1 * 640 + (lane & 4) * 8 + (640 + (head >> 3) * 8 + w))) * 128;
    qq = u32x16{o1[0], o1[1], o1[2], o1[3], tw + 1024, tw + 1280, (tw ^ 64) + (8 * 3840 - 1024), (tw ^ 64) + (8 * 3840 - 2048 + 1280),
                pol1, xa0, xa0 ^ 64, xa0, wa0, wa0 ^ 32, wa0 ^ 64, wa0 ^ 96};
    const unsigned cB = __builtin_elementwise_max((c & 1) + qb, 1u) + (30 * 8 - 1);
    const unsigned pol2 = (jj * 64 + (lane >= 48 ? 30 * 64 + qt : b1 * 640 + (h ? cB : c) * 8 + (640 + w))) * 128 + FQ_OFF;
    const u32x2 s1 = g1_offs(br, cg1, Tg, lane), s2 = g1_offs(br, cg2, Tg, lane);
    const unsigned ab = grp * TILE_B;
    pk = u32x16{s1[0], s1[1], s2[0], s2[1],
                lane * 16 + ab,
                h ? (V_OFF + 320 + ab) : r32 * 16 + (K2_OFF + odd * 8 + ab),
                h * 512 + (r32 < 28 ? __builtin_elementwise_min(r32, 21u) : 28) * 16 + (V_OFF + ab),
                h * OCT + r32 * 16 + o1h * OCT,
                r32 * 16 + (o2 * OCT - 1024),
                h * 1280 + (__builtin_elementwise_min(r32 + 4 * odd, r32 + 4 * odd - 20) + (head & 3) * 20) * 16,
                pol2,
                g * 512 + __builtin_elementwise_min(r16, 11u) * 16 + (mytile + V_OFF + 320),
                r16 == 0 ? 0x3F803F80u : 0u,
                r32 * 16 + (o2 * OCT + odd * 8),
                lane * 512 + (3 * CHUNK_B + V_OFF + 320 + mytile),
                lane};
  }
  asm volatile("" : "+{v[64:79]}"(qq), "+{v[80:95]}"(pk) :: "memory");
  {
    const unsigned oo[4] = {qq[0], qq[1], qq[2], qq[3]};
    glds16x4(sA + mod10(rot + 2) * str1, oo, m0v + 65536);
  }
  asm volatile("s_waitcnt vmcnt(8) lgkmcnt(0)\n\ts_barrier" ::: "memory");
  PINS(ep); PINS(tch[0]); PINS(tch[1]); PINS(tch[2]); PINS(tch[3]);
  const unsigned long long tag = (((unsigned long long)ep[1] << 32) | ep[0]) + 1;
  STAMP(1);
  u32x4 cv4;
  STV_PUT(pk[15]);
  g1_all(vt1, kv1, vt2, qq, pk, at, cv4, sA, mod10(rot + 3) * str1, str1, sB, mod10(rot + grp) * (64 * 3840), m0v, tag, flag1, ws, mytile, soA,
         qt * 512);
  STV_GET(pk[15]);
  STAMP(8);
  if (bid == 255 && grp == 0) {
    const unsigned e = w * 64 + lane_id(), eu = e & 7, em = (e >> 3) & 15;
    poll_flags(ws, FQ_OFF + (((e >> 7) * 30 + (eu < 5 ? 2 * eu : 5 + eu + ((5 * ((em >> 1) & 3)) >> 1))) * 64 + 4 * em) * 128, tag);
  }
  BARRIER();
  STAMP(9);
  if (bid == 255 && w == 0) put_flag(ws, CTL_WS, tag);
  typedef __attribute__((address_space(3))) u32x4* lds_u4;
  *(lds_u4)((lds_ptr)lds + pk[11] + 2 * CHUNK_B) = cv4; *(lds_u4)((lds_ptr)lds + pk[11] + 3 * CHUNK_B) = cv4; *(lds_u4)((lds_ptr)lds + pk[11] + 4 * CHUNK_B) = cv4;
  if (qb == 0) {
    lds_ptr z = (lds_ptr)lds + 3 * CHUNK_B + mytile + qq[15];
    *(lds_f4)(z) = f32x4{}; *(lds_f4)(z + 1024) = f32x4{}; *(lds_f4)(z + 2048) = f32x4{}; *(lds_f4)(z + 2560) = f32x4{};
  }
  const f32x16 zero16 = {};
  auto exact = [&](int b, bf16x8 q1, s16x4 q2, int t0, int t1) {
    __builtin_amdgcn_fence(__ATOMIC_ACQUIRE, "agent");
    const int ln = lane_id(), r32 = ln & 31, h = ln >> 5;
    unsigned head = xcd * 4 + (slot & 3);
    PINS(head);
    const unsigned odd = head & 1, o2 = 5 * (head >> 1) + 2, o1h = odd ? o2 + 1 : o2 - 2;
    if (h) q2 = s16x4{0, 0, 0, 0};
    typedef __attribute__((address_space(1))) const char* gptr;
    gptr kg1 = (gptr)ws + QK_WS + b * QK_BR + (size_t)(80 + o1h + h) * OCT + r32 * 16;
    gptr kg2 = (gptr)ws + QK_WS + b * QK_BR + (size_t)(80 + o2) * OCT + r32 * 16 + odd * 8;
    gptr vg = (gptr)ws + V_WS + b * V_BR + (head >> 2) * (256 * 1280) + h * 1280 +
                     ((head & 3) * 20 + (odd ? (r32 < 16 ? r32 + 4 : r32 - 16) : r32)) * 16;
    const u32x4 vc = r32 == 20 ? u32x4{0x3F803F80u, 0x3F803F80u, 0x3F803F80u, 0x3F803F80u} : u32x4{0u, 0u, 0u, 0u};
    float m = -INFINITY;
    f32x16 r = zero16;
#pragma unroll 1
    for (int ps = 0; ps < 2; ++ps) {
#pragma unroll 1
      for (int t = t0; t <= t1; ++t) {
        f32x16 s = MFMA32(*(__attribute__((address_space(1))) const bf16x8*)(kg1 + t * 512), q1, zero16);
        s = MFMA32K8(*(__attribute__((address_space(1))) const s16x4*)(kg2 + t * 512), q2, s);
        if (b) {
          const int d = (t1 - t) * 32 + r32 - 4 * h;
#pragma unroll
          for (int e = 0; e < 16; ++e) s[e] = (unsigned)(d - ((e & 3) + 8 * (e >> 2))) < 256u ? s[e] : -INFINITY;
        }
        if (ps == 0) { m = max16(s, m); continue; }
#pragma unroll
        for (int e = 0; e < 16; ++e) s[e] = __builtin_amdgcn_exp2f(s[e] - m);
        u32x4 va = vc, vb = vc;
        if (r32 < 20) { va = *(__attribute__((address_space(1))) const u32x4*)(vg + t * 5120); vb = *(__attribute__((address_space(1))) const u32x4*)(vg + t * 5120 + 2560); }
        r = smfmac(__builtin_bit_cast(bf16x8, va), lo8(s), r);
        r = smfmac(__builtin_bit_cast(bf16x8, vb), hi8(s), r);
      }
      m = xhalf_max(m);
    }
    return r;
  };
  auto unsafe = [&](const f32x16& r, float l) {
    float amax = 0.f;
#pragma unroll
    for (int e = 0; e < 12; ++e) amax = __builtin_fmaxf(amax, __builtin_fabsf(r[e]));
    return __any(!(l <= 1.2980742e33f) || !(l >= 8.4703295e-22f) || !(amax <= 3.0e38f));
  };
  char* const oA = ws + (O_WS + qt * 512);
  f32x32 oab, of; f32x8 of2;
  u32x4 lt;
  {
    f32x16 s0, pp;
    f32x32 fr;
    asm volatile(AT_ASM_MACROS
        "v_subrev_u32 v108, %[ab], v129\n\tds_read_b128 v[112:115], v79\n\tds_read_b64 v[116:117], v108\n\t"
        "ds_read_b128 v[80:83], v128\n\tds_read_b64 v[84:85], v129\n\tds_read_b128 v[94:97], v128 offset:7168\n\tds_read_b64 v[98:99], v129 offset:7168\n\t"
        "ds_read_b128 v[86:89], v130\n\tds_read_b128 v[100:103], v130 offset:7168\n\tds_read_b128 v[90:93], v130 offset:1024\n\t"
        "s_mov_b32 exec_lo, 0\n\tv_mov_b64 v[72:73], 0\n\tv_mov_b64 v[74:75], 0\n\ts_mov_b32 exec_lo, -1\n\t"
        "s_waitcnt lgkmcnt(7)\n\t"
        "qk1 32, 112, 64\n\tqk2 32, 116, 72\n\tqk1 48, 112, 68\n\tqk2 48, 116, 74\n\t"
        ".set i_, 0\n\t.rept 10\n\tv_mov_b64 v[i_:i_+1], 0\n\t.set i_, i_ + 2\n\t.endr\n\t"
        "mx 108, 32\n\tv_mov_b32 v109, v108\n\tmx 110, 48\n\tv_mov_b32 v111, v110\n\t"
        "v_permlane32_swap_b32 v108, v109\n\ts_waitcnt lgkmcnt(0)\n\tqk1 32, 80, 64\n\tv_permlane32_swap_b32 v110, v111\n\t"
        "v_max_f32 v108, v108, v109\n\tv_max_f32 v110, v110, v111\n\tv_add_f32 v108, 0x42600000, v108\n\tv_add_f32 v110, 0x42600000, v110\n\t"
        "s_mov_b32 exec_lo, 0\n\tv_cvt_pk_bf16_f32 v72, -v108, 0\n\tv_cvt_pk_bf16_f32 v74, -v110, 0\n\ts_mov_b32 exec_lo, -1\n\ts_nop 0\n\t"
        "qk2 32, 84, 72\n\t"
        ".set i_, 20\n\t.rept 6\n\tv_mov_b64 v[i_:i_+1], 0\n\t.set i_, i_ + 2\n\t.endr\n\t"
        "v_mov_b64 v[104:105], 0\n\tv_mov_b64 v[106:107], 0\n\t"
        "v_mov_b64 v[120:121], 0\n\tv_mov_b64 v[122:123], 0\n\tv_mov_b64 v[124:125], 0\n\tv_mov_b64 v[126:127], 0\n\t"
        AT_ASM_PURGE
        : "=&{v[0:31]}"(oab), "=&{v[112:127]}"(pp), "=&{v[32:47]}"(s0), "=&{v[80:111]}"(fr), "+{v[64:79]}"(qq), "+{v[128:143]}"(at)
        : [ab] "s"(grp * TILE_B)
        : "memory", "v48", "v49", "v50", "v51", "v52", "v53", "v54", "v55", "v56", "v57", "v58", "v59", "v60", "v61", "v62", "v63");
    STAMP(10);
    f32x16 maskF, maskD; u32x8 qv;
    at_a1(oab, s0, pp, fr, qq, at, ws, so, mytile, mytile + (qb ? 3 : 4) * CHUNK_B, tag, flag2, (0x7fu >> u) & 1u, ws + (QK_BR + qt * 512),
          &lane_tab, maskF, maskD, qv, lt, fsrc, 143360 + w * 1024 AT_STV_PASS);
    STAMP(11);
    {
      f32x16 ob;
      at_b(ob, maskF, maskD, qv, 3 * CHUNK_B + 2 * lw * TILE_B, qt, oA, qq, at, oab, s0, fr, pp, 143360 + w * 1024, fdst, ws);
      lt[0] += late[0]; lt[1] += late[1]; lt[2] += grp * 64;
      float l = row_sum(ob);
      if (__builtin_expect(unsafe(ob, l), 0)) {
        ob = exact(1, __builtin_bit_cast(bf16x8, u32x4{qv[0], qv[1], qv[2], qv[3]}), __builtin_bit_cast(s16x4, u32x2{qv[4], qv[5]}),
                   qb ? qt - 8 : 0, qt);
        l = row_sum(ob);
      }
      store_o<false>(ob, l, oA + O_BR, at[9], qq[14]);
    }
    STAMP(13);
    at[13] = __builtin_elementwise_min(qq[15], 24u * 16) * 64 + (FO_OFF + qb * 25 * 1024);
    at_a2(oab, s0, pp, fr, qq, at, ws, so, mytile, tag, flagO, lt, ws + (late[2] + 102400), w * 5120,
          81920 + __builtin_elementwise_min(3 * w, 17u) * 1024, late[2] + ((slot & 3) * 8 + w) * 3200, of, of2);
  }
  STAMP(12);
  {
    lds_ptr xb = (lds_ptr)lds + 3 * CHUNK_B + qq[15];
#pragma unroll
    for (int i = 0; i < 3; ++i) *(lds_f4)(xb + w * 3072 + i * 1024) = f32x4{oab[16 + 4 * i], oab[17 + 4 * i], oab[18 + 4 * i], oab[19 + 4 * i]};
    asm volatile("s_setprio 3\n\ts_waitcnt vmcnt(0) lgkmcnt(0)\n\ts_barrier" : "+{v[184:215]}"(of), "+{v[216:223]}"(of2) :: "memory");
    f32x16 o = {};
#pragma unroll
    for (int i = 0; i < 3; ++i) {
      const f32x4 r = *(lds_f4)(xb + (w ^ 4) * 3072 + i * 1024);
#pragma unroll
      for (int e = 0; e < 4; ++e) o[4 * i + e] = oab[4 * i + e] + r[e];
    }
    float l = row_sum(o);
    if (__builtin_expect(unsafe(o, l), 0)) {
      o = exact(0, __builtin_bit_cast(bf16x8, u32x4{qq[0], qq[1], qq[2], qq[3]}), __builtin_bit_cast(s16x4, u32x2{qq[8], qq[9]}), 0, 63);
      l = row_sum(o);
    }
    store_o<true>(o, l, oA, at[9], qq[14]);
  }
  STAMP(14);
  {
    const unsigned pos = __builtin_elementwise_min(7 * w, 43u) * 1024;
    proj_all(of, of2, qq[15], lt, ws + (late[2] + pos), pos, w * 3072, grp, ws, ptr(kc[6], kc[7]) + late[3],
             0xffffffffull << (32 * grp) AT_STV_PASS);
  }
  while (__builtin_amdgcn_s_memrealtime() - tstart < 6000) __builtin_amdgcn_s_sleep(1);
}

extern "C" void kernel_launch(void* const* d_in, const int* in_sizes, int n_in, void* d_out, int out_size, void* d_ws,
                              size_t ws_size, hipStream_t stream) {
  k_fused<<<256, 512, 0, stream>>>((const char*)d_in[0], (const char*)d_in[1], (const char*)d_in[2], (const char*)d_in[3],
                                   (const char*)d_in[4], (const char*)d_in[5], (char*)d_ws, (char*)d_out);
}
```

```cpp
#include <hip/hip_runtime.h>
#include <hip/hip_bf16.h>
#include <stdint.h>
#include <type_traits>

typedef __attribute__((ext_vector_type(8))) __bf16 bf16x8;
typedef __attribute__((ext_vector_type(16))) __bf16 bf16x16;
typedef __attribute__((ext_vector_type(4))) short s16x4;
typedef __attribute__((ext_vector_type(8))) short s16x8;
typedef __attribute__((ext_vector_type(2))) unsigned u32x2;
typedef __attribute__((ext_vector_type(4))) unsigned u32x4;
typedef __attribute__((ext_vector_type(4))) float f32x4;
typedef __attribute__((ext_vector_type(8))) float f32x8;
typedef __attribute__((ext_vector_type(16))) float f32x16;
typedef __attribute__((ext_vector_type(32))) float f32x32;
typedef __attribute__((address_space(3))) const char* lds_cptr;
typedef __attribute__((address_space(3))) char* lds_ptr;

#define OCT 33280
#define STR_(x) #x
#define STR(x) STR_(x)
constexpr size_t QK_WS = 0, QK_BR = (size_t)160 * OCT;
constexpr size_t V_WS = 2 * QK_BR, V_BR = (size_t)8 * 256 * 1280;
constexpr size_t O_WS = V_WS + 2 * V_BR, O_BR = (size_t)80 * OCT;
constexpr size_t WP_WS = O_WS + 2 * O_BR;
constexpr size_t CTL_WS = WP_WS + 2048 * 1024;
constexpr unsigned FQ_OFF = CTL_WS + 4096, FO_OFF = FQ_OFF + 2 * 30 * 64 * 128;
constexpr float QSCALE = 0.22360679774997896f * 1.4426950408889634f;

#define STAMP_DECL
#define STV_PUT(x)
#define STV_GET(x)
#define STAMP(i) do {} while (0)
#define STAMP_OUT(ws_, kid) do {} while (0)
__device__ __forceinline__ int lane_id() {
  int l; asm volatile("v_mbcnt_lo_u32_b32 %0, -1, 0\n\tv_mbcnt_hi_u32_b32 %0, -1, %0" : "=v"(l)); return l;
}
#define SBAR() __builtin_amdgcn_sched_barrier(0)
#define PIN(x) asm volatile("" : "+v"(x))
#define PINS(x) asm volatile("" : "+s"(x))
#define WAITV(N) asm volatile("s_waitcnt vmcnt(" #N ")" ::: "memory")
#define BARRIER() asm volatile("s_barrier" ::: "memory")
#define WAITV_BAR(N) asm volatile("s_waitcnt vmcnt(" #N ") lgkmcnt(0)\n\ts_barrier" ::: "memory")

#define XPOL ""
__device__ __forceinline__ void glds_xw(const char* x, const char* w, unsigned k, u32x4 o, unsigned lds) {
  unsigned keep;
  asm volatile("s_mov_b32 %0, m0\n\ts_mov_b32 m0, %7\n\ts_nop 0\n\tglobal_load_lds_dwordx4 %1, %5 " XPOL "\n\t"
               "global_load_lds_dwordx4 %2, %5 offset:1024 " XPOL "\n\ts_add_u32 m0, m0, 16384\n\ts_nop 0\n\tglobal_load_lds_dwordx4 %3, %6\n\t"
               "global_load_lds_dwordx4 %4, %6 offset:1024\n\ts_mov_b32 m0, %0"
               : "=&s"(keep) : "v"(o[0]), "v"(o[1]), "v"(o[2]), "v"(o[3]), "s"(x + k * 128), "s"(w + k * (64 * 3840)), "s"(lds) : "memory", "scc");
}
__device__ __forceinline__ unsigned pk_bf16(float lo, float hi) {
  typedef __attribute__((ext_vector_type(2))) float f32x2_;
  typedef __attribute__((ext_vector_type(2))) __bf16 bf16x2_;
  const f32x2_ v = {lo, hi};
  return __builtin_bit_cast(unsigned, __builtin_convertvector(v, bf16x2_));
}
__device__ __forceinline__ void st16(char* sbase, unsigned voff, u32x4 v) {
  asm volatile("global_store_dwordx4 %0, %1, %2 sc1\n\ts_nop 1" :: "v"(voff), "v"(v), "s"(sbase) : "memory");
}
__device__ __forceinline__ void st8lo(char* sbase, unsigned voff, u32x2 v) {
  asm volatile("s_mov_b32 exec_hi, 0\n\tglobal_store_dwordx2 %0, %1, %2 sc1\n\ts_mov_b32 exec_hi, -1" :: "v"(voff), "v"(v), "s"(sbase) : "memory");
}
__device__ __forceinline__ bf16x8 lds_b128(lds_cptr p) { return *(const __attribute__((address_space(3))) bf16x8*)p; }
__device__ __forceinline__ s16x4 lds_b64(lds_cptr p) { return *(const __attribute__((address_space(3))) s16x4*)p; }
__device__ __forceinline__ s16x4 lds_tr(lds_cptr p) {
  return __builtin_amdgcn_ds_read_tr16_b64_v4i16((__attribute__((address_space(3))) s16x4*)p);
}
__device__ __forceinline__ bf16x8 cat8(s16x4 a, s16x4 b) {
  s16x8 v = {a[0], a[1], a[2], a[3], b[0], b[1], b[2], b[3]};
  return __builtin_bit_cast(bf16x8, v);
}
#define MFMA32(a, b, c) __builtin_amdgcn_mfma_f32_32x32x16_bf16(a, b, c, 0, 0, 0)
#define MFMA32K8(a, b, c) __builtin_amdgcn_mfma_f32_32x32x8bf16_1k(a, b, c, 0, 0, 0)
#define MFMA16(a, b, c) __builtin_amdgcn_mfma_f32_16x16x32_bf16(a, b, c, 0, 0, 0)

typedef __attribute__((ext_vector_type(8))) unsigned u32x8;
typedef __attribute__((ext_vector_type(16))) unsigned u32x16;
#define AWAIT(d, d1, off) \
    "s_mov_b32 s48, 0x200000\n\t5:\n\ts_waitcnt vmcnt(0)\n\tv_cmp_ne_u64 vcc, s[50:51], v[" #d ":" #d1 "]\n\ts_sub_u32 s48, s48, 1\n\t" \
    "s_cbranch_vccz 4f\n\ts_cbranch_scc1 4f\n\ts_sleep 2\n\tglobal_load_dwordx2 v[" #d ":" #d1 "], v" #off ", s[54:55] sc1\n\ts_branch 5b\n\t4:\n\t"
#define G1_STAMP(i)
#define LPOS 0
#define NPOLL 22
#define NFLAG 8
#define NLD 8
#define TGAP 1
#define P2PRIO 0
#define NAW 28
#define KBAR 11
__device__ __forceinline__ void g1_all(unsigned vt1, unsigned kv1, unsigned vt2, u32x16& ad, u32x16& pk, u32x16& at, u32x4& cv4,
                                       const char* sX, const char* sW, unsigned off1, const char* sB, unsigned off2, unsigned m0v,
                                       unsigned long long tag, unsigned flag, char* ws, unsigned tile, u32x4 so, unsigned qoff) {
  unsigned keep;
  f32x32 d0, d1, d2, d3, d4; f32x16 d5, d6; f32x8 d7; f32x4 d8;
  asm volatile(
      ".macro pack vt\n\t.set i_, 0\n\t.rept 4\n\t"
      ".if \\vt\n\t.set e_, 8 * i_\n\t.set f_, 8 * i_ + 4\n\t.else\n\t.set e_, 4 * i_\n\t.set f_, 4 * i_ + 16\n\t.endif\n\t"
      "v_cvt_pk_bf16_f32 v[48+4*i_], v[e_], v[e_+1]\n\tv_cvt_pk_bf16_f32 v[49+4*i_], v[e_+2], v[e_+3]\n\t"
      "v_cvt_pk_bf16_f32 v[50+4*i_], v[f_], v[f_+1]\n\tv_cvt_pk_bf16_f32 v[51+4*i_], v[f_+2], v[f_+3]\n\t"
      ".set i_, i_ + 1\n\t.endr\n\t.set i_, 0\n\t.rept 4\n\t"
      "v_permlane16_swap_b32 v[48+4*i_], v[50+4*i_]\n\tv_permlane16_swap_b32 v[49+4*i_], v[51+4*i_]\n\t"
      ".set i_, i_ + 1\n\t.endr\n\t.endm\n\t"
      ".macro mm vt, c, x, w\n\t"
      ".if (\\x) < 104\n\t"
      ".if \\vt\n\tv_mfma_f32_16x16x32_bf16 v[\\c:\\c+3], v[\\x:\\x+3], v[\\w:\\w+3], 0\n\t"
      ".else\n\tv_mfma_f32_16x16x32_bf16 v[\\c:\\c+3], v[\\w:\\w+3], v[\\x:\\x+3], 0\n\t.endif\n\t"
      ".elseif \\vt\n\tv_mfma_f32_16x16x32_bf16 v[\\c:\\c+3], v[\\x:\\x+3], v[\\w:\\w+3], v[\\c:\\c+3]\n\t"
      ".else\n\tv_mfma_f32_16x16x32_bf16 v[\\c:\\c+3], v[\\w:\\w+3], v[\\x:\\x+3], v[\\c:\\c+3]\n\t.endif\n\t"
      ".endm\n\t"
      ".macro rx d, a, off\n\tds_read_b128 v[\\d:\\d+3], v\\a offset:\\off\n\tds_read_b128 v[\\d+4:\\d+7], v\\a offset:\\off+2048\n\t.endm\n\t"
      ".macro rw d, a, off\n\tds_read_b64_tr_b16 v[\\d:\\d+1], v\\a offset:\\off\n\tds_read_b64_tr_b16 v[\\d+2:\\d+3], v\\a offset:\\off+512\n\t.endm\n\t"
      ".macro bump lo, n, val\n\t.set bi_, \\lo\n\t.rept \\n\n\tv_add_u32 v[bi_], \\val, v[bi_]\n\t.set bi_, bi_+1\n\t.endr\n\t.endm\n\t"
      ".macro src2\n\ts_add_u32 s40, s44, s47\n\ts_addc_u32 s41, s45, 0\n\t"
      "s_add_u32 s47, s47, 2*64*3840\n\ts_cmp_ge_u32 s47, 640*3840\n\ts_cselect_b32 s48, 640*3840, 0\n\ts_sub_u32 s47, s47, s48\n\t.endm\n\t"
      ".macro ld1 i\n\t.if st_ < 7\n\t"
      ".if \\i < 2\n\tglobal_load_lds_dwordx4 v[64+\\i], s[40:41] offset:1024*\\i " XPOL "\n\t.if \\i == 1\n\ts_add_u32 m0, m0, 16384\n\t.endif\n\t"
      ".else\n\tglobal_load_lds_dwordx4 v[64+\\i], s[82:83] offset:1024*(\\i-2)\n\t.endif\n\t"
      ".else\n\t.if st_ == 9\n\ts_cbranch_scc1 8f\n\t.endif\n\tglobal_load_lds_dwordx4 v[68+\\i], s[40:41] offset:1024*\\i\n\t8:\n\t.endif\n\t.endm\n\t"
      ".macro p1body vt\n\t"
      ".set st_, 0\n\t"
      ".rept 10\n\t"
      ".set xr_, 96 + 16 * st_\n\t"
      ".set o_, ((st_ %% 5) & 1) * 32768\n\t"
      ".set n_, (((st_ + 1) %% 5) & 1) * 32768 + 16384\n\t.if st_ == 9\n\t.set n_, 0\n\t.endif\n\t"
      "s_waitcnt lgkmcnt(6)\n\t"
      "mm \\vt, 0, xr_, 32\n\trw 48, 76, o_+16384+8192\n\t"
      "mm \\vt, 16, xr_+4, 32\n\trx xr_+8, 74, o_\n\ts_waitcnt lgkmcnt(8)\n\t"
      "mm \\vt, 4, xr_, 36\n\trw 52, 77, o_+16384+8192\n\t"
      "mm \\vt, 20, xr_+4, 36\n\trw 56, 78, o_+16384+8192\n\ts_waitcnt lgkmcnt(10)\n\t"
      "mm \\vt, 8, xr_, 40\n\trw 60, 79, o_+16384+8192\n\t"
      "mm \\vt, 24, xr_+4, 40\n\ts_waitcnt lgkmcnt(10)\n\t"
      "mm \\vt, 12, xr_, 44\n\t"
      ".if st_ < 7\n\t"
      "s_add_u32 s40, s42, s36\n\ts_addc_u32 s41, s43, 0\n\ts_mul_i32 s48, s36, 64*30\n\ts_add_u32 s82, s84, s48\n\ts_addc_u32 s83, s85, 0\n\t"
      "s_add_u32 s36, s36, 128\n\ts_cmp_eq_u32 s36, 1280\n\ts_cselect_b32 s36, 0, s36\n\t"
      ".else\n\tsrc2\n\t.endif\n\t"
      "mm \\vt, 28, xr_+4, 44\n\t"
      ".if st_ < 7\n\ts_add_u32 m0, s37, ((st_ + 3) %% 5) * 32768\n\t.else\n\ts_add_u32 m0, s46, ((st_ + 3) %% 5) * 32768\n\t.endif\n\t"
      ".if (st_ %% 5) == 1 || (st_ %% 5) == 3\n\tbump 76, 4, 0x10000\n\tbump 73, 2, 0x10000\n\t.endif\n\t"
      ".if (st_ %% 5) == 4\n\tbump 76, 4, 0xfffe0000\n\t.if st_ < 9\n\tbump 73, 2, 0xfffe0000\n\t.endif\n\t.endif\n\t"
      "s_waitcnt lgkmcnt(6)\n\t"
      "mm \\vt, 0, xr_+8, 48\n\t"
      "mm \\vt, 16, xr_+12, 48\n\t"
      ".if st_ < 9\n\ts_waitcnt vmcnt(4) lgkmcnt(0)\n\t.else\n\ts_cmp_eq_u32 %[vt2], 2\n\ts_cbranch_scc1 8f\n\ts_waitcnt vmcnt(4)\n\t8:\n\ts_waitcnt lgkmcnt(0)\n\t.endif\n\ts_barrier\n\t"
      "mm \\vt, 4, xr_+8, 52\n\trw 32, 76, n_\n\t"
      "ld1 0\n\t"
      "mm \\vt, 20, xr_+12, 52\n\t"
      ".if st_ < 9\n\trx xr_+16, 73, n_-16384\n\t.endif\n\t"
      "ld1 1\n\t"
      "mm \\vt, 8, xr_+8, 56\n\trw 36, 77, n_\n\t"
      "ld1 2\n\t"
      "mm \\vt, 24, xr_+12, 56\n\trw 40, 78, n_\n\t"
      "ld1 3\n\t"
      "mm \\vt, 12, xr_+8, 60\n\trw 44, 79, n_\n\t"
      "mm \\vt, 28, xr_+12, 60\n\t"
      ".set st_, st_ + 1\n\t"
      ".endr\n\t"
      "v_mov_b64 v[64:65], v[80:81]\n\tv_add_u32 v66, " STR(OCT) "*4 - (" STR(OCT) "*4 - 2560) * \\vt, v80\n\tv_add_u32 v67, " STR(OCT) "*4 - (" STR(OCT) "*4 - 2560) * \\vt, v81\n\t"
      ".endm\n\t"
      ".macro tdk i\n\t"
      "s_add_u32 m0, s72, \\i * 28672\n\ts_add_u32 s40, s54, s[76+2*\\i]\n\ts_addc_u32 s41, s55, 0\n\t"
      "global_load_lds_dwordx4 v137, s[40:41] sc1\n\ts_mov_b32 exec_hi, 0\n\tglobal_load_lds_dwordx4 v138, s[40:41] offset:1024 sc1\n\ts_mov_b32 exec_hi, -1\n\t.endm\n\t"
      ".macro tdv i\n\t"
      "s_add_u32 s40, s54, s[77+2*\\i]\n\ts_addc_u32 s41, s55, 0\n\ts_mov_b32 exec_lo, 0xfffff\n\ts_mov_b32 exec_hi, 0xfffff\n\t"
      "global_load_lds_dwordx4 v139, s[40:41] offset:1536 sc1\n\tglobal_load_lds_dwordx4 v140, s[40:41] offset:2560 sc1\n\ts_mov_b64 exec, -1\n\t"
      ".endm\n\t"
      ".macro slot n, ld=1\n\t.set n_, \\n\n\t"
      ".set l_, n_ - %c[nld]\n\t"
      ".if n_ == %c[nflag] && \\ld\n\t.set y_, l_\n\t.if y_ < 0\n\t.set y_, 0\n\t.endif\n\t.if y_ > 8\n\t.set y_, 8\n\t.endif\n\t"
      "v_mov_b32 v96, s50\n\tv_mov_b32 v97, s51\n\tv_mov_b32 v98, s52\n\ts_mov_b64 exec, 1\n\ts_waitcnt vmcnt(y_)\n\t"
      "global_store_dwordx2 v98, v[96:97], s[54:55] sc1\n\ts_mov_b64 exec, -1\n\t.endif\n\t"
      ".if l_ >= 0 && l_ < 8 && \\ld\n\t"
      ".if (l_ & 3) == 0\n\ts_add_u32 m0, s46, (3 + l_ / 4) * 32768\n\tsrc2\n\t.endif\n\t"
      "global_load_lds_dwordx4 v[68+(l_&3)], s[40:41] offset:1024*(l_&3)\n\t"
      ".endif\n\t"
      ".if n_ == 12\n\tv_mov_b64 v[128:129], v[84:85]\n\tv_mov_b32 v130, v86\n\tv_add_u32 v131, 57344, v84\n\tv_add_u32 v132, 57344, v85\n\t.endif\n\t"
      ".if n_ == 13\n\tv_add_u32 v133, 57344, v86\n\tv_add_u32 v134, 114688, v84\n\tv_add_u32 v135, 114688, v85\n\tv_add_u32 v136, 114688, v86\n\t.endif\n\t"
      ".if n_ == 14\n\tv_mov_b32 v137, v87\n\tv_mov_b64 v[138:139], v[88:89]\n\tv_add_u32 v140, 1536, v89\n\tv_mov_b32 v141, v90\n\t.endif\n\t"
      ".if n_ == 15\n\tv_mov_b32 v104, v92\n\tv_mov_b32 v105, v92\n\tv_mov_b32 v106, v92\n\tv_mov_b32 v107, v92\n\t.endif\n\t"
      ".if n_ == 2 * %c[kbar] + 1\n\tds_write_b128 v91, v[104:107]\n\tds_write_b128 v91, v[104:107] offset:28672\n\t.endif\n\t"
      ".if n_ == %c[npoll]\n\tglobal_load_dwordx2 v[100:101], v72, s[54:55] sc1\n\t.endif\n\t"
      ".if n_ == %c[naw]\n\t" AWAIT(100, 101, 72) G1_STAMP(6) "tdk 0\n\t.endif\n\t"
      ".if n_ == %c[naw] + 1\n\ttdv 0\n\t.endif\n\t"
      ".if n_ == %c[naw] + 2\n\ts_add_u32 s40, s54, s80\n\ts_addc_u32 s41, s55, 0\n\t"
      "global_load_dwordx4 v[64:67], v137, s[40:41] sc1\n\tglobal_load_dwordx2 v[72:73], v93, s[40:41] sc1\n\t.endif\n\t"
      ".if n_ == %c[naw] + 3\n\ts_xor_b32 s48, s80, 512\n\ts_add_u32 s40, s54, s48\n\ts_addc_u32 s41, s55, 0\n\t"
      "global_load_dwordx4 v[68:71], v137, s[40:41] sc1\n\tglobal_load_dwordx2 v[74:75], v93, s[40:41] sc1\n\t.endif\n\t"
      ".if n_ == %c[naw] + 4 + %c[gap]\n\ttdk 1\n\t.endif\n\t"
      ".if n_ == %c[naw] + 5 + 2 * %c[gap]\n\ttdv 1\n\t.endif\n\t"
      ".endm\n\t"
      ".macro st1 i\n\t.if k_ == 0\n\tglobal_store_dwordx4 v[64+\\i], v[48+4*\\i:51+4*\\i], s[54:55] sc1\n\t.endif\n\t.endm\n\t"
      ".macro p2body vt\n\t"
      ".set k_, 0\n\t"
      ".rept 20\n\t"
      ".set xr_, 96 + 8 * k_\n\t"
      ".set c_, 32 + 16 * (k_ & 1)\n\t"
      ".set d_, 48 - 16 * (k_ & 1)\n\t"
      ".set o_, ((k_ + 1) & 7) * 8192\n\t"
      ".if k_ == 3\n\ts_waitcnt vmcnt(4)\n\ts_barrier\n\t.endif\n\t"
      ".if %c[prio] && k_ >= 3 && k_ < 19 && (k_ & 1)\n\ts_setprio 3 - (((k_ - 3) / 2) & 3)\n\t.endif\n\t"
      ".if k_ == %c[kbar]\n\ts_waitcnt vmcnt(0)\n\ts_barrier\n\t.endif\n\t"
      ".if k_ < 19\n\t"
      ".if ((k_ + 1) & 7) == 0\n\tbump 76, 4, 0x10000\n\t.endif\n\t"
      "s_waitcnt lgkmcnt(6)\n\t"
      "mm \\vt, 0, xr_, c_\n\tst1 0\n\trw d_, 76, o_\n\t"
      "mm \\vt, 16, xr_+4, c_\n\tslot 2*k_\n\ts_waitcnt lgkmcnt(6)\n\t"
      "mm \\vt, 4, xr_, c_+4\n\tst1 1\n\trw d_+4, 77, o_\n\t"
      "mm \\vt, 20, xr_+4, c_+4\n\ts_waitcnt lgkmcnt(6)\n\t"
      "mm \\vt, 8, xr_, c_+8\n\tst1 2\n\trw d_+8, 78, o_\n\t"
      "mm \\vt, 24, xr_+4, c_+8\n\tslot 2*k_+1\n\ts_waitcnt lgkmcnt(6)\n\t"
      "mm \\vt, 12, xr_, c_+12\n\tst1 3\n\trw d_+12, 79, o_\n\t"
      "mm \\vt, 28, xr_+4, c_+12\n\t"
      ".else\n\t"
      "s_waitcnt lgkmcnt(0)\n\t"
      "mm \\vt, 0, xr_, c_\n\tmm \\vt, 16, xr_+4, c_\n\tmm \\vt, 4, xr_, c_+4\n\tmm \\vt, 20, xr_+4, c_+4\n\t"
      "mm \\vt, 8, xr_, c_+8\n\tmm \\vt, 24, xr_+4, c_+8\n\tmm \\vt, 12, xr_, c_+12\n\tmm \\vt, 28, xr_+4, c_+12\n\t"
      "s_setprio 3\n\tv_add_u32 v80, " STR(OCT) "*4 - (" STR(OCT) "*4 - 2560) * \\vt, v82\n\tv_add_u32 v81, " STR(OCT) "*4 - (" STR(OCT) "*4 - 2560) * \\vt, v83\n\ts_nop 4\n\tpack \\vt\n\t"
      ".endif\n\t"
      ".set k_, k_ + 1\n\t"
      ".endr\n\t"
      ".endm\n\t"
      "rw 32, 76, 16384\n\trx 96, 73, 0\n\trw 36, 77, 16384\n\trw 40, 78, 16384\n\trw 44, 79, 16384\n\t"
      "s_mov_b32 %[keep], m0\n\t"
      "s_cmp_lg_u32 %[vt1], 0\n\ts_cbranch_scc1 1f\n\t"
      "p1body 0\n\t"
      "s_mov_b32 s40, %[sc]\n\ts_cmp_lg_u32 %[kv1], 0\n\ts_nop 2\n\ts_cbranch_scc1 3f\n\t"
      ".set i_, 0\n\t.rept 16\n\tv_pk_mul_f32 v[i_:i_+1], v[i_:i_+1], s[40:41] op_sel_hi:[1,0]\n\t.set i_, i_ + 2\n\t.endr\n\t"
      "3:\n\tpack 0\n\ts_branch 2f\n\t"
      "1:\n\tp1body 1\n\ts_nop 4\n\tpack 1\n\t"
      "2:\n\t" G1_STAMP(3)
      "s_cmp_eq_u32 %[vt2], 1\n\ts_cbranch_scc1 1f\n\ts_cmp_eq_u32 %[vt2], 2\n\ts_cbranch_scc0 6f\n\t"
      ".set k_, 0\n\tst1 0\n\tst1 1\n\tst1 2\n\tst1 3\n\tslot 12, 0\n\tslot 13, 0\n\tslot 14, 0\n\tslot 15, 0\n\t"
      "v_mov_b32 v96, s50\n\tv_mov_b32 v97, s51\n\tv_mov_b32 v98, s52\n\t"
      "s_waitcnt vmcnt(0) lgkmcnt(0)\n\ts_mov_b64 exec, 1\n\tglobal_store_dwordx2 v98, v[96:97], s[54:55] sc1\n\ts_mov_b64 exec, -1\n\ts_barrier\n\t"
      "slot %c[npoll]\n\t"
      "slot 2*%c[kbar]+1\n\tslot %c[naw]\n\tslot %c[naw]+1\n\tslot %c[naw]+2\n\tslot %c[naw]+3\n\ttdk 1\n\ttdv 1\n\t"
      "s_waitcnt vmcnt(0)\n\ts_setprio 3\n\ts_branch 3f\n\t"
      "6:\n\tp2body 0\n\ts_branch 2f\n\t"
      "1:\n\tp2body 1\n\t"
      "2:\n\tglobal_store_dwordx4 v82, v[48:51], s[54:55] sc1\n\tglobal_store_dwordx4 v83, v[52:55], s[54:55] sc1\n\t"
      "global_store_dwordx4 v80, v[56:59], s[54:55] sc1\n\tglobal_store_dwordx4 v81, v[60:63], s[54:55] sc1\n\t"
      "3:\n\ts_mov_b32 m0, %[keep]\n\tv_mov_b32 v76, 0xdddd\n\tv_mov_b32 v77, v94\n\tv_mov_b32 v78, v93\n\t"
      "v_mbcnt_lo_u32_b32 v79, -1, 0\n\tv_mbcnt_hi_u32_b32 v79, -1, v79\n\tv_lshlrev_b32 v79, 4, v79\n\t"
      "s_waitcnt vmcnt(8) lgkmcnt(0)\n\t"
      ".purgem ld1\n\t.purgem pack\n\t.purgem mm\n\t.purgem rx\n\t.purgem rw\n\t.purgem bump\n\t.purgem src2\n\t.purgem p1body\n\t.purgem tdk\n\t.purgem tdv\n\t"
      ".purgem slot\n\t.purgem st1\n\t.purgem p2body\n\t"
      : "+{v[64:79]}"(ad), "+{v[80:95]}"(pk), "=&{v[128:143]}"(at), "=&{v[104:107]}"(cv4), [keep] "=&s"(keep), "+{s36}"(off1), "+{s47}"(off2),
        "=&{v[0:31]}"(d0), "=&{v[32:63]}"(d1), "=&{v[160:191]}"(d2), "=&{v[192:223]}"(d3), "=&{v[224:255]}"(d4), "=&{v[112:127]}"(d5),
        "=&{v[144:159]}"(d6), "=&{v[96:103]}"(d7), "=&{v[108:111]}"(d8)
      : [vt1] "s"(vt1), [kv1] "s"(kv1), [vt2] "s"(vt2), "{s[42:43]}"(sX), "{s[84:85]}"(sW), "{s37}"(m0v >> 1), "{s[44:45]}"(sB), "{s46}"(m0v),
        "{s[50:51]}"(tag), "{s52}"(flag), "{s[54:55]}"(ws), "{s72}"(tile), "{s[76:79]}"(so), "{s80}"(qoff),
        [sc] "n"(__builtin_bit_cast(unsigned, QSCALE)), [npoll] "n"(NPOLL), [nflag] "n"(NFLAG), [nld] "n"(NLD), [prio] "n"(P2PRIO), [gap] "n"(TGAP), [naw] "n"(NAW), [kbar] "n"(KBAR)
      : "memory", "scc", "vcc", "s40", "s41", "s48", "s82", "s83");
}

__device__ __forceinline__ u32x2 g1_offs(unsigned br, unsigned cg, unsigned T, unsigned lane) {
  const unsigned l16 = (lane & 31) * 16, h = lane >> 5;
  const unsigned a = h * OCT + l16 + (br * (unsigned)QK_BR + cg * (8 * OCT) + T * 512);
  const unsigned G = (cg - 20) * 4 + ((lane >> 4) & 1);
  const unsigned b = h * 1280 + l16 + ((unsigned)V_WS + br * (unsigned)V_BR + (cg - 20) * 1024 + T * 5120);
  return cg < 20 ? u32x2{a, a + 2 * OCT}
                 : u32x2{((G * 52) >> 8) * (256 * 1280 - 1280) + b, (((G + 2) * 52) >> 8) * (256 * 1280 - 1280) + b + 512};
}

constexpr int TILE_B = 3584, K2_OFF = 1024, V_OFF = 1536;
constexpr int CHUNK_B = 8 * TILE_B;

__device__ __forceinline__ f32x16 smfmac(bf16x8 a, f32x8 p, f32x16 c) {
  return __builtin_amdgcn_smfmac_f32_32x32x32_bf16(a, __builtin_bit_cast(bf16x16, p), c, 0xDDDD, 0, 0);
}
__device__ __forceinline__ f32x8 lo8(const f32x16& s) { return __builtin_shufflevector(s, s, 0, 1, 2, 3, 4, 5, 6, 7); }
__device__ __forceinline__ f32x8 hi8(const f32x16& s) { return __builtin_shufflevector(s, s, 8, 9, 10, 11, 12, 13, 14, 15); }
#define MX3(a, b, c) __builtin_fmaxf(__builtin_fmaxf((a), (b)), (c))
__device__ __forceinline__ float max16(const f32x16& s, float m) {
#pragma unroll
  for (int r = 0; r < 16; r += 2) m = MX3(m, s[r], s[r + 1]);
  return m;
}
__device__ __forceinline__ float xhalf_max(float m) {
  auto rr = __builtin_amdgcn_permlane32_swap(__float_as_uint(m), __float_as_uint(m), false, false);
  return __builtin_fmaxf(__uint_as_float(rr[0]), __uint_as_float(rr[1]));
}
__device__ __forceinline__ float row_sum(const f32x16& o) {
  auto rr = __builtin_amdgcn_permlane32_swap(__float_as_uint(o[8]), __float_as_uint(o[8]), false, false);
  return __uint_as_float(rr[1]);
}
template <bool MARK>
__device__ __forceinline__ void store_o(const f32x16& o, float l, char* base, unsigned lo1, unsigned lo2) {
  const float rl = __builtin_amdgcn_rcpf(l);
  const unsigned a0 = pk_bf16(o[0] * rl, o[1] * rl), a1 = pk_bf16(o[2] * rl, o[3] * rl);
  const unsigned b0 = pk_bf16(o[4] * rl, o[5] * rl), b1 = pk_bf16(o[6] * rl, o[7] * rl);
  const auto r0 = __builtin_amdgcn_permlane32_swap(a0, b0, false, false);
  const auto r1 = __builtin_amdgcn_permlane32_swap(a1, b1, false, false);
  u32x4 v = {r0[0], r1[0], r0[1], r1[1]};
  u32x2 c = {pk_bf16(o[8] * rl, o[9] * rl), pk_bf16(o[10] * rl, o[11] * rl)};
  if (MARK) {
    v = __builtin_elementwise_min(v, u32x4{~1u, ~1u, ~1u, ~1u}); c = __builtin_elementwise_min(c, u32x2{~1u, ~1u});
  }
  st16(base, lo1, v);
  st8lo(base, lo2, c);
}

__device__ __forceinline__ void poll_flags(const char* ws, unsigned off, unsigned long long tag) {
  asm volatile("global_load_dwordx2 v[124:125], v126, s[54:55] sc1\n\t" AWAIT(124, 125, 126)
               :: "{v126}"(off), "{s[54:55]}"(ws), "{s[50:51]}"(tag) : "memory", "scc", "vcc", "s48", "v124", "v125");
}
__device__ __forceinline__ void put_flag(char* ws, unsigned off, unsigned long long tag) {
  asm volatile("s_mov_b64 exec, 1\n\tglobal_store_dwordx2 %0, %1, %2 sc1\n\ts_mov_b64 exec, -1" :: "v"(off), "v"(tag), "s"(ws) : "memory");
}
#define AT_ASM_MACROS \
    ".macro mx d, s\n\tv_max3_f32 v[\\d], v[\\s], v[\\s+1], v[\\s+2]\n\t.set i_, \\s + 3\n\t.rept 6\n\tv_max3_f32 v[\\d], v[\\d], v[i_], v[i_+1]\n\t" \
    ".set i_, i_ + 2\n\t.endr\n\tv_max_f32 v[\\d], v[\\d], v[\\s+15]\n\t.endm\n\t" \
    ".macro xp d, s\n\tv_exp_f32 v[\\d], v[\\s]\n\tv_exp_f32 v[\\d+1], v[\\s+1]\n\tv_exp_f32 v[\\d+2], v[\\s+2]\n\tv_exp_f32 v[\\d+3], v[\\s+3]\n\t.endm\n\t" \
    ".macro smf o, v, p\n\tv_smfmac_f32_32x32x32_bf16 v[\\o:\\o+15], v[\\v:\\v+3], v[\\p:\\p+7], v76\n\t.endm\n\t" \
    ".macro qk1 s, k, q\n\tv_mfma_f32_32x32x16_bf16 v[\\s:\\s+15], v[\\k:\\k+3], v[\\q:\\q+3], 0\n\t.endm\n\t" \
    ".macro qk1c s, k, q, c\n\tv_mfma_f32_32x32x16_bf16 v[\\s:\\s+15], v[\\k:\\k+3], v[\\q:\\q+3], v[\\c:\\c+15]\n\t.endm\n\t" \
    ".macro qk2 s, k, q\n\tv_mfma_f32_32x32x8_bf16 v[\\s:\\s+15], v[\\k:\\k+1], v[\\q:\\q+1], v[\\s:\\s+15]\n\t.endm\n\t"
#define AT_ASM_PURGE ".purgem mx\n\t.purgem xp\n\t.purgem smf\n\t.purgem qk1\n\t.purgem qk1c\n\t.purgem qk2\n\t"

#define AT_A_TEXT AT_ASM_MACROS \
      ".macro pos j\n\t.set sl_, (\\j) >> 2\n\t.if sl_ >= 3\n\t.set sl_, sl_ - 3\n\t.endif\n\t" \
      ".set ar_, 128 + 3 * (sl_ >> 1)\n\t.set of_, (sl_ & 1) * 28672 + ((\\j) & 3) * 7168\n\t.endm\n\t" \
      ".macro dma part\n\t" \
      ".if di_ >= 0\n\t" \
      ".if \\part == 0\n\t" \
      ".if di_ == 1\n\ts_mov_b32 m0, s73\n\t.else\n\ts_add_u32 m0, s72, ((di_ + 2) %% 5) * 28672\n\t.endif\n\t" \
      "s_add_u32 s40, s54, s[56+2*di_]\n\ts_addc_u32 s41, s55, 0\n\t" \
      "global_load_lds_dwordx4 v137, s[40:41] sc1\n\t" \
      ".elseif \\part == 1\n\t" \
      "s_mov_b32 exec_hi, 0\n\tglobal_load_lds_dwordx4 v138, s[40:41] offset:1024 sc1\n\ts_mov_b32 exec_hi, -1\n\t" \
      "s_add_u32 s40, s54, s[57+2*di_]\n\ts_addc_u32 s41, s55, 0\n\t" \
      ".else\n\t" \
      "s_mov_b32 exec_lo, 0xfffff\n\ts_mov_b32 exec_hi, 0xfffff\n\t" \
      "global_load_lds_dwordx4 v139, s[40:41] offset:1536 sc1\n\tglobal_load_lds_dwordx4 v140, s[40:41] offset:2560 sc1\n\t" \
      "s_mov_b64 exec, -1\n\t" \
      ".endif\n\t.endif\n\t" \
        \
      ".set n_, -1\n\t" \
      ".if j_ >= 22 && j_ < 25 && 3 * (j_ - 22) + \\part < 8\n\t.set n_, 3 * (j_ - 22) + \\part\n\t.endif\n\t" \
      ".if j_ >= 28 && 3 * (j_ - 28) + \\part < 5\n\t.set n_, 10 + 3 * (j_ - 28) + \\part\n\t.endif\n\t" \
      ".if n_ >= 0\n\t.set pc_, n_ %% 5\n\t" \
      ".if n_ == 0 || n_ == 10\n\ts_add_u32 m0, s44, n_ * 4096\n\ts_add_u32 s40, s42, m0\n\ts_addc_u32 s41, s43, 0\n\t.endif\n\t" \
      ".if n_ == 5\n\ts_add_u32 m0, s45, 143360 - 81920\n\ts_add_u32 s40, s42, s45\n\ts_addc_u32 s41, s43, 0\n\t.endif\n\t" \
      ".if pc_ == 4\n\ts_add_u32 m0, m0, 4096\n\ts_add_u32 s40, s40, 4096\n\ts_addc_u32 s41, s41, 0\n\t.endif\n\t" \
      "global_load_lds_dwordx4 v79, s[40:41] offset:1024*(pc_&3) sc1\n\t.endif\n\t" \
      ".endm\n\t" \
      ".macro flg msk\n\tv_mov_b32 v142, s50\n\tv_mov_b32 v143, s51\n\tv_mov_b32 v108, s52\n\ts_mov_b64 exec, \\msk\n\ts_cbranch_execz 8f\n\ts_waitcnt vmcnt(0)\n\t" \
      "global_store_dwordx2 v108, v[142:143], s[54:55] sc1\n\t8:\n\ts_mov_b64 exec, -1\n\t.endm\n\t" \
      ".macro tabl d, r\n\tglobal_load_dwordx4 v[\\d:\\d+3], v79, s[40:41] offset:1024*(\\r)\n\t.endm\n\t" \
      ".macro old q\n\t.if (\\q) >= 0 && (\\q) < 10\n\tglobal_load_dwordx4 v[184+4*(\\q):187+4*(\\q)], v108, s[54:55] sc1\n\tv_add_u32 v108, " STR(OCT) "*4, v108\n\t.endif\n\t.endm\n\t" \
      "s_mov_b32 %[keep], m0\n\t" \
      ".set j_, %c[j0]\n\t" \
      ".rept %c[j1] - %c[j0]\n\t" \
      ".set a_, 80 + 14 * (j_ & 1)\n\t.set b_, 94 - 14 * (j_ & 1)\n\t" \
      ".if j_ == 4\n\t" AWAIT(142, 143, 141) ".set di_, 0\n\tdma 0\n\tdma 1\n\tdma 2\n\t.endif\n\t" \
      ".if j_ == 6\n\ts_waitcnt vmcnt(10)\n\ts_barrier\n\t.endif\n\t" \
      ".if j_ == 10\n\ts_waitcnt vmcnt(1)\n\ts_barrier\n\t.endif\n\t" \
      ".if j_ == 14 || j_ == 18\n\ts_waitcnt vmcnt(0)\n\ts_barrier\n\t.endif\n\t" \
      ".if j_ == 22\n\t" AWAIT(142, 143, 141) ".endif\n\t" \
      ".if j_ == 28\n\t" AWAIT(142, 143, 252) "v_add_u32 v108, %c[obr], v253\n\t.endif\n\t" \
      ".if j_ == 28 || j_ == 22 || j_ == 2\n\ts_barrier\n\t.endif\n\t" \
      AT_STAMP \
      ".if j_ < 2\n\ts_setprio 1 - j_\n\t" \
      ".elseif j_ == 10 || j_ == 11\n\ts_setprio 3\n\t.elseif j_ == 12 || j_ == 13\n\ts_setprio 0\n\t" \
      ".elseif j_ < 22\n\ts_setprio 3 - ((j_ + 2) & 3)\n\t.elseif j_ >= 28\n\ts_setprio 31 - j_\n\t" \
      ".elseif j_ != 23 && j_ != 25\n\ts_setprio 3 - (j_ - 21) / 2\n\t.endif\n\t" \
      ".if j_ == 14\n\tflg 1\n\t" \
      "v_mov_b32 v108, 0x3f803f80\n\tv_mov_b32 v109, v108\n\tv_mov_b32 v110, v108\n\tv_mov_b32 v111, v108\n\ts_mov_b64 exec, 15\n\t" \
      "ds_write_b128 v77, v[108:111]\n\ts_mov_b64 exec, -1\n\t.endif\n\t" \
      ".set di_, -1\n\t" \
      ".if j_ == 4 || j_ == 5 || j_ == 6\n\t.set di_, j_ - 3\n\t.endif\n\t.if j_ == 10\n\t.set di_, 4\n\t.endif\n\t" \
      ".if j_ >= 14 && j_ < 17\n\t.set di_, j_ - 9\n\t.endif\n\t" \
        \
      "pos j_+2\n\t" \
      "qk1 48, a_, 68\n\t.if j_ < 30\n\tds_read_b128 v[a_:a_+3], v[ar_] offset:of_\n\t.endif\n\told 4*(j_-28)\n\txp 112, 32\n\t" \
      "qk2 48, a_+4, 74\n\t.if j_ < 30\n\tds_read_b64 v[a_+4:a_+5], v[ar_+1] offset:of_\n\t.endif\n\told 4*(j_-28)+1\n\txp 116, 36\n\t" \
      "pos j_+1\n\t" \
      "smf 16, b_+10, 120\n\t.if j_ < 31\n\tds_read_b128 v[b_+10:b_+13], v[ar_+2] offset:of_+1024\n\t.endif\n\told 4*(j_-28)+2\n\txp 120, 40\n\t" \
      "smf 0, a_+6, 112\n\t.if j_ < 30\n\ts_waitcnt lgkmcnt(3)\n\t.elseif j_ == 30\n\ts_waitcnt lgkmcnt(1)\n\t.else\n\ts_waitcnt lgkmcnt(0)\n\t.endif\n\t" \
      "old 4*(j_-28)+3\n\t" \
      ".if j_ == 5\n\tglobal_load_dwordx4 v[236:239], v137, s[42:43] sc1\n\tglobal_load_dwordx2 v[240:241], v78, s[42:43] sc1\n\t.endif\n\t" \
      "xp 124, 44\n\t" \
        \
      ".if j_ < 31\n\tqk1 32, b_, 64\n\t.endif\n\tdma 0\n\t" \
      ".if j_ == 0\n\tflg s[74:75]\n\t.endif\n\t" \
      ".if j_ == 3\n\tglobal_load_dwordx2 v[142:143], v141, s[54:55] sc1\n\t.endif\n\t" \
      ".if j_ == 7\n\ts_mov_b64 s[40:41], s[44:45]\n\ttabl 252, 0\n\ttabl 160, 1\n\t.endif\n\t" \
      ".if j_ == 8\n\ttabl 224, 2\n\ttabl 228, 3\n\t.endif\n\t" \
      ".if j_ == 20\n\tglobal_load_dwordx2 v[142:143], v141, s[54:55] sc1\n\t.endif\n\t" \
      ".if j_ == 25\n\ts_mov_b32 m0, 40960\n\tv_min_u32 v140, 384, v79\n\tv_lshl_add_u32 v140, v140, 3, s46\n\tglobal_load_lds_dword v140, s[54:55] sc1\n\t.endif\n\t" \
      ".if j_ == 26\n\tglobal_load_dwordx2 v[142:143], v252, s[54:55] sc1\n\t.endif\n\t" \
      "xp 112, 48\n\t" \
      ".if j_ < 31\n\tqk2 32, b_+4, 72\n\t.endif\n\tdma 1\n\t" \
      ".if j_ == 7\n\ttabl 164, 2\n\ttabl 168, 3\n\ts_add_u32 s40, s40, 4096\n\ts_addc_u32 s41, s41, 0\n\t.endif\n\t" \
      ".if j_ == 8\n\ts_add_u32 s40, s40, 4096\n\ts_addc_u32 s41, s41, 0\n\ttabl 232, 0\n\t.endif\n\t" \
      "xp 116, 52\n\t" \
      "smf 0, a_+10, 120\n\tdma 2\n\t" \
      ".if j_ == 7\n\ttabl 172, 0\n\ttabl 220, 1\n\t.endif\n\t" \
      ".if j_ == 9\n\tv_lshrrev_b32 v108, 5, v79\n\tv_mul_u32_u24 v108, 1248, v108\n\ts_mov_b32 m0, s47\n\tv_add_u32 v108, v108, v79\n\t" \
      "global_load_lds_dwordx4 v108, s[76:77]\n\t.endif\n\t" \
      "xp 120, 56\n\t" \
      "pos j_+2\n\t" \
      "smf 16, a_+6, 112\n\t.if j_ < 30\n\tds_read_b128 v[a_+6:a_+9], v[ar_+2] offset:of_\n\t.endif\n\txp 124, 60\n\t" \
      ".set j_, j_ + 1\n\t" \
      ".endr\n\t" \
      ".if %c[j1] == 32\n\ts_nop 1\n\tsmf 16, 104, 120\n\ts_nop 15\n\t.endif\n\t" \
      "s_mov_b32 m0, %[keep]\n\t" \
      ".purgem pos\n\t.purgem dma\n\t.purgem flg\n\t.purgem tabl\n\t.purgem old\n\t" AT_ASM_PURGE
#define AT_STAMP
#define AT_STV
#define AT_STV_ARG
#define AT_STV_PASS
#define AT_A_STATE "+{v[0:31]}"(o), "+{v[32:47]}"(s0), "+{v[112:127]}"(pp), "+{v[80:111]}"(fr), "+{v[128:143]}"(ad), [keep] "=&s"(keep)
#define AT_A_CLOB "memory", "scc", "vcc", "s40", "s41", "s48", "v48", "v49", "v50", "v51", "v52", "v53", "v54", "v55", "v56", "v57", "v58", "v59", "v60", "v61", "v62", "v63"
__device__ __forceinline__ void at_a1(f32x32& o, f32x16& s0, f32x16& pp, f32x32& fr, const u32x16& q, u32x16& ad, const char* ws, u32x16 so,
                                      unsigned m0v, unsigned m0b, unsigned long long tag, unsigned flag, unsigned long long fmask,
                                      const char* qB, const void* tab, f32x16& mF, f32x16& mD, u32x8& qv, u32x4& tw,
                                      const char* fsrc, unsigned fl AT_STV_ARG) {
  unsigned keep;
  asm volatile(AT_A_TEXT
      : AT_A_STATE, "=&{v[160:175]}"(mF), "=&{v[220:235]}"(mD), "=&{v[236:243]}"(qv), "=&{v[252:255]}"(tw) AT_STV
      : "{v[64:79]}"(q), "{s[54:55]}"(ws), "{s[56:71]}"(so), "{s72}"(m0v), "{s73}"(m0b), "{s[50:51]}"(tag), "{s52}"(flag), "{s[74:75]}"(fmask),
        "{s[42:43]}"(qB), "{s[44:45]}"(tab), "{s[76:77]}"(fsrc), "{s47}"(fl), [j0] "n"(0), [j1] "n"(12), [obr] "n"(O_BR)
      : AT_A_CLOB);
}
__device__ __forceinline__ void at_a2(f32x32& o, f32x16& s0, f32x16& pp, f32x32& fr, const u32x16& q, u32x16& ad, const char* ws, u32x16 so,
                                      unsigned m0v, unsigned long long tag, unsigned flag, const u32x4& tw, const char* src, unsigned d0,
                                      unsigned d1, unsigned tch, f32x32& of, f32x8& of2) {
  unsigned keep;
  asm volatile(AT_A_TEXT
      : AT_A_STATE, "=&{v[184:215]}"(of), "=&{v[216:223]}"(of2)
      : "{v[64:79]}"(q), "{s[54:55]}"(ws), "{s[56:71]}"(so), "{s72}"(m0v), "{s[50:51]}"(tag), "{s52}"(flag),
        "{v[252:255]}"(tw), "{s[42:43]}"(src), "{s44}"(d0), "{s45}"(d1), "{s46}"(tch), [j0] "n"(12), [j1] "n"(32), [obr] "n"(O_BR)
      : AT_A_CLOB);
}

__device__ __forceinline__ void at_b(f32x16& o, f32x16& mF, const f32x16& mD, u32x8& q, unsigned tb, unsigned qt, char* oA, const u32x16& qa,
                                     u32x16& ad, f32x32& k0, f32x16& k1, f32x32& k2, f32x16& k3, unsigned fl, unsigned fd, char* ws) {
  asm volatile(AT_ASM_MACROS
      "v_add_u32 v242, %[tb], v128\n\tv_add_u32 v243, %[tb], v129\n\tv_add_u32 v245, %[tb], v130\n\t"
      "ds_read_b128 v[176:179], v242 offset:7*3584\n\tds_read_b64 v[180:181], v243 offset:7*3584\n\t"
      "ds_read_b128 v[184:187], v242 offset:8*3584\n\tds_read_b64 v[182:183], v243 offset:8*3584\n\t"
      "ds_read_b128 v[192:195], v242\n\tds_read_b64 v[196:197], v243\n\tds_read_b128 v[206:209], v242 offset:3584\n\tds_read_b64 v[210:211], v243 offset:3584\n\t"
      "ds_read_b128 v[198:201], v245\n\tds_read_b128 v[212:215], v245 offset:3584\n\tds_read_b128 v[202:205], v245 offset:1024\n\t"
      "s_mov_b32 exec_lo, 0\n\tv_mov_b64 v[240:241], 0\n\ts_mov_b32 exec_lo, -1\n\tv_mov_b64 v[188:189], -1\n\tv_mov_b64 v[190:191], -1\n\t"
      "s_waitcnt lgkmcnt(7)\n\t"
      "qk1 144, 176, 236\n\tqk2 144, 180, 240\n\tqk1c 48, 184, 236, 220\n\tqk2 48, 182, 240\n\t"
      "global_store_dwordx4 v137, v[188:191], s[42:43] sc1\n\ts_mov_b32 exec_hi, 0\n\tglobal_store_dwordx2 v78, v[188:189], s[42:43] sc1\n\ts_mov_b32 exec_hi, -1\n\t"
      "s_waitcnt lgkmcnt(5)\n\tqk1c 160, 192, 236, 160\n\tds_read_b128 v[192:195], v242 offset:7168\n\ts_cmp_eq_u32 %[qt], 0\n\ts_nop 1\n\t"
      "mx 216, 144\n\tmx 218, 48\n\ts_cbranch_scc1 1f\n\tv_max_f32 v218, v218, v216\n\t1:\n\tv_mov_b32 v219, v218\n\t"
      "v_mov_b64 v[144:145], 0\n\tv_mov_b64 v[146:147], 0\n\tv_permlane32_swap_b32 v218, v219\n\tv_max_f32 v218, v218, v219\n\tv_add_f32 v218, 0x42600000, v218\n\t"
      "s_mov_b32 exec_lo, 0\n\tv_cvt_pk_bf16_f32 v240, -v218, 0\n\ts_mov_b32 exec_lo, -1\n\tv_mov_b64 v[148:149], 0\n\t"
      "qk2 160, 196, 240\n\tds_read_b64 v[196:197], v243 offset:7168\n\t"
      ".set i_, 150\n\t.rept 5\n\tv_mov_b64 v[i_:i_+1], 0\n\t.set i_, i_ + 2\n\t.endr\n\t"
      "s_waitcnt lgkmcnt(2)\n\ts_nop 1\n\t"
      ".set t_, 0\n\t"
      ".rept 9\n\t"
      ".set a_, 192 + 14 * (t_ & 1)\n\t.set b_, 206 - 14 * (t_ & 1)\n\t.set c_, 160 - 112 * (t_ & 1)\n\t.set d_, 48 + 112 * (t_ & 1)\n\t"
      ".if t_ == 0 || t_ == 5\n\ts_setprio 2 - t_ / 5\n\t.endif\n\t"
      ".if t_ < 7\n\tqk1 d_, b_, 236\n\t.elseif t_ == 7\n\tqk1c d_, b_, 236, 220\n\t.endif\n\t"
      ".if t_ < 6\n\tds_read_b128 v[b_:b_+3], v242 offset:(t_+3)*3584\n\t.endif\n\t"
      "xp 176, c_\n\t"
      ".if t_ < 8\n\tqk2 d_, b_+4, 240\n\t.endif\n\t"
      ".if t_ < 6\n\tds_read_b64 v[b_+4:b_+5], v243 offset:(t_+3)*3584\n\t.endif\n\t"
      ".if t_ == 4\n\tv_and_b32 v142, 0xf0, v79\n\tv_lshrrev_b32 v142, 1, v142\n\tv_sub_u32 v142, v79, v142\n\tv_add_u32 v142, %[fl], v142\n\ts_waitcnt vmcnt(6)\n\t"
      "ds_read_b64_tr_b16 v[108:109], v142\n\tds_read_b64_tr_b16 v[110:111], v142 offset:128\n\tv_add_u32 v142, %[fd], v79\n\t.endif\n\t"
      ".if t_ == 6\n\tglobal_store_dwordx4 v142, v[108:111], %[ws] sc1\n\t.endif\n\t"
      "xp 180, c_+4\n\t"
      ".if t_ > 0\n\tsmf 144, b_+10, 184\n\t.endif\n\t"
      ".if t_ < 8\n\tds_read_b128 v[b_+10:b_+13], v245 offset:(t_+1)*3584+1024\n\t.endif\n\t"
      "xp 184, c_+8\n\t"
      "smf 144, a_+6, 176\n\t"
      ".if t_ < 6\n\ts_waitcnt lgkmcnt(3)\n\t.elseif t_ < 8\n\ts_waitcnt lgkmcnt(1)\n\t.else\n\ts_waitcnt lgkmcnt(0)\n\t.endif\n\t"
      ".if t_ < 7\n\tds_read_b128 v[a_+6:a_+9], v245 offset:(t_+2)*3584\n\t.endif\n\t"
      "xp 188, c_+12\n\t"
      ".set t_, t_ + 1\n\t"
      ".endr\n\t"
      "s_nop 1\n\tsmf 144, 202, 184\n\ts_nop 11\n\t"
      AT_ASM_PURGE
      : "=&{v[144:159]}"(o), "+{v[160:175]}"(mF), "+{v[0:31]}"(k0), "+{v[32:47]}"(k1), "+{v[80:111]}"(k2), "+{v[112:127]}"(k3), "+{v[128:143]}"(ad),
        "+{v[236:243]}"(q)
      : "{v[220:235]}"(mD), "{v[64:79]}"(qa), [tb] "s"(tb), [qt] "s"(qt), "{s[42:43]}"(oA), [fl] "s"(fl), [fd] "s"(fd), [ws] "s"(ws)
      : "memory", "scc", "v245", "v48", "v49", "v50", "v51", "v52", "v53", "v54", "v55", "v56", "v57", "v58", "v59", "v60", "v61", "v62", "v63",
        "v176", "v177", "v178", "v179", "v180", "v181", "v182", "v183", "v184", "v185", "v186", "v187", "v188", "v189", "v190", "v191",
        "v192", "v193", "v194", "v195", "v196", "v197", "v198", "v199", "v200", "v201", "v202", "v203", "v204", "v205", "v206", "v207",
        "v208", "v209", "v210", "v211", "v212", "v213", "v214", "v215", "v216", "v217", "v218", "v219");
}

#define PJ_STAMP(i)
#define KLD 9
__device__ __forceinline__ void proj_all(const f32x32& of, const f32x8& of2, unsigned l16, const u32x4& tw, const char* src, unsigned pos,
                                         unsigned x, unsigned grp, const char* ws, char* out, unsigned long long m5 AT_STV_ARG) {
  f32x32 t0, t1, t2; f32x16 t3, t4, t5; f32x8 t6, t7; f32x4 t8; u32x2 t9;
  asm volatile(
      ".macro rdw k, sp, ab\n\t.set d_, 80 + 20 * ((\\k) & 1)\n\t"
      ".if (\\k) < \\sp\n\t.set a_, \\ab\n\t.set o_, (\\k) * 5120\n\t.else\n\t.set a_, \\ab+1\n\t.set o_, ((\\k) - \\sp) * 5120\n\t.endif\n\t.endm\n\t"
      ".macro rd1 i\n\tds_read_b128 v[d_+4*\\i:d_+4*\\i+3], v[a_] offset:o_+1024*\\i\n\t.endm\n\t"
      ".macro rd5\n\trd1 0\n\trd1 1\n\trd1 2\n\trd1 3\n\trd1 4\n\t.endm\n\t"
      ".macro pm i, ac, of, last\n\t"
      ".if k_ == 0\n\tv_mfma_f32_16x16x32_bf16 v[\\ac+4*\\i:\\ac+4*\\i+3], v[c_+4*\\i:c_+4*\\i+3], v[\\of:\\of+3], 0\n\t"
      ".else\n\tv_mfma_f32_16x16x32_bf16 v[\\ac+4*\\i:\\ac+4*\\i+3], v[c_+4*\\i:c_+4*\\i+3], v[\\of+4*k_:\\of+3+4*k_], v[\\ac+4*\\i:\\ac+4*\\i+3]\n\t.endif\n\t"
      ".if k_ < \\last\n\trd1 \\i\n\ts_waitcnt lgkmcnt(4)\n\t.elseif \\i < 4\n\ts_waitcnt lgkmcnt(3 - \\i)\n\t.endif\n\t.endm\n\t"
      ".macro ldo\n\tv_mov_b32 v72, v253\n\t.set q_, 0\n\t.rept 10\n\t"
      "global_load_dwordx4 v[32+4*q_:35+4*q_], v72, s[54:55] sc1\n\t.if q_ < 9\n\tv_add_u32 v72, " STR(OCT) "*4, v72\n\t.endif\n\t.set q_, q_ + 1\n\t.endr\n\t.endm\n\t"
      ".macro wp n, hf\n\t.set n_, \\n\n\t.if n_ < 7\n\t"
      ".if (n_ & 3) == 0\n\ts_add_u32 m0, s44, 86016 * (1 - \\hf) + 1024 * n_\n\ts_add_u32 s40, s42, 51200 * \\hf + 1024 * n_\n\ts_addc_u32 s41, s43, 0\n\t.endif\n\t"
      "global_load_lds_dwordx4 v79, s[40:41] offset:1024*(n_&3) sc1\n\t.endif\n\t.endm\n\t"
      ".macro snd ac, off\n\tds_write_b128 v124, v[\\ac+8:\\ac+11] offset:\\off\n\tds_write_b128 v124, v[\\ac+12:\\ac+15] offset:\\off+1024\n\t"
      "ds_write_b128 v124, v[\\ac+16:\\ac+19] offset:\\off+2048\n\t.endm\n\t"
      ".macro rcv off\n\tds_read_b128 v[128:131], v125 offset:\\off\n\tds_read_b128 v[132:135], v125 offset:\\off+1024\n\tds_read_b128 v[136:139], v125 offset:\\off+2048\n\t.endm\n\t"
      ".macro fin ac, off\n\t"
      "v_pk_add_f32 v[128:129], v[128:129], v[\\ac:\\ac+1]\n\tv_pk_add_f32 v[130:131], v[130:131], v[\\ac+2:\\ac+3]\n\t"
      "v_pk_add_f32 v[132:133], v[132:133], v[\\ac+4:\\ac+5]\n\tv_pk_add_f32 v[134:135], v[134:135], v[\\ac+6:\\ac+7]\n\t"
      "v_cvt_pk_bf16_f32 v140, v128, v129\n\tv_cvt_pk_bf16_f32 v141, v130, v131\n\tv_cvt_pk_bf16_f32 v142, v132, v133\n\tv_cvt_pk_bf16_f32 v143, v134, v135\n\t"
      "v_pk_add_f32 v[136:137], v[136:137], v[\\ac+16:\\ac+17]\n\tv_pk_add_f32 v[138:139], v[138:139], v[\\ac+18:\\ac+19]\n\t"
      "v_permlane16_swap_b32 v140, v142\n\tv_permlane16_swap_b32 v141, v143\n\t"
      "v_cvt_pk_bf16_f32 v136, v136, v137\n\tv_cvt_pk_bf16_f32 v137, v138, v139\n\t"
      "global_store_dwordx4 v254, v[140:143], s[56:57] offset:\\off\n\t"
      "s_mov_b64 exec, s[58:59]\n\tglobal_store_dwordx2 v255, v[136:137], s[56:57] offset:\\off\n\ts_mov_b64 exec, -1\n\t.endm\n\t"
      "s_mul_i32 s48, s46, 51200\n\tv_add_u32 v120, s48, v79\n\trdw 0, 6, 120\n\trd5\n\t"
      "s_mul_i32 s49, s46, 143360 - 30720\n\ts_add_u32 s49, s49, 30720\n\tv_add_u32 v121, s49, v79\n\t"
      "s_lshr_b32 s48, s48, 1\n\tv_add_u32 v123, s48, v79\n\tv_add_u32 v122, 86016, v123\n\t"
      "s_add_u32 s48, s45, 51200\n\tv_add_u32 v124, s48, v79\n\ts_mul_i32 s49, s46, 24576\n\ts_sub_u32 s49, 12288, s49\n\tv_add_u32 v125, s49, v124\n\t"
      "s_barrier\n\ts_waitcnt lgkmcnt(4)\n\t"
      ".set k_, 0\n\t.rept 10\n\t.set c_, 80 + 20 * (k_ & 1)\n\trdw k_+1, 6, 120\n\t"
      ".if k_ == 0 || k_ == 3 || k_ == 6\n\ts_setprio 2 - k_ / 3\n\t.endif\n\t"
      ".if k_ == %c[kld]\n\tldo\n\t.endif\n\t"
      "pm 0, 0, 184, 9\n\twp 2*k_, 0\n\tpm 1, 0, 184, 9\n\tpm 2, 0, 184, 9\n\twp 2*k_+1, 0\n\tpm 3, 0, 184, 9\n\tpm 4, 0, 184, 9\n\t"
      ".set k_, k_ + 1\n\t.endr\n\t"
      "s_setprio 3\n\ts_mov_b32 s48, 0x40000\n\ts_waitcnt vmcnt(10)\n\ts_barrier\n\t"
      PJ_STAMP(16)
      "rdw 0, 5, 122\n\trd5\n\tsnd 0, 0\n\twp 0, 1\n\twp 1, 1\n\twp 2, 1\n\twp 3, 1\n\twp 4, 1\n\twp 5, 1\n\twp 6, 1\n\ts_waitcnt vmcnt(7)\n\t"
      "v_max3_u32 v72, v32, v33, v34\n\t.set q_, 35\n\t.rept 18\n\tv_max3_u32 v72, v72, v[q_], v[q_+1]\n\t.set q_, q_ + 2\n\t.endr\n\t"
      "v_max_u32 v72, v72, v71\n\tv_cmp_eq_u32 vcc, -1, v72\n\ts_nop 0\n\ts_cbranch_vccz 4f\n\t"
      "5:\n\ts_mov_b32 s49, 0\n\t.set q_, 0\n\t.rept 10\n\t"
      "v_max3_u32 v72, v[32+4*q_], v[33+4*q_], v[34+4*q_]\n\tv_max_u32 v72, v72, v[35+4*q_]\n\tv_cmp_eq_u32 vcc, -1, v72\n\t"
      "v_add_u32 v73, " STR(OCT) "*4*q_, v253\n\ts_cbranch_vccz 6f\n\t"
      "global_load_dwordx4 v[32+4*q_:35+4*q_], v73, s[54:55] sc1\n\ts_mov_b32 s49, 1\n\t6:\n\t.set q_, q_ + 1\n\t.endr\n\t"
      "s_cmp_eq_u32 s49, 0\n\ts_cbranch_scc1 4f\n\ts_sub_u32 s48, s48, 1\n\ts_waitcnt vmcnt(0)\n\ts_cbranch_scc0 5b\n\t4:\n\t"
      PJ_STAMP(17)
      "s_waitcnt lgkmcnt(0)\n\t"
      ".set k_, 0\n\t.rept 5\n\t.set c_, 80 + 20 * (k_ & 1)\n\trdw k_+1, 5, 122\n\t"
      "pm 0, 160, 32, 4\n\tpm 1, 160, 32, 4\n\tpm 2, 160, 32, 4\n\tpm 3, 160, 32, 4\n\tpm 4, 160, 32, 4\n\t"
      ".set k_, k_ + 1\n\t.endr\n\t"
      "s_waitcnt vmcnt(0)\n\ts_barrier\n\t"
      PJ_STAMP(18)
      "rcv 0\n\trd5\n\ts_waitcnt lgkmcnt(4)\n\t"
      ".rept 5\n\t.set c_, 80 + 20 * (k_ & 1)\n\trdw k_+1, 5, 122\n\t"
      "pm 0, 160, 32, 9\n\tpm 1, 160, 32, 9\n\tpm 2, 160, 32, 9\n\tpm 3, 160, 32, 9\n\tpm 4, 160, 32, 9\n\t"
      ".if k_ == 5\n\tfin 0, 1280\n\t.endif\n\t"
      ".set k_, k_ + 1\n\t.endr\n\t"
      "s_nop 6\n\tsnd 160, 86016-51200\n\ts_waitcnt lgkmcnt(0)\n\ts_barrier\n\t"
      PJ_STAMP(20)
      "rcv 86016-51200\n\ts_waitcnt lgkmcnt(0)\n\tfin 160, 0\n\t"
      ".purgem rdw\n\t.purgem rd1\n\t.purgem rd5\n\t.purgem pm\n\t.purgem ldo\n\t.purgem wp\n\t.purgem snd\n\t.purgem rcv\n\t.purgem fin\n\t"
      : "=&{v[0:31]}"(t0), "=&{v[32:63]}"(t1), "=&{v[80:111]}"(t2), "=&{v[112:127]}"(t3), "=&{v[128:143]}"(t4), "=&{v[160:175]}"(t5),
        "=&{v[64:71]}"(t6), "=&{v[176:183]}"(t7), "=&{v[72:73]}"(t9) AT_STV
      : "{v[184:215]}"(of), "{v[216:223]}"(of2), "{v79}"(l16), "{v[252:255]}"(tw), "{s[42:43]}"(src), "{s44}"(pos), "{s45}"(x), "{s46}"(grp),
        "{s[54:55]}"(ws), "{s[56:57]}"(out), "{s[58:59]}"(m5), [kld] "n"(KLD)
      : "memory", "scc", "vcc", "s40", "s41", "s48", "s49");
}
typedef __attribute__((address_space(3))) f32x4* lds_f4;

struct LaneTab { unsigned v[9][64][4]; };
constexpr LaneTab make_tab() {
  LaneTab t = {};
  for (unsigned l = 0; l < 64; ++l) {
    const unsigned g = l >> 4;
    t.v[0][l][0] = (l & 15) * 64 * 128;
    t.v[0][l][1] = g * OCT + (l & 15) * 16;
    t.v[0][l][2] = (l & 15) * 2560 + (g & 1) * 32 + (g >> 1) * 16;
    t.v[0][l][3] = (l & 15) * 2560 + 128 + g * 8;
    for (unsigned r = 0; r < 16; ++r) {
      const unsigned key = (r & 3) + 8 * (r >> 2) + 4 * (l >> 5);
      t.v[1 + (r >> 2)][l][r & 3] = key > (l & 31) ? 0u : 0xFF800000u;
      t.v[5 + (r >> 2)][l][r & 3] = key <= (l & 31) ? 0u : 0xFF800000u;
    }
  }
  return t;
}
__device__ const LaneTab lane_tab = make_tab();
constexpr unsigned CODE_B = 34816;

__global__ void __launch_bounds__(512, 2)
k_fused(const char* __restrict__ xa_, const char* __restrict__ wa_, const char* __restrict__ woa_, const char* __restrict__ xb_,
        const char* __restrict__ wb_, const char* __restrict__ wob_, char* __restrict__ ws_, char* __restrict__ out_) {
  __shared__ __attribute__((aligned(16))) char lds[163840];
  const unsigned bid = blockIdx.x, xcd = bid & 7, slot = bid >> 3, br = xcd >> 2;
  u32x4 ka; u32x2 kb; u32x8 kc;
  {
    const void* kp = (const void*)__builtin_amdgcn_kernarg_segment_ptr();
    asm volatile("s_load_dwordx4 %0, %3, %4\n\ts_load_dwordx8 %2, %3, 0x20\n\ts_load_dwordx2 %1, %3, 0x10"
                 : "=&s"(ka), "=&s"(kb), "=&s"(kc) : "s"(kp), "s"(br * 24));
  }
  STAMP_DECL STAMP(0);
  unsigned lane = threadIdx.x & 63;
  const unsigned w = __builtin_amdgcn_readfirstlane(threadIdx.x >> 6);
  const unsigned grp = w >> 2, lw = w & 3;
  const unsigned mt = (xcd & 3) * 4 + (slot & 3), u = slot >> 2;
  const unsigned win = (5 * ((mt >> 1) & 3)) >> 1;
  const unsigned kv1 = (u + 3) >> 3;
  const unsigned j1 = win + u - 5;
#define ROT (5 * ((u + slot) & 1) + 3 * ((xcd >> 1) & 1) + (xcd & 1))
  auto mod10 = [](unsigned x) { return __builtin_elementwise_min(x, x - 10); };
  const unsigned rot = mod10(ROT);
  const unsigned m0v = w * 4096;
  u32x4 o1; unsigned tw;
  {
    const unsigned row = lane >> 3, pos = lane & 7;
    tw = row * 3840 + ((pos ^ (row & 2)) * 16);
    o1[0] = row * 1280 + ((pos ^ (row >> 1)) * 16);
    o1[1] = (o1[0] ^ 64) + (8 * 1280 - 1024);
    o1[2] = (tw ^ ((w & 1) * 64)) + 1024;
    o1[3] = (tw ^ ((w & 1) * 64)) + (128 + kv1 * 1152);
  }
  const unsigned offX = (mt * 128 + 16 * w) * 1280, offW = 8 * w * 3840 + (kv1 ? 1280 + j1 * 128 : u * 256);
  asm volatile("s_waitcnt lgkmcnt(0)" : "+s"(ka), "+s"(kb), "+s"(kc));
  auto ptr = [](unsigned lo, unsigned hi) { return (char*)(((unsigned long long)hi << 32) | lo); };
  const char* sX = ptr(ka[0], ka[1]) + offX;
  const char* sW = ptr(ka[2], ka[3]) + (offW - 1024ll);
  char* const ws = ptr(kc[4], kc[5]);
  glds_xw(sX, sW, rot, o1, w * 2048);
  u32x2 ep; unsigned tch[4];
  asm volatile("s_load_dwordx2 %0, %1, %2" : "=&s"(ep) : "s"(ws), "s"((unsigned)CTL_WS));
  {
    const unsigned i64 = (slot * 8 + w) * 64;
    unsigned long long pc;
    asm volatile("s_getpc_b64 %0\n\ts_load_dword %1, %0, %5\n\ts_load_dword %2, %0, %6\n\ts_load_dword %3, %0, %7\n\ts_load_dword %4, %8, %9"
                 : "=&s"(pc), "=&s"(tch[0]), "=&s"(tch[1]), "=&s"(tch[2]), "=&s"(tch[3])
                 : "s"(i64), "s"(i64 + 16384), "s"(__builtin_elementwise_min(i64 + 32768, CODE_B)), "s"(&lane_tab),
                   "s"(__builtin_elementwise_min(i64, 9152u)));
  }
  const unsigned j2 = mod10(win + 3 + u);
  const unsigned Tg = mt * 4 + lw;
  const unsigned vt1 = grp & kv1, vt2 = u < 7 ? grp : 2;
  const unsigned cg1 = kv1 ? 10 + 10 * grp + j1 : 2 * u + grp, cg2 = 10 + 10 * grp + j2;
  const unsigned head = xcd * 4 + (slot & 3), qb = u;
  const unsigned odd = head & 1, o2 = 5 * (head >> 1) + 2, o1h = odd ? o2 + 1 : o2 - 2;
  const unsigned qt = qb * 8 + 2 * lw + grp;
  u32x4 soA; u32x16 so;
  {
    const unsigned kb0 = 80 * OCT + w * 512, vb0 = (unsigned)V_WS - V_OFF + xcd * (256 * 1280) + w * 5120;
    unsigned ko[10], vo_[10];
#pragma unroll
    for (int i = 0; i < 8; ++i) { const unsigned c = (((head >> 3) + (i >> 1)) & 3) + 4 * (i & 1); ko[i] = kb0 + c * 4096; vo_[i] = vb0 + c * 40960; }
    const unsigned c8 = qb ? qb - 1 : 0;
    ko[8] = kb0 + (unsigned)QK_BR + c8 * 4096; vo_[8] = vb0 + (unsigned)V_BR + c8 * 40960;
    ko[9] = kb0 + (unsigned)QK_BR + qb * 4096; vo_[9] = vb0 + (unsigned)V_BR + qb * 40960;
    soA = u32x4{ko[0], vo_[0], ko[1], vo_[1]};
    so = u32x16{ko[2], vo_[2], ko[8], vo_[8], ko[9], vo_[9], ko[3], vo_[3], ko[4], vo_[4], ko[5], vo_[5], ko[6], vo_[6], ko[7], vo_[7]};
    asm volatile("" : "+{s[76:79]}"(soA), "+{s[56:71]}"(so));
  }
  unsigned flag1 = FQ_OFF + ((br * 30 + cg1) * 64 + Tg) * 128, flag2 = FQ_OFF + ((br * 30 + cg2) * 64 + Tg) * 128;
  unsigned flagO = FO_OFF + (head * 64 + qt) * 128;
  PINS(flag1); PINS(flag2); PINS(flagO);
  glds_xw(sX, sW, mod10(rot + 1), o1, w * 2048 + 32768);
  const char* sB = ptr(ka[2], ka[3]) + (16 * lw * 3840 + 256 + j2 * 128);
  const unsigned mytile = w * TILE_B;
  const char* fsrc; unsigned fdst;
  {
    const unsigned F = __builtin_elementwise_min((head * 8 + qb) * 8 + w, 1599u);
    const unsigned bl = (F * 13108) >> 16, np = F - 5 * bl, t = (bl * 3277) >> 16, ps = bl - 20 * t;
    const unsigned ks = ((t & 1) || ps < 5 || ps >= 15) ? ps : (ps < 10 ? ps + 5 : ps - 5);
    fsrc = ptr((t & 1) ? kc[2] : kb[0], (t & 1) ? kc[3] : kb[1]) + (ks * (32 * 1280) + (t >> 1) * 160 + ((ks >= 10 && np < 4) ? np ^ 2 : np) * 32);
    fdst = (unsigned)WP_WS + ((head * 8 + qb) * 8 + w) * 1024;
  }
  u32x4 late = {FO_OFF + (grp * 16 * 64 + head * 2 + (lw >> 1)) * 128,
                (unsigned)O_WS + grp * 40 * OCT + (head * 64 + lw * 16) * 16,
                (unsigned)WP_WS + qb * 204800,
                (head * 64 + lw * 16) * 2560 + qb * 160};
  asm volatile("" : "+s"(fsrc), "+s"(fdst), "+s"(late));
  u32x16 qq, pk, at;
  {
    const unsigned r16 = lane & 15, g = lane >> 4, q = r16 >> 2, p = lane & 3, r32 = lane & 31, h = lane >> 5;
    const unsigned xa0 = (lw * 32 + r16) * 128 + ((g ^ (r16 >> 1)) * 16);
    const unsigned wa0 = 2048 * g + 1024 * grp + 128 * q + 16 * ((p >> 1) ^ (4 * (g & 1) + (q & 2))) + 8 * (p & 1);
    const unsigned jj = (20 * head + 19 * (lane & 1)) >> 6, b1 = (lane >> 1) & 1, c = (lane >> 2) & 7;
    const unsigned pol1 = FQ_OFF + (jj * 64 + ((lane & 8) ? (qt ^ b1) : b1 * 640 + (lane & 4) * 8 + (640 + (head >> 3) * 8 + w))) * 128;
    qq = u32x16{o1[0], o1[1], o1[2], o1[3], tw + 1024, tw + 1280, (tw ^ 64) + (8 * 3840 - 1024), (tw ^ 64) + (8 * 3840 - 2048 + 1280),
                pol1, xa0, xa0 ^ 64, xa0, wa0, wa0 ^ 32, wa0 ^ 64, wa0 ^ 96};
    const unsigned cB = __builtin_elementwise_max((c & 1) + qb, 1u) + (30 * 8 - 1);
    const unsigned pol2 = (jj * 64 + (lane >= 48 ? 30 * 64 + qt : b1 * 640 + (h ? cB : c) * 8 + (640 + w))) * 128 + FQ_OFF;
    const u32x2 s1 = g1_offs(br, cg1, Tg, lane), s2 = g1_offs(br, cg2, Tg, lane);
    const unsigned ab = grp * TILE_B;
    pk = u32x16{s1[0], s1[1], s2[0], s2[1],
                lane * 16 + ab,
                h ? (V_OFF + 320 + ab) : r32 * 16 + (K2_OFF + odd * 8 + ab),
                h * 512 + (r32 < 28 ? __builtin_elementwise_min(r32, 21u) : 28) * 16 + (V_OFF + ab),
                h * OCT + r32 * 16 + o1h * OCT,
                r32 * 16 + (o2 * OCT - 1024),
                h * 1280 + (__builtin_elementwise_min(r32 + 4 * odd, r32 + 4 * odd - 20) + (head & 3) * 20) * 16,
                pol2,
                g * 512 + __builtin_elementwise_min(r16, 11u) * 16 + (mytile + V_OFF + 320),
                r16 == 0 ? 0x3F803F80u : 0u,
                r32 * 16 + (o2 * OCT + odd * 8),
                lane * 512 + (3 * CHUNK_B + V_OFF + 320 + mytile),
                lane};
  }
  asm volatile("" : "+{v[64:79]}"(qq), "+{v[80:95]}"(pk) :: "memory");
  glds_xw(sX, sW, mod10(rot + 2), u32x4{qq[0], qq[1], qq[2], qq[3]}, w * 2048 + 65536);
  asm volatile("s_waitcnt vmcnt(8) lgkmcnt(0)\n\ts_barrier" ::: "memory");
  PINS(ep); PINS(tch[0]); PINS(tch[1]); PINS(tch[2]); PINS(tch[3]);
  const unsigned long long tag = (((unsigned long long)ep[1] << 32) | ep[0]) + 1;
  STAMP(1);
  u32x4 cv4;
  STV_PUT(pk[15]);
  g1_all(vt1, kv1, vt2, qq, pk, at, cv4, sX, sW, mod10(rot + 3) * 128, sB, mod10(rot + grp) * (64 * 3840), m0v, tag, flag1, ws, mytile, soA,
         qt * 512);
  STV_GET(pk[15]);
  STAMP(8);
  if (bid == 255 && grp == 0) {
    const unsigned e = w * 64 + lane_id(), eu = e & 7, em = (e >> 3) & 15;
    poll_flags(ws, FQ_OFF + (((e >> 7) * 30 + (eu < 5 ? 2 * eu : 5 + eu + ((5 * ((em >> 1) & 3)) >> 1))) * 64 + 4 * em) * 128, tag);
  }
  BARRIER();
  STAMP(9);
  if (bid == 255 && w == 0) put_flag(ws, CTL_WS, tag);
  typedef __attribute__((address_space(3))) u32x4* lds_u4;
  *(lds_u4)((lds_ptr)lds + pk[11] + 2 * CHUNK_B) = cv4; *(lds_u4)((lds_ptr)lds + pk[11] + 3 * CHUNK_B) = cv4; *(lds_u4)((lds_ptr)lds + pk[11] + 4 * CHUNK_B) = cv4;
  if (qb == 0) {
    lds_ptr z = (lds_ptr)lds + 3 * CHUNK_B + mytile + qq[15];
    *(lds_f4)(z) = f32x4{}; *(lds_f4)(z + 1024) = f32x4{}; *(lds_f4)(z + 2048) = f32x4{}; *(lds_f4)(z + 2560) = f32x4{};
  }
  const f32x16 zero16 = {};
  auto exact = [&](int b, bf16x8 q1, s16x4 q2, int t0, int t1) {
    __builtin_amdgcn_fence(__ATOMIC_ACQUIRE, "agent");
    const int ln = lane_id(), r32 = ln & 31, h = ln >> 5;
    unsigned head = xcd * 4 + (slot & 3);
    PINS(head);
    const unsigned odd = head & 1, o2 = 5 * (head >> 1) + 2, o1h = odd ? o2 + 1 : o2 - 2;
    if (h) q2 = s16x4{0, 0, 0, 0};
    typedef __attribute__((address_space(1))) const char* gptr;
    gptr kg1 = (gptr)ws + QK_WS + b * QK_BR + (size_t)(80 + o1h + h) * OCT + r32 * 16;
    gptr kg2 = (gptr)ws + QK_WS + b * QK_BR + (size_t)(80 + o2) * OCT + r32 * 16 + odd * 8;
    gptr vg = (gptr)ws + V_WS + b * V_BR + (head >> 2) * (256 * 1280) + h * 1280 +
                     ((head & 3) * 20 + (odd ? (r32 < 16 ? r32 + 4 : r32 - 16) : r32)) * 16;
    const u32x4 vc = r32 == 20 ? u32x4{0x3F803F80u, 0x3F803F80u, 0x3F803F80u, 0x3F803F80u} : u32x4{0u, 0u, 0u, 0u};
    float m = -INFINITY;
    f32x16 r = zero16;
#pragma unroll 1
    for (int ps = 0; ps < 2; ++ps) {
#pragma unroll 1
      for (int t = t0; t <= t1; ++t) {
        f32x16 s = MFMA32(*(__attribute__((address_space(1))) const bf16x8*)(kg1 + t * 512), q1, zero16);
        s = MFMA32K8(*(__attribute__((address_space(1))) const s16x4*)(kg2 + t * 512), q2, s);
        if (b) {
          const int d = (t1 - t) * 32 + r32 - 4 * h;
#pragma unroll
          for (int e = 0; e < 16; ++e) s[e] = (unsigned)(d - ((e & 3) + 8 * (e >> 2))) < 256u ? s[e] : -INFINITY;
        }
        if (ps == 0) { m = max16(s, m); continue; }
#pragma unroll
        for (int e = 0; e < 16; ++e) s[e] = __builtin_amdgcn_exp2f(s[e] - m);
        u32x4 va = vc, vb = vc;
        if (r32 < 20) { va = *(__attribute__((address_space(1))) const u32x4*)(vg + t * 5120); vb = *(__attribute__((address_space(1))) const u32x4*)(vg + t * 5120 + 2560); }
        r = smfmac(__builtin_bit_cast(bf16x8, va), lo8(s), r);
        r = smfmac(__builtin_bit_cast(bf16x8, vb), hi8(s), r);
      }
      m = xhalf_max(m);
    }
    return r;
  };
  auto unsafe = [&](const f32x16& r, float l) {
    float amax = 0.f;
#pragma unroll
    for (int e = 0; e < 12; ++e) amax = __builtin_fmaxf(amax, __builtin_fabsf(r[e]));
    return __any(!(l <= 1.2980742e33f) || !(l >= 8.4703295e-22f) || !(amax <= 3.0e38f));
  };
  char* const oA = ws + (O_WS + qt * 512);
  f32x32 oab, of; f32x8 of2;
  u32x4 lt;
  {
    f32x16 s0, pp;
    f32x32 fr;
    asm volatile(AT_ASM_MACROS
        "v_subrev_u32 v108, %[ab], v129\n\tds_read_b128 v[112:115], v79\n\tds_read_b64 v[116:117], v108\n\t"
        "ds_read_b128 v[80:83], v128\n\tds_read_b64 v[84:85], v129\n\tds_read_b128 v[94:97], v128 offset:7168\n\tds_read_b64 v[98:99], v129 offset:7168\n\t"
        "ds_read_b128 v[86:89], v130\n\tds_read_b128 v[100:103], v130 offset:7168\n\tds_read_b128 v[90:93], v130 offset:1024\n\t"
        "s_mov_b32 exec_lo, 0\n\tv_mov_b64 v[72:73], 0\n\tv_mov_b64 v[74:75], 0\n\ts_mov_b32 exec_lo, -1\n\t"
        "s_waitcnt lgkmcnt(7)\n\t"
        "qk1 32, 112, 64\n\tqk2 32, 116, 72\n\tqk1 48, 112, 68\n\tqk2 48, 116, 74\n\t"
        ".set i_, 0\n\t.rept 10\n\tv_mov_b64 v[i_:i_+1], 0\n\t.set i_, i_ + 2\n\t.endr\n\t"
        "mx 108, 32\n\tv_mov_b32 v109, v108\n\tmx 110, 48\n\tv_mov_b32 v111, v110\n\t"
        "v_permlane32_swap_b32 v108, v109\n\ts_waitcnt lgkmcnt(0)\n\tqk1 32, 80, 64\n\tv_permlane32_swap_b32 v110, v111\n\t"
        "v_max_f32 v108, v108, v109\n\tv_max_f32 v110, v110, v111\n\tv_add_f32 v108, 0x42600000, v108\n\tv_add_f32 v110, 0x42600000, v110\n\t"
        "s_mov_b32 exec_lo, 0\n\tv_cvt_pk_bf16_f32 v72, -v108, 0\n\tv_cvt_pk_bf16_f32 v74, -v110, 0\n\ts_mov_b32 exec_lo, -1\n\ts_nop 0\n\t"
        "qk2 32, 84, 72\n\t"
        ".set i_, 20\n\t.rept 6\n\tv_mov_b64 v[i_:i_+1], 0\n\t.set i_, i_ + 2\n\t.endr\n\t"
        "v_mov_b64 v[104:105], 0\n\tv_mov_b64 v[106:107], 0\n\t"
        "v_mov_b64 v[120:121], 0\n\tv_mov_b64 v[122:123], 0\n\tv_mov_b64 v[124:125], 0\n\tv_mov_b64 v[126:127], 0\n\t"
        AT_ASM_PURGE
        : "=&{v[0:31]}"(oab), "=&{v[112:127]}"(pp), "=&{v[32:47]}"(s0), "=&{v[80:111]}"(fr), "+{v[64:79]}"(qq), "+{v[128:143]}"(at)
        : [ab] "s"(grp * TILE_B)
        : "memory", "v48", "v49", "v50", "v51", "v52", "v53", "v54", "v55", "v56", "v57", "v58", "v59", "v60", "v61", "v62", "v63");
    STAMP(10);
    f32x16 maskF, maskD; u32x8 qv;
    at_a1(oab, s0, pp, fr, qq, at, ws, so, mytile, mytile + (qb ? 3 : 4) * CHUNK_B, tag, flag2, (0x7fu >> u) & 1u, ws + (QK_BR + qt * 512),
          &lane_tab, maskF, maskD, qv, lt, fsrc, 143360 + w * 1024 AT_STV_PASS);
    STAMP(11);
    {
      f32x16 ob;
      at_b(ob, maskF, maskD, qv, 3 * CHUNK_B + 2 * lw * TILE_B, qt, oA, qq, at, oab, s0, fr, pp, 143360 + w * 1024, fdst, ws);
      lt[0] += late[0]; lt[1] += late[1]; lt[2] += grp * 64;
      float l = row_sum(ob);
      if (__builtin_expect(unsafe(ob, l), 0)) {
        ob = exact(1, __builtin_bit_cast(bf16x8, u32x4{qv[0], qv[1], qv[2], qv[3]}), __builtin_bit_cast(s16x4, u32x2{qv[4], qv[5]}),
                   qb ? qt - 8 : 0, qt);
        l = row_sum(ob);
      }
      store_o<false>(ob, l, oA + O_BR, at[9], qq[14]);
    }
    STAMP(13);
    at[13] = __builtin_elementwise_min(qq[15], 24u * 16) * 64 + (FO_OFF + qb * 25 * 1024);
    at_a2(oab, s0, pp, fr, qq, at, ws, so, mytile, tag, flagO, lt, ws + (late[2] + 102400), w * 5120,
          81920 + __builtin_elementwise_min(3 * w, 17u) * 1024, late[2] + ((slot & 3) * 8 + w) * 3200, of, of2);
  }
  STAMP(12);
  {
    lds_ptr xb = (lds_ptr)lds + 3 * CHUNK_B + qq[15];
#pragma unroll
    for (int i = 0; i < 3; ++i) *(lds_f4)(xb + w * 3072 + i * 1024) = f32x4{oab[16 + 4 * i], oab[17 + 4 * i], oab[18 + 4 * i], oab[19 + 4 * i]};
    asm volatile("s_setprio 3\n\ts_waitcnt vmcnt(0) lgkmcnt(0)\n\ts_barrier" : "+{v[184:215]}"(of), "+{v[216:223]}"(of2) :: "memory");
    f32x16 o = {};
#pragma unroll
    for (int i = 0; i < 3; ++i) {
      const f32x4 r = *(lds_f4)(xb + (w ^ 4) * 3072 + i * 1024);
#pragma unroll
      for (int e = 0; e < 4; ++e) o[4 * i + e] = oab[4 * i + e] + r[e];
    }
    float l = row_sum(o);
    if (__builtin_expect(unsafe(o, l), 0)) {
      o = exact(0, __builtin_bit_cast(bf16x8, u32x4{qq[0], qq[1], qq[2], qq[3]}), __builtin_bit_cast(s16x4, u32x2{qq[8], qq[9]}), 0, 63);
      l = row_sum(o);
    }
    store_o<true>(o, l, oA, at[9], qq[14]);
  }
  STAMP(14);
  {
    const unsigned pos = __builtin_elementwise_min(7 * w, 43u) * 1024;
    proj_all(of, of2, qq[15], lt, ws + (late[2] + pos), pos, w * 3072, grp, ws, ptr(kc[6], kc[7]) + late[3],
             0xffffffffull << (32 * grp) AT_STV_PASS);
  }
  STAMP_OUT(ws, 0);
}

extern "C" void kernel_launch(void* const* d_in, const int* in_sizes, int n_in, void* d_out, int out_size, void* d_ws,
                              size_t ws_size, hipStream_t stream) {
  k_fused<<<256, 512, 0, stream>>>((const char*)d_in[0], (const char*)d_in[1], (const char*)d_in[2], (const char*)d_in[3],
                                   (const char*)d_in[4], (const char*)d_in[5], (char*)d_ws, (char*)d_out);
}
```

```cpp
#include <hip/hip_runtime.h>
#include <hip/hip_bf16.h>
#include <stdint.h>
#include <type_traits>

typedef __attribute__((ext_vector_type(8))) __bf16 bf16x8;
typedef __attribute__((ext_vector_type(16))) __bf16 bf16x16;
typedef __attribute__((ext_vector_type(4))) short s16x4;
typedef __attribute__((ext_vector_type(8))) short s16x8;
typedef __attribute__((ext_vector_type(2))) unsigned u32x2;
typedef __attribute__((ext_vector_type(4))) unsigned u32x4;
typedef __attribute__((ext_vector_type(4))) float f32x4;
typedef __attribute__((ext_vector_type(8))) float f32x8;
typedef __attribute__((ext_vector_type(16))) float f32x16;
typedef __attribute__((ext_vector_type(32))) float f32x32;
typedef __attribute__((address_space(3))) const char* lds_cptr;
typedef __attribute__((address_space(3))) char* lds_ptr;

#define OCT 33280
#define STR_(x) #x
#define STR(x) STR_(x)
constexpr size_t QK_WS = 0, QK_BR = (size_t)160 * OCT;
constexpr size_t V_WS = 2 * QK_BR, V_BR = (size_t)8 * 256 * 1280;
constexpr size_t O_WS = V_WS + 2 * V_BR, O_BR = (size_t)80 * OCT;
constexpr size_t WP_WS = O_WS + 2 * O_BR;
constexpr size_t CTL_WS = WP_WS + 2048 * 1024;
constexpr unsigned FQ_OFF = CTL_WS + 4096, FO_OFF = FQ_OFF + 2 * 30 * 64 * 128;
constexpr float QSCALE = 0.22360679774997896f * 1.4426950408889634f;

#define STAMP_DECL
#define STV_PUT(x)
#define STV_GET(x)
#define STAMP(i) do {} while (0)
#define STAMP_OUT(ws_, kid) do {} while (0)
__device__ __forceinline__ int lane_id() {
  int l; asm volatile("v_mbcnt_lo_u32_b32 %0, -1, 0\n\tv_mbcnt_hi_u32_b32 %0, -1, %0" : "=v"(l)); return l;
}
#define SBAR() __builtin_amdgcn_sched_barrier(0)
#define PIN(x) asm volatile("" : "+v"(x))
#define PINS(x) asm volatile("" : "+s"(x))
#define WAITV(N) asm volatile("s_waitcnt vmcnt(" #N ")" ::: "memory")
#define BARRIER() asm volatile("s_barrier" ::: "memory")
#define WAITV_BAR(N) asm volatile("s_waitcnt vmcnt(" #N ") lgkmcnt(0)\n\ts_barrier" ::: "memory")

#define XPOL ""
__device__ __forceinline__ void glds_xw(const char* x, const char* w, unsigned k, u32x4 o, unsigned lds) {
  unsigned keep;
  asm volatile("s_mov_b32 %0, m0\n\ts_mov_b32 m0, %7\n\ts_nop 0\n\tglobal_load_lds_dwordx4 %1, %5 " XPOL "\n\t"
               "global_load_lds_dwordx4 %2, %5 offset:1024 " XPOL "\n\ts_add_u32 m0, m0, 16384\n\ts_nop 0\n\tglobal_load_lds_dwordx4 %3, %6\n\t"
               "global_load_lds_dwordx4 %4, %6 offset:1024\n\ts_mov_b32 m0, %0"
               : "=&s"(keep) : "v"(o[0]), "v"(o[1]), "v"(o[2]), "v"(o[3]), "s"(x + k * 128), "s"(w + k * (64 * 3840)), "s"(lds) : "memory", "scc");
}
__device__ __forceinline__ unsigned pk_bf16(float lo, float hi) {
  typedef __attribute__((ext_vector_type(2))) float f32x2_;
  typedef __attribute__((ext_vector_type(2))) __bf16 bf16x2_;
  const f32x2_ v = {lo, hi};
  return __builtin_bit_cast(unsigned, __builtin_convertvector(v, bf16x2_));
}
__device__ __forceinline__ void st16(char* sbase, unsigned voff, u32x4 v) {
  asm volatile("global_store_dwordx4 %0, %1, %2 sc1\n\ts_nop 1" :: "v"(voff), "v"(v), "s"(sbase) : "memory");
}
__device__ __forceinline__ void st8lo(char* sbase, unsigned voff, u32x2 v) {
  asm volatile("s_mov_b32 exec_hi, 0\n\tglobal_store_dwordx2 %0, %1, %2 sc1\n\ts_mov_b32 exec_hi, -1" :: "v"(voff), "v"(v), "s"(sbase) : "memory");
}
__device__ __forceinline__ bf16x8 lds_b128(lds_cptr p) { return *(const __attribute__((address_space(3))) bf16x8*)p; }
__device__ __forceinline__ s16x4 lds_b64(lds_cptr p) { return *(const __attribute__((address_space(3))) s16x4*)p; }
__device__ __forceinline__ s16x4 lds_tr(lds_cptr p) {
  return __builtin_amdgcn_ds_read_tr16_b64_v4i16((__attribute__((address_space(3))) s16x4*)p);
}
__device__ __forceinline__ bf16x8 cat8(s16x4 a, s16x4 b) {
  s16x8 v = {a[0], a[1], a[2], a[3], b[0], b[1], b[2], b[3]};
  return __builtin_bit_cast(bf16x8, v);
}
#define MFMA32(a, b, c) __builtin_amdgcn_mfma_f32_32x32x16_bf16(a, b, c, 0, 0, 0)
#define MFMA32K8(a, b, c) __builtin_amdgcn_mfma_f32_32x32x8bf16_1k(a, b, c, 0, 0, 0)
#define MFMA16(a, b, c) __builtin_amdgcn_mfma_f32_16x16x32_bf16(a, b, c, 0, 0, 0)

typedef __attribute__((ext_vector_type(8))) unsigned u32x8;
typedef __attribute__((ext_vector_type(16))) unsigned u32x16;
#define AWAIT(d, d1, off) \
    "s_mov_b32 s48, 0x200000\n\t5:\n\ts_waitcnt vmcnt(0)\n\tv_cmp_ne_u64 vcc, s[50:51], v[" #d ":" #d1 "]\n\ts_sub_u32 s48, s48, 1\n\t" \
    "s_cbranch_vccz 4f\n\ts_cbranch_scc1 4f\n\ts_sleep 2\n\tglobal_load_dwordx2 v[" #d ":" #d1 "], v" #off ", s[54:55] sc1\n\ts_branch 5b\n\t4:\n\t"
#define G1_STAMP(i)
#define LPOS 0
#define NPOLL 22
#define NFLAG 8
#define NLD 8
#define TGAP 1
#define P2PRIO 0
#define NAW 28
#define KBAR 11
__device__ __forceinline__ void g1_all(unsigned vt1, unsigned kv1, unsigned vt2, u32x16& ad, u32x16& pk, u32x16& at, u32x8& cv, f32x32& o, f32x16& pp,
                                       const char* sX, const char* sW, unsigned off1, const char* sB, unsigned off2, unsigned m0v,
                                       unsigned long long tag, unsigned flag, char* ws, unsigned tile, u32x4 so, unsigned qoff) {
  unsigned keep;
  f32x32 d1, d2, d3, d4; f32x16 d7; f32x8 d6;
  asm volatile(
      ".macro pack vt\n\t.set i_, 0\n\t.rept 4\n\t"
      ".if \\vt\n\t.set e_, 8 * i_\n\t.set f_, 8 * i_ + 4\n\t.else\n\t.set e_, 4 * i_\n\t.set f_, 4 * i_ + 16\n\t.endif\n\t"
      "v_cvt_pk_bf16_f32 v[48+4*i_], v[e_], v[e_+1]\n\tv_cvt_pk_bf16_f32 v[49+4*i_], v[e_+2], v[e_+3]\n\t"
      "v_cvt_pk_bf16_f32 v[50+4*i_], v[f_], v[f_+1]\n\tv_cvt_pk_bf16_f32 v[51+4*i_], v[f_+2], v[f_+3]\n\t"
      ".set i_, i_ + 1\n\t.endr\n\t.set i_, 0\n\t.rept 4\n\t"
      "v_permlane16_swap_b32 v[48+4*i_], v[50+4*i_]\n\tv_permlane16_swap_b32 v[49+4*i_], v[51+4*i_]\n\t"
      ".set i_, i_ + 1\n\t.endr\n\t.endm\n\t"
      ".macro mm vt, c, x, w\n\t"
      ".if (\\x) < 104\n\t"
      ".if \\vt\n\tv_mfma_f32_16x16x32_bf16 v[\\c:\\c+3], v[\\x:\\x+3], v[\\w:\\w+3], 0\n\t"
      ".else\n\tv_mfma_f32_16x16x32_bf16 v[\\c:\\c+3], v[\\w:\\w+3], v[\\x:\\x+3], 0\n\t.endif\n\t"
      ".elseif \\vt\n\tv_mfma_f32_16x16x32_bf16 v[\\c:\\c+3], v[\\x:\\x+3], v[\\w:\\w+3], v[\\c:\\c+3]\n\t"
      ".else\n\tv_mfma_f32_16x16x32_bf16 v[\\c:\\c+3], v[\\w:\\w+3], v[\\x:\\x+3], v[\\c:\\c+3]\n\t.endif\n\t"
      ".endm\n\t"
      ".macro rx d, a, off\n\tds_read_b128 v[\\d:\\d+3], v\\a offset:\\off\n\tds_read_b128 v[\\d+4:\\d+7], v\\a offset:\\off+2048\n\t.endm\n\t"
      ".macro rw d, a, off\n\tds_read_b64_tr_b16 v[\\d:\\d+1], v\\a offset:\\off\n\tds_read_b64_tr_b16 v[\\d+2:\\d+3], v\\a offset:\\off+512\n\t.endm\n\t"
      ".macro bump lo, n, val\n\t.set bi_, \\lo\n\t.rept \\n\n\tv_add_u32 v[bi_], \\val, v[bi_]\n\t.set bi_, bi_+1\n\t.endr\n\t.endm\n\t"
      ".macro src2\n\ts_add_u32 s40, s44, s47\n\ts_addc_u32 s41, s45, 0\n\t"
      "s_add_u32 s47, s47, 2*64*3840\n\ts_cmp_ge_u32 s47, 640*3840\n\ts_cselect_b32 s48, 640*3840, 0\n\ts_sub_u32 s47, s47, s48\n\t.endm\n\t"
      ".macro ld1 i\n\t.if st_ < 7\n\t"
      ".if \\i < 2\n\tglobal_load_lds_dwordx4 v[64+\\i], s[40:41] offset:1024*\\i " XPOL "\n\t.if \\i == 1\n\ts_add_u32 m0, m0, 16384\n\t.endif\n\t"
      ".else\n\tglobal_load_lds_dwordx4 v[64+\\i], s[82:83] offset:1024*(\\i-2)\n\t.endif\n\t"
      ".else\n\t.if st_ == 9\n\ts_cbranch_scc1 8f\n\t.endif\n\tglobal_load_lds_dwordx4 v[68+\\i], s[40:41] offset:1024*\\i\n\t8:\n\t.endif\n\t.endm\n\t"
      ".macro p1body vt\n\t"
      ".set st_, 0\n\t"
      ".rept 10\n\t"
      ".set xr_, 96 + 16 * st_\n\t"
      ".set o_, ((st_ %% 5) & 1) * 32768\n\t"
      ".set n_, (((st_ + 1) %% 5) & 1) * 32768 + 16384\n\t.if st_ == 9\n\t.set n_, 0\n\t.endif\n\t"
      "s_waitcnt lgkmcnt(6)\n\t"
      "mm \\vt, 0, xr_, 32\n\trw 48, 76, o_+16384+8192\n\t"
      "mm \\vt, 16, xr_+4, 32\n\trx xr_+8, 74, o_\n\ts_waitcnt lgkmcnt(8)\n\t"
      "mm \\vt, 4, xr_, 36\n\trw 52, 77, o_+16384+8192\n\t"
      "mm \\vt, 20, xr_+4, 36\n\trw 56, 78, o_+16384+8192\n\ts_waitcnt lgkmcnt(10)\n\t"
      "mm \\vt, 8, xr_, 40\n\trw 60, 79, o_+16384+8192\n\t"
      "mm \\vt, 24, xr_+4, 40\n\ts_waitcnt lgkmcnt(10)\n\t"
      "mm \\vt, 12, xr_, 44\n\t"
      ".if st_ < 7\n\t"
      "s_add_u32 s40, s42, s36\n\ts_addc_u32 s41, s43, 0\n\ts_mul_i32 s48, s36, 64*30\n\ts_add_u32 s82, s84, s48\n\ts_addc_u32 s83, s85, 0\n\t"
      "s_add_u32 s36, s36, 128\n\ts_cmp_eq_u32 s36, 1280\n\ts_cselect_b32 s36, 0, s36\n\t"
      ".else\n\tsrc2\n\t.endif\n\t"
      "mm \\vt, 28, xr_+4, 44\n\t"
      ".if st_ < 7\n\ts_add_u32 m0, s37, ((st_ + 3) %% 5) * 32768\n\t.else\n\ts_add_u32 m0, s46, ((st_ + 3) %% 5) * 32768\n\t.endif\n\t"
      ".if (st_ %% 5) == 1 || (st_ %% 5) == 3\n\tbump 76, 4, 0x10000\n\tbump 73, 2, 0x10000\n\t.endif\n\t"
      ".if (st_ %% 5) == 4\n\tbump 76, 4, 0xfffe0000\n\t.if st_ < 9\n\tbump 73, 2, 0xfffe0000\n\t.endif\n\t.endif\n\t"
      "s_waitcnt lgkmcnt(6)\n\t"
      "mm \\vt, 0, xr_+8, 48\n\t"
      "mm \\vt, 16, xr_+12, 48\n\t"
      ".if st_ < 9\n\ts_waitcnt vmcnt(4) lgkmcnt(0)\n\t.else\n\ts_cmp_eq_u32 %[vt2], 2\n\ts_cbranch_scc1 8f\n\ts_waitcnt vmcnt(4)\n\t8:\n\ts_waitcnt lgkmcnt(0)\n\t.endif\n\ts_barrier\n\t"
      "mm \\vt, 4, xr_+8, 52\n\trw 32, 76, n_\n\t"
      "ld1 0\n\t"
      "mm \\vt, 20, xr_+12, 52\n\t"
      ".if st_ < 9\n\trx xr_+16, 73, n_-16384\n\t.endif\n\t"
      "ld1 1\n\t"
      "mm \\vt, 8, xr_+8, 56\n\trw 36, 77, n_\n\t"
      "ld1 2\n\t"
      "mm \\vt, 24, xr_+12, 56\n\trw 40, 78, n_\n\t"
      "ld1 3\n\t"
      "mm \\vt, 12, xr_+8, 60\n\trw 44, 79, n_\n\t"
      "mm \\vt, 28, xr_+12, 60\n\t"
      ".set st_, st_ + 1\n\t"
      ".endr\n\t"
      "v_mov_b64 v[64:65], v[80:81]\n\tv_add_u32 v66, " STR(OCT) "*4 - (" STR(OCT) "*4 - 2560) * \\vt, v80\n\tv_add_u32 v67, " STR(OCT) "*4 - (" STR(OCT) "*4 - 2560) * \\vt, v81\n\t"
      ".endm\n\t"
      ".macro tdk i\n\t"
      "s_add_u32 m0, s72, \\i * 28672\n\ts_add_u32 s40, s54, s[76+2*\\i]\n\ts_addc_u32 s41, s55, 0\n\t"
      "global_load_lds_dwordx4 v137, s[40:41] sc1\n\ts_mov_b32 exec_hi, 0\n\tglobal_load_lds_dwordx4 v138, s[40:41] offset:1024 sc1\n\ts_mov_b32 exec_hi, -1\n\t.endm\n\t"
      ".macro tdv i\n\t"
      "s_add_u32 s40, s54, s[77+2*\\i]\n\ts_addc_u32 s41, s55, 0\n\ts_mov_b32 exec_lo, 0xfffff\n\ts_mov_b32 exec_hi, 0xfffff\n\t"
      "global_load_lds_dwordx4 v139, s[40:41] offset:1536 sc1\n\tglobal_load_lds_dwordx4 v140, s[40:41] offset:2560 sc1\n\ts_mov_b64 exec, -1\n\t"
      ".endm\n\t"
      ".macro slot n, ld=1\n\t.set n_, \\n\n\t"
      ".set l_, n_ - %c[nld]\n\t"
      ".if n_ == %c[nflag] && \\ld\n\t.set y_, l_\n\t.if y_ < 0\n\t.set y_, 0\n\t.endif\n\t.if y_ > 8\n\t.set y_, 8\n\t.endif\n\t"
      "v_mov_b32 v96, s50\n\tv_mov_b32 v97, s51\n\tv_mov_b32 v98, s52\n\ts_mov_b64 exec, 1\n\ts_waitcnt vmcnt(y_)\n\t"
      "global_store_dwordx2 v98, v[96:97], s[54:55] sc1\n\ts_mov_b64 exec, -1\n\t.endif\n\t"
      ".if l_ >= 0 && l_ < 8 && \\ld\n\t"
      ".if (l_ & 3) == 0\n\ts_add_u32 m0, s46, (3 + l_ / 4) * 32768\n\tsrc2\n\t.endif\n\t"
      "global_load_lds_dwordx4 v[68+(l_&3)], s[40:41] offset:1024*(l_&3)\n\t"
      ".endif\n\t"
      ".if n_ == 12\n\tv_mov_b64 v[128:129], v[84:85]\n\tv_mov_b32 v130, v86\n\tv_add_u32 v131, 57344, v84\n\tv_add_u32 v132, 57344, v85\n\t.endif\n\t"
      ".if n_ == 13\n\tv_add_u32 v133, 57344, v86\n\tv_add_u32 v134, 114688, v84\n\tv_add_u32 v135, 114688, v85\n\tv_add_u32 v136, 114688, v86\n\t.endif\n\t"
      ".if n_ == 14\n\tv_mov_b32 v137, v87\n\tv_mov_b64 v[138:139], v[88:89]\n\tv_add_u32 v140, 1536, v89\n\tv_mov_b32 v141, v90\n\t.endif\n\t"
      ".if n_ == 15\n\tv_mov_b32 v144, v92\n\tv_mov_b32 v145, v92\n\tv_mov_b32 v146, v92\n\tv_mov_b32 v147, v92\n\tv_add_u32 v148, 57344, v91\n\t.endif\n\t"
      ".if n_ == 2 * %c[kbar] + 1\n\tds_write_b128 v91, v[144:147]\n\tds_write_b128 v91, v[144:147] offset:28672\n\t.endif\n\t"
      ".if n_ == %c[npoll]\n\tglobal_load_dwordx2 v[100:101], v72, s[54:55] sc1\n\t.endif\n\t"
      ".if n_ == %c[naw]\n\t" AWAIT(100, 101, 72) G1_STAMP(6) "tdk 0\n\t.endif\n\t"
      ".if n_ == %c[naw] + 1\n\ttdv 0\n\t.endif\n\t"
      ".if n_ == %c[naw] + 2\n\ts_add_u32 s40, s54, s80\n\ts_addc_u32 s41, s55, 0\n\t"
      "global_load_dwordx4 v[64:67], v137, s[40:41] sc1\n\tglobal_load_dwordx2 v[72:73], v93, s[40:41] sc1\n\t.endif\n\t"
      ".if n_ == %c[naw] + 3\n\ts_xor_b32 s48, s80, 512\n\ts_add_u32 s40, s54, s48\n\ts_addc_u32 s41, s55, 0\n\t"
      "global_load_dwordx4 v[68:71], v137, s[40:41] sc1\n\tglobal_load_dwordx2 v[74:75], v93, s[40:41] sc1\n\t.endif\n\t"
      ".if n_ == %c[naw] + 4 + %c[gap]\n\ttdk 1\n\t.endif\n\t"
      ".if n_ == %c[naw] + 5 + 2 * %c[gap]\n\ttdv 1\n\t.endif\n\t"
      ".endm\n\t"
      ".macro st1 i\n\t.if k_ == 0\n\tglobal_store_dwordx4 v[64+\\i], v[48+4*\\i:51+4*\\i], s[54:55] sc1\n\t.endif\n\t.endm\n\t"
      ".macro p2body vt\n\t"
      ".set k_, 0\n\t"
      ".rept 20\n\t"
      ".set xr_, 96 + 8 * k_\n\t"
      ".set c_, 32 + 16 * (k_ & 1)\n\t"
      ".set d_, 48 - 16 * (k_ & 1)\n\t"
      ".set o_, ((k_ + 1) & 7) * 8192\n\t"
      ".if k_ == 3\n\ts_waitcnt vmcnt(4)\n\ts_barrier\n\t.endif\n\t"
      ".if %c[prio] && k_ >= 3 && k_ < 19 && (k_ & 1)\n\ts_setprio 3 - (((k_ - 3) / 2) & 3)\n\t.endif\n\t"
      ".if k_ == %c[kbar]\n\ts_waitcnt vmcnt(0)\n\ts_barrier\n\t.endif\n\t"
      ".if k_ < 19\n\t"
      ".if ((k_ + 1) & 7) == 0\n\tbump 76, 4, 0x10000\n\t.endif\n\t"
      "s_waitcnt lgkmcnt(6)\n\t"
      "mm \\vt, 0, xr_, c_\n\tst1 0\n\trw d_, 76, o_\n\t"
      "mm \\vt, 16, xr_+4, c_\n\tslot 2*k_\n\ts_waitcnt lgkmcnt(6)\n\t"
      "mm \\vt, 4, xr_, c_+4\n\tst1 1\n\trw d_+4, 77, o_\n\t"
      "mm \\vt, 20, xr_+4, c_+4\n\ts_waitcnt lgkmcnt(6)\n\t"
      "mm \\vt, 8, xr_, c_+8\n\tst1 2\n\trw d_+8, 78, o_\n\t"
      "mm \\vt, 24, xr_+4, c_+8\n\tslot 2*k_+1\n\ts_waitcnt lgkmcnt(6)\n\t"
      "mm \\vt, 12, xr_, c_+12\n\tst1 3\n\trw d_+12, 79, o_\n\t"
      "mm \\vt, 28, xr_+4, c_+12\n\t"
      ".else\n\t"
      "s_waitcnt lgkmcnt(0)\n\t"
      "mm \\vt, 0, xr_, c_\n\tmm \\vt, 16, xr_+4, c_\n\tmm \\vt, 4, xr_, c_+4\n\tmm \\vt, 20, xr_+4, c_+4\n\t"
      "mm \\vt, 8, xr_, c_+8\n\tmm \\vt, 24, xr_+4, c_+8\n\tmm \\vt, 12, xr_, c_+12\n\tmm \\vt, 28, xr_+4, c_+12\n\t"
      "s_setprio 3\n\tv_add_u32 v80, " STR(OCT) "*4 - (" STR(OCT) "*4 - 2560) * \\vt, v82\n\tv_add_u32 v81, " STR(OCT) "*4 - (" STR(OCT) "*4 - 2560) * \\vt, v83\n\ts_nop 4\n\tpack \\vt\n\t"
      ".endif\n\t"
      ".set k_, k_ + 1\n\t"
      ".endr\n\t"
      ".endm\n\t"
      "rw 32, 76, 16384\n\trx 96, 73, 0\n\trw 36, 77, 16384\n\trw 40, 78, 16384\n\trw 44, 79, 16384\n\t"
      "s_mov_b32 %[keep], m0\n\t"
      "s_cmp_lg_u32 %[vt1], 0\n\ts_cbranch_scc1 1f\n\t"
      "p1body 0\n\t"
      "s_mov_b32 s40, %[sc]\n\ts_cmp_lg_u32 %[kv1], 0\n\ts_nop 2\n\ts_cbranch_scc1 3f\n\t"
      ".set i_, 0\n\t.rept 16\n\tv_pk_mul_f32 v[i_:i_+1], v[i_:i_+1], s[40:41] op_sel_hi:[1,0]\n\t.set i_, i_ + 2\n\t.endr\n\t"
      "3:\n\tpack 0\n\ts_branch 2f\n\t"
      "1:\n\tp1body 1\n\ts_nop 4\n\tpack 1\n\t"
      "2:\n\t" G1_STAMP(3)
      "s_cmp_eq_u32 %[vt2], 1\n\ts_cbranch_scc1 1f\n\ts_cmp_eq_u32 %[vt2], 2\n\ts_cbranch_scc0 6f\n\t"
      ".set k_, 0\n\tst1 0\n\tst1 1\n\tst1 2\n\tst1 3\n\tslot 12, 0\n\tslot 13, 0\n\tslot 14, 0\n\tslot 15, 0\n\t"
      "v_mov_b32 v96, s50\n\tv_mov_b32 v97, s51\n\tv_mov_b32 v98, s52\n\t"
      "s_waitcnt vmcnt(0) lgkmcnt(0)\n\ts_mov_b64 exec, 1\n\tglobal_store_dwordx2 v98, v[96:97], s[54:55] sc1\n\ts_mov_b64 exec, -1\n\ts_barrier\n\t"
      "slot %c[npoll]\n\t"
      "slot 2*%c[kbar]+1\n\tslot %c[naw]\n\tslot %c[naw]+1\n\tslot %c[naw]+2\n\tslot %c[naw]+3\n\ttdk 1\n\ttdv 1\n\t"
      "s_waitcnt vmcnt(0)\n\ts_setprio 3\n\ts_branch 3f\n\t"
      "6:\n\tp2body 0\n\ts_branch 2f\n\t"
      "1:\n\tp2body 1\n\t"
      "2:\n\tglobal_store_dwordx4 v82, v[48:51], s[54:55] sc1\n\tglobal_store_dwordx4 v83, v[52:55], s[54:55] sc1\n\t"
      "global_store_dwordx4 v80, v[56:59], s[54:55] sc1\n\tglobal_store_dwordx4 v81, v[60:63], s[54:55] sc1\n\t"
      "3:\n\ts_mov_b32 m0, %[keep]\n\tv_mov_b32 v76, 0xdddd\n\tv_mov_b32 v77, v94\n\tv_mov_b32 v78, v93\n\t"
      "v_mbcnt_lo_u32_b32 v79, -1, 0\n\tv_mbcnt_hi_u32_b32 v79, -1, v79\n\tv_lshlrev_b32 v79, 4, v79\n\t"
      ".set i_, 0\n\t.rept 16\n\tv_mov_b64 v[i_:i_+1], 0\n\t.set i_, i_ + 2\n\t.endr\n\t"
      "v_mov_b64 v[120:121], 0\n\tv_mov_b64 v[122:123], 0\n\tv_mov_b64 v[124:125], 0\n\tv_mov_b64 v[126:127], 0\n\t"
      "s_waitcnt vmcnt(8) lgkmcnt(0)\n\t"
      ".purgem ld1\n\t.purgem pack\n\t.purgem mm\n\t.purgem rx\n\t.purgem rw\n\t.purgem bump\n\t.purgem src2\n\t.purgem p1body\n\t.purgem tdk\n\t.purgem tdv\n\t"
      ".purgem slot\n\t.purgem st1\n\t.purgem p2body\n\t"
      : "+{v[64:79]}"(ad), "+{v[80:95]}"(pk), "=&{v[128:143]}"(at), "=&{v[144:151]}"(cv), [keep] "=&s"(keep), "+{s36}"(off1), "+{s47}"(off2),
        "=&{v[0:31]}"(o), "=&{v[32:63]}"(d1), "=&{v[160:191]}"(d2), "=&{v[192:223]}"(d3), "=&{v[224:255]}"(d4), "=&{v[112:127]}"(pp),
        "=&{v[152:159]}"(d6), "=&{v[96:111]}"(d7)
      : [vt1] "s"(vt1), [kv1] "s"(kv1), [vt2] "s"(vt2), "{s[42:43]}"(sX), "{s[84:85]}"(sW), "{s37}"(m0v >> 1), "{s[44:45]}"(sB), "{s46}"(m0v),
        "{s[50:51]}"(tag), "{s52}"(flag), "{s[54:55]}"(ws), "{s72}"(tile), "{s[76:79]}"(so), "{s80}"(qoff),
        [sc] "n"(__builtin_bit_cast(unsigned, QSCALE)), [npoll] "n"(NPOLL), [nflag] "n"(NFLAG), [nld] "n"(NLD), [prio] "n"(P2PRIO), [gap] "n"(TGAP), [naw] "n"(NAW), [kbar] "n"(KBAR)
      : "memory", "scc", "vcc", "s40", "s41", "s48", "s82", "s83");
}

__device__ __forceinline__ u32x2 g1_offs(unsigned br, unsigned cg, unsigned T, unsigned lane) {
  const unsigned l16 = (lane & 31) * 16, h = lane >> 5;
  const unsigned a = h * OCT + l16 + (br * (unsigned)QK_BR + cg * (8 * OCT) + T * 512);
  const unsigned G = (cg - 20) * 4 + ((lane >> 4) & 1);
  const unsigned b = h * 1280 + l16 + ((unsigned)V_WS + br * (unsigned)V_BR + (cg - 20) * 1024 + T * 5120);
  return cg < 20 ? u32x2{a, a + 2 * OCT}
                 : u32x2{((G * 52) >> 8) * (256 * 1280 - 1280) + b, (((G + 2) * 52) >> 8) * (256 * 1280 - 1280) + b + 512};
}

constexpr int TILE_B = 3584, K2_OFF = 1024, V_OFF = 1536;
constexpr int CHUNK_B = 8 * TILE_B;

__device__ __forceinline__ f32x16 smfmac(bf16x8 a, f32x8 p, f32x16 c) {
  return __builtin_amdgcn_smfmac_f32_32x32x32_bf16(a, __builtin_bit_cast(bf16x16, p), c, 0xDDDD, 0, 0);
}
__device__ __forceinline__ f32x8 lo8(const f32x16& s) { return __builtin_shufflevector(s, s, 0, 1, 2, 3, 4, 5, 6, 7); }
__device__ __forceinline__ f32x8 hi8(const f32x16& s) { return __builtin_shufflevector(s, s, 8, 9, 10, 11, 12, 13, 14, 15); }
#define MX3(a, b, c) __builtin_fmaxf(__builtin_fmaxf((a), (b)), (c))
__device__ __forceinline__ float max16(const f32x16& s, float m) {
#pragma unroll
  for (int r = 0; r < 16; r += 2) m = MX3(m, s[r], s[r + 1]);
  return m;
}
__device__ __forceinline__ float xhalf_max(float m) {
  auto rr = __builtin_amdgcn_permlane32_swap(__float_as_uint(m), __float_as_uint(m), false, false);
  return __builtin_fmaxf(__uint_as_float(rr[0]), __uint_as_float(rr[1]));
}
__device__ __forceinline__ float row_sum(const f32x16& o) {
  auto rr = __builtin_amdgcn_permlane32_swap(__float_as_uint(o[8]), __float_as_uint(o[8]), false, false);
  return __uint_as_float(rr[1]);
}
template <bool MARK>
__device__ __forceinline__ void store_o(const f32x16& o, float l, char* base, unsigned lo1, unsigned lo2) {
  const float rl = __builtin_amdgcn_rcpf(l);
  const unsigned a0 = pk_bf16(o[0] * rl, o[1] * rl), a1 = pk_bf16(o[2] * rl, o[3] * rl);
  const unsigned b0 = pk_bf16(o[4] * rl, o[5] * rl), b1 = pk_bf16(o[6] * rl, o[7] * rl);
  const auto r0 = __builtin_amdgcn_permlane32_swap(a0, b0, false, false);
  const auto r1 = __builtin_amdgcn_permlane32_swap(a1, b1, false, false);
  u32x4 v = {r0[0], r1[0], r0[1], r1[1]};
  u32x2 c = {pk_bf16(o[8] * rl, o[9] * rl), pk_bf16(o[10] * rl, o[11] * rl)};
  if (MARK) {
    v = __builtin_elementwise_min(v, u32x4{~1u, ~1u, ~1u, ~1u}); c = __builtin_elementwise_min(c, u32x2{~1u, ~1u});
  }
  st16(base, lo1, v);
  st8lo(base, lo2, c);
}

__device__ __forceinline__ void poll_flags(const char* ws, unsigned off, unsigned long long tag) {
  asm volatile("global_load_dwordx2 v[60:61], v62, s[54:55] sc1\n\t" AWAIT(60, 61, 62)
               :: "{v62}"(off), "{s[54:55]}"(ws), "{s[50:51]}"(tag) : "memory", "scc", "vcc", "s48", "v60", "v61");
}
__device__ __forceinline__ void put_flag(char* ws, unsigned off, unsigned long long tag) {
  asm volatile("s_mov_b64 exec, 1\n\tglobal_store_dwordx2 %0, %1, %2 sc1\n\ts_mov_b64 exec, -1" :: "v"(off), "v"(tag), "s"(ws) : "memory");
}
#define AT_ASM_MACROS \
    ".macro mx d, s\n\tv_max3_f32 v[\\d], v[\\s], v[\\s+1], v[\\s+2]\n\t.set i_, \\s + 3\n\t.rept 6\n\tv_max3_f32 v[\\d], v[\\d], v[i_], v[i_+1]\n\t" \
    ".set i_, i_ + 2\n\t.endr\n\tv_max_f32 v[\\d], v[\\d], v[\\s+15]\n\t.endm\n\t" \
    ".macro xp d, s\n\tv_exp_f32 v[\\d], v[\\s]\n\tv_exp_f32 v[\\d+1], v[\\s+1]\n\tv_exp_f32 v[\\d+2], v[\\s+2]\n\tv_exp_f32 v[\\d+3], v[\\s+3]\n\t.endm\n\t" \
    ".macro smf o, v, p\n\tv_smfmac_f32_32x32x32_bf16 v[\\o:\\o+15], v[\\v:\\v+3], v[\\p:\\p+7], v76\n\t.endm\n\t" \
    ".macro qk1 s, k, q\n\tv_mfma_f32_32x32x16_bf16 v[\\s:\\s+15], v[\\k:\\k+3], v[\\q:\\q+3], 0\n\t.endm\n\t" \
    ".macro qk1c s, k, q, c\n\tv_mfma_f32_32x32x16_bf16 v[\\s:\\s+15], v[\\k:\\k+3], v[\\q:\\q+3], v[\\c:\\c+15]\n\t.endm\n\t" \
    ".macro qk2 s, k, q\n\tv_mfma_f32_32x32x8_bf16 v[\\s:\\s+15], v[\\k:\\k+1], v[\\q:\\q+1], v[\\s:\\s+15]\n\t.endm\n\t"
#define AT_ASM_PURGE ".purgem mx\n\t.purgem xp\n\t.purgem smf\n\t.purgem qk1\n\t.purgem qk1c\n\t.purgem qk2\n\t"

#define JPOLL 3
#define AT_A_TEXT AT_ASM_MACROS \
      ".macro pos j\n\t.set sl_, (\\j) >> 2\n\t.if sl_ >= 3\n\t.set sl_, sl_ - 3\n\t.endif\n\t" \
      ".set ar_, 128 + 3 * (sl_ >> 1)\n\t.set of_, (sl_ & 1) * 28672 + ((\\j) & 3) * 7168\n\t.endm\n\t" \
      ".macro dma part\n\t" \
      ".if di_ >= 0\n\t" \
      ".if \\part == 0\n\t" \
      ".if di_ == 1\n\ts_mov_b32 m0, s73\n\t.else\n\ts_add_u32 m0, s72, ((di_ + 2) %% 5) * 28672\n\t.endif\n\t" \
      "s_add_u32 s40, s54, s[56+2*di_]\n\ts_addc_u32 s41, s55, 0\n\t" \
      "global_load_lds_dwordx4 v137, s[40:41] sc1\n\t" \
      ".elseif \\part == 1\n\t" \
      "s_mov_b32 exec_hi, 0\n\tglobal_load_lds_dwordx4 v138, s[40:41] offset:1024 sc1\n\ts_mov_b32 exec_hi, -1\n\t" \
      "s_add_u32 s40, s54, s[57+2*di_]\n\ts_addc_u32 s41, s55, 0\n\t" \
      ".else\n\t" \
      "s_mov_b32 exec_lo, 0xfffff\n\ts_mov_b32 exec_hi, 0xfffff\n\t" \
      "global_load_lds_dwordx4 v139, s[40:41] offset:1536 sc1\n\tglobal_load_lds_dwordx4 v140, s[40:41] offset:2560 sc1\n\t" \
      "s_mov_b64 exec, -1\n\t" \
      ".endif\n\t.endif\n\t" \
        \
      ".set n_, -1\n\t" \
      ".if j_ >= 22 && j_ < 25 && 3 * (j_ - 22) + \\part < 8\n\t.set n_, 3 * (j_ - 22) + \\part\n\t.endif\n\t" \
      ".if j_ >= 28 && 3 * (j_ - 28) + \\part < 9\n\t.set n_, 10 + 3 * (j_ - 28) + \\part\n\t.endif\n\t" \
      ".if n_ >= 0\n\t.set pc_, n_ %% 5\n\t" \
      ".if n_ == 0 || n_ == 10\n\ts_add_u32 m0, s44, n_ * 4096\n\ts_add_u32 s40, s42, m0\n\ts_addc_u32 s41, s43, 0\n\t.endif\n\t" \
      ".if n_ == 5\n\ts_add_u32 m0, s45, 143360 - 81920\n\ts_add_u32 s40, s42, s45\n\ts_addc_u32 s41, s43, 0\n\t.endif\n\t" \
      ".if n_ == 15\n\ts_add_u32 m0, s47, 81920\n\ts_sub_u32 s48, 102400, s47\n\ts_sub_u32 s40, s42, s48\n\ts_subb_u32 s41, s43, 0\n\t.endif\n\t" \
      ".if pc_ == 4\n\ts_add_u32 m0, m0, 4096\n\ts_add_u32 s40, s40, 4096\n\ts_addc_u32 s41, s41, 0\n\t.endif\n\t" \
      "global_load_lds_dwordx4 v79, s[40:41] offset:1024*(pc_&3) sc1\n\t.endif\n\t" \
      ".endm\n\t" \
      ".macro flg msk\n\tv_mov_b32 v142, s50\n\tv_mov_b32 v143, s51\n\tv_mov_b32 v108, s52\n\ts_mov_b64 exec, \\msk\n\ts_cbranch_execz 8f\n\ts_waitcnt vmcnt(0)\n\t" \
      "global_store_dwordx2 v108, v[142:143], s[54:55] sc1\n\t8:\n\ts_mov_b64 exec, -1\n\t.endm\n\t" \
      ".macro tabl d, r\n\tglobal_load_dwordx4 v[\\d:\\d+3], v79, s[40:41] offset:1024*(\\r)\n\t.endm\n\t" \
      ".macro old q\n\t.if (\\q) >= 0 && (\\q) < 10\n\tglobal_load_dwordx4 v[184+4*(\\q):187+4*(\\q)], v108, s[54:55] sc1\n\tv_add_u32 v108, " STR(OCT) "*4, v108\n\t.endif\n\t.endm\n\t" \
      "s_mov_b32 %[keep], m0\n\t" \
      ".set j_, %c[j0]\n\t" \
      ".rept %c[j1] - %c[j0]\n\t" \
      ".set a_, 80 + 14 * (j_ & 1)\n\t.set b_, 94 - 14 * (j_ & 1)\n\t" \
      ".if j_ == 5\n\t" AWAIT(142, 143, 141) ".set di_, 0\n\tdma 0\n\tdma 1\n\tdma 2\n\t.endif\n\t" \
        \
      ".if j_ == 7\n\ts_waitcnt vmcnt(10)\n\ts_barrier\n\tpos 8\n\tds_read_b128 v[80:83], v[ar_] offset:of_\n\tds_read_b64 v[84:85], v[ar_+1] offset:of_\n\t" \
      "ds_read_b128 v[86:89], v[ar_+2] offset:of_\n\t.endif\n\t" \
      ".if j_ == 10\n\ts_waitcnt vmcnt(1)\n\ts_barrier\n\t.endif\n\t" \
      ".if j_ == 14 || j_ == 18\n\ts_waitcnt vmcnt(0)\n\ts_barrier\n\t.endif\n\t" \
      ".if j_ == 22\n\t" AWAIT(142, 143, 141) AWAIT(110, 111, 252) "v_add_u32 v108, %c[obr], v253\n\t.endif\n\t" \
      ".if j_ == 28\n\ts_waitcnt vmcnt(10)\n\t.endif\n\t.if j_ == 30\n\ts_waitcnt lgkmcnt(0)\n\t.endif\n\t" \
      ".if j_ == 30 || j_ == 28 || j_ == 22 || j_ == 2\n\ts_barrier\n\t.endif\n\t" \
      AT_STAMP \
      ".if j_ < 2\n\ts_setprio 1 - j_\n\t.elseif j_ < 7\n\t.if j_ != 3\n\ts_setprio 3 - (j_ - 2) + (j_ > 3) * (-1)\n\t.endif\n\t.elseif j_ < 10\n\ts_setprio 9 - j_\n\t" \
      ".elseif j_ == 10 || j_ == 11\n\ts_setprio 3\n\t.elseif j_ == 12 || j_ == 13\n\ts_setprio 0\n\t" \
      ".elseif j_ < 22\n\ts_setprio 3 - ((j_ + 2) & 3)\n\t.elseif j_ >= 28\n\ts_setprio 1 - (j_ & 1)\n\t" \
      ".elseif j_ != 23 && j_ != 25\n\ts_setprio 3 - (j_ - 21) / 2\n\t.endif\n\t" \
      ".if j_ == 14\n\tflg 1\n\t" \
      "v_mov_b32 v108, 0x3f803f80\n\tv_mov_b32 v109, v108\n\tv_mov_b32 v110, v108\n\tv_mov_b32 v111, v108\n\ts_mov_b64 exec, 15\n\t" \
      "ds_write_b128 v77, v[108:111]\n\ts_mov_b64 exec, -1\n\t.endif\n\t" \
      ".set di_, -1\n\t" \
      ".if j_ == 5 || j_ == 6 || j_ == 7\n\t.set di_, j_ - 4\n\t.endif\n\t.if j_ == 10\n\t.set di_, 4\n\t.endif\n\t" \
      ".if j_ >= 14 && j_ < 17\n\t.set di_, j_ - 9\n\t.endif\n\t" \
        \
      "pos j_+2\n\t" \
      "qk1 48, a_, 68\n\t.if j_ < 30 && j_ != 6\n\tds_read_b128 v[a_:a_+3], v[ar_] offset:of_\n\t.endif\n\told 4*(j_-25)\n\txp 112, 32\n\t" \
      "qk2 48, a_+4, 74\n\t.if j_ < 30 && j_ != 6\n\tds_read_b64 v[a_+4:a_+5], v[ar_+1] offset:of_\n\t.endif\n\told 4*(j_-25)+1\n\txp 116, 36\n\t" \
      "pos j_+1\n\t" \
      "smf 16, b_+10, 120\n\t.if j_ < 30\n\tds_read_b128 v[b_+10:b_+13], v[ar_+2] offset:of_+1024\n\t.endif\n\told 4*(j_-25)+2\n\txp 120, 40\n\t" \
      "smf 0, a_+6, 112\n\t.if j_ < 30 && j_ != 6\n\ts_waitcnt lgkmcnt(3)\n\t.elseif j_ == 6\n\ts_waitcnt lgkmcnt(1)\n\t.endif\n\t" \
      "old 4*(j_-25)+3\n\t" \
      ".if j_ == 6\n\tglobal_load_dwordx4 v[236:239], v137, s[42:43] sc1\n\tglobal_load_dwordx2 v[240:241], v78, s[42:43] sc1\n\t.endif\n\t" \
      "xp 124, 44\n\t" \
        \
      ".if j_ < 31\n\tqk1 32, b_, 64\n\t.endif\n\tdma 0\n\t" \
      ".if j_ == 0\n\tflg s[74:75]\n\t.endif\n\t" \
      ".if j_ == " STR(JPOLL) "\n\tglobal_load_dwordx2 v[142:143], v141, s[54:55] sc1\n\t.endif\n\t" \
      ".if j_ == 1\n\ts_mov_b64 s[40:41], s[44:45]\n\ttabl 252, 0\n\ttabl 160, 1\n\t.endif\n\t" \
      ".if j_ == 2\n\ttabl 224, 2\n\ttabl 228, 3\n\t.endif\n\t" \
      ".if j_ == 20\n\tglobal_load_dwordx2 v[142:143], v141, s[54:55] sc1\n\tglobal_load_dwordx2 v[110:111], v252, s[54:55] sc1\n\t.endif\n\t" \
      ".if j_ == 29\n\tpos 31\n\tds_read_b128 v[108:111], v[ar_+2] offset:of_+1024\n\t.endif\n\t" \
      "xp 112, 48\n\t" \
      ".if j_ < 31\n\tqk2 32, b_+4, 72\n\t.endif\n\tdma 1\n\t" \
      ".if j_ == 1\n\ttabl 164, 2\n\ttabl 168, 3\n\ts_add_u32 s40, s40, 4096\n\ts_addc_u32 s41, s41, 0\n\t.endif\n\t" \
      ".if j_ == 2\n\ts_add_u32 s40, s40, 4096\n\ts_addc_u32 s41, s41, 0\n\ttabl 232, 0\n\t.endif\n\t" \
      "xp 116, 52\n\t" \
      ".if j_ < 31\n\tsmf 0, a_+10, 120\n\t.else\n\tsmf 0, 108, 120\n\t.endif\n\tdma 2\n\t" \
      ".if j_ == 1\n\ttabl 172, 0\n\ttabl 220, 1\n\t.endif\n\t" \
      ".if j_ == 9\n\tv_lshrrev_b32 v108, 5, v79\n\tv_mul_u32_u24 v108, 1248, v108\n\ts_mov_b32 m0, s47\n\tv_add_u32 v108, v108, v79\n\t" \
      "global_load_lds_dwordx4 v108, s[76:77]\n\t.endif\n\t" \
      ".if j_ == 24\n\ts_mov_b32 m0, 40960\n\tv_min_u32 v140, 384, v79\n\tv_lshl_add_u32 v140, v140, 3, s46\n\tglobal_load_lds_dword v140, s[54:55] sc1\n\t.endif\n\t" \
      "xp 120, 56\n\t" \
      "pos j_+2\n\t" \
      "smf 16, a_+6, 112\n\t.if j_ < 30 && j_ != 6\n\tds_read_b128 v[a_+6:a_+9], v[ar_+2] offset:of_\n\t.endif\n\txp 124, 60\n\t" \
      ".set j_, j_ + 1\n\t" \
      ".endr\n\t" \
      ".if %c[j1] == 32\n\ts_nop 1\n\tsmf 16, 108, 120\n\ts_nop 15\n\t.endif\n\t" \
      "s_mov_b32 m0, %[keep]\n\t" \
      ".purgem pos\n\t.purgem dma\n\t.purgem flg\n\t.purgem tabl\n\t.purgem old\n\t" AT_ASM_PURGE
#define AT_STAMP
#define AT_STV
#define AT_STV_ARG
#define AT_STV_PASS
#define AT_A_STATE "+{v[0:31]}"(o), "+{v[32:47]}"(s0), "+{v[112:127]}"(pp), "+{v[80:111]}"(fr), "+{v[128:143]}"(ad), [keep] "=&s"(keep)
#define AT_A_CLOB "memory", "scc", "vcc", "s40", "s41", "s48", "v48", "v49", "v50", "v51", "v52", "v53", "v54", "v55", "v56", "v57", "v58", "v59", "v60", "v61", "v62", "v63"
__device__ __forceinline__ void at_a1(f32x32& o, f32x16& s0, f32x16& pp, f32x32& fr, const u32x16& q, u32x16& ad, const char* ws, u32x16 so,
                                      unsigned m0v, unsigned m0b, unsigned long long tag, unsigned flag, unsigned long long fmask,
                                      const char* qB, const void* tab, f32x16& mF, f32x16& mD, u32x8& qv, u32x4& tw,
                                      const char* fsrc, unsigned fl AT_STV_ARG) {
  unsigned keep;
  asm volatile(AT_A_TEXT
      : AT_A_STATE, "=&{v[160:175]}"(mF), "=&{v[220:235]}"(mD), "=&{v[236:243]}"(qv), "=&{v[252:255]}"(tw) AT_STV
      : "{v[64:79]}"(q), "{s[54:55]}"(ws), "{s[56:71]}"(so), "{s72}"(m0v), "{s73}"(m0b), "{s[50:51]}"(tag), "{s52}"(flag), "{s[74:75]}"(fmask),
        "{s[42:43]}"(qB), "{s[44:45]}"(tab), "{s[76:77]}"(fsrc), "{s47}"(fl), [j0] "n"(0), [j1] "n"(12), [obr] "n"(O_BR)
      : AT_A_CLOB);
}
__device__ __forceinline__ void at_a2(f32x32& o, f32x16& s0, f32x16& pp, f32x32& fr, const u32x16& q, u32x16& ad, const char* ws, u32x16 so,
                                      unsigned m0v, unsigned long long tag, unsigned flag, const u32x4& tw, const char* src, unsigned d0,
                                      unsigned d1, unsigned tch, unsigned ea, f32x32& of, f32x8& of2) {
  unsigned keep;
  asm volatile(AT_A_TEXT
      : AT_A_STATE, "=&{v[184:215]}"(of), "=&{v[216:223]}"(of2)
      : "{v[64:79]}"(q), "{s[54:55]}"(ws), "{s[56:71]}"(so), "{s72}"(m0v), "{s[50:51]}"(tag), "{s52}"(flag),
        "{v[252:255]}"(tw), "{s[42:43]}"(src), "{s44}"(d0), "{s45}"(d1), "{s46}"(tch), "{s47}"(ea), [j0] "n"(12), [j1] "n"(32), [obr] "n"(O_BR)
      : AT_A_CLOB);
}

__device__ __forceinline__ void at_b(f32x16& o, f32x16& mF, const f32x16& mD, u32x8& q, unsigned tb, unsigned qt, char* oA, const u32x16& qa,
                                     u32x16& ad, f32x32& k0, f32x16& k1, f32x32& k2, f32x16& k3, unsigned fl, unsigned fd, char* ws) {
  asm volatile(AT_ASM_MACROS
      "v_add_u32 v242, %[tb], v128\n\tv_add_u32 v243, %[tb], v129\n\tv_add_u32 v245, %[tb], v130\n\t"
      "ds_read_b128 v[176:179], v242 offset:7*3584\n\tds_read_b64 v[180:181], v243 offset:7*3584\n\t"
      "ds_read_b128 v[184:187], v242 offset:8*3584\n\tds_read_b64 v[182:183], v243 offset:8*3584\n\t"
      "ds_read_b128 v[192:195], v242\n\tds_read_b64 v[196:197], v243\n\tds_read_b128 v[206:209], v242 offset:3584\n\tds_read_b64 v[210:211], v243 offset:3584\n\t"
      "ds_read_b128 v[198:201], v245\n\tds_read_b128 v[212:215], v245 offset:3584\n\tds_read_b128 v[202:205], v245 offset:1024\n\t"
      "s_mov_b32 exec_lo, 0\n\tv_mov_b64 v[240:241], 0\n\ts_mov_b32 exec_lo, -1\n\tv_mov_b64 v[188:189], -1\n\tv_mov_b64 v[190:191], -1\n\t"
      "s_waitcnt lgkmcnt(7)\n\t"
      "qk1 144, 176, 236\n\tqk2 144, 180, 240\n\tqk1c 48, 184, 236, 220\n\tqk2 48, 182, 240\n\t"
      "global_store_dwordx4 v137, v[188:191], s[42:43] sc1\n\ts_mov_b32 exec_hi, 0\n\tglobal_store_dwordx2 v78, v[188:189], s[42:43] sc1\n\ts_mov_b32 exec_hi, -1\n\t"
      "s_waitcnt lgkmcnt(5)\n\tqk1c 160, 192, 236, 160\n\tds_read_b128 v[192:195], v242 offset:7168\n\ts_cmp_eq_u32 %[qt], 0\n\ts_nop 1\n\t"
      "mx 216, 144\n\tmx 218, 48\n\ts_cbranch_scc1 1f\n\tv_max_f32 v218, v218, v216\n\t1:\n\tv_mov_b32 v219, v218\n\t"
      "v_mov_b64 v[144:145], 0\n\tv_mov_b64 v[146:147], 0\n\tv_permlane32_swap_b32 v218, v219\n\tv_max_f32 v218, v218, v219\n\tv_add_f32 v218, 0x42600000, v218\n\t"
      "s_mov_b32 exec_lo, 0\n\tv_cvt_pk_bf16_f32 v240, -v218, 0\n\ts_mov_b32 exec_lo, -1\n\tv_mov_b64 v[148:149], 0\n\t"
      "qk2 160, 196, 240\n\tds_read_b64 v[196:197], v243 offset:7168\n\t"
      ".set i_, 150\n\t.rept 5\n\tv_mov_b64 v[i_:i_+1], 0\n\t.set i_, i_ + 2\n\t.endr\n\t"
      "s_waitcnt lgkmcnt(2)\n\ts_nop 1\n\t"
      ".set t_, 0\n\t"
      ".rept 9\n\t"
      ".set a_, 192 + 14 * (t_ & 1)\n\t.set b_, 206 - 14 * (t_ & 1)\n\t.set c_, 160 - 112 * (t_ & 1)\n\t.set d_, 48 + 112 * (t_ & 1)\n\t"
      ".if t_ == 0 || t_ == 5\n\ts_setprio 2 - t_ / 5\n\t.endif\n\t"
      ".if t_ < 7\n\tqk1 d_, b_, 236\n\t.elseif t_ == 7\n\tqk1c d_, b_, 236, 220\n\t.endif\n\t"
      ".if t_ < 6\n\tds_read_b128 v[b_:b_+3], v242 offset:(t_+3)*3584\n\t.endif\n\t"
      "xp 176, c_\n\t"
      ".if t_ < 8\n\tqk2 d_, b_+4, 240\n\t.endif\n\t"
      ".if t_ < 6\n\tds_read_b64 v[b_+4:b_+5], v243 offset:(t_+3)*3584\n\t.endif\n\t"
      ".if t_ == 4\n\tv_and_b32 v142, 0xf0, v79\n\tv_lshrrev_b32 v142, 1, v142\n\tv_sub_u32 v142, v79, v142\n\tv_add_u32 v142, %[fl], v142\n\ts_waitcnt vmcnt(6)\n\t"
      "ds_read_b64_tr_b16 v[108:109], v142\n\tds_read_b64_tr_b16 v[110:111], v142 offset:128\n\tv_add_u32 v142, %[fd], v79\n\t.endif\n\t"
      ".if t_ == 6\n\tglobal_store_dwordx4 v142, v[108:111], %[ws] sc1\n\t.endif\n\t"
      "xp 180, c_+4\n\t"
      ".if t_ > 0\n\tsmf 144, b_+10, 184\n\t.endif\n\t"
      ".if t_ < 8\n\tds_read_b128 v[b_+10:b_+13], v245 offset:(t_+1)*3584+1024\n\t.endif\n\t"
      "xp 184, c_+8\n\t"
      "smf 144, a_+6, 176\n\t"
      ".if t_ < 6\n\ts_waitcnt lgkmcnt(3)\n\t.elseif t_ < 8\n\ts_waitcnt lgkmcnt(1)\n\t.else\n\ts_waitcnt lgkmcnt(0)\n\t.endif\n\t"
      ".if t_ < 7\n\tds_read_b128 v[a_+6:a_+9], v245 offset:(t_+2)*3584\n\t.endif\n\t"
      "xp 188, c_+12\n\t"
      ".set t_, t_ + 1\n\t"
      ".endr\n\t"
      "s_nop 1\n\tsmf 144, 202, 184\n\ts_nop 11\n\t"
      AT_ASM_PURGE
      : "=&{v[144:159]}"(o), "+{v[160:175]}"(mF), "+{v[0:31]}"(k0), "+{v[32:47]}"(k1), "+{v[80:111]}"(k2), "+{v[112:127]}"(k3), "+{v[128:143]}"(ad),
        "+{v[236:243]}"(q)
      : "{v[220:235]}"(mD), "{v[64:79]}"(qa), [tb] "s"(tb), [qt] "s"(qt), "{s[42:43]}"(oA), [fl] "s"(fl), [fd] "s"(fd), [ws] "s"(ws)
      : "memory", "scc", "v245", "v48", "v49", "v50", "v51", "v52", "v53", "v54", "v55", "v56", "v57", "v58", "v59", "v60", "v61", "v62", "v63",
        "v176", "v177", "v178", "v179", "v180", "v181", "v182", "v183", "v184", "v185", "v186", "v187", "v188", "v189", "v190", "v191",
        "v192", "v193", "v194", "v195", "v196", "v197", "v198", "v199", "v200", "v201", "v202", "v203", "v204", "v205", "v206", "v207",
        "v208", "v209", "v210", "v211", "v212", "v213", "v214", "v215", "v216", "v217", "v218", "v219");
}

#define PJ_STAMP(i)
#define KLD 5
#define LPK 2
__device__ __forceinline__ void proj_all(const f32x32& of, const f32x8& of2, unsigned l16, const u32x4& tw, const char* src, unsigned pos, unsigned pos2,
                                         unsigned x, unsigned grp, const char* ws, char* out, unsigned long long m5 AT_STV_ARG) {
  f32x32 t0, t1, t2; f32x16 t3, t4, t5; f32x8 t6, t7; f32x4 t8; u32x2 t9;
  asm volatile(
      ".macro rdw k, sp, ab\n\t.set d_, 80 + 20 * ((\\k) & 1)\n\t"
      ".if (\\k) < \\sp\n\t.set a_, \\ab\n\t.set o_, (\\k) * 5120\n\t.else\n\t.set a_, \\ab+1\n\t.set o_, ((\\k) - \\sp) * 5120\n\t.endif\n\t.endm\n\t"
      ".macro rd1 i\n\tds_read_b128 v[d_+4*\\i:d_+4*\\i+3], v[a_] offset:o_+1024*\\i\n\t.endm\n\t"
      ".macro rd5\n\trd1 0\n\trd1 1\n\trd1 2\n\trd1 3\n\trd1 4\n\t.endm\n\t"
      ".macro pm i, ac, of, last\n\t"
      ".if k_ == 0\n\tv_mfma_f32_16x16x32_bf16 v[\\ac+4*\\i:\\ac+4*\\i+3], v[c_+4*\\i:c_+4*\\i+3], v[\\of:\\of+3], 0\n\t"
      ".else\n\tv_mfma_f32_16x16x32_bf16 v[\\ac+4*\\i:\\ac+4*\\i+3], v[c_+4*\\i:c_+4*\\i+3], v[\\of+4*k_:\\of+3+4*k_], v[\\ac+4*\\i:\\ac+4*\\i+3]\n\t.endif\n\t"
      ".if k_ < \\last\n\trd1 \\i\n\ts_waitcnt lgkmcnt(4)\n\t.elseif \\i < 4\n\ts_waitcnt lgkmcnt(3 - \\i)\n\t.endif\n\t.endm\n\t"
      ".macro ld1 q\n\t.if (\\q) >= 0 && (\\q) < 10\n\tv_add_u32 v72, " STR(OCT) "*4*(\\q), v253\n\t"
      "global_load_dwordx4 v[32+4*(\\q):35+4*(\\q)], v72, s[54:55] sc1\n\t.endif\n\t.endm\n\t"
      ".macro wp n, hf\n\t.set n_, \\n\n\t.if n_ < 4 + \\hf\n\t"
      ".if (n_ & 3) == 0\n\ts_add_u32 m0, s[44+8*\\hf], 81920 * (1 - \\hf) + 1024 * n_\n\ts_add_u32 s49, s[44+8*\\hf], 61440 * \\hf + 1024 * n_\n\t"
      "s_add_u32 s40, s42, s49\n\ts_addc_u32 s41, s43, 0\n\t.endif\n\t"
      "global_load_lds_dwordx4 v79, s[40:41] offset:1024*(n_&3) sc1\n\t.endif\n\t.endm\n\t"
      ".macro snd ac, off\n\tds_write_b128 v124, v[\\ac+8:\\ac+11] offset:\\off\n\tds_write_b128 v124, v[\\ac+12:\\ac+15] offset:\\off+1024\n\t"
      "ds_write_b128 v124, v[\\ac+16:\\ac+19] offset:\\off+2048\n\t.endm\n\t"
      ".macro rcv off\n\tds_read_b128 v[128:131], v125 offset:\\off\n\tds_read_b128 v[132:135], v125 offset:\\off+1024\n\tds_read_b128 v[136:139], v125 offset:\\off+2048\n\t.endm\n\t"
      ".macro fin ac, off\n\t"
      "v_pk_add_f32 v[128:129], v[128:129], v[\\ac:\\ac+1]\n\tv_pk_add_f32 v[130:131], v[130:131], v[\\ac+2:\\ac+3]\n\t"
      "v_pk_add_f32 v[132:133], v[132:133], v[\\ac+4:\\ac+5]\n\tv_pk_add_f32 v[134:135], v[134:135], v[\\ac+6:\\ac+7]\n\t"
      "v_cvt_pk_bf16_f32 v140, v128, v129\n\tv_cvt_pk_bf16_f32 v141, v130, v131\n\tv_cvt_pk_bf16_f32 v142, v132, v133\n\tv_cvt_pk_bf16_f32 v143, v134, v135\n\t"
      "v_pk_add_f32 v[136:137], v[136:137], v[\\ac+16:\\ac+17]\n\tv_pk_add_f32 v[138:139], v[138:139], v[\\ac+18:\\ac+19]\n\t"
      "v_permlane16_swap_b32 v140, v142\n\tv_permlane16_swap_b32 v141, v143\n\t"
      "v_cvt_pk_bf16_f32 v136, v136, v137\n\tv_cvt_pk_bf16_f32 v137, v138, v139\n\t"
      "global_store_dwordx4 v254, v[140:143], s[56:57] offset:\\off\n\t"
      "s_mov_b64 exec, s[58:59]\n\tglobal_store_dwordx2 v255, v[136:137], s[56:57] offset:\\off\n\ts_mov_b64 exec, -1\n\t.endm\n\t"
      "s_mul_i32 s48, s46, 51200\n\tv_add_u32 v120, s48, v79\n\trdw 0, 6, 120\n\trd5\n\t"
      "s_mul_i32 s49, s46, 143360 - 30720\n\ts_add_u32 s49, s49, 30720\n\tv_add_u32 v121, s49, v79\n\t"
      "s_mul_i32 s48, s46, 20480\n\tv_add_u32 v123, s48, v79\n\ts_mul_i32 s48, s46, 30720\n\ts_add_u32 s48, s48, 81920\n\tv_add_u32 v122, s48, v79\n\t"
      "s_add_u32 s48, s45, 51200\n\tv_add_u32 v124, s48, v79\n\ts_mul_i32 s49, s46, 24576\n\ts_sub_u32 s49, 12288, s49\n\tv_add_u32 v125, s49, v124\n\t"
      "s_barrier\n\ts_waitcnt lgkmcnt(4)\n\t"
      ".set k_, 0\n\t.rept 10\n\t.set c_, 80 + 20 * (k_ & 1)\n\trdw k_+1, 6, 120\n\t"
      ".if k_ == 0 || k_ == 3 || k_ == 6\n\ts_setprio 2 - k_ / 3\n\t.endif\n\t"
      ".set z_, %c[lpk] * (k_ - %c[kld])\n\t"
      "pm 0, 0, 184, 9\n\twp 2*k_, 0\n\tld1 z_\n\tpm 1, 0, 184, 9\n\t.if %c[lpk] > 3\n\tld1 z_+3\n\t.endif\n\tpm 2, 0, 184, 9\n\twp 2*k_+1, 0\n\tld1 z_+1\n\tpm 3, 0, 184, 9\n\t"
      ".if %c[lpk] > 2\n\tld1 z_+2\n\t.endif\n\tpm 4, 0, 184, 9\n\t"
      ".set k_, k_ + 1\n\t.endr\n\t"
      "s_setprio 3\n\ts_mov_b32 s48, 0x40000\n\ts_waitcnt vmcnt(10)\n\ts_barrier\n\t"
      PJ_STAMP(16)
      "rdw 0, 6, 122\n\trd5\n\tsnd 0, 0\n\twp 0, 1\n\twp 1, 1\n\twp 2, 1\n\twp 3, 1\n\twp 4, 1\n\ts_waitcnt vmcnt(5)\n\t"
      "v_max3_u32 v72, v32, v33, v34\n\t.set q_, 35\n\t.rept 18\n\tv_max3_u32 v72, v72, v[q_], v[q_+1]\n\t.set q_, q_ + 2\n\t.endr\n\t"
      "v_max_u32 v72, v72, v71\n\tv_cmp_eq_u32 vcc, -1, v72\n\ts_nop 0\n\ts_cbranch_vccz 4f\n\t"
      "5:\n\ts_mov_b32 s49, 0\n\t.set q_, 0\n\t.rept 10\n\t"
      "v_max3_u32 v72, v[32+4*q_], v[33+4*q_], v[34+4*q_]\n\tv_max_u32 v72, v72, v[35+4*q_]\n\tv_cmp_eq_u32 vcc, -1, v72\n\t"
      "v_add_u32 v73, " STR(OCT) "*4*q_, v253\n\ts_cbranch_vccz 6f\n\t"
      "global_load_dwordx4 v[32+4*q_:35+4*q_], v73, s[54:55] sc1\n\ts_mov_b32 s49, 1\n\t6:\n\t.set q_, q_ + 1\n\t.endr\n\t"
      "s_cmp_eq_u32 s49, 0\n\ts_cbranch_scc1 4f\n\ts_sub_u32 s48, s48, 1\n\ts_waitcnt vmcnt(0)\n\ts_cbranch_scc0 5b\n\t4:\n\t"
      PJ_STAMP(17)
      "s_waitcnt lgkmcnt(0)\n\t"
      ".set k_, 0\n\t.rept 6\n\t.set c_, 80 + 20 * (k_ & 1)\n\trdw k_+1, 6, 122\n\t"
      "pm 0, 160, 32, 5\n\tpm 1, 160, 32, 5\n\tpm 2, 160, 32, 5\n\tpm 3, 160, 32, 5\n\tpm 4, 160, 32, 5\n\t"
      ".set k_, k_ + 1\n\t.endr\n\t"
      "s_waitcnt vmcnt(0)\n\ts_barrier\n\t"
      PJ_STAMP(18)
      "rcv 0\n\trd5\n\ts_waitcnt lgkmcnt(4)\n\t"
      ".rept 4\n\t.set c_, 80 + 20 * (k_ & 1)\n\trdw k_+1, 6, 122\n\t"
      "pm 0, 160, 32, 9\n\tpm 1, 160, 32, 9\n\tpm 2, 160, 32, 9\n\tpm 3, 160, 32, 9\n\tpm 4, 160, 32, 9\n\t"
      ".if k_ == 6\n\tfin 0, 1280\n\t.endif\n\t"
      ".set k_, k_ + 1\n\t.endr\n\t"
      "s_nop 6\n\tsnd 160, 86016-51200\n\ts_waitcnt lgkmcnt(0)\n\ts_barrier\n\t"
      PJ_STAMP(20)
      "rcv 86016-51200\n\ts_waitcnt lgkmcnt(0)\n\tfin 160, 0\n\t"
      ".purgem rdw\n\t.purgem rd1\n\t.purgem rd5\n\t.purgem pm\n\t.purgem ld1\n\t.purgem wp\n\t.purgem snd\n\t.purgem rcv\n\t.purgem fin\n\t"
      : "=&{v[0:31]}"(t0), "=&{v[32:63]}"(t1), "=&{v[80:111]}"(t2), "=&{v[112:127]}"(t3), "=&{v[128:143]}"(t4), "=&{v[160:175]}"(t5),
        "=&{v[64:71]}"(t6), "=&{v[176:183]}"(t7), "=&{v[72:73]}"(t9) AT_STV
      : "{v[184:215]}"(of), "{v[216:223]}"(of2), "{v79}"(l16), "{v[252:255]}"(tw), "{s[42:43]}"(src), "{s44}"(pos), "{s52}"(pos2), "{s45}"(x), "{s46}"(grp),
        "{s[54:55]}"(ws), "{s[56:57]}"(out), "{s[58:59]}"(m5), [kld] "n"(KLD), [lpk] "n"(LPK)
      : "memory", "scc", "vcc", "s40", "s41", "s48", "s49");
}
typedef __attribute__((address_space(3))) f32x4* lds_f4;

struct LaneTab { unsigned v[9][64][4]; };
constexpr LaneTab make_tab() {
  LaneTab t = {};
  for (unsigned l = 0; l < 64; ++l) {
    const unsigned g = l >> 4;
    t.v[0][l][0] = (l & 15) * 64 * 128;
    t.v[0][l][1] = g * OCT + (l & 15) * 16;
    t.v[0][l][2] = (l & 15) * 2560 + (g & 1) * 32 + (g >> 1) * 16;
    t.v[0][l][3] = (l & 15) * 2560 + 128 + g * 8;
    for (unsigned r = 0; r < 16; ++r) {
      const unsigned key = (r & 3) + 8 * (r >> 2) + 4 * (l >> 5);
      t.v[1 + (r >> 2)][l][r & 3] = key > (l & 31) ? 0u : 0xFF800000u;
      t.v[5 + (r >> 2)][l][r & 3] = key <= (l & 31) ? 0u : 0xFF800000u;
    }
  }
  return t;
}
__device__ const LaneTab lane_tab = make_tab();
constexpr unsigned CODE_B = 34816;

__global__ void __launch_bounds__(512, 2)
k_fused(const char* __restrict__ xa_, const char* __restrict__ wa_, const char* __restrict__ woa_, const char* __restrict__ xb_,
        const char* __restrict__ wb_, const char* __restrict__ wob_, char* __restrict__ ws_, char* __restrict__ out_) {
  __shared__ __attribute__((aligned(16))) char lds[163840];
  const unsigned bid = blockIdx.x, xcd = bid & 7, slot = bid >> 3, br = xcd >> 2;
  u32x4 ka; u32x2 kb; u32x8 kc;
  {
    const void* kp = (const void*)__builtin_amdgcn_kernarg_segment_ptr();
    asm volatile("s_load_dwordx4 %0, %3, %4\n\ts_load_dwordx8 %2, %3, 0x20\n\ts_load_dwordx2 %1, %3, 0x10"
                 : "=&s"(ka), "=&s"(kb), "=&s"(kc) : "s"(kp), "s"(br * 24));
  }
  STAMP_DECL STAMP(0);
  unsigned lane = threadIdx.x & 63;
  const unsigned w = __builtin_amdgcn_readfirstlane(threadIdx.x >> 6);
  const unsigned grp = w >> 2, lw = w & 3;
  const unsigned mt = (xcd & 3) * 4 + (slot & 3), u = slot >> 2;
  const unsigned win = (5 * ((mt >> 1) & 3)) >> 1;
  const unsigned kv1 = (u + 3) >> 3;
  const unsigned j1 = win + u - 5;
#define ROT ((5 * (u & 3)) / 2 + 5 * (slot & 1) + 3 * ((xcd >> 1) & 1) + (xcd & 1))
  auto mod10 = [](unsigned x) { return __builtin_elementwise_min(x, x - 10); };
  const unsigned rot = mod10(ROT);
  const unsigned m0v = w * 4096;
  u32x4 o1; unsigned tw;
  {
    const unsigned row = lane >> 3, pos = lane & 7;
    tw = row * 3840 + ((pos ^ (row & 2)) * 16);
    o1[0] = row * 1280 + ((pos ^ (row >> 1)) * 16);
    o1[1] = (o1[0] ^ 64) + (8 * 1280 - 1024);
    o1[2] = (tw ^ ((w & 1) * 64)) + 1024;
    o1[3] = (tw ^ ((w & 1) * 64)) + (128 + kv1 * 1152);
  }
  const unsigned offX = (mt * 128 + 16 * w) * 1280, offW = 8 * w * 3840 + (kv1 ? 1280 + j1 * 128 : u * 256);
  asm volatile("s_waitcnt lgkmcnt(0)" : "+s"(ka), "+s"(kb), "+s"(kc));
  auto ptr = [](unsigned lo, unsigned hi) { return (char*)(((unsigned long long)hi << 32) | lo); };
  const char* sX = ptr(ka[0], ka[1]) + offX;
  const char* sW = ptr(ka[2], ka[3]) + (offW - 1024ll);
  char* const ws = ptr(kc[4], kc[5]);
  glds_xw(sX, sW, rot, o1, w * 2048);
  u32x2 ep; unsigned tch[4];
  asm volatile("s_load_dwordx2 %0, %1, %2" : "=&s"(ep) : "s"(ws), "s"((unsigned)CTL_WS));
  {
    const unsigned i64 = (slot * 8 + w) * 64;
    unsigned long long pc;
    asm volatile("s_getpc_b64 %0\n\ts_load_dword %1, %0, %5\n\ts_load_dword %2, %0, %6\n\ts_load_dword %3, %0, %7\n\ts_load_dword %4, %8, %9"
                 : "=&s"(pc), "=&s"(tch[0]), "=&s"(tch[1]), "=&s"(tch[2]), "=&s"(tch[3])
                 : "s"(i64), "s"(i64 + 16384), "s"(__builtin_elementwise_min(i64 + 32768, CODE_B)), "s"(&lane_tab),
                   "s"(__builtin_elementwise_min(i64, 9152u)));
  }
  const unsigned j2 = mod10(win + 3 + u);
  const unsigned Tg = mt * 4 + lw;
  const unsigned vt1 = grp & kv1, vt2 = u < 7 ? grp : 2;
  const unsigned cg1 = kv1 ? 10 + 10 * grp + j1 : 2 * u + grp, cg2 = 10 + 10 * grp + j2;
  const unsigned head = xcd * 4 + (slot & 3), qb = u;
  const unsigned odd = head & 1, o2 = 5 * (head >> 1) + 2, o1h = odd ? o2 + 1 : o2 - 2;
  const unsigned qt = qb * 8 + 2 * lw + grp;
  u32x4 soA; u32x16 so;
  {
    const unsigned kb0 = 80 * OCT + w * 512, vb0 = (unsigned)V_WS - V_OFF + xcd * (256 * 1280) + w * 5120;
    unsigned ko[10], vo_[10];
#pragma unroll
    for (int i = 0; i < 8; ++i) { const unsigned c = (((head >> 3) + (i >> 1)) & 3) + 4 * (i & 1); ko[i] = kb0 + c * 4096; vo_[i] = vb0 + c * 40960; }
    const unsigned c8 = qb ? qb - 1 : 0;
    ko[8] = kb0 + (unsigned)QK_BR + c8 * 4096; vo_[8] = vb0 + (unsigned)V_BR + c8 * 40960;
    ko[9] = kb0 + (unsigned)QK_BR + qb * 4096; vo_[9] = vb0 + (unsigned)V_BR + qb * 40960;
    soA = u32x4{ko[0], vo_[0], ko[1], vo_[1]};
    so = u32x16{ko[2], vo_[2], ko[8], vo_[8], ko[9], vo_[9], ko[3], vo_[3], ko[4], vo_[4], ko[5], vo_[5], ko[6], vo_[6], ko[7], vo_[7]};
    asm volatile("" : "+{s[76:79]}"(soA), "+{s[56:71]}"(so));
  }
  unsigned flag1 = FQ_OFF + ((br * 30 + cg1) * 64 + Tg) * 128, flag2 = FQ_OFF + ((br * 30 + cg2) * 64 + Tg) * 128;
  unsigned flagO = FO_OFF + (head * 64 + qt) * 128;
  PINS(flag1); PINS(flag2); PINS(flagO);
  glds_xw(sX, sW, mod10(rot + 1), o1, w * 2048 + 32768);
  const char* sB = ptr(ka[2], ka[3]) + (16 * lw * 3840 + 256 + j2 * 128);
  const unsigned mytile = w * TILE_B;
  const char* fsrc; unsigned fdst;
  {
    const unsigned F = __builtin_elementwise_min((head * 8 + qb) * 8 + w, 1599u);
    const unsigned bl = (F * 13108) >> 16, np = F - 5 * bl, t = (bl * 3277) >> 16, ps = bl - 20 * t;
    const unsigned ks = ((t & 1) || ps < 6 || ps >= 16) ? ps : (ps < 12 ? ps + 4 : ps - 6);
    fsrc = ptr((t & 1) ? kc[2] : kb[0], (t & 1) ? kc[3] : kb[1]) + (ks * (32 * 1280) + (t >> 1) * 160 + ((ks >= 10 && np < 4) ? np ^ 2 : np) * 32);
    fdst = (unsigned)WP_WS + ((head * 8 + qb) * 8 + w) * 1024;
  }
  u32x4 late = {FO_OFF + (grp * 16 * 64 + head * 2 + (lw >> 1)) * 128,
                (unsigned)O_WS + grp * 40 * OCT + (head * 64 + lw * 16) * 16,
                (unsigned)WP_WS + qb * 204800,
                (head * 64 + lw * 16) * 2560 + qb * 160};
  asm volatile("" : "+s"(fsrc), "+s"(fdst), "+s"(late));
  u32x16 qq, pk, at;
  {
    const unsigned r16 = lane & 15, g = lane >> 4, q = r16 >> 2, p = lane & 3, r32 = lane & 31, h = lane >> 5;
    const unsigned xa0 = (lw * 32 + r16) * 128 + ((g ^ (r16 >> 1)) * 16);
    const unsigned wa0 = 2048 * g + 1024 * grp + 128 * q + 16 * ((p >> 1) ^ (4 * (g & 1) + (q & 2))) + 8 * (p & 1);
    const unsigned jj = (20 * head + 19 * (lane & 1)) >> 6, b1 = (lane >> 1) & 1, c = (lane >> 2) & 7;
    const unsigned pol1 = FQ_OFF + (jj * 64 + ((lane & 8) ? (qt ^ b1) : b1 * 640 + (lane & 4) * 8 + (640 + (head >> 3) * 8 + w))) * 128;
    qq = u32x16{o1[0], o1[1], o1[2], o1[3], tw + 1024, tw + 1280, (tw ^ 64) + (8 * 3840 - 1024), (tw ^ 64) + (8 * 3840 - 2048 + 1280),
                pol1, xa0, xa0 ^ 64, xa0, wa0, wa0 ^ 32, wa0 ^ 64, wa0 ^ 96};
    const unsigned cB = __builtin_elementwise_max((c & 1) + qb, 1u) + (30 * 8 - 1);
    const unsigned pol2 = (jj * 64 + (lane >= 48 ? 30 * 64 + qt : b1 * 640 + (h ? cB : c) * 8 + (640 + w))) * 128 + FQ_OFF;
    const u32x2 s1 = g1_offs(br, cg1, Tg, lane), s2 = g1_offs(br, cg2, Tg, lane);
    const unsigned ab = grp * TILE_B;
    pk = u32x16{s1[0], s1[1], s2[0], s2[1],
                lane * 16 + ab,
                h ? (V_OFF + 320 + ab) : r32 * 16 + (K2_OFF + odd * 8 + ab),
                h * 512 + (r32 < 28 ? __builtin_elementwise_min(r32, 21u) : 28) * 16 + (V_OFF + ab),
                h * OCT + r32 * 16 + o1h * OCT,
                r32 * 16 + (o2 * OCT - 1024),
                h * 1280 + (__builtin_elementwise_min(r32 + 4 * odd, r32 + 4 * odd - 20) + (head & 3) * 20) * 16,
                pol2,
                g * 512 + __builtin_elementwise_min(r16, 11u) * 16 + (mytile + V_OFF + 320),
                r16 == 0 ? 0x3F803F80u : 0u,
                r32 * 16 + (o2 * OCT + odd * 8),
                lane * 512 + (3 * CHUNK_B + V_OFF + 320 + mytile),
                lane};
  }
  asm volatile("" : "+{v[64:79]}"(qq), "+{v[80:95]}"(pk) :: "memory");
  glds_xw(sX, sW, mod10(rot + 2), u32x4{qq[0], qq[1], qq[2], qq[3]}, w * 2048 + 65536);
  asm volatile("s_waitcnt vmcnt(8) lgkmcnt(0)\n\ts_barrier" ::: "memory");
  PINS(ep); PINS(tch[0]); PINS(tch[1]); PINS(tch[2]); PINS(tch[3]);
  const unsigned long long tag = (((unsigned long long)ep[1] << 32) | ep[0]) + 1;
  STAMP(1);
  u32x8 cv; f32x32 oab; f32x16 pp;
  STV_PUT(pk[15]);
  g1_all(vt1, kv1, vt2, qq, pk, at, cv, oab, pp, sX, sW, mod10(rot + 3) * 128, sB, mod10(rot + grp) * (64 * 3840), m0v, tag, flag1, ws, mytile, soA,
         qt * 512);
  STV_GET(pk[15]);
  STAMP(8);
  if (bid == 255 && grp == 0) {
    const unsigned e = w * 64 + lane_id(), eu = e & 7, em = (e >> 3) & 15;
    poll_flags(ws, FQ_OFF + (((e >> 7) * 30 + (eu < 5 ? 2 * eu : 5 + eu + ((5 * ((em >> 1) & 3)) >> 1))) * 64 + 4 * em) * 128, tag);
  }
  BARRIER();
  STAMP(9);
  if (bid == 255 && w == 0) put_flag(ws, CTL_WS, tag);
  const f32x16 zero16 = {};
  auto exact = [&](int b, bf16x8 q1, s16x4 q2, int t0, int t1) {
    __builtin_amdgcn_fence(__ATOMIC_ACQUIRE, "agent");
    const int ln = lane_id(), r32 = ln & 31, h = ln >> 5;
    unsigned head = xcd * 4 + (slot & 3);
    PINS(head);
    const unsigned odd = head & 1, o2 = 5 * (head >> 1) + 2, o1h = odd ? o2 + 1 : o2 - 2;
    if (h) q2 = s16x4{0, 0, 0, 0};
    typedef __attribute__((address_space(1))) const char* gptr;
    gptr kg1 = (gptr)ws + QK_WS + b * QK_BR + (size_t)(80 + o1h + h) * OCT + r32 * 16;
    gptr kg2 = (gptr)ws + QK_WS + b * QK_BR + (size_t)(80 + o2) * OCT + r32 * 16 + odd * 8;
    gptr vg = (gptr)ws + V_WS + b * V_BR + (head >> 2) * (256 * 1280) + h * 1280 +
                     ((head & 3) * 20 + (odd ? (r32 < 16 ? r32 + 4 : r32 - 16) : r32)) * 16;
    const u32x4 vc = r32 == 20 ? u32x4{0x3F803F80u, 0x3F803F80u, 0x3F803F80u, 0x3F803F80u} : u32x4{0u, 0u, 0u, 0u};
    float m = -INFINITY;
    f32x16 r = zero16;
#pragma unroll 1
    for (int ps = 0; ps < 2; ++ps) {
#pragma unroll 1
      for (int t = t0; t <= t1; ++t) {
        f32x16 s = MFMA32(*(__attribute__((address_space(1))) const bf16x8*)(kg1 + t * 512), q1, zero16);
        s = MFMA32K8(*(__attribute__((address_space(1))) const s16x4*)(kg2 + t * 512), q2, s);
        if (b) {
          const int d = (t1 - t) * 32 + r32 - 4 * h;
#pragma unroll
          for (int e = 0; e < 16; ++e) s[e] = (unsigned)(d - ((e & 3) + 8 * (e >> 2))) < 256u ? s[e] : -INFINITY;
        }
        if (ps == 0) { m = max16(s, m); continue; }
#pragma unroll
        for (int e = 0; e < 16; ++e) s[e] = __builtin_amdgcn_exp2f(s[e] - m);
        u32x4 va = vc, vb = vc;
        if (r32 < 20) { va = *(__attribute__((address_space(1))) const u32x4*)(vg + t * 5120); vb = *(__attribute__((address_space(1))) const u32x4*)(vg + t * 5120 + 2560); }
        r = smfmac(__builtin_bit_cast(bf16x8, va), lo8(s), r);
        r = smfmac(__builtin_bit_cast(bf16x8, vb), hi8(s), r);
      }
      m = xhalf_max(m);
    }
    return r;
  };
  auto unsafe = [&](const f32x16& r, float l) {
    float amax = 0.f;
#pragma unroll
    for (int e = 0; e < 12; ++e) amax = __builtin_fmaxf(amax, __builtin_fabsf(r[e]));
    return __any(!(l <= 1.2980742e33f) || !(l >= 8.4703295e-22f) || !(amax <= 3.0e38f));
  };
  char* const oA = ws + (O_WS + qt * 512);
  f32x32 of; f32x8 of2;
  u32x4 lt;
  {
    f32x16 s0;
    f32x32 fr;
    asm volatile(AT_ASM_MACROS
        "v_subrev_u32 v108, %[ab], v129\n\tds_read_b128 v[112:115], v79\n\tds_read_b64 v[116:117], v108\n\t"
        "ds_read_b128 v[80:83], v128\n\tds_read_b64 v[84:85], v129\n\tds_read_b128 v[94:97], v128 offset:7168\n\tds_read_b64 v[98:99], v129 offset:7168\n\t"
        "ds_read_b128 v[86:89], v130\n\tds_read_b128 v[100:103], v130 offset:7168\n\tds_read_b128 v[90:93], v130 offset:1024\n\t"
        "s_mov_b32 exec_lo, 0\n\tv_mov_b64 v[72:73], 0\n\tv_mov_b64 v[74:75], 0\n\ts_mov_b32 exec_lo, -1\n\t"
        "s_waitcnt lgkmcnt(7)\n\t"
        "qk1 32, 112, 64\n\tqk2 32, 116, 72\n\tqk1 48, 112, 68\n\tqk2 48, 116, 74\n\t"
        "ds_write_b128 v148, v[144:147]\n\tds_write_b128 v148, v[144:147] offset:28672\n\tds_write_b128 v148, v[144:147] offset:57344\n\t"
        "v_mov_b64 v[104:105], 0\n\tv_mov_b64 v[106:107], 0\n\tv_add_u32 v148, %[zt], v79\n\ts_cmp_lg_u32 %[qb], 0\n\ts_cbranch_scc1 1f\n\t"
        "ds_write_b128 v148, v[104:107]\n\tds_write_b128 v148, v[104:107] offset:1024\n\tds_write_b128 v148, v[104:107] offset:2048\n\t"
        "ds_write_b128 v148, v[104:107] offset:2560\n\t1:\n\ts_nop 1\n\t"
        "mx 108, 32\n\tv_mov_b32 v109, v108\n\tmx 110, 48\n\tv_mov_b32 v111, v110\n\t"
        "v_permlane32_swap_b32 v108, v109\n\ts_waitcnt lgkmcnt(0)\n\tqk1 32, 80, 64\n\tv_permlane32_swap_b32 v110, v111\n\t"
        "v_max_f32 v108, v108, v109\n\tv_max_f32 v110, v110, v111\n\tv_add_f32 v108, 0x42600000, v108\n\tv_add_f32 v110, 0x42600000, v110\n\t"
        "s_mov_b32 exec_lo, 0\n\tv_cvt_pk_bf16_f32 v72, -v108, 0\n\tv_cvt_pk_bf16_f32 v74, -v110, 0\n\ts_mov_b32 exec_lo, -1\n\ts_nop 0\n\t"
        "qk2 32, 84, 72\n\ts_nop 8\n\t"
        AT_ASM_PURGE
        : "+{v[112:127]}"(pp), "=&{v[32:47]}"(s0), "=&{v[80:111]}"(fr), "+{v[64:79]}"(qq), "+{v[128:143]}"(at), "+{v[144:151]}"(cv)
        : [ab] "s"(grp * TILE_B), [zt] "s"(3 * CHUNK_B + mytile), [qb] "s"(qb)
        : "memory", "scc", "v48", "v49", "v50", "v51", "v52", "v53", "v54", "v55", "v56", "v57", "v58", "v59", "v60", "v61", "v62", "v63");
    STAMP(10);
    f32x16 maskF, maskD; u32x8 qv;
    at_a1(oab, s0, pp, fr, qq, at, ws, so, mytile, mytile + (qb ? 3 : 4) * CHUNK_B, tag, flag2, (0x7fu >> u) & 1u, ws + (QK_BR + qt * 512),
          &lane_tab, maskF, maskD, qv, lt, fsrc, 143360 + w * 1024 AT_STV_PASS);
    STAMP(11);
    {
      f32x16 ob;
      at_b(ob, maskF, maskD, qv, 3 * CHUNK_B + 2 * lw * TILE_B, qt, oA, qq, at, oab, s0, fr, pp, 143360 + w * 1024, fdst, ws);
      lt[0] += late[0]; lt[1] += late[1]; lt[2] += grp * 64;
      float l = row_sum(ob);
      if (__builtin_expect(unsafe(ob, l), 0)) {
        ob = exact(1, __builtin_bit_cast(bf16x8, u32x4{qv[0], qv[1], qv[2], qv[3]}), __builtin_bit_cast(s16x4, u32x2{qv[4], qv[5]}),
                   qb ? qt - 8 : 0, qt);
        l = row_sum(ob);
      }
      store_o<false>(ob, l, oA + O_BR, at[9], qq[14]);
    }
    STAMP(13);
    at[13] = __builtin_elementwise_min(qq[15], 24u * 16) * 64 + (FO_OFF + qb * 25 * 1024);
    at_a2(oab, s0, pp, fr, qq, at, ws, so, mytile, tag, flagO, lt, ws + (late[2] + 102400), w * 5120,
          81920 + __builtin_elementwise_min(3 * w, 17u) * 1024, late[2] + ((slot & 3) * 8 + w) * 3200, w * 4096, of, of2);
  }
  STAMP(12);
  {
    lds_ptr xb = (lds_ptr)lds + 4 * CHUNK_B + qq[15];
#pragma unroll
    for (int i = 0; i < 3; ++i) *(lds_f4)(xb + w * 3072 + i * 1024) = f32x4{oab[16 + 4 * i], oab[17 + 4 * i], oab[18 + 4 * i], oab[19 + 4 * i]};
    asm volatile("s_setprio 3\n\ts_waitcnt vmcnt(4) lgkmcnt(0)\n\ts_barrier" : "+{v[184:215]}"(of), "+{v[216:223]}"(of2) :: "memory");
    f32x16 o = {};
#pragma unroll
    for (int i = 0; i < 3; ++i) {
      const f32x4 r = *(lds_f4)(xb + (w ^ 4) * 3072 + i * 1024);
#pragma unroll
      for (int e = 0; e < 4; ++e) o[4 * i + e] = oab[4 * i + e] + r[e];
    }
    float l = row_sum(o);
    if (__builtin_expect(unsafe(o, l), 0)) {
      o = exact(0, __builtin_bit_cast(bf16x8, u32x4{qq[0], qq[1], qq[2], qq[3]}), __builtin_bit_cast(s16x4, u32x2{qq[8], qq[9]}), 0, 63);
      l = row_sum(o);
    }
    store_o<true>(o, l, oA, at[9], qq[14]);
  }
  STAMP(14);
  {
    proj_all(of, of2, qq[15], lt, ws + late[2], 32768 + __builtin_elementwise_min(w, 6u) * 4096, w * 5120, w * 3072, grp, ws, ptr(kc[6], kc[7]) + late[3],
             0xffffffffull << (32 * grp) AT_STV_PASS);
  }
  { unsigned long long t0 = __hip_atomic_load((unsigned long long*)(ws + CTL_WS + 256), __ATOMIC_RELAXED, __HIP_MEMORY_SCOPE_AGENT);
    unsigned long long now = __builtin_amdgcn_s_memrealtime();
    if (now - t0 > 7000) t0 = now;
    if (bid == 5 && w == 0) while (__builtin_amdgcn_s_memrealtime() - t0 < 7000) __builtin_amdgcn_s_sleep(1); }
}

__global__ void k_stamp(char* ws) { *(unsigned long long*)(ws + CTL_WS + 256) = __builtin_amdgcn_s_memrealtime(); }
extern "C" void kernel_launch(void* const* d_in, const int* in_sizes, int n_in, void* d_out, int out_size, void* d_ws,
                              size_t ws_size, hipStream_t stream) {
  k_stamp<<<1, 64, 0, stream>>>((char*)d_ws);
  k_fused<<<256, 512, 0, stream>>>((const char*)d_in[0], (const char*)d_in[1], (const char*)d_in[2], (const char*)d_in[3],
                                   (const char*)d_in[4], (const char*)d_in[5], (char*)d_ws, (char*)d_out);
}
```

```cpp
#include <hip/hip_runtime.h>
#include <hip/hip_bf16.h>
#include <stdint.h>
#include <type_traits>

typedef __attribute__((ext_vector_type(8))) __bf16 bf16x8;
typedef __attribute__((ext_vector_type(16))) __bf16 bf16x16;
typedef __attribute__((ext_vector_type(4))) short s16x4;
typedef __attribute__((ext_vector_type(8))) short s16x8;
typedef __attribute__((ext_vector_type(2))) unsigned u32x2;
typedef __attribute__((ext_vector_type(4))) unsigned u32x4;
typedef __attribute__((ext_vector_type(4))) float f32x4;
typedef __attribute__((ext_vector_type(8))) float f32x8;
typedef __attribute__((ext_vector_type(16))) float f32x16;
typedef __attribute__((ext_vector_type(32))) float f32x32;
typedef __attribute__((address_space(3))) const char* lds_cptr;
typedef __attribute__((address_space(3))) char* lds_ptr;

#define OCT 33280
#define STR_(x) #x
#define STR(x) STR_(x)
constexpr size_t QK_WS = 0, QK_BR = (size_t)160 * OCT;
constexpr size_t V_WS = 2 * QK_BR, V_BR = (size_t)8 * 256 * 1280;
constexpr size_t O_WS = V_WS + 2 * V_BR, O_BR = (size_t)80 * OCT;
constexpr size_t WP_WS = O_WS + 2 * O_BR;
constexpr size_t CTL_WS = WP_WS + 2048 * 1024;
constexpr unsigned FQ_OFF = CTL_WS + 4096, FO_OFF = FQ_OFF + 2 * 30 * 64 * 128;
constexpr float QSCALE = 0.22360679774997896f * 1.4426950408889634f;

#define STAMP_DECL
#define STV_PUT(x)
#define STV_GET(x)
#define STAMP(i) do {} while (0)
#define STAMP_OUT(ws_, kid) do {} while (0)
__device__ __forceinline__ int lane_id() {
  int l; asm volatile("v_mbcnt_lo_u32_b32 %0, -1, 0\n\tv_mbcnt_hi_u32_b32 %0, -1, %0" : "=v"(l)); return l;
}
#define SBAR() __builtin_amdgcn_sched_barrier(0)
#define PIN(x) asm volatile("" : "+v"(x))
#define PINS(x) asm volatile("" : "+s"(x))
#define WAITV(N) asm volatile("s_waitcnt vmcnt(" #N ")" ::: "memory")
#define BARRIER() asm volatile("s_barrier" ::: "memory")
#define WAITV_BAR(N) asm volatile("s_waitcnt vmcnt(" #N ") lgkmcnt(0)\n\ts_barrier" ::: "memory")

#define XPOL ""
__device__ __forceinline__ void glds_xw(const char* x, const char* w, unsigned k, u32x4 o, unsigned lds) {
  unsigned keep;
  asm volatile("s_mov_b32 %0, m0\n\ts_mov_b32 m0, %7\n\ts_nop 0\n\tglobal_load_lds_dwordx4 %1, %5 " XPOL "\n\t"
               "global_load_lds_dwordx4 %2, %5 offset:1024 " XPOL "\n\ts_add_u32 m0, m0, 16384\n\ts_nop 0\n\tglobal_load_lds_dwordx4 %3, %6\n\t"
               "global_load_lds_dwordx4 %4, %6 offset:1024\n\ts_mov_b32 m0, %0"
               : "=&s"(keep) : "v"(o[0]), "v"(o[1]), "v"(o[2]), "v"(o[3]), "s"(x + k * 128), "s"(w + k * (64 * 3840)), "s"(lds) : "memory", "scc");
}
__device__ __forceinline__ unsigned pk_bf16(float lo, float hi) {
  typedef __attribute__((ext_vector_type(2))) float f32x2_;
  typedef __attribute__((ext_vector_type(2))) __bf16 bf16x2_;
  const f32x2_ v = {lo, hi};
  return __builtin_bit_cast(unsigned, __builtin_convertvector(v, bf16x2_));
}
__device__ __forceinline__ void st16(char* sbase, unsigned voff, u32x4 v) {
  asm volatile("global_store_dwordx4 %0, %1, %2 sc1\n\ts_nop 1" :: "v"(voff), "v"(v), "s"(sbase) : "memory");
}
__device__ __forceinline__ void st8lo(char* sbase, unsigned voff, u32x2 v) {
  asm volatile("s_mov_b32 exec_hi, 0\n\tglobal_store_dwordx2 %0, %1, %2 sc1\n\ts_mov_b32 exec_hi, -1" :: "v"(voff), "v"(v), "s"(sbase) : "memory");
}
__device__ __forceinline__ bf16x8 lds_b128(lds_cptr p) { return *(const __attribute__((address_space(3))) bf16x8*)p; }
__device__ __forceinline__ s16x4 lds_b64(lds_cptr p) { return *(const __attribute__((address_space(3))) s16x4*)p; }
__device__ __forceinline__ s16x4 lds_tr(lds_cptr p) {
  return __builtin_amdgcn_ds_read_tr16_b64_v4i16((__attribute__((address_space(3))) s16x4*)p);
}
__device__ __forceinline__ bf16x8 cat8(s16x4 a, s16x4 b) {
  s16x8 v = {a[0], a[1], a[2], a[3], b[0], b[1], b[2], b[3]};
  return __builtin_bit_cast(bf16x8, v);
}
#define MFMA32(a, b, c) __builtin_amdgcn_mfma_f32_32x32x16_bf16(a, b, c, 0, 0, 0)
#define MFMA32K8(a, b, c) __builtin_amdgcn_mfma_f32_32x32x8bf16_1k(a, b, c, 0, 0, 0)
#define MFMA16(a, b, c) __builtin_amdgcn_mfma_f32_16x16x32_bf16(a, b, c, 0, 0, 0)

typedef __attribute__((ext_vector_type(8))) unsigned u32x8;
typedef __attribute__((ext_vector_type(16))) unsigned u32x16;
#define AWAIT(d, d1, off) \
    "s_mov_b32 s48, 0x200000\n\t5:\n\ts_waitcnt vmcnt(0)\n\tv_cmp_ne_u64 vcc, s[50:51], v[" #d ":" #d1 "]\n\ts_sub_u32 s48, s48, 1\n\t" \
    "s_cbranch_vccz 4f\n\ts_cbranch_scc1 4f\n\ts_sleep 2\n\tglobal_load_dwordx2 v[" #d ":" #d1 "], v" #off ", s[54:55] sc1\n\ts_branch 5b\n\t4:\n\t"
#define G1_STAMP(i)
#define LPOS 0
#define NPOLL 22
#define NFLAG 8
#define NLD 8
#define TGAP 1
#define P2PRIO 0
#define NAW 28
#define KBAR 11
__device__ __forceinline__ void g1_all(unsigned vt1, unsigned kv1, unsigned vt2, u32x16& ad, u32x16& pk, u32x16& at, u32x8& cv, f32x32& o, f32x16& pp,
                                       const char* sX, const char* sW, unsigned off1, const char* sB, unsigned off2, unsigned m0v,
                                       unsigned long long tag, unsigned flag, char* ws, unsigned tile, u32x4 so, unsigned qoff) {
  unsigned keep;
  f32x32 d1, d2, d3, d4; f32x16 d7; f32x8 d6;
  asm volatile(
      ".macro pack vt\n\t.set i_, 0\n\t.rept 4\n\t"
      ".if \\vt\n\t.set e_, 8 * i_\n\t.set f_, 8 * i_ + 4\n\t.else\n\t.set e_, 4 * i_\n\t.set f_, 4 * i_ + 16\n\t.endif\n\t"
      "v_cvt_pk_bf16_f32 v[48+4*i_], v[e_], v[e_+1]\n\tv_cvt_pk_bf16_f32 v[49+4*i_], v[e_+2], v[e_+3]\n\t"
      "v_cvt_pk_bf16_f32 v[50+4*i_], v[f_], v[f_+1]\n\tv_cvt_pk_bf16_f32 v[51+4*i_], v[f_+2], v[f_+3]\n\t"
      ".set i_, i_ + 1\n\t.endr\n\t.set i_, 0\n\t.rept 4\n\t"
      "v_permlane16_swap_b32 v[48+4*i_], v[50+4*i_]\n\tv_permlane16_swap_b32 v[49+4*i_], v[51+4*i_]\n\t"
      ".set i_, i_ + 1\n\t.endr\n\t.endm\n\t"
      ".macro mm vt, c, x, w\n\t"
      ".if (\\x) < 104\n\t"
      ".if \\vt\n\tv_mfma_f32_16x16x32_bf16 v[\\c:\\c+3], v[\\x:\\x+3], v[\\w:\\w+3], 0\n\t"
      ".else\n\tv_mfma_f32_16x16x32_bf16 v[\\c:\\c+3], v[\\w:\\w+3], v[\\x:\\x+3], 0\n\t.endif\n\t"
      ".elseif \\vt\n\tv_mfma_f32_16x16x32_bf16 v[\\c:\\c+3], v[\\x:\\x+3], v[\\w:\\w+3], v[\\c:\\c+3]\n\t"
      ".else\n\tv_mfma_f32_16x16x32_bf16 v[\\c:\\c+3], v[\\w:\\w+3], v[\\x:\\x+3], v[\\c:\\c+3]\n\t.endif\n\t"
      ".endm\n\t"
      ".macro rx d, a, off\n\tds_read_b128 v[\\d:\\d+3], v\\a offset:\\off\n\tds_read_b128 v[\\d+4:\\d+7], v\\a offset:\\off+2048\n\t.endm\n\t"
      ".macro rw d, a, off\n\tds_read_b64_tr_b16 v[\\d:\\d+1], v\\a offset:\\off\n\tds_read_b64_tr_b16 v[\\d+2:\\d+3], v\\a offset:\\off+512\n\t.endm\n\t"
      ".macro bump lo, n, val\n\t.set bi_, \\lo\n\t.rept \\n\n\tv_add_u32 v[bi_], \\val, v[bi_]\n\t.set bi_, bi_+1\n\t.endr\n\t.endm\n\t"
      ".macro src2\n\ts_add_u32 s40, s44, s47\n\ts_addc_u32 s41, s45, 0\n\t"
      "s_add_u32 s47, s47, 2*64*3840\n\ts_cmp_ge_u32 s47, 640*3840\n\ts_cselect_b32 s48, 640*3840, 0\n\ts_sub_u32 s47, s47, s48\n\t.endm\n\t"
      ".macro ld1 i\n\t.if st_ < 7\n\t"
      ".if \\i < 2\n\tglobal_load_lds_dwordx4 v[64+\\i], s[40:41] offset:1024*\\i " XPOL "\n\t.if \\i == 1\n\ts_add_u32 m0, m0, 16384\n\t.endif\n\t"
      ".else\n\tglobal_load_lds_dwordx4 v[64+\\i], s[82:83] offset:1024*(\\i-2)\n\t.endif\n\t"
      ".else\n\t.if st_ == 9\n\ts_cbranch_scc1 8f\n\t.endif\n\tglobal_load_lds_dwordx4 v[68+\\i], s[40:41] offset:1024*\\i\n\t8:\n\t.endif\n\t.endm\n\t"
      ".macro p1body vt\n\t"
      ".set st_, 0\n\t"
      ".rept 10\n\t"
      ".set xr_, 96 + 16 * st_\n\t"
      ".set o_, ((st_ %% 5) & 1) * 32768\n\t"
      ".set n_, (((st_ + 1) %% 5) & 1) * 32768 + 16384\n\t.if st_ == 9\n\t.set n_, 0\n\t.endif\n\t"
      "s_waitcnt lgkmcnt(6)\n\t"
      "mm \\vt, 0, xr_, 32\n\trw 48, 76, o_+16384+8192\n\t"
      "mm \\vt, 16, xr_+4, 32\n\trx xr_+8, 74, o_\n\ts_waitcnt lgkmcnt(8)\n\t"
      "mm \\vt, 4, xr_, 36\n\trw 52, 77, o_+16384+8192\n\t"
      "mm \\vt, 20, xr_+4, 36\n\trw 56, 78, o_+16384+8192\n\ts_waitcnt lgkmcnt(10)\n\t"
      "mm \\vt, 8, xr_, 40\n\trw 60, 79, o_+16384+8192\n\t"
      "mm \\vt, 24, xr_+4, 40\n\ts_waitcnt lgkmcnt(10)\n\t"
      "mm \\vt, 12, xr_, 44\n\t"
      ".if st_ < 7\n\t"
      "s_add_u32 s40, s42, s36\n\ts_addc_u32 s41, s43, 0\n\ts_mul_i32 s48, s36, 64*30\n\ts_add_u32 s82, s84, s48\n\ts_addc_u32 s83, s85, 0\n\t"
      "s_add_u32 s36, s36, 128\n\ts_cmp_eq_u32 s36, 1280\n\ts_cselect_b32 s36, 0, s36\n\t"
      ".else\n\tsrc2\n\t.endif\n\t"
      "mm \\vt, 28, xr_+4, 44\n\t"
      ".if st_ < 7\n\ts_add_u32 m0, s37, ((st_ + 3) %% 5) * 32768\n\t.else\n\ts_add_u32 m0, s46, ((st_ + 3) %% 5) * 32768\n\t.endif\n\t"
      ".if (st_ %% 5) == 1 || (st_ %% 5) == 3\n\tbump 76, 4, 0x10000\n\tbump 73, 2, 0x10000\n\t.endif\n\t"
      ".if (st_ %% 5) == 4\n\tbump 76, 4, 0xfffe0000\n\t.if st_ < 9\n\tbump 73, 2, 0xfffe0000\n\t.endif\n\t.endif\n\t"
      "s_waitcnt lgkmcnt(6)\n\t"
      "mm \\vt, 0, xr_+8, 48\n\t"
      "mm \\vt, 16, xr_+12, 48\n\t"
      ".if st_ < 9\n\ts_waitcnt vmcnt(4) lgkmcnt(0)\n\t.else\n\ts_cmp_eq_u32 %[vt2], 2\n\ts_cbranch_scc1 8f\n\ts_waitcnt vmcnt(4)\n\t8:\n\ts_waitcnt lgkmcnt(0)\n\t.endif\n\ts_barrier\n\t"
      "mm \\vt, 4, xr_+8, 52\n\trw 32, 76, n_\n\t"
      "ld1 0\n\t"
      "mm \\vt, 20, xr_+12, 52\n\t"
      ".if st_ < 9\n\trx xr_+16, 73, n_-16384\n\t.endif\n\t"
      "ld1 1\n\t"
      "mm \\vt, 8, xr_+8, 56\n\trw 36, 77, n_\n\t"
      "ld1 2\n\t"
      "mm \\vt, 24, xr_+12, 56\n\trw 40, 78, n_\n\t"
      "ld1 3\n\t"
      "mm \\vt, 12, xr_+8, 60\n\trw 44, 79, n_\n\t"
      "mm \\vt, 28, xr_+12, 60\n\t"
      ".set st_, st_ + 1\n\t"
      ".endr\n\t"
      "v_mov_b64 v[64:65], v[80:81]\n\tv_add_u32 v66, " STR(OCT) "*4 - (" STR(OCT) "*4 - 2560) * \\vt, v80\n\tv_add_u32 v67, " STR(OCT) "*4 - (" STR(OCT) "*4 - 2560) * \\vt, v81\n\t"
      ".endm\n\t"
      ".macro tdk i\n\t"
      "s_add_u32 m0, s72, \\i * 28672\n\ts_add_u32 s40, s54, s[76+2*\\i]\n\ts_addc_u32 s41, s55, 0\n\t"
      "global_load_lds_dwordx4 v137, s[40:41] sc1\n\ts_mov_b32 exec_hi, 0\n\tglobal_load_lds_dwordx4 v138, s[40:41] offset:1024 sc1\n\ts_mov_b32 exec_hi, -1\n\t.endm\n\t"
      ".macro tdv i\n\t"
      "s_add_u32 s40, s54, s[77+2*\\i]\n\ts_addc_u32 s41, s55, 0\n\ts_mov_b32 exec_lo, 0xfffff\n\ts_mov_b32 exec_hi, 0xfffff\n\t"
      "global_load_lds_dwordx4 v139, s[40:41] offset:1536 sc1\n\tglobal_load_lds_dwordx4 v140, s[40:41] offset:2560 sc1\n\ts_mov_b64 exec, -1\n\t"
      ".endm\n\t"
      ".macro slot n, ld=1\n\t.set n_, \\n\n\t"
      ".set l_, n_ - %c[nld]\n\t"
      ".if n_ == %c[nflag] && \\ld\n\t.set y_, l_\n\t.if y_ < 0\n\t.set y_, 0\n\t.endif\n\t.if y_ > 8\n\t.set y_, 8\n\t.endif\n\t"
      "v_mov_b32 v96, s50\n\tv_mov_b32 v97, s51\n\tv_mov_b32 v98, s52\n\ts_mov_b64 exec, 1\n\ts_waitcnt vmcnt(y_)\n\t"
      "global_store_dwordx2 v98, v[96:97], s[54:55] sc1\n\ts_mov_b64 exec, -1\n\t.endif\n\t"
      ".if l_ >= 0 && l_ < 8 && \\ld\n\t"
      ".if (l_ & 3) == 0\n\ts_add_u32 m0, s46, (3 + l_ / 4) * 32768\n\tsrc2\n\t.endif\n\t"
      "global_load_lds_dwordx4 v[68+(l_&3)], s[40:41] offset:1024*(l_&3)\n\t"
      ".endif\n\t"
      ".if n_ == 12\n\tv_mov_b64 v[128:129], v[84:85]\n\tv_mov_b32 v130, v86\n\tv_add_u32 v131, 57344, v84\n\tv_add_u32 v132, 57344, v85\n\t.endif\n\t"
      ".if n_ == 13\n\tv_add_u32 v133, 57344, v86\n\tv_add_u32 v134, 114688, v84\n\tv_add_u32 v135, 114688, v85\n\tv_add_u32 v136, 114688, v86\n\t.endif\n\t"
      ".if n_ == 14\n\tv_mov_b32 v137, v87\n\tv_mov_b64 v[138:139], v[88:89]\n\tv_add_u32 v140, 1536, v89\n\tv_mov_b32 v141, v90\n\t.endif\n\t"
      ".if n_ == 15\n\tv_mov_b32 v144, v92\n\tv_mov_b32 v145, v92\n\tv_mov_b32 v146, v92\n\tv_mov_b32 v147, v92\n\tv_add_u32 v148, 57344, v91\n\t.endif\n\t"
      ".if n_ == 2 * %c[kbar] + 1\n\tds_write_b128 v91, v[144:147]\n\tds_write_b128 v91, v[144:147] offset:28672\n\t.endif\n\t"
      ".if n_ == %c[npoll]\n\tglobal_load_dwordx2 v[100:101], v72, s[54:55] sc1\n\t.endif\n\t"
      ".if n_ == %c[naw]\n\t" AWAIT(100, 101, 72) G1_STAMP(6) "tdk 0\n\t.endif\n\t"
      ".if n_ == %c[naw] + 1\n\ttdv 0\n\t.endif\n\t"
      ".if n_ == %c[naw] + 2\n\ts_add_u32 s40, s54, s80\n\ts_addc_u32 s41, s55, 0\n\t"
      "global_load_dwordx4 v[64:67], v137, s[40:41] sc1\n\tglobal_load_dwordx2 v[72:73], v93, s[40:41] sc1\n\t.endif\n\t"
      ".if n_ == %c[naw] + 3\n\ts_xor_b32 s48, s80, 512\n\ts_add_u32 s40, s54, s48\n\ts_addc_u32 s41, s55, 0\n\t"
      "global_load_dwordx4 v[68:71], v137, s[40:41] sc1\n\tglobal_load_dwordx2 v[74:75], v93, s[40:41] sc1\n\t.endif\n\t"
      ".if n_ == %c[naw] + 4 + %c[gap]\n\ttdk 1\n\t.endif\n\t"
      ".if n_ == %c[naw] + 5 + 2 * %c[gap]\n\ttdv 1\n\t.endif\n\t"
      ".endm\n\t"
      ".macro st1 i\n\t.if k_ == 0\n\tglobal_store_dwordx4 v[64+\\i], v[48+4*\\i:51+4*\\i], s[54:55] sc1\n\t.endif\n\t.endm\n\t"
      ".macro p2body vt\n\t"
      ".set k_, 0\n\t"
      ".rept 20\n\t"
      ".set xr_, 96 + 8 * k_\n\t"
      ".set c_, 32 + 16 * (k_ & 1)\n\t"
      ".set d_, 48 - 16 * (k_ & 1)\n\t"
      ".set o_, ((k_ + 1) & 7) * 8192\n\t"
      ".if k_ == 3\n\ts_waitcnt vmcnt(4)\n\ts_barrier\n\t.endif\n\t"
      ".if %c[prio] && k_ >= 3 && k_ < 19 && (k_ & 1)\n\ts_setprio 3 - (((k_ - 3) / 2) & 3)\n\t.endif\n\t"
      ".if k_ == %c[kbar]\n\ts_waitcnt vmcnt(0)\n\ts_barrier\n\t.endif\n\t"
      ".if k_ < 19\n\t"
      ".if ((k_ + 1) & 7) == 0\n\tbump 76, 4, 0x10000\n\t.endif\n\t"
      "s_waitcnt lgkmcnt(6)\n\t"
      "mm \\vt, 0, xr_, c_\n\tst1 0\n\trw d_, 76, o_\n\t"
      "mm \\vt, 16, xr_+4, c_\n\tslot 2*k_\n\ts_waitcnt lgkmcnt(6)\n\t"
      "mm \\vt, 4, xr_, c_+4\n\tst1 1\n\trw d_+4, 77, o_\n\t"
      "mm \\vt, 20, xr_+4, c_+4\n\ts_waitcnt lgkmcnt(6)\n\t"
      "mm \\vt, 8, xr_, c_+8\n\tst1 2\n\trw d_+8, 78, o_\n\t"
      "mm \\vt, 24, xr_+4, c_+8\n\tslot 2*k_+1\n\ts_waitcnt lgkmcnt(6)\n\t"
      "mm \\vt, 12, xr_, c_+12\n\tst1 3\n\trw d_+12, 79, o_\n\t"
      "mm \\vt, 28, xr_+4, c_+12\n\t"
      ".else\n\t"
      "s_waitcnt lgkmcnt(0)\n\t"
      "mm \\vt, 0, xr_, c_\n\tmm \\vt, 16, xr_+4, c_\n\tmm \\vt, 4, xr_, c_+4\n\tmm \\vt, 20, xr_+4, c_+4\n\t"
      "mm \\vt, 8, xr_, c_+8\n\tmm \\vt, 24, xr_+4, c_+8\n\tmm \\vt, 12, xr_, c_+12\n\tmm \\vt, 28, xr_+4, c_+12\n\t"
      "s_setprio 3\n\tv_add_u32 v80, " STR(OCT) "*4 - (" STR(OCT) "*4 - 2560) * \\vt, v82\n\tv_add_u32 v81, " STR(OCT) "*4 - (" STR(OCT) "*4 - 2560) * \\vt, v83\n\ts_nop 4\n\tpack \\vt\n\t"
      ".endif\n\t"
      ".set k_, k_ + 1\n\t"
      ".endr\n\t"
      ".endm\n\t"
      "rw 32, 76, 16384\n\trx 96, 73, 0\n\trw 36, 77, 16384\n\trw 40, 78, 16384\n\trw 44, 79, 16384\n\t"
      "s_mov_b32 %[keep], m0\n\t"
      "s_cmp_lg_u32 %[vt1], 0\n\ts_cbranch_scc1 1f\n\t"
      "p1body 0\n\t"
      "s_mov_b32 s40, %[sc]\n\ts_cmp_lg_u32 %[kv1], 0\n\ts_nop 2\n\ts_cbranch_scc1 3f\n\t"
      ".set i_, 0\n\t.rept 16\n\tv_pk_mul_f32 v[i_:i_+1], v[i_:i_+1], s[40:41] op_sel_hi:[1,0]\n\t.set i_, i_ + 2\n\t.endr\n\t"
      "3:\n\tpack 0\n\ts_branch 2f\n\t"
      "1:\n\tp1body 1\n\ts_nop 4\n\tpack 1\n\t"
      "2:\n\t" G1_STAMP(3)
      "s_cmp_eq_u32 %[vt2], 1\n\ts_cbranch_scc1 1f\n\ts_cmp_eq_u32 %[vt2], 2\n\ts_cbranch_scc0 6f\n\t"
      ".set k_, 0\n\tst1 0\n\tst1 1\n\tst1 2\n\tst1 3\n\tslot 12, 0\n\tslot 13, 0\n\tslot 14, 0\n\tslot 15, 0\n\t"
      "v_mov_b32 v96, s50\n\tv_mov_b32 v97, s51\n\tv_mov_b32 v98, s52\n\t"
      "s_waitcnt vmcnt(0) lgkmcnt(0)\n\ts_mov_b64 exec, 1\n\tglobal_store_dwordx2 v98, v[96:97], s[54:55] sc1\n\ts_mov_b64 exec, -1\n\ts_barrier\n\t"
      "slot %c[npoll]\n\t"
      "slot 2*%c[kbar]+1\n\tslot %c[naw]\n\tslot %c[naw]+1\n\tslot %c[naw]+2\n\tslot %c[naw]+3\n\ttdk 1\n\ttdv 1\n\t"
      "s_waitcnt vmcnt(0)\n\ts_setprio 3\n\ts_branch 3f\n\t"
      "6:\n\tp2body 0\n\ts_branch 2f\n\t"
      "1:\n\tp2body 1\n\t"
      "2:\n\tglobal_store_dwordx4 v82, v[48:51], s[54:55] sc1\n\tglobal_store_dwordx4 v83, v[52:55], s[54:55] sc1\n\t"
      "global_store_dwordx4 v80, v[56:59], s[54:55] sc1\n\tglobal_store_dwordx4 v81, v[60:63], s[54:55] sc1\n\t"
      "3:\n\ts_mov_b32 m0, %[keep]\n\tv_mov_b32 v76, 0xdddd\n\tv_mov_b32 v77, v94\n\tv_mov_b32 v78, v93\n\t"
      "v_mbcnt_lo_u32_b32 v79, -1, 0\n\tv_mbcnt_hi_u32_b32 v79, -1, v79\n\tv_lshlrev_b32 v79, 4, v79\n\t"
      ".set i_, 0\n\t.rept 16\n\tv_mov_b64 v[i_:i_+1], 0\n\t.set i_, i_ + 2\n\t.endr\n\t"
      "v_mov_b64 v[120:121], 0\n\tv_mov_b64 v[122:123], 0\n\tv_mov_b64 v[124:125], 0\n\tv_mov_b64 v[126:127], 0\n\t"
      "s_waitcnt vmcnt(8) lgkmcnt(0)\n\t"
      ".purgem ld1\n\t.purgem pack\n\t.purgem mm\n\t.purgem rx\n\t.purgem rw\n\t.purgem bump\n\t.purgem src2\n\t.purgem p1body\n\t.purgem tdk\n\t.purgem tdv\n\t"
      ".purgem slot\n\t.purgem st1\n\t.purgem p2body\n\t"
      : "+{v[64:79]}"(ad), "+{v[80:95]}"(pk), "=&{v[128:143]}"(at), "=&{v[144:151]}"(cv), [keep] "=&s"(keep), "+{s36}"(off1), "+{s47}"(off2),
        "=&{v[0:31]}"(o), "=&{v[32:63]}"(d1), "=&{v[160:191]}"(d2), "=&{v[192:223]}"(d3), "=&{v[224:255]}"(d4), "=&{v[112:127]}"(pp),
        "=&{v[152:159]}"(d6), "=&{v[96:111]}"(d7)
      : [vt1] "s"(vt1), [kv1] "s"(kv1), [vt2] "s"(vt2), "{s[42:43]}"(sX), "{s[84:85]}"(sW), "{s37}"(m0v >> 1), "{s[44:45]}"(sB), "{s46}"(m0v),
        "{s[50:51]}"(tag), "{s52}"(flag), "{s[54:55]}"(ws), "{s72}"(tile), "{s[76:79]}"(so), "{s80}"(qoff),
        [sc] "n"(__builtin_bit_cast(unsigned, QSCALE)), [npoll] "n"(NPOLL), [nflag] "n"(NFLAG), [nld] "n"(NLD), [prio] "n"(P2PRIO), [gap] "n"(TGAP), [naw] "n"(NAW), [kbar] "n"(KBAR)
      : "memory", "scc", "vcc", "s40", "s41", "s48", "s82", "s83");
}

__device__ __forceinline__ u32x2 g1_offs(unsigned br, unsigned cg, unsigned T, unsigned lane) {
  const unsigned l16 = (lane & 31) * 16, h = lane >> 5;
  const unsigned a = h * OCT + l16 + (br * (unsigned)QK_BR + cg * (8 * OCT) + T * 512);
  const unsigned G = (cg - 20) * 4 + ((lane >> 4) & 1);
  const unsigned b = h * 1280 + l16 + ((unsigned)V_WS + br * (unsigned)V_BR + (cg - 20) * 1024 + T * 5120);
  return cg < 20 ? u32x2{a, a + 2 * OCT}
                 : u32x2{((G * 52) >> 8) * (256 * 1280 - 1280) + b, (((G + 2) * 52) >> 8) * (256 * 1280 - 1280) + b + 512};
}

constexpr int TILE_B = 3584, K2_OFF = 1024, V_OFF = 1536;
constexpr int CHUNK_B = 8 * TILE_B;

__device__ __forceinline__ f32x16 smfmac(bf16x8 a, f32x8 p, f32x16 c) {
  return __builtin_amdgcn_smfmac_f32_32x32x32_bf16(a, __builtin_bit_cast(bf16x16, p), c, 0xDDDD, 0, 0);
}
__device__ __forceinline__ f32x8 lo8(const f32x16& s) { return __builtin_shufflevector(s, s, 0, 1, 2, 3, 4, 5, 6, 7); }
__device__ __forceinline__ f32x8 hi8(const f32x16& s) { return __builtin_shufflevector(s, s, 8, 9, 10, 11, 12, 13, 14, 15); }
#define MX3(a, b, c) __builtin_fmaxf(__builtin_fmaxf((a), (b)), (c))
__device__ __forceinline__ float max16(const f32x16& s, float m) {
#pragma unroll
  for (int r = 0; r < 16; r += 2) m = MX3(m, s[r], s[r + 1]);
  return m;
}
__device__ __forceinline__ float xhalf_max(float m) {
  auto rr = __builtin_amdgcn_permlane32_swap(__float_as_uint(m), __float_as_uint(m), false, false);
  return __builtin_fmaxf(__uint_as_float(rr[0]), __uint_as_float(rr[1]));
}
__device__ __forceinline__ float row_sum(const f32x16& o) {
  auto rr = __builtin_amdgcn_permlane32_swap(__float_as_uint(o[8]), __float_as_uint(o[8]), false, false);
  return __uint_as_float(rr[1]);
}
template <bool MARK>
__device__ __forceinline__ void store_o(const f32x16& o, float l, char* base, unsigned lo1, unsigned lo2) {
  const float rl = __builtin_amdgcn_rcpf(l);
  const unsigned a0 = pk_bf16(o[0] * rl, o[1] * rl), a1 = pk_bf16(o[2] * rl, o[3] * rl);
  const unsigned b0 = pk_bf16(o[4] * rl, o[5] * rl), b1 = pk_bf16(o[6] * rl, o[7] * rl);
  const auto r0 = __builtin_amdgcn_permlane32_swap(a0, b0, false, false);
  const auto r1 = __builtin_amdgcn_permlane32_swap(a1, b1, false, false);
  u32x4 v = {r0[0], r1[0], r0[1], r1[1]};
  u32x2 c = {pk_bf16(o[8] * rl, o[9] * rl), pk_bf16(o[10] * rl, o[11] * rl)};
  if (MARK) {
    v = __builtin_elementwise_min(v, u32x4{~1u, ~1u, ~1u, ~1u}); c = __builtin_elementwise_min(c, u32x2{~1u, ~1u});
  }
  st16(base, lo1, v);
  st8lo(base, lo2, c);
}

__device__ __forceinline__ void poll_flags(const char* ws, unsigned off, unsigned long long tag) {
  asm volatile("global_load_dwordx2 v[60:61], v62, s[54:55] sc1\n\t" AWAIT(60, 61, 62)
               :: "{v62}"(off), "{s[54:55]}"(ws), "{s[50:51]}"(tag) : "memory", "scc", "vcc", "s48", "v60", "v61");
}
__device__ __forceinline__ void put_flag(char* ws, unsigned off, unsigned long long tag) {
  asm volatile("s_mov_b64 exec, 1\n\tglobal_store_dwordx2 %0, %1, %2 sc1\n\ts_mov_b64 exec, -1" :: "v"(off), "v"(tag), "s"(ws) : "memory");
}
#define AT_ASM_MACROS \
    ".macro mx d, s\n\tv_max3_f32 v[\\d], v[\\s], v[\\s+1], v[\\s+2]\n\t.set i_, \\s + 3\n\t.rept 6\n\tv_max3_f32 v[\\d], v[\\d], v[i_], v[i_+1]\n\t" \
    ".set i_, i_ + 2\n\t.endr\n\tv_max_f32 v[\\d], v[\\d], v[\\s+15]\n\t.endm\n\t" \
    ".macro xp d, s\n\tv_exp_f32 v[\\d], v[\\s]\n\tv_exp_f32 v[\\d+1], v[\\s+1]\n\tv_exp_f32 v[\\d+2], v[\\s+2]\n\tv_exp_f32 v[\\d+3], v[\\s+3]\n\t.endm\n\t" \
    ".macro smf o, v, p\n\tv_smfmac_f32_32x32x32_bf16 v[\\o:\\o+15], v[\\v:\\v+3], v[\\p:\\p+7], v76\n\t.endm\n\t" \
    ".macro qk1 s, k, q\n\tv_mfma_f32_32x32x16_bf16 v[\\s:\\s+15], v[\\k:\\k+3], v[\\q:\\q+3], 0\n\t.endm\n\t" \
    ".macro qk1c s, k, q, c\n\tv_mfma_f32_32x32x16_bf16 v[\\s:\\s+15], v[\\k:\\k+3], v[\\q:\\q+3], v[\\c:\\c+15]\n\t.endm\n\t" \
    ".macro qk2 s, k, q\n\tv_mfma_f32_32x32x8_bf16 v[\\s:\\s+15], v[\\k:\\k+1], v[\\q:\\q+1], v[\\s:\\s+15]\n\t.endm\n\t"
#define AT_ASM_PURGE ".purgem mx\n\t.purgem xp\n\t.purgem smf\n\t.purgem qk1\n\t.purgem qk1c\n\t.purgem qk2\n\t"

#define JPOLL 3
#define AT_A_TEXT AT_ASM_MACROS \
      ".macro pos j\n\t.set sl_, (\\j) >> 2\n\t.if sl_ >= 3\n\t.set sl_, sl_ - 3\n\t.endif\n\t" \
      ".set ar_, 128 + 3 * (sl_ >> 1)\n\t.set of_, (sl_ & 1) * 28672 + ((\\j) & 3) * 7168\n\t.endm\n\t" \
      ".macro dma part\n\t" \
      ".if di_ >= 0\n\t" \
      ".if \\part == 0\n\t" \
      ".if di_ == 1\n\ts_mov_b32 m0, s73\n\t.else\n\ts_add_u32 m0, s72, ((di_ + 2) %% 5) * 28672\n\t.endif\n\t" \
      "s_add_u32 s40, s54, s[56+2*di_]\n\ts_addc_u32 s41, s55, 0\n\t" \
      "global_load_lds_dwordx4 v137, s[40:41] sc1\n\t" \
      ".elseif \\part == 1\n\t" \
      "s_mov_b32 exec_hi, 0\n\tglobal_load_lds_dwordx4 v138, s[40:41] offset:1024 sc1\n\ts_mov_b32 exec_hi, -1\n\t" \
      "s_add_u32 s40, s54, s[57+2*di_]\n\ts_addc_u32 s41, s55, 0\n\t" \
      ".else\n\t" \
      "s_mov_b32 exec_lo, 0xfffff\n\ts_mov_b32 exec_hi, 0xfffff\n\t" \
      "global_load_lds_dwordx4 v139, s[40:41] offset:1536 sc1\n\tglobal_load_lds_dwordx4 v140, s[40:41] offset:2560 sc1\n\t" \
      "s_mov_b64 exec, -1\n\t" \
      ".endif\n\t.endif\n\t" \
        \
      ".set n_, -1\n\t" \
      ".if j_ >= 22 && j_ < 25 && 3 * (j_ - 22) + \\part < 8\n\t.set n_, 3 * (j_ - 22) + \\part\n\t.endif\n\t" \
      ".if j_ >= 28 && 3 * (j_ - 28) + \\part < 9\n\t.set n_, 10 + 3 * (j_ - 28) + \\part\n\t.endif\n\t" \
      ".if n_ >= 0\n\t.set pc_, n_ %% 5\n\t" \
      ".if n_ == 0 || n_ == 10\n\ts_add_u32 m0, s44, n_ * 4096\n\ts_add_u32 s40, s42, m0\n\ts_addc_u32 s41, s43, 0\n\t.endif\n\t" \
      ".if n_ == 5\n\ts_add_u32 m0, s45, 143360 - 81920\n\ts_add_u32 s40, s42, s45\n\ts_addc_u32 s41, s43, 0\n\t.endif\n\t" \
      ".if n_ == 15\n\ts_add_u32 m0, s47, 81920\n\ts_sub_u32 s48, 102400, s47\n\ts_sub_u32 s40, s42, s48\n\ts_subb_u32 s41, s43, 0\n\t.endif\n\t" \
      ".if pc_ == 4\n\ts_add_u32 m0, m0, 4096\n\ts_add_u32 s40, s40, 4096\n\ts_addc_u32 s41, s41, 0\n\t.endif\n\t" \
      "global_load_lds_dwordx4 v79, s[40:41] offset:1024*(pc_&3) sc1\n\t.endif\n\t" \
      ".endm\n\t" \
      ".macro flg msk\n\tv_mov_b32 v142, s50\n\tv_mov_b32 v143, s51\n\tv_mov_b32 v108, s52\n\ts_mov_b64 exec, \\msk\n\ts_cbranch_execz 8f\n\ts_waitcnt vmcnt(0)\n\t" \
      "global_store_dwordx2 v108, v[142:143], s[54:55] sc1\n\t8:\n\ts_mov_b64 exec, -1\n\t.endm\n\t" \
      ".macro tabl d, r\n\tglobal_load_dwordx4 v[\\d:\\d+3], v79, s[40:41] offset:1024*(\\r)\n\t.endm\n\t" \
      ".macro old q\n\t.if (\\q) >= 0 && (\\q) < 10\n\tglobal_load_dwordx4 v[184+4*(\\q):187+4*(\\q)], v108, s[54:55] sc1\n\tv_add_u32 v108, " STR(OCT) "*4, v108\n\t.endif\n\t.endm\n\t" \
      "s_mov_b32 %[keep], m0\n\t" \
      ".set j_, %c[j0]\n\t" \
      ".rept %c[j1] - %c[j0]\n\t" \
      ".set a_, 80 + 14 * (j_ & 1)\n\t.set b_, 94 - 14 * (j_ & 1)\n\t" \
      ".if j_ == 5\n\t" AWAIT(142, 143, 141) ".set di_, 0\n\tdma 0\n\tdma 1\n\tdma 2\n\t.endif\n\t" \
        \
      ".if j_ == 7\n\ts_waitcnt vmcnt(10)\n\ts_barrier\n\tpos 8\n\tds_read_b128 v[80:83], v[ar_] offset:of_\n\tds_read_b64 v[84:85], v[ar_+1] offset:of_\n\t" \
      "ds_read_b128 v[86:89], v[ar_+2] offset:of_\n\t.endif\n\t" \
      ".if j_ == 10\n\ts_waitcnt vmcnt(1)\n\ts_barrier\n\t.endif\n\t" \
      ".if j_ == 14 || j_ == 18\n\ts_waitcnt vmcnt(0)\n\ts_barrier\n\t.endif\n\t" \
      ".if j_ == 22\n\t" AWAIT(142, 143, 141) AWAIT(110, 111, 252) "v_add_u32 v108, %c[obr], v253\n\t.endif\n\t" \
      ".if j_ == 28\n\ts_waitcnt vmcnt(10)\n\t.endif\n\t.if j_ == 30\n\ts_waitcnt lgkmcnt(0)\n\t.endif\n\t" \
      ".if j_ == 30 || j_ == 28 || j_ == 22 || j_ == 2\n\ts_barrier\n\t.endif\n\t" \
      AT_STAMP \
      ".if j_ < 2\n\ts_setprio 1 - j_\n\t.elseif j_ < 7\n\t.if j_ != 3\n\ts_setprio 3 - (j_ - 2) + (j_ > 3) * (-1)\n\t.endif\n\t.elseif j_ < 10\n\ts_setprio 9 - j_\n\t" \
      ".elseif j_ == 10 || j_ == 11\n\ts_setprio 3\n\t.elseif j_ == 12 || j_ == 13\n\ts_setprio 0\n\t" \
      ".elseif j_ < 22\n\ts_setprio 3 - ((j_ + 2) & 3)\n\t.elseif j_ >= 28\n\ts_setprio 1 - (j_ & 1)\n\t" \
      ".elseif j_ != 23 && j_ != 25\n\ts_setprio 3 - (j_ - 21) / 2\n\t.endif\n\t" \
      ".if j_ == 14\n\tflg 1\n\t" \
      "v_mov_b32 v108, 0x3f803f80\n\tv_mov_b32 v109, v108\n\tv_mov_b32 v110, v108\n\tv_mov_b32 v111, v108\n\ts_mov_b64 exec, 15\n\t" \
      "ds_write_b128 v77, v[108:111]\n\ts_mov_b64 exec, -1\n\t.endif\n\t" \
      ".set di_, -1\n\t" \
      ".if j_ == 5 || j_ == 6 || j_ == 7\n\t.set di_, j_ - 4\n\t.endif\n\t.if j_ == 10\n\t.set di_, 4\n\t.endif\n\t" \
      ".if j_ >= 14 && j_ < 17\n\t.set di_, j_ - 9\n\t.endif\n\t" \
        \
      "pos j_+2\n\t" \
      "qk1 48, a_, 68\n\t.if j_ < 30 && j_ != 6\n\tds_read_b128 v[a_:a_+3], v[ar_] offset:of_\n\t.endif\n\told 4*(j_-25)\n\txp 112, 32\n\t" \
      "qk2 48, a_+4, 74\n\t.if j_ < 30 && j_ != 6\n\tds_read_b64 v[a_+4:a_+5], v[ar_+1] offset:of_\n\t.endif\n\told 4*(j_-25)+1\n\txp 116, 36\n\t" \
      "pos j_+1\n\t" \
      "smf 16, b_+10, 120\n\t.if j_ < 30\n\tds_read_b128 v[b_+10:b_+13], v[ar_+2] offset:of_+1024\n\t.endif\n\told 4*(j_-25)+2\n\txp 120, 40\n\t" \
      "smf 0, a_+6, 112\n\t.if j_ < 30 && j_ != 6\n\ts_waitcnt lgkmcnt(3)\n\t.elseif j_ == 6\n\ts_waitcnt lgkmcnt(1)\n\t.endif\n\t" \
      "old 4*(j_-25)+3\n\t" \
      ".if j_ == 6\n\tglobal_load_dwordx4 v[236:239], v137, s[42:43] sc1\n\tglobal_load_dwordx2 v[240:241], v78, s[42:43] sc1\n\t.endif\n\t" \
      "xp 124, 44\n\t" \
        \
      ".if j_ < 31\n\tqk1 32, b_, 64\n\t.endif\n\tdma 0\n\t" \
      ".if j_ == 0\n\tflg s[74:75]\n\t.endif\n\t" \
      ".if j_ == " STR(JPOLL) "\n\tglobal_load_dwordx2 v[142:143], v141, s[54:55] sc1\n\t.endif\n\t" \
      ".if j_ == 1\n\ts_mov_b64 s[40:41], s[44:45]\n\ttabl 252, 0\n\ttabl 160, 1\n\t.endif\n\t" \
      ".if j_ == 2\n\ttabl 224, 2\n\ttabl 228, 3\n\t.endif\n\t" \
      ".if j_ == 20\n\tglobal_load_dwordx2 v[142:143], v141, s[54:55] sc1\n\tglobal_load_dwordx2 v[110:111], v252, s[54:55] sc1\n\t.endif\n\t" \
      ".if j_ == 29\n\tpos 31\n\tds_read_b128 v[108:111], v[ar_+2] offset:of_+1024\n\t.endif\n\t" \
      "xp 112, 48\n\t" \
      ".if j_ < 31\n\tqk2 32, b_+4, 72\n\t.endif\n\tdma 1\n\t" \
      ".if j_ == 1\n\ttabl 164, 2\n\ttabl 168, 3\n\ts_add_u32 s40, s40, 4096\n\ts_addc_u32 s41, s41, 0\n\t.endif\n\t" \
      ".if j_ == 2\n\ts_add_u32 s40, s40, 4096\n\ts_addc_u32 s41, s41, 0\n\ttabl 232, 0\n\t.endif\n\t" \
      "xp 116, 52\n\t" \
      ".if j_ < 31\n\tsmf 0, a_+10, 120\n\t.else\n\tsmf 0, 108, 120\n\t.endif\n\tdma 2\n\t" \
      ".if j_ == 1\n\ttabl 172, 0\n\ttabl 220, 1\n\t.endif\n\t" \
      ".if j_ == 9\n\tv_lshrrev_b32 v108, 5, v79\n\tv_mul_u32_u24 v108, 1248, v108\n\ts_mov_b32 m0, s47\n\tv_add_u32 v108, v108, v79\n\t" \
      "global_load_lds_dwordx4 v108, s[76:77]\n\t.endif\n\t" \
      ".if j_ == 24\n\ts_mov_b32 m0, 40960\n\tv_min_u32 v140, 384, v79\n\tv_lshl_add_u32 v140, v140, 3, s46\n\tglobal_load_lds_dword v140, s[54:55] sc1\n\t.endif\n\t" \
      "xp 120, 56\n\t" \
      "pos j_+2\n\t" \
      "smf 16, a_+6, 112\n\t.if j_ < 30 && j_ != 6\n\tds_read_b128 v[a_+6:a_+9], v[ar_+2] offset:of_\n\t.endif\n\txp 124, 60\n\t" \
      ".set j_, j_ + 1\n\t" \
      ".endr\n\t" \
      ".if %c[j1] == 32\n\ts_nop 1\n\tsmf 16, 108, 120\n\ts_nop 15\n\t.endif\n\t" \
      "s_mov_b32 m0, %[keep]\n\t" \
      ".purgem pos\n\t.purgem dma\n\t.purgem flg\n\t.purgem tabl\n\t.purgem old\n\t" AT_ASM_PURGE
#define AT_STAMP
#define AT_STV
#define AT_STV_ARG
#define AT_STV_PASS
#define AT_A_STATE "+{v[0:31]}"(o), "+{v[32:47]}"(s0), "+{v[112:127]}"(pp), "+{v[80:111]}"(fr), "+{v[128:143]}"(ad), [keep] "=&s"(keep)
#define AT_A_CLOB "memory", "scc", "vcc", "s40", "s41", "s48", "v48", "v49", "v50", "v51", "v52", "v53", "v54", "v55", "v56", "v57", "v58", "v59", "v60", "v61", "v62", "v63"
__device__ __forceinline__ void at_a1(f32x32& o, f32x16& s0, f32x16& pp, f32x32& fr, const u32x16& q, u32x16& ad, const char* ws, u32x16 so,
                                      unsigned m0v, unsigned m0b, unsigned long long tag, unsigned flag, unsigned long long fmask,
                                      const char* qB, const void* tab, f32x16& mF, f32x16& mD, u32x8& qv, u32x4& tw,
                                      const char* fsrc, unsigned fl AT_STV_ARG) {
  unsigned keep;
  asm volatile(AT_A_TEXT
      : AT_A_STATE, "=&{v[160:175]}"(mF), "=&{v[220:235]}"(mD), "=&{v[236:243]}"(qv), "=&{v[252:255]}"(tw) AT_STV
      : "{v[64:79]}"(q), "{s[54:55]}"(ws), "{s[56:71]}"(so), "{s72}"(m0v), "{s73}"(m0b), "{s[50:51]}"(tag), "{s52}"(flag), "{s[74:75]}"(fmask),
        "{s[42:43]}"(qB), "{s[44:45]}"(tab), "{s[76:77]}"(fsrc), "{s47}"(fl), [j0] "n"(0), [j1] "n"(12), [obr] "n"(O_BR)
      : AT_A_CLOB);
}
__device__ __forceinline__ void at_a2(f32x32& o, f32x16& s0, f32x16& pp, f32x32& fr, const u32x16& q, u32x16& ad, const char* ws, u32x16 so,
                                      unsigned m0v, unsigned long long tag, unsigned flag, const u32x4& tw, const char* src, unsigned d0,
                                      unsigned d1, unsigned tch, unsigned ea, f32x32& of, f32x8& of2) {
  unsigned keep;
  asm volatile(AT_A_TEXT
      : AT_A_STATE, "=&{v[184:215]}"(of), "=&{v[216:223]}"(of2)
      : "{v[64:79]}"(q), "{s[54:55]}"(ws), "{s[56:71]}"(so), "{s72}"(m0v), "{s[50:51]}"(tag), "{s52}"(flag),
        "{v[252:255]}"(tw), "{s[42:43]}"(src), "{s44}"(d0), "{s45}"(d1), "{s46}"(tch), "{s47}"(ea), [j0] "n"(12), [j1] "n"(32), [obr] "n"(O_BR)
      : AT_A_CLOB);
}

__device__ __forceinline__ void at_b(f32x16& o, f32x16& mF, const f32x16& mD, u32x8& q, unsigned tb, unsigned qt, char* oA, const u32x16& qa,
                                     u32x16& ad, f32x32& k0, f32x16& k1, f32x32& k2, f32x16& k3, unsigned fl, unsigned fd, char* ws) {
  asm volatile(AT_ASM_MACROS
      "v_add_u32 v242, %[tb], v128\n\tv_add_u32 v243, %[tb], v129\n\tv_add_u32 v245, %[tb], v130\n\t"
      "ds_read_b128 v[176:179], v242 offset:7*3584\n\tds_read_b64 v[180:181], v243 offset:7*3584\n\t"
      "ds_read_b128 v[184:187], v242 offset:8*3584\n\tds_read_b64 v[182:183], v243 offset:8*3584\n\t"
      "ds_read_b128 v[192:195], v242\n\tds_read_b64 v[196:197], v243\n\tds_read_b128 v[206:209], v242 offset:3584\n\tds_read_b64 v[210:211], v243 offset:3584\n\t"
      "ds_read_b128 v[198:201], v245\n\tds_read_b128 v[212:215], v245 offset:3584\n\tds_read_b128 v[202:205], v245 offset:1024\n\t"
      "s_mov_b32 exec_lo, 0\n\tv_mov_b64 v[240:241], 0\n\ts_mov_b32 exec_lo, -1\n\tv_mov_b64 v[188:189], -1\n\tv_mov_b64 v[190:191], -1\n\t"
      "s_waitcnt lgkmcnt(7)\n\t"
      "qk1 144, 176, 236\n\tqk2 144, 180, 240\n\tqk1c 48, 184, 236, 220\n\tqk2 48, 182, 240\n\t"
      "global_store_dwordx4 v137, v[188:191], s[42:43] sc1\n\ts_mov_b32 exec_hi, 0\n\tglobal_store_dwordx2 v78, v[188:189], s[42:43] sc1\n\ts_mov_b32 exec_hi, -1\n\t"
      "s_waitcnt lgkmcnt(5)\n\tqk1c 160, 192, 236, 160\n\tds_read_b128 v[192:195], v242 offset:7168\n\ts_cmp_eq_u32 %[qt], 0\n\ts_nop 1\n\t"
      "mx 216, 144\n\tmx 218, 48\n\ts_cbranch_scc1 1f\n\tv_max_f32 v218, v218, v216\n\t1:\n\tv_mov_b32 v219, v218\n\t"
      "v_mov_b64 v[144:145], 0\n\tv_mov_b64 v[146:147], 0\n\tv_permlane32_swap_b32 v218, v219\n\tv_max_f32 v218, v218, v219\n\tv_add_f32 v218, 0x42600000, v218\n\t"
      "s_mov_b32 exec_lo, 0\n\tv_cvt_pk_bf16_f32 v240, -v218, 0\n\ts_mov_b32 exec_lo, -1\n\tv_mov_b64 v[148:149], 0\n\t"
      "qk2 160, 196, 240\n\tds_read_b64 v[196:197], v243 offset:7168\n\t"
      ".set i_, 150\n\t.rept 5\n\tv_mov_b64 v[i_:i_+1], 0\n\t.set i_, i_ + 2\n\t.endr\n\t"
      "s_waitcnt lgkmcnt(2)\n\ts_nop 1\n\t"
      ".set t_, 0\n\t"
      ".rept 9\n\t"
      ".set a_, 192 + 14 * (t_ & 1)\n\t.set b_, 206 - 14 * (t_ & 1)\n\t.set c_, 160 - 112 * (t_ & 1)\n\t.set d_, 48 + 112 * (t_ & 1)\n\t"
      ".if t_ == 0 || t_ == 5\n\ts_setprio 2 - t_ / 5\n\t.endif\n\t"
      ".if t_ < 7\n\tqk1 d_, b_, 236\n\t.elseif t_ == 7\n\tqk1c d_, b_, 236, 220\n\t.endif\n\t"
      ".if t_ < 6\n\tds_read_b128 v[b_:b_+3], v242 offset:(t_+3)*3584\n\t.endif\n\t"
      "xp 176, c_\n\t"
      ".if t_ < 8\n\tqk2 d_, b_+4, 240\n\t.endif\n\t"
      ".if t_ < 6\n\tds_read_b64 v[b_+4:b_+5], v243 offset:(t_+3)*3584\n\t.endif\n\t"
      ".if t_ == 4\n\tv_and_b32 v142, 0xf0, v79\n\tv_lshrrev_b32 v142, 1, v142\n\tv_sub_u32 v142, v79, v142\n\tv_add_u32 v142, %[fl], v142\n\ts_waitcnt vmcnt(6)\n\t"
      "ds_read_b64_tr_b16 v[108:109], v142\n\tds_read_b64_tr_b16 v[110:111], v142 offset:128\n\tv_add_u32 v142, %[fd], v79\n\t.endif\n\t"
      ".if t_ == 6\n\tglobal_store_dwordx4 v142, v[108:111], %[ws] sc1\n\t.endif\n\t"
      "xp 180, c_+4\n\t"
      ".if t_ > 0\n\tsmf 144, b_+10, 184\n\t.endif\n\t"
      ".if t_ < 8\n\tds_read_b128 v[b_+10:b_+13], v245 offset:(t_+1)*3584+1024\n\t.endif\n\t"
      "xp 184, c_+8\n\t"
      "smf 144, a_+6, 176\n\t"
      ".if t_ < 6\n\ts_waitcnt lgkmcnt(3)\n\t.elseif t_ < 8\n\ts_waitcnt lgkmcnt(1)\n\t.else\n\ts_waitcnt lgkmcnt(0)\n\t.endif\n\t"
      ".if t_ < 7\n\tds_read_b128 v[a_+6:a_+9], v245 offset:(t_+2)*3584\n\t.endif\n\t"
      "xp 188, c_+12\n\t"
      ".set t_, t_ + 1\n\t"
      ".endr\n\t"
      "s_nop 1\n\tsmf 144, 202, 184\n\ts_nop 11\n\t"
      AT_ASM_PURGE
      : "=&{v[144:159]}"(o), "+{v[160:175]}"(mF), "+{v[0:31]}"(k0), "+{v[32:47]}"(k1), "+{v[80:111]}"(k2), "+{v[112:127]}"(k3), "+{v[128:143]}"(ad),
        "+{v[236:243]}"(q)
      : "{v[220:235]}"(mD), "{v[64:79]}"(qa), [tb] "s"(tb), [qt] "s"(qt), "{s[42:43]}"(oA), [fl] "s"(fl), [fd] "s"(fd), [ws] "s"(ws)
      : "memory", "scc", "v245", "v48", "v49", "v50", "v51", "v52", "v53", "v54", "v55", "v56", "v57", "v58", "v59", "v60", "v61", "v62", "v63",
        "v176", "v177", "v178", "v179", "v180", "v181", "v182", "v183", "v184", "v185", "v186", "v187", "v188", "v189", "v190", "v191",
        "v192", "v193", "v194", "v195", "v196", "v197", "v198", "v199", "v200", "v201", "v202", "v203", "v204", "v205", "v206", "v207",
        "v208", "v209", "v210", "v211", "v212", "v213", "v214", "v215", "v216", "v217", "v218", "v219");
}

#define PJ_STAMP(i)
#define KLD 5
#define LPK 2
__device__ __forceinline__ void proj_all(const f32x32& of, const f32x8& of2, unsigned l16, const u32x4& tw, const char* src, unsigned pos, unsigned pos2,
                                         unsigned x, unsigned grp, const char* ws, char* out, unsigned long long m5 AT_STV_ARG) {
  f32x32 t0, t1, t2; f32x16 t3, t4, t5; f32x8 t6, t7; f32x4 t8; u32x2 t9;
  asm volatile(
      ".macro rdw k, sp, ab\n\t.set d_, 80 + 20 * ((\\k) & 1)\n\t"
      ".if (\\k) < \\sp\n\t.set a_, \\ab\n\t.set o_, (\\k) * 5120\n\t.else\n\t.set a_, \\ab+1\n\t.set o_, ((\\k) - \\sp) * 5120\n\t.endif\n\t.endm\n\t"
      ".macro rd1 i\n\tds_read_b128 v[d_+4*\\i:d_+4*\\i+3], v[a_] offset:o_+1024*\\i\n\t.endm\n\t"
      ".macro rd5\n\trd1 0\n\trd1 1\n\trd1 2\n\trd1 3\n\trd1 4\n\t.endm\n\t"
      ".macro pm i, ac, of, last\n\t"
      ".if k_ == 0\n\tv_mfma_f32_16x16x32_bf16 v[\\ac+4*\\i:\\ac+4*\\i+3], v[c_+4*\\i:c_+4*\\i+3], v[\\of:\\of+3], 0\n\t"
      ".else\n\tv_mfma_f32_16x16x32_bf16 v[\\ac+4*\\i:\\ac+4*\\i+3], v[c_+4*\\i:c_+4*\\i+3], v[\\of+4*k_:\\of+3+4*k_], v[\\ac+4*\\i:\\ac+4*\\i+3]\n\t.endif\n\t"
      ".if k_ < \\last\n\trd1 \\i\n\ts_waitcnt lgkmcnt(4)\n\t.elseif \\i < 4\n\ts_waitcnt lgkmcnt(3 - \\i)\n\t.endif\n\t.endm\n\t"
      ".macro ld1 q\n\t.if (\\q) >= 0 && (\\q) < 10\n\tv_add_u32 v72, " STR(OCT) "*4*(\\q), v253\n\t"
      "global_load_dwordx4 v[32+4*(\\q):35+4*(\\q)], v72, s[54:55] sc1\n\t.endif\n\t.endm\n\t"
      ".macro wp n, hf\n\t.set n_, \\n\n\t.if n_ < 4 + \\hf\n\t"
      ".if (n_ & 3) == 0\n\ts_add_u32 m0, s[44+8*\\hf], 81920 * (1 - \\hf) + 1024 * n_\n\ts_add_u32 s49, s[44+8*\\hf], 61440 * \\hf + 1024 * n_\n\t"
      "s_add_u32 s40, s42, s49\n\ts_addc_u32 s41, s43, 0\n\t.endif\n\t"
      "global_load_lds_dwordx4 v79, s[40:41] offset:1024*(n_&3) sc1\n\t.endif\n\t.endm\n\t"
      ".macro snd ac, off\n\tds_write_b128 v124, v[\\ac+8:\\ac+11] offset:\\off\n\tds_write_b128 v124, v[\\ac+12:\\ac+15] offset:\\off+1024\n\t"
      "ds_write_b128 v124, v[\\ac+16:\\ac+19] offset:\\off+2048\n\t.endm\n\t"
      ".macro rcv off\n\tds_read_b128 v[128:131], v125 offset:\\off\n\tds_read_b128 v[132:135], v125 offset:\\off+1024\n\tds_read_b128 v[136:139], v125 offset:\\off+2048\n\t.endm\n\t"
      ".macro fin ac, off\n\t"
      "v_pk_add_f32 v[128:129], v[128:129], v[\\ac:\\ac+1]\n\tv_pk_add_f32 v[130:131], v[130:131], v[\\ac+2:\\ac+3]\n\t"
      "v_pk_add_f32 v[132:133], v[132:133], v[\\ac+4:\\ac+5]\n\tv_pk_add_f32 v[134:135], v[134:135], v[\\ac+6:\\ac+7]\n\t"
      "v_cvt_pk_bf16_f32 v140, v128, v129\n\tv_cvt_pk_bf16_f32 v141, v130, v131\n\tv_cvt_pk_bf16_f32 v142, v132, v133\n\tv_cvt_pk_bf16_f32 v143, v134, v135\n\t"
      "v_pk_add_f32 v[136:137], v[136:137], v[\\ac+16:\\ac+17]\n\tv_pk_add_f32 v[138:139], v[138:139], v[\\ac+18:\\ac+19]\n\t"
      "v_permlane16_swap_b32 v140, v142\n\tv_permlane16_swap_b32 v141, v143\n\t"
      "v_cvt_pk_bf16_f32 v136, v136, v137\n\tv_cvt_pk_bf16_f32 v137, v138, v139\n\t"
      "global_store_dwordx4 v254, v[140:143], s[56:57] offset:\\off\n\t"
      "s_mov_b64 exec, s[58:59]\n\tglobal_store_dwordx2 v255, v[136:137], s[56:57] offset:\\off\n\ts_mov_b64 exec, -1\n\t.endm\n\t"
      "s_mul_i32 s48, s46, 51200\n\tv_add_u32 v120, s48, v79\n\trdw 0, 6, 120\n\trd5\n\t"
      "s_mul_i32 s49, s46, 143360 - 30720\n\ts_add_u32 s49, s49, 30720\n\tv_add_u32 v121, s49, v79\n\t"
      "s_mul_i32 s48, s46, 20480\n\tv_add_u32 v123, s48, v79\n\ts_mul_i32 s48, s46, 30720\n\ts_add_u32 s48, s48, 81920\n\tv_add_u32 v122, s48, v79\n\t"
      "s_add_u32 s48, s45, 51200\n\tv_add_u32 v124, s48, v79\n\ts_mul_i32 s49, s46, 24576\n\ts_sub_u32 s49, 12288, s49\n\tv_add_u32 v125, s49, v124\n\t"
      "s_barrier\n\ts_waitcnt lgkmcnt(4)\n\t"
      ".set k_, 0\n\t.rept 10\n\t.set c_, 80 + 20 * (k_ & 1)\n\trdw k_+1, 6, 120\n\t"
      ".if k_ == 0 || k_ == 3 || k_ == 6\n\ts_setprio 2 - k_ / 3\n\t.endif\n\t"
      ".set z_, %c[lpk] * (k_ - %c[kld])\n\t"
      "pm 0, 0, 184, 9\n\twp 2*k_, 0\n\tld1 z_\n\tpm 1, 0, 184, 9\n\t.if %c[lpk] > 3\n\tld1 z_+3\n\t.endif\n\tpm 2, 0, 184, 9\n\twp 2*k_+1, 0\n\tld1 z_+1\n\tpm 3, 0, 184, 9\n\t"
      ".if %c[lpk] > 2\n\tld1 z_+2\n\t.endif\n\tpm 4, 0, 184, 9\n\t"
      ".set k_, k_ + 1\n\t.endr\n\t"
      "s_setprio 3\n\ts_mov_b32 s48, 0x40000\n\ts_waitcnt vmcnt(10)\n\ts_barrier\n\t"
      PJ_STAMP(16)
      "rdw 0, 6, 122\n\trd5\n\tsnd 0, 0\n\twp 0, 1\n\twp 1, 1\n\twp 2, 1\n\twp 3, 1\n\twp 4, 1\n\ts_waitcnt vmcnt(5)\n\t"
      "v_max3_u32 v72, v32, v33, v34\n\t.set q_, 35\n\t.rept 18\n\tv_max3_u32 v72, v72, v[q_], v[q_+1]\n\t.set q_, q_ + 2\n\t.endr\n\t"
      "v_max_u32 v72, v72, v71\n\tv_cmp_eq_u32 vcc, -1, v72\n\ts_nop 0\n\ts_cbranch_vccz 4f\n\t"
      "5:\n\ts_mov_b32 s49, 0\n\t.set q_, 0\n\t.rept 10\n\t"
      "v_max3_u32 v72, v[32+4*q_], v[33+4*q_], v[34+4*q_]\n\tv_max_u32 v72, v72, v[35+4*q_]\n\tv_cmp_eq_u32 vcc, -1, v72\n\t"
      "v_add_u32 v73, " STR(OCT) "*4*q_, v253\n\ts_cbranch_vccz 6f\n\t"
      "global_load_dwordx4 v[32+4*q_:35+4*q_], v73, s[54:55] sc1\n\ts_mov_b32 s49, 1\n\t6:\n\t.set q_, q_ + 1\n\t.endr\n\t"
      "s_cmp_eq_u32 s49, 0\n\ts_cbranch_scc1 4f\n\ts_sub_u32 s48, s48, 1\n\ts_waitcnt vmcnt(0)\n\ts_cbranch_scc0 5b\n\t4:\n\t"
      PJ_STAMP(17)
      "s_waitcnt lgkmcnt(0)\n\t"
      ".set k_, 0\n\t.rept 6\n\t.set c_, 80 + 20 * (k_ & 1)\n\trdw k_+1, 6, 122\n\t"
      "pm 0, 160, 32, 5\n\tpm 1, 160, 32, 5\n\tpm 2, 160, 32, 5\n\tpm 3, 160, 32, 5\n\tpm 4, 160, 32, 5\n\t"
      ".set k_, k_ + 1\n\t.endr\n\t"
      "s_waitcnt vmcnt(0)\n\ts_barrier\n\t"
      PJ_STAMP(18)
      "rcv 0\n\trd5\n\ts_waitcnt lgkmcnt(4)\n\t"
      ".rept 4\n\t.set c_, 80 + 20 * (k_ & 1)\n\trdw k_+1, 6, 122\n\t"
      "pm 0, 160, 32, 9\n\tpm 1, 160, 32, 9\n\tpm 2, 160, 32, 9\n\tpm 3, 160, 32, 9\n\tpm 4, 160, 32, 9\n\t"
      ".if k_ == 6\n\tfin 0, 1280\n\t.endif\n\t"
      ".set k_, k_ + 1\n\t.endr\n\t"
      "s_nop 6\n\tsnd 160, 86016-51200\n\ts_waitcnt lgkmcnt(0)\n\ts_barrier\n\t"
      PJ_STAMP(20)
      "rcv 86016-51200\n\ts_waitcnt lgkmcnt(0)\n\tfin 160, 0\n\t"
      ".purgem rdw\n\t.purgem rd1\n\t.purgem rd5\n\t.purgem pm\n\t.purgem ld1\n\t.purgem wp\n\t.purgem snd\n\t.purgem rcv\n\t.purgem fin\n\t"
      : "=&{v[0:31]}"(t0), "=&{v[32:63]}"(t1), "=&{v[80:111]}"(t2), "=&{v[112:127]}"(t3), "=&{v[128:143]}"(t4), "=&{v[160:175]}"(t5),
        "=&{v[64:71]}"(t6), "=&{v[176:183]}"(t7), "=&{v[72:73]}"(t9) AT_STV
      : "{v[184:215]}"(of), "{v[216:223]}"(of2), "{v79}"(l16), "{v[252:255]}"(tw), "{s[42:43]}"(src), "{s44}"(pos), "{s52}"(pos2), "{s45}"(x), "{s46}"(grp),
        "{s[54:55]}"(ws), "{s[56:57]}"(out), "{s[58:59]}"(m5), [kld] "n"(KLD), [lpk] "n"(LPK)
      : "memory", "scc", "vcc", "s40", "s41", "s48", "s49");
}
typedef __attribute__((address_space(3))) f32x4* lds_f4;

struct LaneTab { unsigned v[9][64][4]; };
constexpr LaneTab make_tab() {
  LaneTab t = {};
  for (unsigned l = 0; l < 64; ++l) {
    const unsigned g = l >> 4;
    t.v[0][l][0] = (l & 15) * 64 * 128;
    t.v[0][l][1] = g * OCT + (l & 15) * 16;
    t.v[0][l][2] = (l & 15) * 2560 + (g & 1) * 32 + (g >> 1) * 16;
    t.v[0][l][3] = (l & 15) * 2560 + 128 + g * 8;
    for (unsigned r = 0; r < 16; ++r) {
      const unsigned key = (r & 3) + 8 * (r >> 2) + 4 * (l >> 5);
      t.v[1 + (r >> 2)][l][r & 3] = key > (l & 31) ? 0u : 0xFF800000u;
      t.v[5 + (r >> 2)][l][r & 3] = key <= (l & 31) ? 0u : 0xFF800000u;
    }
  }
  return t;
}
__device__ const LaneTab lane_tab = make_tab();
constexpr unsigned CODE_B = 34816;

__global__ void __launch_bounds__(512, 2)
k_fused(const char* __restrict__ xa_, const char* __restrict__ wa_, const char* __restrict__ woa_, const char* __restrict__ xb_,
        const char* __restrict__ wb_, const char* __restrict__ wob_, char* __restrict__ ws_, char* __restrict__ out_) {
  __shared__ __attribute__((aligned(16))) char lds[163840];
  const unsigned bid = blockIdx.x, xcd = bid & 7, slot = bid >> 3, br = xcd >> 2;
  u32x4 ka; u32x2 kb; u32x8 kc;
  {
    const void* kp = (const void*)__builtin_amdgcn_kernarg_segment_ptr();
    asm volatile("s_load_dwordx4 %0, %3, %4\n\ts_load_dwordx8 %2, %3, 0x20\n\ts_load_dwordx2 %1, %3, 0x10"
                 : "=&s"(ka), "=&s"(kb), "=&s"(kc) : "s"(kp), "s"(br * 24));
  }
  STAMP_DECL STAMP(0);
  unsigned lane = threadIdx.x & 63;
  const unsigned w = __builtin_amdgcn_readfirstlane(threadIdx.x >> 6);
  const unsigned grp = w >> 2, lw = w & 3;
  const unsigned mt = (xcd & 3) * 4 + (slot & 3), u = slot >> 2;
  const unsigned win = (5 * ((mt >> 1) & 3)) >> 1;
  const unsigned kv1 = (u + 3) >> 3;
  const unsigned j1 = win + u - 5;
#define ROT ((5 * (u & 3)) / 2 + 5 * (slot & 1) + 3 * ((xcd >> 1) & 1) + (xcd & 1))
  auto mod10 = [](unsigned x) { return __builtin_elementwise_min(x, x - 10); };
  const unsigned rot = mod10(ROT);
  const unsigned m0v = w * 4096;
  u32x4 o1; unsigned tw;
  {
    const unsigned row = lane >> 3, pos = lane & 7;
    tw = row * 3840 + ((pos ^ (row & 2)) * 16);
    o1[0] = row * 1280 + ((pos ^ (row >> 1)) * 16);
    o1[1] = (o1[0] ^ 64) + (8 * 1280 - 1024);
    o1[2] = (tw ^ ((w & 1) * 64)) + 1024;
    o1[3] = (tw ^ ((w & 1) * 64)) + (128 + kv1 * 1152);
  }
  const unsigned offX = (mt * 128 + 16 * w) * 1280, offW = 8 * w * 3840 + (kv1 ? 1280 + j1 * 128 : u * 256);
  asm volatile("s_waitcnt lgkmcnt(0)" : "+s"(ka), "+s"(kb), "+s"(kc));
  auto ptr = [](unsigned lo, unsigned hi) { return (char*)(((unsigned long long)hi << 32) | lo); };
  const char* sX = ptr(ka[0], ka[1]) + offX;
  const char* sW = ptr(ka[2], ka[3]) + (offW - 1024ll);
  char* const ws = ptr(kc[4], kc[5]);
  glds_xw(sX, sW, rot, o1, w * 2048);
  u32x2 ep; unsigned tch[4];
  asm volatile("s_load_dwordx2 %0, %1, %2" : "=&s"(ep) : "s"(ws), "s"((unsigned)CTL_WS));
  {
    const unsigned i64 = (slot * 8 + w) * 64;
    unsigned long long pc;
    asm volatile("s_getpc_b64 %0\n\ts_load_dword %1, %0, %5\n\ts_load_dword %2, %0, %6\n\ts_load_dword %3, %0, %7\n\ts_load_dword %4, %8, %9"
                 : "=&s"(pc), "=&s"(tch[0]), "=&s"(tch[1]), "=&s"(tch[2]), "=&s"(tch[3])
                 : "s"(i64), "s"(i64 + 16384), "s"(__builtin_elementwise_min(i64 + 32768, CODE_B)), "s"(&lane_tab),
                   "s"(__builtin_elementwise_min(i64, 9152u)));
  }
  const unsigned j2 = mod10(win + 3 + u);
  const unsigned Tg = mt * 4 + lw;
  const unsigned vt1 = grp & kv1, vt2 = u < 7 ? grp : 2;
  const unsigned cg1 = kv1 ? 10 + 10 * grp + j1 : 2 * u + grp, cg2 = 10 + 10 * grp + j2;
  const unsigned head = xcd * 4 + (slot & 3), qb = u;
  const unsigned odd = head & 1, o2 = 5 * (head >> 1) + 2, o1h = odd ? o2 + 1 : o2 - 2;
  const unsigned qt = qb * 8 + 2 * lw + grp;
  u32x4 soA; u32x16 so;
  {
    const unsigned kb0 = 80 * OCT + w * 512, vb0 = (unsigned)V_WS - V_OFF + xcd * (256 * 1280) + w * 5120;
    unsigned ko[10], vo_[10];
#pragma unroll
    for (int i = 0; i < 8; ++i) { const unsigned c = (((head >> 3) + (i >> 1)) & 3) + 4 * (i & 1); ko[i] = kb0 + c * 4096; vo_[i] = vb0 + c * 40960; }
    const unsigned c8 = qb ? qb - 1 : 0;
    ko[8] = kb0 + (unsigned)QK_BR + c8 * 4096; vo_[8] = vb0 + (unsigned)V_BR + c8 * 40960;
    ko[9] = kb0 + (unsigned)QK_BR + qb * 4096; vo_[9] = vb0 + (unsigned)V_BR + qb * 40960;
    soA = u32x4{ko[0], vo_[0], ko[1], vo_[1]};
    so = u32x16{ko[2], vo_[2], ko[8], vo_[8], ko[9], vo_[9], ko[3], vo_[3], ko[4], vo_[4], ko[5], vo_[5], ko[6], vo_[6], ko[7], vo_[7]};
    asm volatile("" : "+{s[76:79]}"(soA), "+{s[56:71]}"(so));
  }
  unsigned flag1 = FQ_OFF + ((br * 30 + cg1) * 64 + Tg) * 128, flag2 = FQ_OFF + ((br * 30 + cg2) * 64 + Tg) * 128;
  unsigned flagO = FO_OFF + (head * 64 + qt) * 128;
  PINS(flag1); PINS(flag2); PINS(flagO);
  glds_xw(sX, sW, mod10(rot + 1), o1, w * 2048 + 32768);
  const char* sB = ptr(ka[2], ka[3]) + (16 * lw * 3840 + 256 + j2 * 128);
  const unsigned mytile = w * TILE_B;
  const char* fsrc; unsigned fdst;
  {
    const unsigned F = __builtin_elementwise_min((head * 8 + qb) * 8 + w, 1599u);
    const unsigned bl = (F * 13108) >> 16, np = F - 5 * bl, t = (bl * 3277) >> 16, ps = bl - 20 * t;
    const unsigned ks = ((t & 1) || ps < 6 || ps >= 16) ? ps : (ps < 12 ? ps + 4 : ps - 6);
    fsrc = ptr((t & 1) ? kc[2] : kb[0], (t & 1) ? kc[3] : kb[1]) + (ks * (32 * 1280) + (t >> 1) * 160 + ((ks >= 10 && np < 4) ? np ^ 2 : np) * 32);
    fdst = (unsigned)WP_WS + ((head * 8 + qb) * 8 + w) * 1024;
  }
  u32x4 late = {FO_OFF + (grp * 16 * 64 + head * 2 + (lw >> 1)) * 128,
                (unsigned)O_WS + grp * 40 * OCT + (head * 64 + lw * 16) * 16,
                (unsigned)WP_WS + qb * 204800,
                (head * 64 + lw * 16) * 2560 + qb * 160};
  asm volatile("" : "+s"(fsrc), "+s"(fdst), "+s"(late));
  u32x16 qq, pk, at;
  {
    const unsigned r16 = lane & 15, g = lane >> 4, q = r16 >> 2, p = lane & 3, r32 = lane & 31, h = lane >> 5;
    const unsigned xa0 = (lw * 32 + r16) * 128 + ((g ^ (r16 >> 1)) * 16);
    const unsigned wa0 = 2048 * g + 1024 * grp + 128 * q + 16 * ((p >> 1) ^ (4 * (g & 1) + (q & 2))) + 8 * (p & 1);
    const unsigned jj = (20 * head + 19 * (lane & 1)) >> 6, b1 = (lane >> 1) & 1, c = (lane >> 2) & 7;
    const unsigned pol1 = FQ_OFF + (jj * 64 + ((lane & 8) ? (qt ^ b1) : b1 * 640 + (lane & 4) * 8 + (640 + (head >> 3) * 8 + w))) * 128;
    qq = u32x16{o1[0], o1[1], o1[2], o1[3], tw + 1024, tw + 1280, (tw ^ 64) + (8 * 3840 - 1024), (tw ^ 64) + (8 * 3840 - 2048 + 1280),
                pol1, xa0, xa0 ^ 64, xa0, wa0, wa0 ^ 32, wa0 ^ 64, wa0 ^ 96};
    const unsigned cB = __builtin_elementwise_max((c & 1) + qb, 1u) + (30 * 8 - 1);
    const unsigned pol2 = (jj * 64 + (lane >= 48 ? 30 * 64 + qt : b1 * 640 + (h ? cB : c) * 8 + (640 + w))) * 128 + FQ_OFF;
    const u32x2 s1 = g1_offs(br, cg1, Tg, lane), s2 = g1_offs(br, cg2, Tg, lane);
    const unsigned ab = grp * TILE_B;
    pk = u32x16{s1[0], s1[1], s2[0], s2[1],
                lane * 16 + ab,
                h ? (V_OFF + 320 + ab) : r32 * 16 + (K2_OFF + odd * 8 + ab),
                h * 512 + (r32 < 28 ? __builtin_elementwise_min(r32, 21u) : 28) * 16 + (V_OFF + ab),
                h * OCT + r32 * 16 + o1h * OCT,
                r32 * 16 + (o2 * OCT - 1024),
                h * 1280 + (__builtin_elementwise_min(r32 + 4 * odd, r32 + 4 * odd - 20) + (head & 3) * 20) * 16,
                pol2,
                g * 512 + __builtin_elementwise_min(r16, 11u) * 16 + (mytile + V_OFF + 320),
                r16 == 0 ? 0x3F803F80u : 0u,
                r32 * 16 + (o2 * OCT + odd * 8),
                lane * 512 + (3 * CHUNK_B + V_OFF + 320 + mytile),
                lane};
  }
  asm volatile("" : "+{v[64:79]}"(qq), "+{v[80:95]}"(pk) :: "memory");
  glds_xw(sX, sW, mod10(rot + 2), u32x4{qq[0], qq[1], qq[2], qq[3]}, w * 2048 + 65536);
  asm volatile("s_waitcnt vmcnt(8) lgkmcnt(0)\n\ts_barrier" ::: "memory");
  PINS(ep); PINS(tch[0]); PINS(tch[1]); PINS(tch[2]); PINS(tch[3]);
  const unsigned long long tag = (((unsigned long long)ep[1] << 32) | ep[0]) + 1;
  STAMP(1);
  u32x8 cv; f32x32 oab; f32x16 pp;
  STV_PUT(pk[15]);
  g1_all(vt1, kv1, vt2, qq, pk, at, cv, oab, pp, sX, sW, mod10(rot + 3) * 128, sB, mod10(rot + grp) * (64 * 3840), m0v, tag, flag1, ws, mytile, soA,
         qt * 512);
  STV_GET(pk[15]);
  STAMP(8);
  if (bid == 255 && grp == 0) {
    const unsigned e = w * 64 + lane_id(), eu = e & 7, em = (e >> 3) & 15;
    poll_flags(ws, FQ_OFF + (((e >> 7) * 30 + (eu < 5 ? 2 * eu : 5 + eu + ((5 * ((em >> 1) & 3)) >> 1))) * 64 + 4 * em) * 128, tag);
  }
  BARRIER();
  STAMP(9);
  if (bid == 255 && w == 0) put_flag(ws, CTL_WS, tag);
  const f32x16 zero16 = {};
  auto exact = [&](int b, bf16x8 q1, s16x4 q2, int t0, int t1) {
    __builtin_amdgcn_fence(__ATOMIC_ACQUIRE, "agent");
    const int ln = lane_id(), r32 = ln & 31, h = ln >> 5;
    unsigned head = xcd * 4 + (slot & 3);
    PINS(head);
    const unsigned odd = head & 1, o2 = 5 * (head >> 1) + 2, o1h = odd ? o2 + 1 : o2 - 2;
    if (h) q2 = s16x4{0, 0, 0, 0};
    typedef __attribute__((address_space(1))) const char* gptr;
    gptr kg1 = (gptr)ws + QK_WS + b * QK_BR + (size_t)(80 + o1h + h) * OCT + r32 * 16;
    gptr kg2 = (gptr)ws + QK_WS + b * QK_BR + (size_t)(80 + o2) * OCT + r32 * 16 + odd * 8;
    gptr vg = (gptr)ws + V_WS + b * V_BR + (head >> 2) * (256 * 1280) + h * 1280 +
                     ((head & 3) * 20 + (odd ? (r32 < 16 ? r32 + 4 : r32 - 16) : r32)) * 16;
    const u32x4 vc = r32 == 20 ? u32x4{0x3F803F80u, 0x3F803F80u, 0x3F803F80u, 0x3F803F80u} : u32x4{0u, 0u, 0u, 0u};
    float m = -INFINITY;
    f32x16 r = zero16;
#pragma unroll 1
    for (int ps = 0; ps < 2; ++ps) {
#pragma unroll 1
      for (int t = t0; t <= t1; ++t) {
        f32x16 s = MFMA32(*(__attribute__((address_space(1))) const bf16x8*)(kg1 + t * 512), q1, zero16);
        s = MFMA32K8(*(__attribute__((address_space(1))) const s16x4*)(kg2 + t * 512), q2, s);
        if (b) {
          const int d = (t1 - t) * 32 + r32 - 4 * h;
#pragma unroll
          for (int e = 0; e < 16; ++e) s[e] = (unsigned)(d - ((e & 3) + 8 * (e >> 2))) < 256u ? s[e] : -INFINITY;
        }
        if (ps == 0) { m = max16(s, m); continue; }
#pragma unroll
        for (int e = 0; e < 16; ++e) s[e] = __builtin_amdgcn_exp2f(s[e] - m);
        u32x4 va = vc, vb = vc;
        if (r32 < 20) { va = *(__attribute__((address_space(1))) const u32x4*)(vg + t * 5120); vb = *(__attribute__((address_space(1))) const u32x4*)(vg + t * 5120 + 2560); }
        r = smfmac(__builtin_bit_cast(bf16x8, va), lo8(s), r);
        r = smfmac(__builtin_bit_cast(bf16x8, vb), hi8(s), r);
      }
      m = xhalf_max(m);
    }
    return r;
  };
  auto unsafe = [&](const f32x16& r, float l) {
    float amax = 0.f;
#pragma unroll
    for (int e = 0; e < 12; ++e) amax = __builtin_fmaxf(amax, __builtin_fabsf(r[e]));
    return __any(!(l <= 1.2980742e33f) || !(l >= 8.4703295e-22f) || !(amax <= 3.0e38f));
  };
  char* const oA = ws + (O_WS + qt * 512);
  f32x32 of; f32x8 of2;
  u32x4 lt;
  {
    f32x16 s0;
    f32x32 fr;
    asm volatile(AT_ASM_MACROS
        "v_subrev_u32 v108, %[ab], v129\n\tds_read_b128 v[112:115], v79\n\tds_read_b64 v[116:117], v108\n\t"
        "ds_read_b128 v[80:83], v128\n\tds_read_b64 v[84:85], v129\n\tds_read_b128 v[94:97], v128 offset:7168\n\tds_read_b64 v[98:99], v129 offset:7168\n\t"
        "ds_read_b128 v[86:89], v130\n\tds_read_b128 v[100:103], v130 offset:7168\n\tds_read_b128 v[90:93], v130 offset:1024\n\t"
        "s_mov_b32 exec_lo, 0\n\tv_mov_b64 v[72:73], 0\n\tv_mov_b64 v[74:75], 0\n\ts_mov_b32 exec_lo, -1\n\t"
        "s_waitcnt lgkmcnt(7)\n\t"
        "qk1 32, 112, 64\n\tqk2 32, 116, 72\n\tqk1 48, 112, 68\n\tqk2 48, 116, 74\n\t"
        "ds_write_b128 v148, v[144:147]\n\tds_write_b128 v148, v[144:147] offset:28672\n\tds_write_b128 v148, v[144:147] offset:57344\n\t"
        "v_mov_b64 v[104:105], 0\n\tv_mov_b64 v[106:107], 0\n\tv_add_u32 v148, %[zt], v79\n\ts_cmp_lg_u32 %[qb], 0\n\ts_cbranch_scc1 1f\n\t"
        "ds_write_b128 v148, v[104:107]\n\tds_write_b128 v148, v[104:107] offset:1024\n\tds_write_b128 v148, v[104:107] offset:2048\n\t"
        "ds_write_b128 v148, v[104:107] offset:2560\n\t1:\n\ts_nop 1\n\t"
        "mx 108, 32\n\tv_mov_b32 v109, v108\n\tmx 110, 48\n\tv_mov_b32 v111, v110\n\t"
        "v_permlane32_swap_b32 v108, v109\n\ts_waitcnt lgkmcnt(0)\n\tqk1 32, 80, 64\n\tv_permlane32_swap_b32 v110, v111\n\t"
        "v_max_f32 v108, v108, v109\n\tv_max_f32 v110, v110, v111\n\tv_add_f32 v108, 0x42600000, v108\n\tv_add_f32 v110, 0x42600000, v110\n\t"
        "s_mov_b32 exec_lo, 0\n\tv_cvt_pk_bf16_f32 v72, -v108, 0\n\tv_cvt_pk_bf16_f32 v74, -v110, 0\n\ts_mov_b32 exec_lo, -1\n\ts_nop 0\n\t"
        "qk2 32, 84, 72\n\ts_nop 8\n\t"
        AT_ASM_PURGE
        : "+{v[112:127]}"(pp), "=&{v[32:47]}"(s0), "=&{v[80:111]}"(fr), "+{v[64:79]}"(qq), "+{v[128:143]}"(at), "+{v[144:151]}"(cv)
        : [ab] "s"(grp * TILE_B), [zt] "s"(3 * CHUNK_B + mytile), [qb] "s"(qb)
        : "memory", "scc", "v48", "v49", "v50", "v51", "v52", "v53", "v54", "v55", "v56", "v57", "v58", "v59", "v60", "v61", "v62", "v63");
    STAMP(10);
    f32x16 maskF, maskD; u32x8 qv;
    at_a1(oab, s0, pp, fr, qq, at, ws, so, mytile, mytile + (qb ? 3 : 4) * CHUNK_B, tag, flag2, (0x7fu >> u) & 1u, ws + (QK_BR + qt * 512),
          &lane_tab, maskF, maskD, qv, lt, fsrc, 143360 + w * 1024 AT_STV_PASS);
    STAMP(11);
    {
      f32x16 ob;
      at_b(ob, maskF, maskD, qv, 3 * CHUNK_B + 2 * lw * TILE_B, qt, oA, qq, at, oab, s0, fr, pp, 143360 + w * 1024, fdst, ws);
      lt[0] += late[0]; lt[1] += late[1]; lt[2] += grp * 64;
      float l = row_sum(ob);
      if (__builtin_expect(unsafe(ob, l), 0)) {
        ob = exact(1, __builtin_bit_cast(bf16x8, u32x4{qv[0], qv[1], qv[2], qv[3]}), __builtin_bit_cast(s16x4, u32x2{qv[4], qv[5]}),
                   qb ? qt - 8 : 0, qt);
        l = row_sum(ob);
      }
      store_o<false>(ob, l, oA + O_BR, at[9], qq[14]);
    }
    STAMP(13);
    at[13] = __builtin_elementwise_min(qq[15], 24u * 16) * 64 + (FO_OFF + qb * 25 * 1024);
    at_a2(oab, s0, pp, fr, qq, at, ws, so, mytile, tag, flagO, lt, ws + (late[2] + 102400), w * 5120,
          81920 + __builtin_elementwise_min(3 * w, 17u) * 1024, late[2] + ((slot & 3) * 8 + w) * 3200, w * 4096, of, of2);
  }
  STAMP(12);
  {
    lds_ptr xb = (lds_ptr)lds + 4 * CHUNK_B + qq[15];
#pragma unroll
    for (int i = 0; i < 3; ++i) *(lds_f4)(xb + w * 3072 + i * 1024) = f32x4{oab[16 + 4 * i], oab[17 + 4 * i], oab[18 + 4 * i], oab[19 + 4 * i]};
    asm volatile("s_setprio 3\n\ts_waitcnt vmcnt(4) lgkmcnt(0)\n\ts_barrier" : "+{v[184:215]}"(of), "+{v[216:223]}"(of2) :: "memory");
    f32x16 o = {};
#pragma unroll
    for (int i = 0; i < 3; ++i) {
      const f32x4 r = *(lds_f4)(xb + (w ^ 4) * 3072 + i * 1024);
#pragma unroll
      for (int e = 0; e < 4; ++e) o[4 * i + e] = oab[4 * i + e] + r[e];
    }
    float l = row_sum(o);
    if (__builtin_expect(unsafe(o, l), 0)) {
      o = exact(0, __builtin_bit_cast(bf16x8, u32x4{qq[0], qq[1], qq[2], qq[3]}), __builtin_bit_cast(s16x4, u32x2{qq[8], qq[9]}), 0, 63);
      l = row_sum(o);
    }
    store_o<true>(o, l, oA, at[9], qq[14]);
  }
  STAMP(14);
  {
    proj_all(of, of2, qq[15], lt, ws + late[2], 32768 + __builtin_elementwise_min(w, 6u) * 4096, w * 5120, w * 3072, grp, ws, ptr(kc[6], kc[7]) + late[3],
             0xffffffffull << (32 * grp) AT_STV_PASS);
  }
  STAMP_OUT(ws, 0);
}

extern "C" void kernel_launch(void* const* d_in, const int* in_sizes, int n_in, void* d_out, int out_size, void* d_ws,
                              size_t ws_size, hipStream_t stream) {
  k_fused<<<256, 512, 0, stream>>>((const char*)d_in[0], (const char*)d_in[1], (const char*)d_in[2], (const char*)d_in[3],
                                   (const char*)d_in[4], (const char*)d_in[5], (char*)d_ws, (char*)d_out);
}
```
